```python
import torch

def _softmax(x, axis=-1):
    return torch.softmax(x, dim=axis)
def _gelu(x):
    return torch.nn.functional.gelu(x, approximate="tanh")
def _silu(x):
    return torch.nn.functional.silu(x)
def _sigmoid(x):
    return torch.sigmoid(x)
def _layernorm(x, axis=-1, eps=1e-5):
    m = x.mean(dim=axis, keepdim=True)
    v = x.var(dim=axis, keepdim=True, unbiased=False)
    return (x - m) / torch.sqrt(v + eps)
def _scatter_add(tgt, idx, src, axis=0):
    return tgt.clone().index_add_(axis, idx, src)
def _segment_sum(src, seg, n):
    out = torch.zeros((n,) + src.shape[1:], dtype=src.dtype)
    return out.index_add_(0, seg, src)
def _pad(x, pw):
    flat = tuple(v for pair in reversed(pw) for v in pair)
    return torch.nn.functional.pad(x, flat)

kernel_name = 'parallel_sum__compressed_kv_cla__mixer_token_mlp__prompt'

def setup_inputs():

    _w = (lambda a: (lambda t: t.to(torch.bfloat16) if t.is_floating_point() else t)(torch.as_tensor(a)))
    def _A_setup():
        B, H, Sq, Sk, D, M = 1, 32, 2048, 2048, 128, 640
        Dc = 4 * D
        return {
            "x": _w((lambda *s: torch.randn(*s, dtype=torch.bfloat16) * 0.05)(B, Sq, M)),
            "c_kv": _w((lambda *s: torch.randn(*s, dtype=torch.bfloat16) * 0.2)(B, Sk, Dc)),
            "w_q": _w((lambda *s: torch.randn(*s, dtype=torch.bfloat16) * 6.4)(M, H, D) * (M ** -0.5)),
            "w_uk": _w((lambda *s: torch.randn(*s, dtype=torch.bfloat16) * 6.4)(Dc, H, D) * (Dc ** -0.5)),
            "w_uv": _w((lambda *s: torch.randn(*s, dtype=torch.bfloat16) * 0.2)(Dc, H, D) * (Dc ** -0.5)),
            "w_o": _w((lambda *s: torch.randn(*s, dtype=torch.bfloat16) * 0.2)(H, D, M) * ((H * D) ** -0.5)),
        }
    _A_in = _A_setup()

    def _B_setup():
        B, S, D, F = 1, 2048, 640, 1728
        return {
            "x": _w((lambda *s: torch.randn(*s, dtype=torch.bfloat16) * 0.05)(B, S, D)),
            "w1": _w((lambda *s: torch.randn(*s, dtype=torch.bfloat16) * 0.4)(S, F) * (S ** -0.5)),
            "b1": _w((lambda *s: torch.randn(*s, dtype=torch.bfloat16) * 3.2)(F) * 0.02),
            "w2": _w((lambda *s: torch.randn(*s, dtype=torch.bfloat16) * 0.2)(F, S) * (F ** -0.5)),
            "b2": _w((lambda *s: torch.randn(*s, dtype=torch.bfloat16) * 0.8)(S) * 0.02),
        }
    _B_in = _B_setup()

    return {
        "A_x": _A_in["x"],
        "A_c_kv": _A_in["c_kv"],
        "A_w_q": _A_in["w_q"],
        "A_w_uk": _A_in["w_uk"],
        "A_w_uv": _A_in["w_uv"],
        "A_w_o": _A_in["w_o"],
        "B_x": _B_in["x"],
        "B_w1": _B_in["w1"],
        "B_b1": _B_in["b1"],
        "B_w2": _B_in["w2"],
        "B_b2": _B_in["b2"],
    }

def reference(A_x, A_c_kv, A_w_q, A_w_uk, A_w_uv, A_w_o, B_x, B_w1, B_b1, B_w2, B_b2):
    def _A_ref(x, c_kv, w_q, w_uk, w_uv, w_o):
        B, Sq, M = x.shape[0], x.shape[1], x.shape[2]
        Sk = c_kv.shape[1]
        H, D = w_q.shape[1], w_q.shape[2]
        q = torch.einsum("bqm,mhd->bhqd", x, w_q)
        k = torch.einsum("bkc,chd->bhkd", c_kv, w_uk)
        v = torch.einsum("bkc,chd->bhkd", c_kv, w_uv)
        scores = torch.einsum("bhqd,bhkd->bhqk", q, k) * (D ** -0.5)
        off = Sk - Sq
        qi = torch.arange(Sq)[:, None]
        kj = torch.arange(Sk)[None, :]
        cmask = kj <= (qi + off)
        scores = torch.where(cmask[None, None], scores, -1e9)
        p = _softmax(scores, axis=-1)
        ctx = torch.einsum("bhqk,bhkd->bhqd", p, v)
        return torch.einsum("bhqd,hdm->bqm", ctx, w_o)

    def _B_ref(x, w1, b1, w2, b2):
        B, S, D = x.shape
        h = _layernorm(x, axis=-1)
        u = torch.einsum("bsd,sf->bdf", h, w1) + b1
        u = _gelu(u)
        y = torch.einsum("bdf,fs->bds", u, w2) + b2
        return (lambda x, *d: x.permute(*d))(y, 0, 2, 1)

    x = A_x
    xn = _layernorm(x, axis=-1)
    return x + _A_ref(xn, A_c_kv, A_w_q, A_w_uk, A_w_uv, A_w_o) + _B_ref(xn, B_w1, B_b1, B_w2, B_b2)
```

```cpp
#include <hip/hip_runtime.h>
#include <hip/hip_bf16.h>
#include <stdint.h>

constexpr int S_LEN = 2048;
constexpr int DM = 640;
constexpr int NH = 32;
constexpr int HD = 128;
constexpr int DC = 512;
constexpr int FF = 1728;
constexpr int HDN = NH * HD;

typedef unsigned short bf16_t;
typedef short bf16x8 __attribute__((ext_vector_type(8)));
typedef short s16x4 __attribute__((ext_vector_type(4)));
typedef float f32x16 __attribute__((ext_vector_type(16)));
typedef float f32x4 __attribute__((ext_vector_type(4)));
typedef unsigned u32x4 __attribute__((ext_vector_type(4)));
typedef unsigned u32x2 __attribute__((ext_vector_type(2)));
typedef __bf16 bf16x2_t __attribute__((ext_vector_type(2)));
#define LAS __attribute__((address_space(3)))

__device__ __forceinline__ float bf2f(bf16_t v) { return __uint_as_float(((unsigned)v) << 16); }
__device__ __forceinline__ unsigned pack2(float a, float b) {
  bf16x2_t v = {(__bf16)a, (__bf16)b};
  return *reinterpret_cast<unsigned*>(&v);
}
__device__ __forceinline__ float rbf(float a) {
  __bf16 v = (__bf16)a;
  return __uint_as_float(((unsigned)*reinterpret_cast<unsigned short*>(&v)) << 16);
}

constexpr size_t WS_XN = 0;
constexpr size_t WS_H = WS_XN + (size_t)S_LEN * DM * 2;
constexpr size_t WS_Q = WS_H + (size_t)S_LEN * DM * 2;
constexpr size_t WS_K = WS_Q + (size_t)S_LEN * HDN * 2;
constexpr size_t WS_V = WS_K + (size_t)S_LEN * HDN * 2;
constexpr size_t WS_U = WS_V + (size_t)S_LEN * HDN * 2;
constexpr size_t WS_CTX = WS_U + (size_t)DM * FF * 2;
constexpr size_t WS_PART = WS_CTX + (size_t)S_LEN * HDN * 2;

__device__ __forceinline__ float wave_sum(float v) {
#pragma unroll
  for (int o = 32; o > 0; o >>= 1) v += __shfl_xor(v, o);
  return v;
}
__device__ __forceinline__ void ln_row(float (&v)[10]) {
  float s = 0.f;
#pragma unroll
  for (int i = 0; i < 10; ++i) s += v[i];
  s = wave_sum(s);
  const float mean = s * (1.0f / DM);
  float q = 0.f;
#pragma unroll
  for (int i = 0; i < 10; ++i) { float d = v[i] - mean; q += d * d; }
  q = wave_sum(q);
  const float mb = rbf(mean);
  const float vb = rbf(q * (1.0f / DM));
  const float den = rbf(sqrtf(rbf(vb + 1e-5f)));
#pragma unroll
  for (int i = 0; i < 10; ++i) v[i] = rbf(rbf(v[i] - mb) / den);
}
__global__ __launch_bounds__(256) void ln_kernel(const bf16_t* __restrict__ x, bf16_t* __restrict__ xn,
                                                 bf16_t* __restrict__ h) {
  const int lane = threadIdx.x & 63, w = threadIdx.x >> 6;
  const int row = blockIdx.x * 4 + w;
  const unsigned* xr = reinterpret_cast<const unsigned*>(x + (size_t)row * DM);
  float v[10];
#pragma unroll
  for (int j = 0; j < 5; ++j) {
    unsigned u = xr[lane + 64 * j];
    v[2 * j] = __uint_as_float(u << 16);
    v[2 * j + 1] = __uint_as_float(u & 0xffff0000u);
  }
  ln_row(v);
  unsigned* xo = reinterpret_cast<unsigned*>(xn + (size_t)row * DM);
#pragma unroll
  for (int j = 0; j < 5; ++j) xo[lane + 64 * j] = pack2(v[2 * j], v[2 * j + 1]);
  ln_row(v);
  unsigned* ho = reinterpret_cast<unsigned*>(h + (size_t)row * DM);
#pragma unroll
  for (int j = 0; j < 5; ++j) ho[lane + 64 * j] = pack2(v[2 * j], v[2 * j + 1]);
}

constexpr int STAGE_BYTES = 49152;
constexpr int RBASE = 32768;

__device__ __forceinline__ int swapmid(int p) { return ((p & 1) << 1) | ((p >> 1) & 1); }
__device__ __forceinline__ int rperm(int r) {
  int g = r >> 2;
  int pg = (g & 4) | swapmid(g & 3);
  return pg * 4 + (r & 3);
}
__device__ __forceinline__ s16x4 tr_read(const char LAS* p) {
  return __builtin_amdgcn_ds_read_tr16_b64_v4i16((LAS s16x4*)p);
}
__device__ __forceinline__ bf16x8 cat8(s16x4 a, s16x4 b) {
  return (bf16x8){a[0], a[1], a[2], a[3], b[0], b[1], b[2], b[3]};
}

template <bool P_TR, bool R_TR, bool ROWS_P>
struct GemmCore {
  int pOff[4], rOff[2];
  int pAddr[4], rAddr[4];
  int w, l;
  __device__ __forceinline__ void init(int ldp, int ldr, int pcol_limit  ) {
    const int tid = threadIdx.x;
    l = tid & 63;
    w = __builtin_amdgcn_readfirstlane(tid >> 6);
    const int wp = w >> 1, wr = w & 1;
#pragma unroll
    for (int i = 0; i < 4; ++i) {
      const int j = w * 4 + i;
      if (!P_TR) {
        const int row = 8 * j + (l >> 3), slot = l & 7;
        const int chunk = slot ^ ((row >> 1) & 7);
        pOff[i] = row * ldp + chunk * 8;
      } else {
        const int half = j >> 4, jj = j & 15;
        const int krow = 4 * jj + (l >> 4), s16 = l & 15;
        const int c64 = (s16 >> 2) ^ (krow & 3);
        int col = half * 128 + c64 * 32 + (s16 & 3) * 8;
        if (col >= pcol_limit) col = pcol_limit - 8;
        pOff[i] = krow * ldp + col;
      }
    }
#pragma unroll
    for (int i = 0; i < 2; ++i) {
      const int j = w * 2 + i;
      if (!R_TR) {
        const int row = 8 * j + (l >> 3), slot = l & 7;
        const int chunk = slot ^ ((row >> 1) & 7);
        rOff[i] = row * ldr + chunk * 8;
      } else {
        const int krow = 4 * j + (l >> 4), s16 = l & 15;
        const int c64 = (s16 >> 2) ^ (krow & 3);
        const int col = c64 * 32 + (s16 & 3) * 8;
        rOff[i] = krow * ldr + col;
      }
    }
    const int r32 = l & 31, hi = l >> 5, blk = (l >> 4) & 1, q = (l & 15) >> 2, p = l & 3;
    if (!P_TR) {
      const int prow = 64 * wp + (ROWS_P ? rperm(r32) : r32);
      const int s = (prow >> 1) & 7;
#pragma unroll
      for (int kk = 0; kk < 4; ++kk) pAddr[kk] = prow * 128 + (((2 * kk + hi) ^ s) << 4);
    } else {
      const int half = wp >> 1, pp = ROWS_P ? swapmid(p) : p;
#pragma unroll
      for (int i = 0; i < 2; ++i) {
        const int c64 = 2 * (wp & 1) + i;
        pAddr[i] = half * 16384 + (8 * hi + q) * 256 + ((c64 ^ q) * 64) + blk * 32 + pp * 8;
      }
      pAddr[2] = pAddr[3] = 0;
    }
    if (!R_TR) {
      const int rrow = 64 * wr + (!ROWS_P ? rperm(r32) : r32);
      const int s = (rrow >> 1) & 7;
#pragma unroll
      for (int kk = 0; kk < 4; ++kk) rAddr[kk] = RBASE + rrow * 128 + (((2 * kk + hi) ^ s) << 4);
    } else {
      const int pp = !ROWS_P ? swapmid(p) : p;
#pragma unroll
      for (int i = 0; i < 2; ++i) {
        const int c64 = 2 * wr + i;
        rAddr[i] = RBASE + (8 * hi + q) * 256 + ((c64 ^ q) * 64) + blk * 32 + pp * 8;
      }
      rAddr[2] = rAddr[3] = 0;
    }
  }
  __device__ __forceinline__ void stage(char LAS* buf, const bf16_t* P, const bf16_t* R) {
#pragma unroll
    for (int i = 0; i < 4; ++i)
      __builtin_amdgcn_global_load_lds((const unsigned*)(P + pOff[i]), (LAS unsigned*)(buf + (w * 4 + i) * 1024), 16, 0, 0);
#pragma unroll
    for (int i = 0; i < 2; ++i)
      __builtin_amdgcn_global_load_lds((const unsigned*)(R + rOff[i]), (LAS unsigned*)(buf + RBASE + (w * 2 + i) * 1024), 16, 0, 0);
  }
  __device__ __forceinline__ void compute(const char LAS* buf, f32x16 (&acc)[2][2]) {
#pragma unroll
    for (int kk = 0; kk < 4; ++kk) {
      bf16x8 pf[2], rf[2];
#pragma unroll
      for (int i = 0; i < 2; ++i) {
        if (!P_TR) pf[i] = *(const LAS bf16x8*)(buf + pAddr[kk] + i * 4096);
        else pf[i] = cat8(tr_read(buf + pAddr[i] + kk * 4096), tr_read(buf + pAddr[i] + kk * 4096 + 1024));
        if (!R_TR) rf[i] = *(const LAS bf16x8*)(buf + rAddr[kk] + i * 4096);
        else rf[i] = cat8(tr_read(buf + rAddr[i] + kk * 4096), tr_read(buf + rAddr[i] + kk * 4096 + 1024));
      }
#pragma unroll
      for (int i = 0; i < 2; ++i)
#pragma unroll
        for (int j = 0; j < 2; ++j) {
          if (ROWS_P) acc[i][j] = __builtin_amdgcn_mfma_f32_32x32x16_bf16(pf[i], rf[j], acc[i][j], 0, 0, 0);
          else acc[i][j] = __builtin_amdgcn_mfma_f32_32x32x16_bf16(rf[j], pf[i], acc[i][j], 0, 0, 0);
        }
    }
  }
};

#define WAIT_VM0() asm volatile("s_waitcnt vmcnt(0)" ::: "memory")
#define BARRIER() __builtin_amdgcn_s_barrier()

__device__ __forceinline__ float gelu_tanh(float x) {
  const float y = 0.7978845608028654f * (x + 0.044715f * x * x * x);
  const float e = __expf(2.0f * y);
  const float t = 1.0f - 2.0f / (e + 1.0f);
  return 0.5f * x * (1.0f + t);
}

__global__ __launch_bounds__(512, 2) void gemm1_kernel(const bf16_t* __restrict__ xn, const bf16_t* __restrict__ hbuf,
                                                       const bf16_t* __restrict__ ckv, const bf16_t* __restrict__ wq,
                                                       const bf16_t* __restrict__ wuk, const bf16_t* __restrict__ wuv,
                                                       const bf16_t* __restrict__ w1, const bf16_t* __restrict__ b1,
                                                       bf16_t* __restrict__ Qo, bf16_t* __restrict__ Ko,
                                                       bf16_t* __restrict__ Vo, bf16_t* __restrict__ Uo) {
  __shared__ __attribute__((aligned(16))) char smem[2 * STAGE_BYTES];
  char LAS* lds = (char LAS*)smem;
  const int xcd = blockIdx.x & 7, c = blockIdx.x >> 3;
  const int l = threadIdx.x & 63, w = __builtin_amdgcn_readfirstlane(threadIdx.x >> 6);
  const int wp = w >> 1, wr = w & 1, r32 = l & 31, hi = l >> 5;
  if (c < 27) {
    const bool isQ = c >= 16;
    int ntile, tile0;
    const bf16_t* A; const bf16_t* W; bf16_t* O; int K;
    if (!isQ) {
      ntile = 4; tile0 = (c >> 1) * 4;
      A = ckv; K = DC;
      W = (c & 1) ? wuv : wuk; O = (c & 1) ? Vo : Ko;
    } else {
      tile0 = (c - 16) * 3; ntile = (tile0 + 3 <= 32) ? 3 : 32 - tile0;
      A = xn; K = DM; W = wq; O = Qo;
    }
    const int nk = K / 64;
    GemmCore<false, true, false> g;
    g.init(K, HDN, 0);
    int buf = 0;
    {
      const int tb = tile0 >> 2, head = xcd * 4 + (tile0 & 3);
      g.stage(lds, A + (size_t)tb * 256 * K, W + head * 128);
    }
    for (int t = 0; t < ntile; ++t) {
      const int id = tile0 + t, tb = id >> 2, head = xcd * 4 + (id & 3);
      const bf16_t* Ap = A + (size_t)tb * 256 * K;
      const bf16_t* Wp = W + head * 128;
      f32x16 acc[2][2] = {};
      for (int kt = 0; kt < nk; ++kt) {
        WAIT_VM0(); BARRIER();
        if (kt + 1 < nk) g.stage(lds + (buf ^ 1) * STAGE_BYTES, Ap + (kt + 1) * 64, Wp + (size_t)(kt + 1) * 64 * HDN);
        else if (t + 1 < ntile) {
          const int id2 = id + 1, tb2 = id2 >> 2, head2 = xcd * 4 + (id2 & 3);
          g.stage(lds + (buf ^ 1) * STAGE_BYTES, A + (size_t)tb2 * 256 * K, W + head2 * 128);
        }
        g.compute(lds + buf * STAGE_BYTES, acc);
        buf ^= 1;
      }
#pragma unroll
      for (int i = 0; i < 2; ++i) {
        const int tok = tb * 256 + 64 * wp + 32 * i + r32;
        bf16_t* orow = O + ((size_t)head * S_LEN + tok) * HD + 64 * wr + 8 * hi;
#pragma unroll
        for (int j = 0; j < 2; ++j) {
          const f32x16 a = acc[i][j];
          u32x4 v0 = {pack2(a[0], a[1]), pack2(a[2], a[3]), pack2(a[4], a[5]), pack2(a[6], a[7])};
          u32x4 v1 = {pack2(a[8], a[9]), pack2(a[10], a[11]), pack2(a[12], a[13]), pack2(a[14], a[15])};
          *(u32x4*)(orow + 32 * j) = v0;
          *(u32x4*)(orow + 32 * j + 16) = v1;
        }
      }
    }
  } else {
    const int id = xcd * 5 + (c - 27);
    if (id >= 35) return;
    const int ft = id / 5, dt = id % 5;
    const int f0 = ft * 256, d0 = dt * 128;
    GemmCore<true, true, true> g;
    g.init(FF, DM, FF - f0);
    const bf16_t* Pp = w1 + f0;
    const bf16_t* Rp = hbuf + d0;
    int buf = 0;
    g.stage(lds, Pp, Rp);
    f32x16 acc[2][2] = {};
    const int nk = S_LEN / 64;
    for (int kt = 0; kt < nk; ++kt) {
      WAIT_VM0(); BARRIER();
      if (kt + 1 < nk) g.stage(lds + (buf ^ 1) * STAGE_BYTES, Pp + (size_t)(kt + 1) * 64 * FF, Rp + (size_t)(kt + 1) * 64 * DM);
      g.compute(lds + buf * STAGE_BYTES, acc);
      buf ^= 1;
    }
#pragma unroll
    for (int i = 0; i < 2; ++i) {
      const int fb = f0 + 64 * wp + 32 * i;
      if (fb >= FF) continue;
      float bias[16];
#pragma unroll
      for (int r = 0; r < 8; ++r) { bias[r] = bf2f(b1[fb + 8 * hi + r]); bias[8 + r] = bf2f(b1[fb + 16 + 8 * hi + r]); }
#pragma unroll
      for (int j = 0; j < 2; ++j) {
        const int d = d0 + 64 * wr + 32 * j + r32;
        float a[16];
#pragma unroll
        for (int r = 0; r < 16; ++r) a[r] = gelu_tanh(rbf(rbf(acc[i][j][r]) + bias[r]));
        u32x4 v0 = {pack2(a[0], a[1]), pack2(a[2], a[3]), pack2(a[4], a[5]), pack2(a[6], a[7])};
        u32x4 v1 = {pack2(a[8], a[9]), pack2(a[10], a[11]), pack2(a[12], a[13]), pack2(a[14], a[15])};
        bf16_t* orow = Uo + (size_t)d * FF + fb + 8 * hi;
        *(u32x4*)(orow) = v0;
        *(u32x4*)(orow + 16) = v1;
      }
    }
  }
}

constexpr int ATT_BUF = 65536;
__global__ __launch_bounds__(512, 2) void attn_kernel(const bf16_t* __restrict__ Q, const bf16_t* __restrict__ K,
                                                      const bf16_t* __restrict__ V, bf16_t* __restrict__ ctx) {
  __shared__ __attribute__((aligned(16))) char smem[2 * ATT_BUF + 4096];
  char LAS* lds = (char LAS*)smem;
  const int tid = threadIdx.x, l = tid & 63, w = __builtin_amdgcn_readfirstlane(tid >> 6);
  const int rg = w & 3, kh = w >> 2, r32 = l & 31, hi = l >> 5;
  const int xcd = blockIdx.x & 7, jj = blockIdx.x >> 3;
  const int head = xcd * 4 + (jj >> 3), pair = jj & 7;
  const bf16_t* Kh = K + (size_t)head * S_LEN * HD;
  const bf16_t* Vh = V + (size_t)head * S_LEN * HD;
  const bf16_t* Qh = Q + (size_t)head * S_LEN * HD;
  int kOff[4], vOff[4];
#pragma unroll
  for (int i = 0; i < 4; ++i) {
    const int j = w * 4 + i, row = 4 * j + (l >> 4), s16 = l & 15;
    kOff[i] = row * HD + ((s16 ^ (row & 15)) * 8);
    vOff[i] = row * HD + ((((s16 >> 2) ^ (row & 3)) * 4 + (s16 & 3)) * 8);
  }
  auto stage = [&](int b, int t) {
    const bf16_t* kp = Kh + (size_t)t * 128 * HD;
    const bf16_t* vp = Vh + (size_t)t * 128 * HD;
    char LAS* base = lds + b * ATT_BUF;
#pragma unroll
    for (int i = 0; i < 4; ++i)
      __builtin_amdgcn_global_load_lds((const unsigned*)(kp + kOff[i]), (LAS unsigned*)(base + (w * 4 + i) * 1024), 16, 0, 0);
#pragma unroll
    for (int i = 0; i < 4; ++i)
      __builtin_amdgcn_global_load_lds((const unsigned*)(vp + vOff[i]), (LAS unsigned*)(base + 32768 + (w * 4 + i) * 1024), 16, 0, 0);
  };
  const int krow = 64 * kh + r32;
  const int ksw = krow & 15;
  const int blk = (l >> 4) & 1, q4 = (l & 15) >> 2, p4 = l & 3;
  int vAddr[4];
#pragma unroll
  for (int db = 0; db < 4; ++db)
    vAddr[db] = 32768 + (64 * kh + 4 * hi + q4) * 256 + ((db ^ q4) * 64) + blk * 32 + swapmid(p4) * 8;
  constexpr float CS = 0.08838834764831845f * 1.4426950408889634f;

  int buf = 0;
  stage(0, 0);
  for (int pass = 0; pass < 2; ++pass) {
    const int qb = pass == 0 ? (15 - pair) : pair;
    const int q0 = qb * 128 + 32 * rg;
    bf16x8 qf[8];
#pragma unroll
    for (int d0 = 0; d0 < 8; ++d0) qf[d0] = *(const bf16x8*)(Qh + (size_t)(q0 + r32) * HD + d0 * 16 + hi * 8);
    float m_run = -1e30f, l_run = 0.f;
    f32x16 o[4] = {};
    for (int t = 0; t <= qb; ++t) {
      WAIT_VM0(); BARRIER();
      if (t < qb) stage(buf ^ 1, t + 1);
      else if (pass == 0) stage(buf ^ 1, 0);
      const char LAS* base = lds + buf * ATT_BUF;
      const bool diag = (t == qb);
      const bool active = !(diag && (64 * kh > 32 * rg + 31));
      if (active) {
        f32x16 s0 = {}, s1 = {};
#pragma unroll
        for (int d0 = 0; d0 < 8; ++d0) {
          const int a = krow * 256 + (((2 * d0 + hi) ^ ksw) << 4);
          bf16x8 k0 = *(const LAS bf16x8*)(base + a);
          bf16x8 k1 = *(const LAS bf16x8*)(base + a + 32 * 256);
          s0 = __builtin_amdgcn_mfma_f32_32x32x16_bf16(k0, qf[d0], s0, 0, 0, 0);
          s1 = __builtin_amdgcn_mfma_f32_32x32x16_bf16(k1, qf[d0], s1, 0, 0, 0);
        }
        if (diag) {
          const int qi = 32 * rg + r32;
#pragma unroll
          for (int r = 0; r < 16; ++r) {
            const int key = 64 * kh + (r & 3) + 8 * (r >> 2) + 4 * hi;
            if (key > qi) s0[r] = -__builtin_inff();
            if (key + 32 > qi) s1[r] = -__builtin_inff();
          }
        }
        float pm = s0[0];
#pragma unroll
        for (int r = 1; r < 16; ++r) pm = fmaxf(pm, s0[r]);
#pragma unroll
        for (int r = 0; r < 16; ++r) pm = fmaxf(pm, s1[r]);
        pm = fmaxf(pm, __shfl_xor(pm, 32));
        const float mn = fmaxf(m_run, pm);
        const float alpha = __builtin_amdgcn_exp2f((m_run - mn) * CS);
        m_run = mn;
        const float mc = -mn * CS;
        float ps = 0.f;
#pragma unroll
        for (int r = 0; r < 16; ++r) { s0[r] = __builtin_amdgcn_exp2f(fmaf(s0[r], CS, mc)); ps += s0[r]; }
#pragma unroll
        for (int r = 0; r < 16; ++r) { s1[r] = __builtin_amdgcn_exp2f(fmaf(s1[r], CS, mc)); ps += s1[r]; }
        ps += __shfl_xor(ps, 32);
        l_run = l_run * alpha + ps;
        if (__any(alpha != 1.0f)) {
#pragma unroll
          for (int db = 0; db < 4; ++db)
#pragma unroll
            for (int r = 0; r < 16; ++r) o[db][r] *= alpha;
        }
        bf16x8 pf[4];
        {
          u32x4 a0 = {pack2(s0[0], s0[1]), pack2(s0[2], s0[3]), pack2(s0[4], s0[5]), pack2(s0[6], s0[7])};
          u32x4 a1 = {pack2(s0[8], s0[9]), pack2(s0[10], s0[11]), pack2(s0[12], s0[13]), pack2(s0[14], s0[15])};
          u32x4 a2 = {pack2(s1[0], s1[1]), pack2(s1[2], s1[3]), pack2(s1[4], s1[5]), pack2(s1[6], s1[7])};
          u32x4 a3 = {pack2(s1[8], s1[9]), pack2(s1[10], s1[11]), pack2(s1[12], s1[13]), pack2(s1[14], s1[15])};
          pf[0] = *(bf16x8*)&a0; pf[1] = *(bf16x8*)&a1; pf[2] = *(bf16x8*)&a2; pf[3] = *(bf16x8*)&a3;
        }
#pragma unroll
        for (int db = 0; db < 4; ++db) {
#pragma unroll
          for (int s = 0; s < 4; ++s) {
            bf16x8 vf = cat8(tr_read(base + vAddr[db] + s * 4096), tr_read(base + vAddr[db] + s * 4096 + 2048));
            o[db] = __builtin_amdgcn_mfma_f32_32x32x16_bf16(vf, pf[s], o[db], 0, 0, 0);
          }
        }
      }
      buf ^= 1;
    }
    BARRIER();
    {
      char LAS* xb = lds + (buf ^ 1) * ATT_BUF;
      float LAS* ml = (float LAS*)(lds + 2 * ATT_BUF);
      float LAS* dst = (float LAS*)(xb + w * 8192);
#pragma unroll
      for (int dd = 0; dd < 2; ++dd) {
        const f32x16 a = kh == 0 ? o[2 + dd] : o[dd];
#pragma unroll
        for (int g = 0; g < 4; ++g)
          *(f32x4 LAS*)(dst + ((dd * 4 + g) * 64 + l) * 4) = (f32x4){a[4 * g], a[4 * g + 1], a[4 * g + 2], a[4 * g + 3]};
      }
      ml[w * 128 + l] = m_run;
      ml[w * 128 + 64 + l] = l_run;
      asm volatile("s_waitcnt lgkmcnt(0)" ::: "memory");
      BARRIER();
      const int wo = w ^ 4;
      const float m2 = ml[wo * 128 + l], l2 = ml[wo * 128 + 64 + l];
      const float mt = fmaxf(m_run, m2);
      const float a1 = __builtin_amdgcn_exp2f((m_run - mt) * CS), a2 = __builtin_amdgcn_exp2f((m2 - mt) * CS);
      const float inv = 1.0f / (l_run * a1 + l2 * a2);
      const float f1 = a1 * inv, f2 = a2 * inv;
      const float LAS* src = (const float LAS*)(xb + wo * 8192);
      bf16_t* orow = ctx + (size_t)(q0 + r32) * HDN + head * HD + 8 * hi;
#pragma unroll
      for (int dd = 0; dd < 2; ++dd) {
        const f32x16 a = kh == 0 ? o[dd] : o[2 + dd];
        float r[16];
#pragma unroll
        for (int g = 0; g < 4; ++g) {
          const f32x4 b = *(const f32x4 LAS*)(src + ((dd * 4 + g) * 64 + l) * 4);
#pragma unroll
          for (int e = 0; e < 4; ++e) r[4 * g + e] = a[4 * g + e] * f1 + b[e] * f2;
        }
        u32x4 v0 = {pack2(r[0], r[1]), pack2(r[2], r[3]), pack2(r[4], r[5]), pack2(r[6], r[7])};
        u32x4 v1 = {pack2(r[8], r[9]), pack2(r[10], r[11]), pack2(r[12], r[13]), pack2(r[14], r[15])};
        const int dcol = 32 * (2 * kh + dd);
        *(u32x4*)(orow + dcol) = v0;
        *(u32x4*)(orow + dcol + 16) = v1;
      }
      asm volatile("s_waitcnt lgkmcnt(0)" ::: "memory");
    }
  }
}

__global__ __launch_bounds__(512, 2) void gemm2_kernel(const bf16_t* __restrict__ ctx, const bf16_t* __restrict__ wo,
                                                       const bf16_t* __restrict__ U, const bf16_t* __restrict__ w2,
                                                       float* __restrict__ part) {
  __shared__ __attribute__((aligned(16))) char smem[2 * STAGE_BYTES];
  char LAS* lds = (char LAS*)smem;
  const int b = blockIdx.x;
  const int sp = b % 6, tile = b / 6;
  const int st = tile / 5, mt = tile % 5;
  const int s0 = st * 256, m0 = mt * 128;
  const int l = threadIdx.x & 63, w = __builtin_amdgcn_readfirstlane(threadIdx.x >> 6);
  const int wp = w >> 1, wr = w & 1, r32 = l & 31, hi = l >> 5;
  f32x16 acc[2][2] = {};
  int buf = 0;
  if (sp < 4) {
    GemmCore<false, true, false> g;
    g.init(HDN, DM, 0);
    const bf16_t* Pp = ctx + (size_t)s0 * HDN + sp * 1024;
    const bf16_t* Rp = wo + (size_t)sp * 1024 * DM + m0;
    g.stage(lds, Pp, Rp);
    const int nk = 16;
    for (int kt = 0; kt < nk; ++kt) {
      WAIT_VM0(); BARRIER();
      if (kt + 1 < nk) g.stage(lds + (buf ^ 1) * STAGE_BYTES, Pp + (kt + 1) * 64, Rp + (size_t)(kt + 1) * 64 * DM);
      g.compute(lds + buf * STAGE_BYTES, acc);
      buf ^= 1;
    }
  } else {
    GemmCore<true, false, false> g;
    g.init(S_LEN, FF, 256);
    const int k0 = (sp == 4) ? 0 : 14 * 64;
    const int nk = (sp == 4) ? 14 : 13;
    const bf16_t* Pp = w2 + (size_t)k0 * S_LEN + s0;
    const bf16_t* Rp = U + (size_t)m0 * FF + k0;
    g.stage(lds, Pp, Rp);
    for (int kt = 0; kt < nk; ++kt) {
      WAIT_VM0(); BARRIER();
      if (kt + 1 < nk) g.stage(lds + (buf ^ 1) * STAGE_BYTES, Pp + (size_t)(kt + 1) * 64 * S_LEN, Rp + (kt + 1) * 64);
      g.compute(lds + buf * STAGE_BYTES, acc);
      buf ^= 1;
    }
  }
#pragma unroll
  for (int i = 0; i < 2; ++i) {
    const int s = s0 + 64 * wp + 32 * i + r32;
    float* orow = part + ((size_t)sp * S_LEN + s) * DM + m0 + 64 * wr + 8 * hi;
#pragma unroll
    for (int j = 0; j < 2; ++j) {
      const f32x16 a = acc[i][j];
      *(f32x4*)(orow + 32 * j) = (f32x4){a[0], a[1], a[2], a[3]};
      *(f32x4*)(orow + 32 * j + 4) = (f32x4){a[4], a[5], a[6], a[7]};
      *(f32x4*)(orow + 32 * j + 16) = (f32x4){a[8], a[9], a[10], a[11]};
      *(f32x4*)(orow + 32 * j + 20) = (f32x4){a[12], a[13], a[14], a[15]};
    }
  }
}

__global__ __launch_bounds__(256) void reduce_kernel(const float* __restrict__ part, const bf16_t* __restrict__ x,
                                                     const bf16_t* __restrict__ b2, bf16_t* __restrict__ out) {
  const int i = blockIdx.x * 256 + threadIdx.x;
  const int e = i * 4;
  const int s = e / DM;
  f32x4 a = *(const f32x4*)(part + e);
#pragma unroll
  for (int sp = 1; sp < 6; ++sp) a += *(const f32x4*)(part + (size_t)sp * S_LEN * DM + e);
  const u32x2 xv = *(const u32x2*)(x + e);
  const float bb = bf2f(b2[s]);
  float r0 = __uint_as_float(xv[0] << 16) + a[0] + bb;
  float r1 = __uint_as_float(xv[0] & 0xffff0000u) + a[1] + bb;
  float r2 = __uint_as_float(xv[1] << 16) + a[2] + bb;
  float r3 = __uint_as_float(xv[1] & 0xffff0000u) + a[3] + bb;
  u32x2 o = {pack2(r0, r1), pack2(r2, r3)};
  *(u32x2*)(out + e) = o;
}

extern "C" void kernel_launch(void* const* d_in, const int* in_sizes, int n_in, void* d_out, int out_size, void* d_ws,
                              size_t ws_size, hipStream_t stream) {
  const bf16_t* x = (const bf16_t*)d_in[0];
  const bf16_t* ckv = (const bf16_t*)d_in[1];
  const bf16_t* wq = (const bf16_t*)d_in[2];
  const bf16_t* wuk = (const bf16_t*)d_in[3];
  const bf16_t* wuv = (const bf16_t*)d_in[4];
  const bf16_t* wo = (const bf16_t*)d_in[5];
  const bf16_t* w1 = (const bf16_t*)d_in[7];
  const bf16_t* b1 = (const bf16_t*)d_in[8];
  const bf16_t* w2 = (const bf16_t*)d_in[9];
  const bf16_t* b2 = (const bf16_t*)d_in[10];
  char* ws = (char*)d_ws;
  bf16_t* xn = (bf16_t*)(ws + WS_XN);
  bf16_t* h = (bf16_t*)(ws + WS_H);
  bf16_t* Q = (bf16_t*)(ws + WS_Q);
  bf16_t* K = (bf16_t*)(ws + WS_K);
  bf16_t* V = (bf16_t*)(ws + WS_V);
  bf16_t* U = (bf16_t*)(ws + WS_U);
  bf16_t* ctx = (bf16_t*)(ws + WS_CTX);
  float* part = (float*)(ws + WS_PART);
  ln_kernel<<<S_LEN / 4, 256, 0, stream>>>(x, xn, h);
  gemm1_kernel<<<256, 512, 0, stream>>>(xn, h, ckv, wq, wuk, wuv, w1, b1, Q, K, V, U);
  attn_kernel<<<256, 512, 0, stream>>>(Q, K, V, ctx);
  gemm2_kernel<<<240, 512, 0, stream>>>(ctx, wo, U, w2, part);
  reduce_kernel<<<S_LEN * DM / 4 / 256, 256, 0, stream>>>(part, x, b2, (bf16_t*)d_out);
}
```

```cpp
#include <hip/hip_runtime.h>
#include <hip/hip_bf16.h>
#include <stdint.h>

constexpr int S_LEN = 2048;
constexpr int DM = 640;
constexpr int NH = 32;
constexpr int HD = 128;
constexpr int DC = 512;
constexpr int FF = 1728;
constexpr int HDN = NH * HD;
constexpr int CTX_LD = HDN + 64;

typedef unsigned short bf16_t;
typedef short bf16x8 __attribute__((ext_vector_type(8)));
typedef short s16x4 __attribute__((ext_vector_type(4)));
typedef float f32x16 __attribute__((ext_vector_type(16)));
typedef float f32x4 __attribute__((ext_vector_type(4)));
typedef unsigned u32x4 __attribute__((ext_vector_type(4)));
typedef unsigned u32x2 __attribute__((ext_vector_type(2)));
typedef __bf16 bf16x2_t __attribute__((ext_vector_type(2)));
#define LAS __attribute__((address_space(3)))

__device__ __forceinline__ float bf2f(bf16_t v) { return __uint_as_float(((unsigned)v) << 16); }
__device__ __forceinline__ unsigned pack2(float a, float b) {
  bf16x2_t v = {(__bf16)a, (__bf16)b};
  return *reinterpret_cast<unsigned*>(&v);
}
__device__ __forceinline__ float rbf(float a) {
  __bf16 v = (__bf16)a;
  return __uint_as_float(((unsigned)*reinterpret_cast<unsigned short*>(&v)) << 16);
}

constexpr size_t WS_XN = 0;
constexpr size_t WS_H = WS_XN + (size_t)S_LEN * DM * 2;
constexpr size_t WS_Q = WS_H + (size_t)S_LEN * DM * 2;
constexpr size_t WS_K = WS_Q + (size_t)S_LEN * HDN * 2;
constexpr size_t WS_V = WS_K + (size_t)S_LEN * HDN * 2;
constexpr size_t WS_U = WS_V + (size_t)S_LEN * HDN * 2;
constexpr size_t WS_CTX = WS_U + (size_t)DM * FF * 2;
constexpr size_t WS_PART = WS_CTX + (size_t)S_LEN * CTX_LD * 2;
constexpr size_t WS_UP = WS_PART + (size_t)6 * S_LEN * DM * 4;

__device__ __forceinline__ float wave_sum(float v) {
#pragma unroll
  for (int o = 32; o > 0; o >>= 1) v += __shfl_xor(v, o);
  return v;
}
__device__ __forceinline__ void ln_row(float (&v)[10]) {
  float s = 0.f;
#pragma unroll
  for (int i = 0; i < 10; ++i) s += v[i];
  s = wave_sum(s);
  const float mean = s * (1.0f / DM);
  float q = 0.f;
#pragma unroll
  for (int i = 0; i < 10; ++i) { float d = v[i] - mean; q += d * d; }
  q = wave_sum(q);
  const float mb = rbf(mean);
  const float vb = rbf(q * (1.0f / DM));
  const float den = rbf(sqrtf(rbf(vb + 1e-5f)));
#pragma unroll
  for (int i = 0; i < 10; ++i) v[i] = rbf(rbf(v[i] - mb) / den);
}
__global__ __launch_bounds__(256) void ln_kernel(const bf16_t* __restrict__ x, bf16_t* __restrict__ xn,
                                                 bf16_t* __restrict__ h) {
  const int lane = threadIdx.x & 63, w = threadIdx.x >> 6;
  const int row = blockIdx.x * 4 + w;
  const unsigned* xr = reinterpret_cast<const unsigned*>(x + (size_t)row * DM);
  float v[10];
#pragma unroll
  for (int j = 0; j < 5; ++j) {
    unsigned u = xr[lane + 64 * j];
    v[2 * j] = __uint_as_float(u << 16);
    v[2 * j + 1] = __uint_as_float(u & 0xffff0000u);
  }
  ln_row(v);
  unsigned* xo = reinterpret_cast<unsigned*>(xn + (size_t)row * DM);
#pragma unroll
  for (int j = 0; j < 5; ++j) xo[lane + 64 * j] = pack2(v[2 * j], v[2 * j + 1]);
  ln_row(v);
  unsigned* ho = reinterpret_cast<unsigned*>(h + (size_t)row * DM);
#pragma unroll
  for (int j = 0; j < 5; ++j) ho[lane + 64 * j] = pack2(v[2 * j], v[2 * j + 1]);
}

constexpr int STAGE_BYTES = 49152;
constexpr int RBASE = 32768;

__device__ __forceinline__ int swapmid(int p) { return ((p & 1) << 1) | ((p >> 1) & 1); }
__device__ __forceinline__ int rperm(int r) {
  int g = r >> 2;
  int pg = (g & 4) | swapmid(g & 3);
  return pg * 4 + (r & 3);
}
__device__ __forceinline__ s16x4 tr_read(const char LAS* p) {
  return __builtin_amdgcn_ds_read_tr16_b64_v4i16((LAS s16x4*)p);
}
__device__ __forceinline__ bf16x8 cat8(s16x4 a, s16x4 b) {
  return (bf16x8){a[0], a[1], a[2], a[3], b[0], b[1], b[2], b[3]};
}

__device__ __forceinline__ void glds16(const void* sbase, int voff, unsigned lds_dst) {
  unsigned keep;
  asm volatile("s_mov_b32 %0, m0\n\ts_mov_b32 m0, %3\n\ts_nop 0\n\tglobal_load_lds_dwordx4 %1, %2\n\ts_mov_b32 m0, %0"
               : "=&s"(keep) : "v"(voff), "s"(sbase), "s"(lds_dst) : "memory");
}

#define ST_MODE 0
__device__ __forceinline__ void st16(void* p, u32x4 v) {
  *(u32x4*)p = v;
}
__device__ __forceinline__ void st16f(float* p, float a, float b, float c, float d) {
  st16(p, (u32x4){__float_as_uint(a), __float_as_uint(b), __float_as_uint(c), __float_as_uint(d)});
}

template <bool P_TR, bool R_TR, bool ROWS_P>
struct GemmCore {
  int pOff[4], rOff[2];
  int pAddr[4], rAddr[4];
  int w, l;
  __device__ __forceinline__ void init(int ldp, int ldr, int pcol_limit  ) {
    const int tid = threadIdx.x;
    l = tid & 63;
    w = __builtin_amdgcn_readfirstlane(tid >> 6);
    const int wp = w >> 1, wr = w & 1;
#pragma unroll
    for (int i = 0; i < 4; ++i) {
      const int j = w * 4 + i;
      if (!P_TR) {
        const int row = 8 * j + (l >> 3), slot = l & 7;
        const int chunk = slot ^ ((row >> 1) & 7);
        pOff[i] = row * ldp + chunk * 8;
      } else {
        const int half = j >> 4, jj = j & 15;
        const int krow = 4 * jj + (l >> 4), s16 = l & 15;
        const int c64 = (s16 >> 2) ^ (krow & 3);
        int col = half * 128 + c64 * 32 + (s16 & 3) * 8;
        if (col >= pcol_limit) col = pcol_limit - 8;
        pOff[i] = krow * ldp + col;
      }
    }
#pragma unroll
    for (int i = 0; i < 2; ++i) {
      const int j = w * 2 + i;
      if (!R_TR) {
        const int row = 8 * j + (l >> 3), slot = l & 7;
        const int chunk = slot ^ ((row >> 1) & 7);
        rOff[i] = row * ldr + chunk * 8;
      } else {
        const int krow = 4 * j + (l >> 4), s16 = l & 15;
        const int c64 = (s16 >> 2) ^ (krow & 3);
        const int col = c64 * 32 + (s16 & 3) * 8;
        rOff[i] = krow * ldr + col;
      }
    }
    const int r32 = l & 31, hi = l >> 5, blk = (l >> 4) & 1, q = (l & 15) >> 2, p = l & 3;
    if (!P_TR) {
      const int prow = 64 * wp + (ROWS_P ? rperm(r32) : r32);
      const int s = (prow >> 1) & 7;
#pragma unroll
      for (int kk = 0; kk < 4; ++kk) pAddr[kk] = prow * 128 + (((2 * kk + hi) ^ s) << 4);
    } else {
      const int half = wp >> 1, pp = ROWS_P ? swapmid(p) : p;
#pragma unroll
      for (int i = 0; i < 2; ++i) {
        const int c64 = 2 * (wp & 1) + i;
        pAddr[i] = half * 16384 + (8 * hi + q) * 256 + ((c64 ^ q) * 64) + blk * 32 + pp * 8;
      }
      pAddr[2] = pAddr[3] = 0;
    }
    if (!R_TR) {
      const int rrow = 64 * wr + (!ROWS_P ? rperm(r32) : r32);
      const int s = (rrow >> 1) & 7;
#pragma unroll
      for (int kk = 0; kk < 4; ++kk) rAddr[kk] = RBASE + rrow * 128 + (((2 * kk + hi) ^ s) << 4);
    } else {
      const int pp = !ROWS_P ? swapmid(p) : p;
#pragma unroll
      for (int i = 0; i < 2; ++i) {
        const int c64 = 2 * wr + i;
        rAddr[i] = RBASE + (8 * hi + q) * 256 + ((c64 ^ q) * 64) + blk * 32 + pp * 8;
      }
      rAddr[2] = rAddr[3] = 0;
    }
  }
  __device__ __forceinline__ void stage(char LAS* buf, const bf16_t* P, const bf16_t* R) {
    const unsigned b = (unsigned)(uintptr_t)buf;
#pragma unroll
    for (int i = 0; i < 4; ++i) glds16(P, pOff[i] * 2, b + (w * 4 + i) * 1024);
#pragma unroll
    for (int i = 0; i < 2; ++i) glds16(R, rOff[i] * 2, b + RBASE + (w * 2 + i) * 1024);
  }
  __device__ __forceinline__ void compute(const char LAS* buf, f32x16 (&acc)[2][2]) {
#pragma unroll
    for (int kk = 0; kk < 4; ++kk) {
      bf16x8 pf[2], rf[2];
#pragma unroll
      for (int i = 0; i < 2; ++i) {
        if (!P_TR) pf[i] = *(const LAS bf16x8*)(buf + pAddr[kk] + i * 4096);
        else pf[i] = cat8(tr_read(buf + pAddr[i] + kk * 4096), tr_read(buf + pAddr[i] + kk * 4096 + 1024));
        if (!R_TR) rf[i] = *(const LAS bf16x8*)(buf + rAddr[kk] + i * 4096);
        else rf[i] = cat8(tr_read(buf + rAddr[i] + kk * 4096), tr_read(buf + rAddr[i] + kk * 4096 + 1024));
      }
#pragma unroll
      for (int i = 0; i < 2; ++i)
#pragma unroll
        for (int j = 0; j < 2; ++j) {
          if (ROWS_P) acc[i][j] = __builtin_amdgcn_mfma_f32_32x32x16_bf16(pf[i], rf[j], acc[i][j], 0, 0, 0);
          else acc[i][j] = __builtin_amdgcn_mfma_f32_32x32x16_bf16(rf[j], pf[i], acc[i][j], 0, 0, 0);
        }
    }
  }
};

#define WAIT_VM0() asm volatile("s_waitcnt vmcnt(0)" ::: "memory")
#define WAIT_VM6() asm volatile("s_waitcnt vmcnt(6)" ::: "memory")
#define WAIT_VM16() asm volatile("s_waitcnt vmcnt(16)" ::: "memory")
#define BARRIER() __builtin_amdgcn_s_barrier()
#define TS(k, i)

__device__ __forceinline__ float gelu_tanh(float x) {
  const float y = 0.7978845608028654f * (x + 0.044715f * x * x * x);
  const float e = __expf(2.0f * y);
  const float t = 1.0f - 2.0f / (e + 1.0f);
  return 0.5f * x * (1.0f + t);
}

constexpr int STAGE256 = 65536;
struct Core256 {
  int pOff[4], rOff[4];
  int pAddr[4], rAddr[2];
  int w;
  __device__ __forceinline__ void init(int ldp, int ldr) {
    const int tid = threadIdx.x, l = tid & 63;
    w = __builtin_amdgcn_readfirstlane(tid >> 6);
    const int wp = w >> 2, wr = w & 3;
#pragma unroll
    for (int i = 0; i < 4; ++i) {
      const int j = w * 4 + i;
      const int row = 8 * j + (l >> 3), slot = l & 7;
      pOff[i] = (row * ldp + (slot ^ ((row >> 1) & 7)) * 8) * 2;
      const int half = j >> 4, jj = j & 15;
      const int krow = 4 * jj + (l >> 4), s16 = l & 15;
      const int c64 = (s16 >> 2) ^ (krow & 3);
      rOff[i] = (krow * ldr + half * 128 + c64 * 32 + (s16 & 3) * 8) * 2;
    }
    const int r32 = l & 31, hi = l >> 5, blk = (l >> 4) & 1, q = (l & 15) >> 2, p = l & 3;
    const int prow = 128 * wp + r32, s = (prow >> 1) & 7;
#pragma unroll
    for (int kk = 0; kk < 4; ++kk) pAddr[kk] = prow * 128 + (((2 * kk + hi) ^ s) << 4);
#pragma unroll
    for (int j = 0; j < 2; ++j) {
      const int c64 = 2 * (wr & 1) + j;
      rAddr[j] = 32768 + (wr >> 1) * 16384 + (8 * hi + q) * 256 + ((c64 ^ q) * 64) + blk * 32 + swapmid(p) * 8;
    }
  }
  __device__ __forceinline__ void stage(char LAS* buf, const bf16_t* P, const bf16_t* R) {
    const unsigned b = (unsigned)(uintptr_t)buf;
#pragma unroll
    for (int i = 0; i < 4; ++i) glds16(P, pOff[i], b + (w * 4 + i) * 1024);
#pragma unroll
    for (int i = 0; i < 4; ++i) glds16(R, rOff[i], b + 32768 + (w * 4 + i) * 1024);
  }
  __device__ __forceinline__ void compute(const char LAS* buf, f32x16 (&acc)[4][2]) {
#pragma unroll
    for (int kk = 0; kk < 4; ++kk) {
      bf16x8 pf[4], rf[2];
#pragma unroll
      for (int j = 0; j < 2; ++j) rf[j] = cat8(tr_read(buf + rAddr[j] + kk * 4096), tr_read(buf + rAddr[j] + kk * 4096 + 1024));
#pragma unroll
      for (int i = 0; i < 4; ++i) pf[i] = *(const LAS bf16x8*)(buf + pAddr[kk] + i * 4096);
#pragma unroll
      for (int i = 0; i < 4; ++i)
#pragma unroll
        for (int j = 0; j < 2; ++j) acc[i][j] = __builtin_amdgcn_mfma_f32_32x32x16_bf16(rf[j], pf[i], acc[i][j], 0, 0, 0);
    }
  }
};

__global__ __launch_bounds__(512, 2) void gemm1_kernel(const bf16_t* __restrict__ xn, const bf16_t* __restrict__ hbuf,
                                                       const bf16_t* __restrict__ ckv, const bf16_t* __restrict__ wq,
                                                       const bf16_t* __restrict__ wuk, const bf16_t* __restrict__ wuv,
                                                       const bf16_t* __restrict__ w1,
                                                       bf16_t* __restrict__ Qo, bf16_t* __restrict__ Ko,
                                                       bf16_t* __restrict__ Vo, float* __restrict__ Up) {
  __shared__ __attribute__((aligned(16))) char smem[3 * STAGE_BYTES];
  char LAS* lds = (char LAS*)smem;
  const int xcd = blockIdx.x & 7, c = blockIdx.x >> 3;
  const int l = threadIdx.x & 63, w = __builtin_amdgcn_readfirstlane(threadIdx.x >> 6);
  const int r32 = l & 31, hi = l >> 5;
  TS(0, 0);
  const bool isQ = c >= 16;
  {
    int ntile, tb0, hp;
    const bf16_t* A; const bf16_t* W; bf16_t* O; int K;
    if (!isQ) {
      ntile = 2; tb0 = c >> 2; hp = xcd * 2 + (c & 1);
      A = ckv; K = DC;
      W = (c & 2) ? wuv : wuk; O = (c & 2) ? Vo : Ko;
    } else {
      ntile = 1; tb0 = (c - 16) >> 1; hp = xcd * 2 + (c & 1);
      A = xn; K = DM; W = wq; O = Qo;
    }
    const int nk = K / 64;
    Core256 g;
    g.init(K, HDN);
    const bf16_t* Wp = W + hp * 256;
    const int wp = w >> 2, wr = w & 3;
    g.stage(lds, A + (size_t)tb0 * 256 * K, Wp);
    int buf = 0;
    for (int t = 0; t < ntile; ++t) {
      const int tb = tb0 + 4 * t;
      const bf16_t* Ap = A + (size_t)tb * 256 * K;
      f32x16 acc[4][2] = {};
      for (int kt = 0; kt < nk; ++kt) {
        if (kt == 0 && t > 0) WAIT_VM16(); else WAIT_VM0();
        BARRIER();
        if (kt + 1 < nk) g.stage(lds + (buf ^ 1) * STAGE256, Ap + (kt + 1) * 64, Wp + (size_t)(kt + 1) * 64 * HDN);
        else if (t + 1 < ntile) g.stage(lds + (buf ^ 1) * STAGE256, Ap + (size_t)4 * 256 * K, Wp);
        g.compute(lds + buf * STAGE256, acc);
        buf ^= 1;
      }
      const int head = hp * 2 + (wr >> 1);
#pragma unroll
      for (int i = 0; i < 4; ++i) {
        const int tok = tb * 256 + 128 * wp + 32 * i + r32;
        bf16_t* orow = O + ((size_t)head * S_LEN + tok) * HD + 64 * (wr & 1) + 8 * hi;
#pragma unroll
        for (int j = 0; j < 2; ++j) {
          const f32x16 a = acc[i][j];
          u32x4 v0 = {pack2(a[0], a[1]), pack2(a[2], a[3]), pack2(a[4], a[5]), pack2(a[6], a[7])};
          u32x4 v1 = {pack2(a[8], a[9]), pack2(a[10], a[11]), pack2(a[12], a[13]), pack2(a[14], a[15])};
          st16(orow + 32 * j, v0);
          st16(orow + 32 * j + 16, v1);
        }
      }
    }
  }
  TS(0, 1);
  if (isQ) {
    const int e = c - 16, n4 = (xcd & 1) ? 11 : 12;
    int tile, part, k0, nk;
    if (e < n4) { tile = (xcd & 1) * 12 + e; part = xcd >> 1; k0 = 8 * part; nk = 8; }
    else {
      const int j = (xcd >> 1) * 9 + (xcd & 1) * 4 + (e - n4);
      part = j / 12; tile = 23 + j % 12; k0 = 11 * part; nk = part == 2 ? 10 : 11;
    }
    const int ft = tile / 5, dt = tile % 5;
    const int f0 = ft * 256, d0 = dt * 128;
    const int wp = w >> 1, wr = w & 1;
    const bf16_t* Pp = w1 + (size_t)k0 * 64 * FF + f0;
    const bf16_t* Rp = hbuf + (size_t)k0 * 64 * DM + d0;
    GemmCore<true, true, true> g;
    g.init(FF, DM, FF - f0);
    int buf = 0, pslot = 2;
    BARRIER();
    g.stage(lds, Pp, Rp);
    g.stage(lds + STAGE_BYTES, Pp + (size_t)64 * FF, Rp + (size_t)64 * DM);
    f32x16 acc[2][2] = {};
    for (int kt = 0; kt < nk; ++kt) {
      if (kt + 1 >= nk) WAIT_VM0(); else WAIT_VM6();
      BARRIER();
      if (kt + 2 < nk) g.stage(lds + pslot * STAGE_BYTES, Pp + (size_t)(kt + 2) * 64 * FF, Rp + (size_t)(kt + 2) * 64 * DM);
      g.compute(lds + buf * STAGE_BYTES, acc);
      if (++buf == 3) buf = 0;
      if (++pslot == 3) pslot = 0;
    }
#pragma unroll
    for (int i = 0; i < 2; ++i) {
      const int fb = f0 + 64 * wp + 32 * i;
      if (fb >= FF) continue;
#pragma unroll
      for (int j = 0; j < 2; ++j) {
        const int d = d0 + 64 * wr + 32 * j + r32;
        const f32x16 a = acc[i][j];
        float* orow = Up + ((size_t)part * DM + d) * FF + fb + 8 * hi;
        st16f(orow, a[0], a[1], a[2], a[3]);
        st16f(orow + 4, a[4], a[5], a[6], a[7]);
        st16f(orow + 16, a[8], a[9], a[10], a[11]);
        st16f(orow + 20, a[12], a[13], a[14], a[15]);
      }
    }
  }
  TS(0, 2);
}

constexpr int ATT_BUF = 65536;
__global__ __launch_bounds__(512, 2) void attn_kernel(const bf16_t* __restrict__ Q, const bf16_t* __restrict__ K,
                                                      const bf16_t* __restrict__ V, bf16_t* __restrict__ ctx,
                                                      const float* __restrict__ Up, const bf16_t* __restrict__ b1,
                                                      bf16_t* __restrict__ Uo) {
  __shared__ __attribute__((aligned(16))) char smem[2 * ATT_BUF + 4096];
  char LAS* lds = (char LAS*)smem;
  const int tid = threadIdx.x, l = tid & 63, w = __builtin_amdgcn_readfirstlane(tid >> 6);
  const int rg = w & 3, kh = w >> 2, r32 = l & 31, hi = l >> 5;
  const int xcd = blockIdx.x & 7, jj = blockIdx.x >> 3;
  const int head = xcd * 4 + (jj >> 3), pair = jj & 7;
  const bf16_t* Kh = K + (size_t)head * S_LEN * HD;
  const bf16_t* Vh = V + (size_t)head * S_LEN * HD;
  const bf16_t* Qh = Q + (size_t)head * S_LEN * HD;
  int kOff[4], vOff[4];
#pragma unroll
  for (int i = 0; i < 4; ++i) {
    const int j = w * 4 + i, row = 4 * j + (l >> 4), s16 = l & 15;
    kOff[i] = row * HD + ((s16 ^ (row & 15)) * 8);
    vOff[i] = row * HD + ((((s16 >> 2) ^ (row & 3)) * 4 + (s16 & 3)) * 8);
  }
  auto stage = [&](int b, int t) {
    const bf16_t* kp = Kh + (size_t)t * 128 * HD;
    const bf16_t* vp = Vh + (size_t)t * 128 * HD;
    char LAS* base = lds + b * ATT_BUF;
    const unsigned bb = (unsigned)(uintptr_t)base;
#pragma unroll
    for (int i = 0; i < 4; ++i) glds16(kp, kOff[i] * 2, bb + (w * 4 + i) * 1024);
#pragma unroll
    for (int i = 0; i < 4; ++i) glds16(vp, vOff[i] * 2, bb + 32768 + (w * 4 + i) * 1024);
  };
  const int krow = 64 * kh + r32;
  const int ksw = krow & 15;
  const int blk = (l >> 4) & 1, q4 = (l & 15) >> 2, p4 = l & 3;
  int vAddr[4];
#pragma unroll
  for (int db = 0; db < 4; ++db)
    vAddr[db] = 32768 + (64 * kh + 4 * hi + q4) * 256 + ((db ^ q4) * 64) + blk * 32 + swapmid(p4) * 8;
  constexpr float CS = 0.08838834764831845f * 1.4426950408889634f;

  TS(1, 0);
  int buf = 0;
  stage(0, 0);
  for (int g4 = tid; g4 < 1080; g4 += 512) {
    const int e = blockIdx.x * 4320 + g4 * 4;
    const int d = e / FF, f = e - d * FF;
    const int np = ((f >> 8) * 5 + (d >> 7)) >= 23 ? 3 : 4;
    f32x4 a = *(const f32x4*)(Up + e) + *(const f32x4*)(Up + (size_t)DM * FF + e) + *(const f32x4*)(Up + (size_t)2 * DM * FF + e);
    if (np == 4) a += *(const f32x4*)(Up + (size_t)3 * DM * FF + e);
    const u32x2 bv = *(const u32x2*)(b1 + f);
    const float r0 = gelu_tanh(rbf(rbf(a[0]) + __uint_as_float(bv[0] << 16)));
    const float r1 = gelu_tanh(rbf(rbf(a[1]) + __uint_as_float(bv[0] & 0xffff0000u)));
    const float r2 = gelu_tanh(rbf(rbf(a[2]) + __uint_as_float(bv[1] << 16)));
    const float r3 = gelu_tanh(rbf(rbf(a[3]) + __uint_as_float(bv[1] & 0xffff0000u)));
    *(u32x2*)(Uo + e) = (u32x2){pack2(r0, r1), pack2(r2, r3)};
  }
  for (int pass = 0; pass < 2; ++pass) {
    const int qb = pass == 0 ? (15 - pair) : pair;
    const int q0 = qb * 128 + 32 * rg;
    bf16x8 qf[8];
#pragma unroll
    for (int d0 = 0; d0 < 8; ++d0) qf[d0] = *(const bf16x8*)(Qh + (size_t)(q0 + r32) * HD + d0 * 16 + hi * 8);
    float m_run = -1e30f, l_run = 0.f;
    f32x16 o[4] = {};
    for (int t = 0; t <= qb; ++t) {
      WAIT_VM0(); BARRIER();
      if (t < qb) stage(buf ^ 1, t + 1);
      else if (pass == 0) stage(buf ^ 1, 0);
      const char LAS* base = lds + buf * ATT_BUF;
      const bool diag = (t == qb);
      const bool active = !(diag && (64 * kh > 32 * rg + 31));
      if (active) {
        f32x16 s0 = {}, s1 = {};
#pragma unroll
        for (int d0 = 0; d0 < 8; ++d0) {
          const int a = krow * 256 + (((2 * d0 + hi) ^ ksw) << 4);
          bf16x8 k0 = *(const LAS bf16x8*)(base + a);
          bf16x8 k1 = *(const LAS bf16x8*)(base + a + 32 * 256);
          s0 = __builtin_amdgcn_mfma_f32_32x32x16_bf16(k0, qf[d0], s0, 0, 0, 0);
          s1 = __builtin_amdgcn_mfma_f32_32x32x16_bf16(k1, qf[d0], s1, 0, 0, 0);
        }
        if (diag) {
          const int qi = 32 * rg + r32;
#pragma unroll
          for (int r = 0; r < 16; ++r) {
            const int key = 64 * kh + (r & 3) + 8 * (r >> 2) + 4 * hi;
            if (key > qi) s0[r] = -__builtin_inff();
            if (key + 32 > qi) s1[r] = -__builtin_inff();
          }
        }
        float pm = s0[0];
#pragma unroll
        for (int r = 1; r < 16; ++r) pm = fmaxf(pm, s0[r]);
#pragma unroll
        for (int r = 0; r < 16; ++r) pm = fmaxf(pm, s1[r]);
        pm = fmaxf(pm, __shfl_xor(pm, 32));
        const float mn = fmaxf(m_run, pm);
        const float alpha = __builtin_amdgcn_exp2f((m_run - mn) * CS);
        m_run = mn;
        const float mc = -mn * CS;
        float ps = 0.f;
#pragma unroll
        for (int r = 0; r < 16; ++r) { s0[r] = __builtin_amdgcn_exp2f(fmaf(s0[r], CS, mc)); ps += s0[r]; }
#pragma unroll
        for (int r = 0; r < 16; ++r) { s1[r] = __builtin_amdgcn_exp2f(fmaf(s1[r], CS, mc)); ps += s1[r]; }
        ps += __shfl_xor(ps, 32);
        l_run = l_run * alpha + ps;
        if (__any(alpha != 1.0f)) {
#pragma unroll
          for (int db = 0; db < 4; ++db)
#pragma unroll
            for (int r = 0; r < 16; ++r) o[db][r] *= alpha;
        }
        bf16x8 pf[4];
        {
          u32x4 a0 = {pack2(s0[0], s0[1]), pack2(s0[2], s0[3]), pack2(s0[4], s0[5]), pack2(s0[6], s0[7])};
          u32x4 a1 = {pack2(s0[8], s0[9]), pack2(s0[10], s0[11]), pack2(s0[12], s0[13]), pack2(s0[14], s0[15])};
          u32x4 a2 = {pack2(s1[0], s1[1]), pack2(s1[2], s1[3]), pack2(s1[4], s1[5]), pack2(s1[6], s1[7])};
          u32x4 a3 = {pack2(s1[8], s1[9]), pack2(s1[10], s1[11]), pack2(s1[12], s1[13]), pack2(s1[14], s1[15])};
          pf[0] = *(bf16x8*)&a0; pf[1] = *(bf16x8*)&a1; pf[2] = *(bf16x8*)&a2; pf[3] = *(bf16x8*)&a3;
        }
#pragma unroll
        for (int db = 0; db < 4; ++db) {
#pragma unroll
          for (int s = 0; s < 4; ++s) {
            bf16x8 vf = cat8(tr_read(base + vAddr[db] + s * 4096), tr_read(base + vAddr[db] + s * 4096 + 2048));
            o[db] = __builtin_amdgcn_mfma_f32_32x32x16_bf16(vf, pf[s], o[db], 0, 0, 0);
          }
        }
      }
      buf ^= 1;
    }
    BARRIER();
    {
      char LAS* xb = lds + (buf ^ 1) * ATT_BUF;
      float LAS* ml = (float LAS*)(lds + 2 * ATT_BUF);
      float LAS* dst = (float LAS*)(xb + w * 8192);
#pragma unroll
      for (int dd = 0; dd < 2; ++dd) {
        const f32x16 a = kh == 0 ? o[2 + dd] : o[dd];
#pragma unroll
        for (int g = 0; g < 4; ++g)
          *(f32x4 LAS*)(dst + ((dd * 4 + g) * 64 + l) * 4) = (f32x4){a[4 * g], a[4 * g + 1], a[4 * g + 2], a[4 * g + 3]};
      }
      ml[w * 128 + l] = m_run;
      ml[w * 128 + 64 + l] = l_run;
      asm volatile("s_waitcnt lgkmcnt(0)" ::: "memory");
      BARRIER();
      const int wo = w ^ 4;
      const float m2 = ml[wo * 128 + l], l2 = ml[wo * 128 + 64 + l];
      const float mt = fmaxf(m_run, m2);
      const float a1 = __builtin_amdgcn_exp2f((m_run - mt) * CS), a2 = __builtin_amdgcn_exp2f((m2 - mt) * CS);
      const float inv = 1.0f / (l_run * a1 + l2 * a2);
      const float f1 = a1 * inv, f2 = a2 * inv;
      const float LAS* src = (const float LAS*)(xb + wo * 8192);
      bf16_t* orow = ctx + (size_t)(q0 + r32) * CTX_LD + head * HD + 8 * hi;
#pragma unroll
      for (int dd = 0; dd < 2; ++dd) {
        const f32x16 a = kh == 0 ? o[dd] : o[2 + dd];
        float r[16];
#pragma unroll
        for (int g = 0; g < 4; ++g) {
          const f32x4 b = *(const f32x4 LAS*)(src + ((dd * 4 + g) * 64 + l) * 4);
#pragma unroll
          for (int e = 0; e < 4; ++e) r[4 * g + e] = a[4 * g + e] * f1 + b[e] * f2;
        }
        u32x4 v0 = {pack2(r[0], r[1]), pack2(r[2], r[3]), pack2(r[4], r[5]), pack2(r[6], r[7])};
        u32x4 v1 = {pack2(r[8], r[9]), pack2(r[10], r[11]), pack2(r[12], r[13]), pack2(r[14], r[15])};
        const int dcol = 32 * (2 * kh + dd);
        st16(orow + dcol, v0);
        st16(orow + dcol + 16, v1);
      }
      asm volatile("s_waitcnt lgkmcnt(0)" ::: "memory");
    }
    if (pass == 0) TS(1, 1);
  }
  TS(1, 2);
}

__global__ __launch_bounds__(512, 2) void gemm2_kernel(const bf16_t* __restrict__ ctx, const bf16_t* __restrict__ wo,
                                                       const bf16_t* __restrict__ U, const bf16_t* __restrict__ w2,
                                                       float* __restrict__ part) {
  __shared__ __attribute__((aligned(16))) char smem[3 * STAGE_BYTES];
  char LAS* lds = (char LAS*)smem;
  const int b = blockIdx.x;
  const int st = b & 7, mt = (b >> 3) % 5, sp = (b >> 3) / 5;
  const int s0 = st * 256, m0 = mt * 128;
  const int l = threadIdx.x & 63, w = __builtin_amdgcn_readfirstlane(threadIdx.x >> 6);
  const int wp = w >> 1, wr = w & 1, r32 = l & 31, hi = l >> 5;
  TS(2, 0);
  f32x16 acc[2][2] = {};
  int buf = 0, pslot = 2;
  if (sp < 4) {
    const bf16_t* Pp = ctx + (size_t)s0 * CTX_LD + sp * 1024;
    const bf16_t* Rp = wo + (size_t)sp * 1024 * DM + m0;
    GemmCore<false, true, false> g;
    g.init(CTX_LD, DM, 0);
    g.stage(lds, Pp, Rp);
    g.stage(lds + STAGE_BYTES, Pp + 64, Rp + (size_t)64 * DM);
    const int nk = 16;
    for (int kt = 0; kt < nk; ++kt) {
      if (kt + 1 >= nk) WAIT_VM0(); else WAIT_VM6();
      BARRIER();
      if (kt + 2 < nk) g.stage(lds + pslot * STAGE_BYTES, Pp + (kt + 2) * 64, Rp + (size_t)(kt + 2) * 64 * DM);
      g.compute(lds + buf * STAGE_BYTES, acc);
      if (++buf == 3) buf = 0;
      if (++pslot == 3) pslot = 0;
    }
  } else {
    const int k0 = (sp == 4) ? 0 : 14 * 64;
    const int nk = (sp == 4) ? 14 : 13;
    const bf16_t* Pp = w2 + (size_t)k0 * S_LEN + s0;
    const bf16_t* Rp = U + (size_t)m0 * FF + k0;
    GemmCore<true, false, false> g;
    g.init(S_LEN, FF, 256);
    g.stage(lds, Pp, Rp);
    g.stage(lds + STAGE_BYTES, Pp + (size_t)64 * S_LEN, Rp + 64);
    for (int kt = 0; kt < nk; ++kt) {
      if (kt + 1 >= nk) WAIT_VM0(); else WAIT_VM6();
      BARRIER();
      if (kt + 2 < nk) g.stage(lds + pslot * STAGE_BYTES, Pp + (size_t)(kt + 2) * 64 * S_LEN, Rp + (kt + 2) * 64);
      g.compute(lds + buf * STAGE_BYTES, acc);
      if (++buf == 3) buf = 0;
      if (++pslot == 3) pslot = 0;
    }
  }
  TS(2, 1);
#pragma unroll
  for (int i = 0; i < 2; ++i) {
    const int s = s0 + 64 * wp + 32 * i + r32;
    float* orow = part + ((size_t)sp * S_LEN + s) * DM + m0 + 64 * wr + 8 * hi;
#pragma unroll
    for (int j = 0; j < 2; ++j) {
      const f32x16 a = acc[i][j];
      st16f(orow + 32 * j, a[0], a[1], a[2], a[3]);
      st16f(orow + 32 * j + 4, a[4], a[5], a[6], a[7]);
      st16f(orow + 32 * j + 16, a[8], a[9], a[10], a[11]);
      st16f(orow + 32 * j + 20, a[12], a[13], a[14], a[15]);
    }
  }
  TS(2, 2);
}

__global__ __launch_bounds__(256) void reduce_kernel(const float* __restrict__ part, const bf16_t* __restrict__ x,
                                                     const bf16_t* __restrict__ b2, bf16_t* __restrict__ out) {
  const int i = blockIdx.x * 256 + threadIdx.x;
  const int e = i * 4;
  const int s = e / DM;
  f32x4 a = *(const f32x4*)(part + e);
#pragma unroll
  for (int sp = 1; sp < 6; ++sp) a += *(const f32x4*)(part + (size_t)sp * S_LEN * DM + e);
  const u32x2 xv = *(const u32x2*)(x + e);
  const float bb = bf2f(b2[s]);
  float r0 = __uint_as_float(xv[0] << 16) + a[0] + bb;
  float r1 = __uint_as_float(xv[0] & 0xffff0000u) + a[1] + bb;
  float r2 = __uint_as_float(xv[1] << 16) + a[2] + bb;
  float r3 = __uint_as_float(xv[1] & 0xffff0000u) + a[3] + bb;
  u32x2 o = {pack2(r0, r1), pack2(r2, r3)};
  *(u32x2*)(out + e) = o;
}

extern "C" void kernel_launch(void* const* d_in, const int* in_sizes, int n_in, void* d_out, int out_size, void* d_ws,
                              size_t ws_size, hipStream_t stream) {
  const bf16_t* x = (const bf16_t*)d_in[0];
  const bf16_t* ckv = (const bf16_t*)d_in[1];
  const bf16_t* wq = (const bf16_t*)d_in[2];
  const bf16_t* wuk = (const bf16_t*)d_in[3];
  const bf16_t* wuv = (const bf16_t*)d_in[4];
  const bf16_t* wo = (const bf16_t*)d_in[5];
  const bf16_t* w1 = (const bf16_t*)d_in[7];
  const bf16_t* b1 = (const bf16_t*)d_in[8];
  const bf16_t* w2 = (const bf16_t*)d_in[9];
  const bf16_t* b2 = (const bf16_t*)d_in[10];
  char* ws = (char*)d_ws;
  bf16_t* xn = (bf16_t*)(ws + WS_XN);
  bf16_t* h = (bf16_t*)(ws + WS_H);
  bf16_t* Q = (bf16_t*)(ws + WS_Q);
  bf16_t* K = (bf16_t*)(ws + WS_K);
  bf16_t* V = (bf16_t*)(ws + WS_V);
  bf16_t* U = (bf16_t*)(ws + WS_U);
  bf16_t* ctx = (bf16_t*)(ws + WS_CTX);
  float* part = (float*)(ws + WS_PART);
  float* up = (float*)(ws + WS_UP);
  ln_kernel<<<S_LEN / 4, 256, 0, stream>>>(x, xn, h);
  gemm1_kernel<<<256, 512, 0, stream>>>(xn, h, ckv, wq, wuk, wuv, w1, Q, K, V, up);
  attn_kernel<<<256, 512, 0, stream>>>(Q, K, V, ctx, up, b1, U);
  gemm2_kernel<<<240, 512, 0, stream>>>(ctx, wo, U, w2, part);
  reduce_kernel<<<S_LEN * DM / 4 / 256, 256, 0, stream>>>(part, x, b2, (bf16_t*)d_out);
}
```

```cpp
#include <hip/hip_runtime.h>
#include <hip/hip_bf16.h>
#include <stdint.h>

constexpr int S_LEN = 2048;
constexpr int DM = 640;
constexpr int NH = 32;
constexpr int HD = 128;
constexpr int DC = 512;
constexpr int FF = 1728;
constexpr int HDN = NH * HD;
constexpr int CTX_LD = HDN + 64;

typedef unsigned short bf16_t;
typedef short bf16x8 __attribute__((ext_vector_type(8)));
typedef short s16x4 __attribute__((ext_vector_type(4)));
typedef float f32x16 __attribute__((ext_vector_type(16)));
typedef float f32x4 __attribute__((ext_vector_type(4)));
typedef unsigned u32x4 __attribute__((ext_vector_type(4)));
typedef unsigned u32x2 __attribute__((ext_vector_type(2)));
typedef __bf16 bf16x2_t __attribute__((ext_vector_type(2)));
#define LAS __attribute__((address_space(3)))

__device__ __forceinline__ float bf2f(bf16_t v) { return __uint_as_float(((unsigned)v) << 16); }
__device__ __forceinline__ unsigned pack2(float a, float b) {
  bf16x2_t v = {(__bf16)a, (__bf16)b};
  return *reinterpret_cast<unsigned*>(&v);
}
__device__ __forceinline__ float rbf(float a) {
  __bf16 v = (__bf16)a;
  return __uint_as_float(((unsigned)*reinterpret_cast<unsigned short*>(&v)) << 16);
}

constexpr size_t WS_XN = 0;
constexpr size_t WS_H = WS_XN + (size_t)S_LEN * DM * 2;
constexpr size_t WS_Q = WS_H + (size_t)S_LEN * DM * 2;
constexpr size_t WS_K = WS_Q + (size_t)S_LEN * HDN * 2;
constexpr size_t WS_V = WS_K + (size_t)S_LEN * HDN * 2;
constexpr size_t WS_U = WS_V + (size_t)S_LEN * HDN * 2;
constexpr size_t WS_CTX = WS_U + (size_t)DM * FF * 2;
constexpr size_t WS_PART = WS_CTX + (size_t)S_LEN * CTX_LD * 2;
constexpr size_t WS_UP = WS_PART + (size_t)6 * S_LEN * DM * 4;

__device__ __forceinline__ float wave_sum(float v) {
#pragma unroll
  for (int o = 32; o > 0; o >>= 1) v += __shfl_xor(v, o);
  return v;
}
__device__ __forceinline__ void ln_row(float (&v)[10]) {
  float s = 0.f;
#pragma unroll
  for (int i = 0; i < 10; ++i) s += v[i];
  s = wave_sum(s);
  const float mean = s * (1.0f / DM);
  float q = 0.f;
#pragma unroll
  for (int i = 0; i < 10; ++i) { float d = v[i] - mean; q += d * d; }
  q = wave_sum(q);
  const float mb = rbf(mean);
  const float vb = rbf(q * (1.0f / DM));
  const float den = rbf(sqrtf(rbf(vb + 1e-5f)));
#pragma unroll
  for (int i = 0; i < 10; ++i) v[i] = rbf(rbf(v[i] - mb) / den);
}
__global__ __launch_bounds__(256) void ln_kernel(const bf16_t* __restrict__ x, bf16_t* __restrict__ xn,
                                                 bf16_t* __restrict__ h) {
  const int lane = threadIdx.x & 63, w = threadIdx.x >> 6;
  const int row = blockIdx.x * 4 + w;
  const unsigned* xr = reinterpret_cast<const unsigned*>(x + (size_t)row * DM);
  float v[10];
#pragma unroll
  for (int j = 0; j < 5; ++j) {
    unsigned u = xr[lane + 64 * j];
    v[2 * j] = __uint_as_float(u << 16);
    v[2 * j + 1] = __uint_as_float(u & 0xffff0000u);
  }
  ln_row(v);
  unsigned* xo = reinterpret_cast<unsigned*>(xn + (size_t)row * DM);
#pragma unroll
  for (int j = 0; j < 5; ++j) xo[lane + 64 * j] = pack2(v[2 * j], v[2 * j + 1]);
  ln_row(v);
  unsigned* ho = reinterpret_cast<unsigned*>(h + (size_t)row * DM);
#pragma unroll
  for (int j = 0; j < 5; ++j) ho[lane + 64 * j] = pack2(v[2 * j], v[2 * j + 1]);
}

constexpr int STAGE_BYTES = 49152;
constexpr int RBASE = 32768;

__device__ __forceinline__ int swapmid(int p) { return ((p & 1) << 1) | ((p >> 1) & 1); }
__device__ __forceinline__ int rperm(int r) {
  int g = r >> 2;
  int pg = (g & 4) | swapmid(g & 3);
  return pg * 4 + (r & 3);
}
__device__ __forceinline__ s16x4 tr_read(const char LAS* p) {
  return __builtin_amdgcn_ds_read_tr16_b64_v4i16((LAS s16x4*)p);
}
__device__ __forceinline__ bf16x8 cat8(s16x4 a, s16x4 b) {
  return (bf16x8){a[0], a[1], a[2], a[3], b[0], b[1], b[2], b[3]};
}

__device__ __forceinline__ void glds16(const void* sbase, int voff, unsigned lds_dst) {
  unsigned keep;
  asm volatile("s_mov_b32 %0, m0\n\ts_mov_b32 m0, %3\n\ts_nop 0\n\tglobal_load_lds_dwordx4 %1, %2\n\ts_mov_b32 m0, %0"
               : "=&s"(keep) : "v"(voff), "s"(sbase), "s"(lds_dst) : "memory");
}

#define ST_MODE 0
__device__ __forceinline__ void st16(void* p, u32x4 v) {
  *(u32x4*)p = v;
}
__device__ __forceinline__ void st16f(float* p, float a, float b, float c, float d) {
  st16(p, (u32x4){__float_as_uint(a), __float_as_uint(b), __float_as_uint(c), __float_as_uint(d)});
}

template <bool P_TR, bool R_TR, bool ROWS_P>
struct GemmCore {
  int pOff[4], rOff[2];
  int pAddr[4], rAddr[4];
  int w, l;
  __device__ __forceinline__ void init(int ldp, int ldr, int pcol_limit  ) {
    const int tid = threadIdx.x;
    l = tid & 63;
    w = __builtin_amdgcn_readfirstlane(tid >> 6);
    const int wp = w >> 1, wr = w & 1;
#pragma unroll
    for (int i = 0; i < 4; ++i) {
      const int j = w * 4 + i;
      if (!P_TR) {
        const int row = 8 * j + (l >> 3), slot = l & 7;
        const int chunk = slot ^ ((row >> 1) & 7);
        pOff[i] = row * ldp + chunk * 8;
      } else {
        const int half = j >> 4, jj = j & 15;
        const int krow = 4 * jj + (l >> 4), s16 = l & 15;
        const int c64 = (s16 >> 2) ^ (krow & 3);
        int col = half * 128 + c64 * 32 + (s16 & 3) * 8;
        if (col >= pcol_limit) col = pcol_limit - 8;
        pOff[i] = krow * ldp + col;
      }
    }
#pragma unroll
    for (int i = 0; i < 2; ++i) {
      const int j = w * 2 + i;
      if (!R_TR) {
        const int row = 8 * j + (l >> 3), slot = l & 7;
        const int chunk = slot ^ ((row >> 1) & 7);
        rOff[i] = row * ldr + chunk * 8;
      } else {
        const int krow = 4 * j + (l >> 4), s16 = l & 15;
        const int c64 = (s16 >> 2) ^ (krow & 3);
        const int col = c64 * 32 + (s16 & 3) * 8;
        rOff[i] = krow * ldr + col;
      }
    }
    const int r32 = l & 31, hi = l >> 5, blk = (l >> 4) & 1, q = (l & 15) >> 2, p = l & 3;
    if (!P_TR) {
      const int prow = 64 * wp + (ROWS_P ? rperm(r32) : r32);
      const int s = (prow >> 1) & 7;
#pragma unroll
      for (int kk = 0; kk < 4; ++kk) pAddr[kk] = prow * 128 + (((2 * kk + hi) ^ s) << 4);
    } else {
      const int half = wp >> 1, pp = ROWS_P ? swapmid(p) : p;
#pragma unroll
      for (int i = 0; i < 2; ++i) {
        const int c64 = 2 * (wp & 1) + i;
        pAddr[i] = half * 16384 + (8 * hi + q) * 256 + ((c64 ^ q) * 64) + blk * 32 + pp * 8;
      }
      pAddr[2] = pAddr[3] = 0;
    }
    if (!R_TR) {
      const int rrow = 64 * wr + (!ROWS_P ? rperm(r32) : r32);
      const int s = (rrow >> 1) & 7;
#pragma unroll
      for (int kk = 0; kk < 4; ++kk) rAddr[kk] = RBASE + rrow * 128 + (((2 * kk + hi) ^ s) << 4);
    } else {
      const int pp = !ROWS_P ? swapmid(p) : p;
#pragma unroll
      for (int i = 0; i < 2; ++i) {
        const int c64 = 2 * wr + i;
        rAddr[i] = RBASE + (8 * hi + q) * 256 + ((c64 ^ q) * 64) + blk * 32 + pp * 8;
      }
      rAddr[2] = rAddr[3] = 0;
    }
  }
  __device__ __forceinline__ void stage(char LAS* buf, const bf16_t* P, const bf16_t* R) {
    const unsigned b = (unsigned)(uintptr_t)buf;
#pragma unroll
    for (int i = 0; i < 4; ++i) glds16(P, pOff[i] * 2, b + (w * 4 + i) * 1024);
#pragma unroll
    for (int i = 0; i < 2; ++i) glds16(R, rOff[i] * 2, b + RBASE + (w * 2 + i) * 1024);
  }
  __device__ __forceinline__ void compute(const char LAS* buf, f32x16 (&acc)[2][2]) {
#pragma unroll
    for (int kk = 0; kk < 4; ++kk) {
      bf16x8 pf[2], rf[2];
#pragma unroll
      for (int i = 0; i < 2; ++i) {
        if (!P_TR) pf[i] = *(const LAS bf16x8*)(buf + pAddr[kk] + i * 4096);
        else pf[i] = cat8(tr_read(buf + pAddr[i] + kk * 4096), tr_read(buf + pAddr[i] + kk * 4096 + 1024));
        if (!R_TR) rf[i] = *(const LAS bf16x8*)(buf + rAddr[kk] + i * 4096);
        else rf[i] = cat8(tr_read(buf + rAddr[i] + kk * 4096), tr_read(buf + rAddr[i] + kk * 4096 + 1024));
      }
#pragma unroll
      for (int i = 0; i < 2; ++i)
#pragma unroll
        for (int j = 0; j < 2; ++j) {
          if (ROWS_P) acc[i][j] = __builtin_amdgcn_mfma_f32_32x32x16_bf16(pf[i], rf[j], acc[i][j], 0, 0, 0);
          else acc[i][j] = __builtin_amdgcn_mfma_f32_32x32x16_bf16(rf[j], pf[i], acc[i][j], 0, 0, 0);
        }
    }
  }
};

#define WAIT_VM0() asm volatile("s_waitcnt vmcnt(0)" ::: "memory")
#define WAIT_VM6() asm volatile("s_waitcnt vmcnt(6)" ::: "memory")
#define WAIT_VM16() asm volatile("s_waitcnt vmcnt(16)" ::: "memory")
#define BARRIER() __builtin_amdgcn_s_barrier()
#define TS(k, i)

__device__ __forceinline__ float gelu_tanh(float x) {
  const float y = 0.7978845608028654f * (x + 0.044715f * x * x * x);
  const float e = __expf(2.0f * y);
  const float t = 1.0f - 2.0f / (e + 1.0f);
  return 0.5f * x * (1.0f + t);
}

constexpr int STAGE256 = 32768;
#define NST256 4
struct Core256 {
  int pOff[2], rOff[2];
  int pAddr[2], rAddr[2];
  int w;
  __device__ __forceinline__ void init(int ldp, int ldr) {
    const int tid = threadIdx.x, l = tid & 63;
    w = __builtin_amdgcn_readfirstlane(tid >> 6);
    const int wp = w >> 2, wr = w & 3;
#pragma unroll
    for (int i = 0; i < 2; ++i) {
      const int j = w * 2 + i;
      const int row = 16 * j + (l >> 2), slot = l & 3;
      pOff[i] = (row * ldp + (slot ^ ((row >> 2) & 3)) * 8) * 2;
      const int half = j >> 3, jj = j & 7;
      const int krow = 4 * jj + (l >> 4), s16 = l & 15;
      const int c64 = (s16 >> 2) ^ (krow & 3);
      rOff[i] = (krow * ldr + half * 128 + c64 * 32 + (s16 & 3) * 8) * 2;
    }
    const int r32 = l & 31, hi = l >> 5, blk = (l >> 4) & 1, q = (l & 15) >> 2, p = l & 3;
    const int prow = 128 * wp + r32, s = (prow >> 2) & 3;
#pragma unroll
    for (int kk = 0; kk < 2; ++kk) pAddr[kk] = prow * 64 + (((2 * kk + hi) ^ s) << 4);
#pragma unroll
    for (int j = 0; j < 2; ++j) {
      const int c64 = 2 * (wr & 1) + j;
      rAddr[j] = 16384 + (wr >> 1) * 8192 + (8 * hi + q) * 256 + ((c64 ^ q) * 64) + blk * 32 + swapmid(p) * 8;
    }
  }
  __device__ __forceinline__ void stage(char LAS* buf, const bf16_t* P, const bf16_t* R) {
    const unsigned b = (unsigned)(uintptr_t)buf;
#pragma unroll
    for (int i = 0; i < 2; ++i) glds16(P, pOff[i], b + (w * 2 + i) * 1024);
#pragma unroll
    for (int i = 0; i < 2; ++i) glds16(R, rOff[i], b + 16384 + (w * 2 + i) * 1024);
  }
  __device__ __forceinline__ void compute(const char LAS* buf, f32x16 (&acc)[4][2]) {
#pragma unroll
    for (int kk = 0; kk < 2; ++kk) {
      bf16x8 pf[4], rf[2];
#pragma unroll
      for (int j = 0; j < 2; ++j) rf[j] = cat8(tr_read(buf + rAddr[j] + kk * 4096), tr_read(buf + rAddr[j] + kk * 4096 + 1024));
#pragma unroll
      for (int i = 0; i < 4; ++i) pf[i] = *(const LAS bf16x8*)(buf + pAddr[kk] + i * 2048);
#pragma unroll
      for (int i = 0; i < 4; ++i)
#pragma unroll
        for (int j = 0; j < 2; ++j) acc[i][j] = __builtin_amdgcn_mfma_f32_32x32x16_bf16(rf[j], pf[i], acc[i][j], 0, 0, 0);
    }
  }
};
template <int N> __device__ __forceinline__ void wait_vm() { asm volatile("s_waitcnt vmcnt(%0)" ::"n"(N) : "memory"); }

__global__ __launch_bounds__(512, 2) void gemm1_kernel(const bf16_t* __restrict__ xn, const bf16_t* __restrict__ hbuf,
                                                       const bf16_t* __restrict__ ckv, const bf16_t* __restrict__ wq,
                                                       const bf16_t* __restrict__ wuk, const bf16_t* __restrict__ wuv,
                                                       const bf16_t* __restrict__ w1,
                                                       bf16_t* __restrict__ Qo, bf16_t* __restrict__ Ko,
                                                       bf16_t* __restrict__ Vo, float* __restrict__ Up) {
  __shared__ __attribute__((aligned(16))) char smem[NST256 * STAGE256 > 3 * STAGE_BYTES ? NST256 * STAGE256 : 3 * STAGE_BYTES];
  char LAS* lds = (char LAS*)smem;
  const int xcd = blockIdx.x & 7, c = blockIdx.x >> 3;
  const int l = threadIdx.x & 63, w = __builtin_amdgcn_readfirstlane(threadIdx.x >> 6);
  const int r32 = l & 31, hi = l >> 5;
  TS(0, 0);
  const bool isQ = c >= 16;
  {
    int ntile, tb0, hp;
    const bf16_t* A; const bf16_t* W; bf16_t* O; int K;
    if (!isQ) {
      ntile = 2; tb0 = c >> 2; hp = xcd * 2 + (c & 1);
      A = ckv; K = DC;
      W = (c & 2) ? wuv : wuk; O = (c & 2) ? Vo : Ko;
    } else {
      ntile = 1; tb0 = (c - 16) >> 1; hp = xcd * 2 + (c & 1);
      A = xn; K = DM; W = wq; O = Qo;
    }
    const int nk = K / 32;
    Core256 g;
    g.init(K, HDN);
    const bf16_t* Wp = W + hp * 256;
    const int wp = w >> 2, wr = w & 3;
    const int total = ntile * nk;
    int pt = 0, pk = 0, pslot = 0, issued = 0;
    auto issue = [&]() {
      g.stage(lds + pslot * STAGE256, A + (size_t)(tb0 + 4 * pt) * 256 * K + pk * 32, Wp + (size_t)pk * 32 * HDN);
      if (++pk == nk) { pk = 0; ++pt; }
      if (++pslot == NST256) pslot = 0;
      ++issued;
    };
#pragma unroll
    for (int i = 0; i < NST256 - 1; ++i) issue();
    int buf = 0, step = 0;
    for (int t = 0; t < ntile; ++t) {
      const int tb = tb0 + 4 * t;
      f32x16 acc[4][2] = {};
      for (int kt = 0; kt < nk; ++kt) {
        if (step + NST256 - 1 > total) WAIT_VM0();
        else if (kt == 0 && t > 0) wait_vm<4 * (NST256 - 2) + 16>();
        else wait_vm<4 * (NST256 - 2)>();
        BARRIER();
        if (issued < total) issue();
        g.compute(lds + buf * STAGE256, acc);
        if (++buf == NST256) buf = 0;
        ++step;
      }
      const int head = hp * 2 + (wr >> 1);
#pragma unroll
      for (int i = 0; i < 4; ++i) {
        const int tok = tb * 256 + 128 * wp + 32 * i + r32;
        bf16_t* orow = O + ((size_t)head * S_LEN + tok) * HD + 64 * (wr & 1) + 8 * hi;
#pragma unroll
        for (int j = 0; j < 2; ++j) {
          const f32x16 a = acc[i][j];
          u32x4 v0 = {pack2(a[0], a[1]), pack2(a[2], a[3]), pack2(a[4], a[5]), pack2(a[6], a[7])};
          u32x4 v1 = {pack2(a[8], a[9]), pack2(a[10], a[11]), pack2(a[12], a[13]), pack2(a[14], a[15])};
          st16(orow + 32 * j, v0);
          st16(orow + 32 * j + 16, v1);
        }
      }
    }
  }
  TS(0, 1);
  if (isQ) {
    const int e = c - 16, n4 = (xcd & 1) ? 11 : 12;
    int tile, part, k0, nk;
    if (e < n4) { tile = (xcd & 1) * 12 + e; part = xcd >> 1; k0 = 8 * part; nk = 8; }
    else {
      const int j = (xcd >> 1) * 9 + (xcd & 1) * 4 + (e - n4);
      part = j / 12; tile = 23 + j % 12; k0 = 11 * part; nk = part == 2 ? 10 : 11;
    }
    const int ft = tile / 5, dt = tile % 5;
    const int f0 = ft * 256, d0 = dt * 128;
    const int wp = w >> 1, wr = w & 1;
    const bf16_t* Pp = w1 + (size_t)k0 * 64 * FF + f0;
    const bf16_t* Rp = hbuf + (size_t)k0 * 64 * DM + d0;
    GemmCore<true, true, true> g;
    g.init(FF, DM, FF - f0);
    int buf = 0, pslot = 2;
    BARRIER();
    g.stage(lds, Pp, Rp);
    g.stage(lds + STAGE_BYTES, Pp + (size_t)64 * FF, Rp + (size_t)64 * DM);
    f32x16 acc[2][2] = {};
    for (int kt = 0; kt < nk; ++kt) {
      if (kt + 1 >= nk) WAIT_VM0(); else WAIT_VM6();
      BARRIER();
      if (kt + 2 < nk) g.stage(lds + pslot * STAGE_BYTES, Pp + (size_t)(kt + 2) * 64 * FF, Rp + (size_t)(kt + 2) * 64 * DM);
      g.compute(lds + buf * STAGE_BYTES, acc);
      if (++buf == 3) buf = 0;
      if (++pslot == 3) pslot = 0;
    }
#pragma unroll
    for (int i = 0; i < 2; ++i) {
      const int fb = f0 + 64 * wp + 32 * i;
      if (fb >= FF) continue;
#pragma unroll
      for (int j = 0; j < 2; ++j) {
        const int d = d0 + 64 * wr + 32 * j + r32;
        const f32x16 a = acc[i][j];
        float* orow = Up + ((size_t)part * DM + d) * FF + fb + 8 * hi;
        st16f(orow, a[0], a[1], a[2], a[3]);
        st16f(orow + 4, a[4], a[5], a[6], a[7]);
        st16f(orow + 16, a[8], a[9], a[10], a[11]);
        st16f(orow + 20, a[12], a[13], a[14], a[15]);
      }
    }
  }
  TS(0, 2);
}

constexpr int ATT_BUF = 65536;
__global__ __launch_bounds__(512, 2) void attn_kernel(const bf16_t* __restrict__ Q, const bf16_t* __restrict__ K,
                                                      const bf16_t* __restrict__ V, bf16_t* __restrict__ ctx,
                                                      const float* __restrict__ Up, const bf16_t* __restrict__ b1,
                                                      bf16_t* __restrict__ Uo) {
  __shared__ __attribute__((aligned(16))) char smem[2 * ATT_BUF + 4096];
  char LAS* lds = (char LAS*)smem;
  const int tid = threadIdx.x, l = tid & 63, w = __builtin_amdgcn_readfirstlane(tid >> 6);
  const int rg = w & 3, kh = w >> 2, r32 = l & 31, hi = l >> 5;
  const int xcd = blockIdx.x & 7, jj = blockIdx.x >> 3;
  const int head = xcd * 4 + (jj >> 3), pair = jj & 7;
  const bf16_t* Kh = K + (size_t)head * S_LEN * HD;
  const bf16_t* Vh = V + (size_t)head * S_LEN * HD;
  const bf16_t* Qh = Q + (size_t)head * S_LEN * HD;
  int kOff[4], vOff[4];
#pragma unroll
  for (int i = 0; i < 4; ++i) {
    const int j = w * 4 + i, row = 4 * j + (l >> 4), s16 = l & 15;
    kOff[i] = row * HD + ((s16 ^ (row & 15)) * 8);
    vOff[i] = row * HD + ((((s16 >> 2) ^ (row & 3)) * 4 + (s16 & 3)) * 8);
  }
  auto stage = [&](int b, int t) {
    const bf16_t* kp = Kh + (size_t)t * 128 * HD;
    const bf16_t* vp = Vh + (size_t)t * 128 * HD;
    char LAS* base = lds + b * ATT_BUF;
    const unsigned bb = (unsigned)(uintptr_t)base;
#pragma unroll
    for (int i = 0; i < 4; ++i) glds16(kp, kOff[i] * 2, bb + (w * 4 + i) * 1024);
#pragma unroll
    for (int i = 0; i < 4; ++i) glds16(vp, vOff[i] * 2, bb + 32768 + (w * 4 + i) * 1024);
  };
  const int krow = 64 * kh + r32;
  const int ksw = krow & 15;
  const int blk = (l >> 4) & 1, q4 = (l & 15) >> 2, p4 = l & 3;
  int vAddr[4];
#pragma unroll
  for (int db = 0; db < 4; ++db)
    vAddr[db] = 32768 + (64 * kh + 4 * hi + q4) * 256 + ((db ^ q4) * 64) + blk * 32 + swapmid(p4) * 8;
  constexpr float CS = 0.08838834764831845f * 1.4426950408889634f;

  TS(1, 0);
  int buf = 0;
  stage(0, 0);
  for (int g4 = tid; g4 < 1080; g4 += 512) {
    const int e = blockIdx.x * 4320 + g4 * 4;
    const int d = e / FF, f = e - d * FF;
    const int np = ((f >> 8) * 5 + (d >> 7)) >= 23 ? 3 : 4;
    f32x4 a = *(const f32x4*)(Up + e) + *(const f32x4*)(Up + (size_t)DM * FF + e) + *(const f32x4*)(Up + (size_t)2 * DM * FF + e);
    if (np == 4) a += *(const f32x4*)(Up + (size_t)3 * DM * FF + e);
    const u32x2 bv = *(const u32x2*)(b1 + f);
    const float r0 = gelu_tanh(rbf(rbf(a[0]) + __uint_as_float(bv[0] << 16)));
    const float r1 = gelu_tanh(rbf(rbf(a[1]) + __uint_as_float(bv[0] & 0xffff0000u)));
    const float r2 = gelu_tanh(rbf(rbf(a[2]) + __uint_as_float(bv[1] << 16)));
    const float r3 = gelu_tanh(rbf(rbf(a[3]) + __uint_as_float(bv[1] & 0xffff0000u)));
    *(u32x2*)(Uo + e) = (u32x2){pack2(r0, r1), pack2(r2, r3)};
  }
  for (int pass = 0; pass < 2; ++pass) {
    const int qb = pass == 0 ? (15 - pair) : pair;
    const int q0 = qb * 128 + 32 * rg;
    bf16x8 qf[8];
#pragma unroll
    for (int d0 = 0; d0 < 8; ++d0) qf[d0] = *(const bf16x8*)(Qh + (size_t)(q0 + r32) * HD + d0 * 16 + hi * 8);
    float m_run = -1e30f, l_run = 0.f;
    f32x16 o[4] = {};
    for (int t = 0; t <= qb; ++t) {
      WAIT_VM0(); BARRIER();
      if (t < qb) stage(buf ^ 1, t + 1);
      else if (pass == 0) stage(buf ^ 1, 0);
      const char LAS* base = lds + buf * ATT_BUF;
      const bool diag = (t == qb);
      const bool active = !(diag && (64 * kh > 32 * rg + 31));
      if (active) {
        f32x16 s0 = {}, s1 = {};
#pragma unroll
        for (int d0 = 0; d0 < 8; ++d0) {
          const int a = krow * 256 + (((2 * d0 + hi) ^ ksw) << 4);
          bf16x8 k0 = *(const LAS bf16x8*)(base + a);
          bf16x8 k1 = *(const LAS bf16x8*)(base + a + 32 * 256);
          s0 = __builtin_amdgcn_mfma_f32_32x32x16_bf16(k0, qf[d0], s0, 0, 0, 0);
          s1 = __builtin_amdgcn_mfma_f32_32x32x16_bf16(k1, qf[d0], s1, 0, 0, 0);
        }
        if (diag) {
          const int qi = 32 * rg + r32;
#pragma unroll
          for (int r = 0; r < 16; ++r) {
            const int key = 64 * kh + (r & 3) + 8 * (r >> 2) + 4 * hi;
            if (key > qi) s0[r] = -__builtin_inff();
            if (key + 32 > qi) s1[r] = -__builtin_inff();
          }
        }
        float pm = s0[0];
#pragma unroll
        for (int r = 1; r < 16; ++r) pm = fmaxf(pm, s0[r]);
#pragma unroll
        for (int r = 0; r < 16; ++r) pm = fmaxf(pm, s1[r]);
        pm = fmaxf(pm, __shfl_xor(pm, 32));
        const float mn = fmaxf(m_run, pm);
        const float alpha = __builtin_amdgcn_exp2f((m_run - mn) * CS);
        m_run = mn;
        const float mc = -mn * CS;
        float ps = 0.f;
#pragma unroll
        for (int r = 0; r < 16; ++r) { s0[r] = __builtin_amdgcn_exp2f(fmaf(s0[r], CS, mc)); ps += s0[r]; }
#pragma unroll
        for (int r = 0; r < 16; ++r) { s1[r] = __builtin_amdgcn_exp2f(fmaf(s1[r], CS, mc)); ps += s1[r]; }
        ps += __shfl_xor(ps, 32);
        l_run = l_run * alpha + ps;
        if (__any(alpha != 1.0f)) {
#pragma unroll
          for (int db = 0; db < 4; ++db)
#pragma unroll
            for (int r = 0; r < 16; ++r) o[db][r] *= alpha;
        }
        bf16x8 pf[4];
        {
          u32x4 a0 = {pack2(s0[0], s0[1]), pack2(s0[2], s0[3]), pack2(s0[4], s0[5]), pack2(s0[6], s0[7])};
          u32x4 a1 = {pack2(s0[8], s0[9]), pack2(s0[10], s0[11]), pack2(s0[12], s0[13]), pack2(s0[14], s0[15])};
          u32x4 a2 = {pack2(s1[0], s1[1]), pack2(s1[2], s1[3]), pack2(s1[4], s1[5]), pack2(s1[6], s1[7])};
          u32x4 a3 = {pack2(s1[8], s1[9]), pack2(s1[10], s1[11]), pack2(s1[12], s1[13]), pack2(s1[14], s1[15])};
          pf[0] = *(bf16x8*)&a0; pf[1] = *(bf16x8*)&a1; pf[2] = *(bf16x8*)&a2; pf[3] = *(bf16x8*)&a3;
        }
#pragma unroll
        for (int db = 0; db < 4; ++db) {
#pragma unroll
          for (int s = 0; s < 4; ++s) {
            bf16x8 vf = cat8(tr_read(base + vAddr[db] + s * 4096), tr_read(base + vAddr[db] + s * 4096 + 2048));
            o[db] = __builtin_amdgcn_mfma_f32_32x32x16_bf16(vf, pf[s], o[db], 0, 0, 0);
          }
        }
      }
      buf ^= 1;
    }
    BARRIER();
    {
      char LAS* xb = lds + (buf ^ 1) * ATT_BUF;
      float LAS* ml = (float LAS*)(lds + 2 * ATT_BUF);
      float LAS* dst = (float LAS*)(xb + w * 8192);
#pragma unroll
      for (int dd = 0; dd < 2; ++dd) {
        const f32x16 a = kh == 0 ? o[2 + dd] : o[dd];
#pragma unroll
        for (int g = 0; g < 4; ++g)
          *(f32x4 LAS*)(dst + ((dd * 4 + g) * 64 + l) * 4) = (f32x4){a[4 * g], a[4 * g + 1], a[4 * g + 2], a[4 * g + 3]};
      }
      ml[w * 128 + l] = m_run;
      ml[w * 128 + 64 + l] = l_run;
      asm volatile("s_waitcnt lgkmcnt(0)" ::: "memory");
      BARRIER();
      const int wo = w ^ 4;
      const float m2 = ml[wo * 128 + l], l2 = ml[wo * 128 + 64 + l];
      const float mt = fmaxf(m_run, m2);
      const float a1 = __builtin_amdgcn_exp2f((m_run - mt) * CS), a2 = __builtin_amdgcn_exp2f((m2 - mt) * CS);
      const float inv = 1.0f / (l_run * a1 + l2 * a2);
      const float f1 = a1 * inv, f2 = a2 * inv;
      const float LAS* src = (const float LAS*)(xb + wo * 8192);
      bf16_t* orow = ctx + (size_t)(q0 + r32) * CTX_LD + head * HD + 8 * hi;
#pragma unroll
      for (int dd = 0; dd < 2; ++dd) {
        const f32x16 a = kh == 0 ? o[dd] : o[2 + dd];
        float r[16];
#pragma unroll
        for (int g = 0; g < 4; ++g) {
          const f32x4 b = *(const f32x4 LAS*)(src + ((dd * 4 + g) * 64 + l) * 4);
#pragma unroll
          for (int e = 0; e < 4; ++e) r[4 * g + e] = a[4 * g + e] * f1 + b[e] * f2;
        }
        u32x4 v0 = {pack2(r[0], r[1]), pack2(r[2], r[3]), pack2(r[4], r[5]), pack2(r[6], r[7])};
        u32x4 v1 = {pack2(r[8], r[9]), pack2(r[10], r[11]), pack2(r[12], r[13]), pack2(r[14], r[15])};
        const int dcol = 32 * (2 * kh + dd);
        st16(orow + dcol, v0);
        st16(orow + dcol + 16, v1);
      }
      asm volatile("s_waitcnt lgkmcnt(0)" ::: "memory");
    }
    if (pass == 0) TS(1, 1);
  }
  TS(1, 2);
}

constexpr int G2_STAGE = 36864;
constexpr int G2_NST = 4;
__global__ __launch_bounds__(512, 2) void gemm2_kernel(const bf16_t* __restrict__ ctx, const bf16_t* __restrict__ wo,
                                                       const bf16_t* __restrict__ U, const bf16_t* __restrict__ w2,
                                                       float* __restrict__ part) {
  __shared__ __attribute__((aligned(16))) char smem[G2_NST * G2_STAGE];
  char LAS* lds = (char LAS*)smem;
  const int b = blockIdx.x, xcd = b & 7, c = b >> 3;
  const int ks = xcd >> 1;
  const int s0 = (xcd & 1) * 1024 + (c >> 2) * 128, m0 = (c & 3) * 160;
  const int l = threadIdx.x & 63, w = __builtin_amdgcn_readfirstlane(threadIdx.x >> 6);
  const int ws = w & 3, kh = w >> 2, r32 = l & 31, hi = l >> 5;
  const int blk = (l >> 4) & 1, q = (l & 15) >> 2, p = l & 3;
  TS(2, 0);
  int pOffA[2], pOffB[2], rOffA[3], rOffB[3];
#pragma unroll
  for (int i = 0; i < 2; ++i) {
    const int j = w * 2 + i;
    const int row = 8 * j + (l >> 3), slot = l & 7;
    pOffA[i] = (row * CTX_LD + (slot ^ ((row >> 1) & 7)) * 8) * 2;
    const int krow = 4 * j + (l >> 4), s16 = l & 15;
    pOffB[i] = (krow * S_LEN + ((s16 >> 2) ^ (krow & 3)) * 32 + (s16 & 3) * 8) * 2;
  }
#pragma unroll
  for (int i = 0; i < 3; ++i) {
    const int j = w + 8 * i;
    const int cb = j >> 2, kr = (j & 3) * 16 + (l >> 2);
    rOffA[i] = (kr * DM + cb * 32 + (l & 3) * 8) * 2;
    const int row = 8 * j + (l >> 3), slot = l & 7;
    rOffB[i] = (row * FF + (slot ^ ((row >> 1) & 7)) * 8) * 2;
  }
  const int f0 = ks * 7, nB = ks == 3 ? 6 : 7, total = 16 + nB;
  const bf16_t* PA = ctx + (size_t)s0 * CTX_LD + ks * 1024;
  const bf16_t* RA = wo + (size_t)ks * 1024 * DM + m0;
  const bf16_t* PB = w2 + (size_t)f0 * 64 * S_LEN + s0;
  const bf16_t* RB = U + (size_t)m0 * FF + f0 * 64;
  int pslot = 0, issued = 0;
  auto issue = [&]() {
    const unsigned bb = (unsigned)(uintptr_t)(lds + pslot * G2_STAGE);
    if (issued < 16) {
      const bf16_t* P = PA + issued * 64;
      const bf16_t* R = RA + (size_t)issued * 64 * DM;
      glds16(P, pOffA[0], bb + (w * 2) * 1024);
      glds16(P, pOffA[1], bb + (w * 2 + 1) * 1024);
      glds16(R, rOffA[0], bb + 16384 + w * 1024);
      glds16(R, rOffA[1], bb + 16384 + (w + 8) * 1024);
      if (w < 4) glds16(R, rOffA[2], bb + 16384 + (w + 16) * 1024);
    } else {
      const bf16_t* P = PB + (size_t)(issued - 16) * 64 * S_LEN;
      const bf16_t* R = RB + (issued - 16) * 64;
      glds16(P, pOffB[0], bb + (w * 2) * 1024);
      glds16(P, pOffB[1], bb + (w * 2 + 1) * 1024);
      glds16(R, rOffB[0], bb + 16384 + w * 1024);
      glds16(R, rOffB[1], bb + 16384 + (w + 8) * 1024);
      if (w < 4) glds16(R, rOffB[2], bb + 16384 + (w + 16) * 1024);
    }
    if (++pslot == G2_NST) pslot = 0;
    ++issued;
  };
  const int prow = 32 * ws + r32, psw = (prow >> 1) & 7;
  const int pA0 = prow * 128 + (((4 * kh + hi) ^ psw) << 4), pA1 = prow * 128 + (((4 * kh + 2 + hi) ^ psw) << 4);
  const int rA = 16384 + (32 * kh + 8 * hi + q) * 64 + blk * 32 + swapmid(p) * 8;
  const int pB = (32 * kh + 8 * hi + q) * 256 + ((ws ^ q) * 64) + blk * 32 + p * 8;
  const int rrow = rperm(r32), rsw = (rrow >> 1) & 7;
  const int rB0 = 16384 + rrow * 128 + (((4 * kh + hi) ^ rsw) << 4), rB1 = 16384 + rrow * 128 + (((4 * kh + 2 + hi) ^ rsw) << 4);
#pragma unroll
  for (int i = 0; i < G2_NST - 1; ++i) issue();
  f32x16 acc[5] = {};
  int buf = 0;
  for (int step = 0; step < total; ++step) {
    if (step + G2_NST - 1 > total) WAIT_VM0();
    else if (w < 4) wait_vm<5 * (G2_NST - 2)>();
    else wait_vm<4 * (G2_NST - 2)>();
    BARRIER();
    if (issued < total) issue();
    const char LAS* bp = lds + buf * G2_STAGE;
    if (step < 16) {
#pragma unroll
      for (int k2 = 0; k2 < 2; ++k2) {
        const bf16x8 pf = *(const LAS bf16x8*)(bp + (k2 ? pA1 : pA0));
#pragma unroll
        for (int j = 0; j < 5; ++j) {
          const bf16x8 rf = cat8(tr_read(bp + rA + j * 4096 + k2 * 1024), tr_read(bp + rA + j * 4096 + k2 * 1024 + 256));
          acc[j] = __builtin_amdgcn_mfma_f32_32x32x16_bf16(rf, pf, acc[j], 0, 0, 0);
        }
      }
    } else {
#pragma unroll
      for (int k2 = 0; k2 < 2; ++k2) {
        const bf16x8 pf = cat8(tr_read(bp + pB + k2 * 4096), tr_read(bp + pB + k2 * 4096 + 1024));
#pragma unroll
        for (int j = 0; j < 5; ++j) {
          const bf16x8 rf = *(const LAS bf16x8*)(bp + (k2 ? rB1 : rB0) + j * 4096);
          acc[j] = __builtin_amdgcn_mfma_f32_32x32x16_bf16(rf, pf, acc[j], 0, 0, 0);
        }
      }
    }
    if (++buf == G2_NST) buf = 0;
  }
  TS(2, 1);
  BARRIER();
  {
    float LAS* dst = (float LAS*)(lds + w * 12288);
    if (kh == 0) {
#pragma unroll
      for (int j = 3; j < 5; ++j)
#pragma unroll
        for (int g = 0; g < 4; ++g)
          *(f32x4 LAS*)(dst + (((j - 3) * 4 + g) * 64 + l) * 4) = (f32x4){acc[j][4 * g], acc[j][4 * g + 1], acc[j][4 * g + 2], acc[j][4 * g + 3]};
    } else {
#pragma unroll
      for (int j = 0; j < 3; ++j)
#pragma unroll
        for (int g = 0; g < 4; ++g)
          *(f32x4 LAS*)(dst + ((j * 4 + g) * 64 + l) * 4) = (f32x4){acc[j][4 * g], acc[j][4 * g + 1], acc[j][4 * g + 2], acc[j][4 * g + 3]};
    }
    asm volatile("s_waitcnt lgkmcnt(0)" ::: "memory");
    BARRIER();
    const float LAS* src = (const float LAS*)(lds + (w ^ 4) * 12288);
    float* orow = part + ((size_t)ks * S_LEN + s0 + 32 * ws + r32) * DM + m0 + 8 * hi;
    if (kh == 0) {
#pragma unroll
      for (int j = 0; j < 3; ++j)
#pragma unroll
        for (int g = 0; g < 4; ++g) {
          const f32x4 o = *(const f32x4 LAS*)(src + ((j * 4 + g) * 64 + l) * 4);
          st16f(orow + 32 * j + (g >> 1) * 16 + (g & 1) * 4, acc[j][4 * g] + o[0], acc[j][4 * g + 1] + o[1], acc[j][4 * g + 2] + o[2], acc[j][4 * g + 3] + o[3]);
        }
    } else {
#pragma unroll
      for (int j = 3; j < 5; ++j)
#pragma unroll
        for (int g = 0; g < 4; ++g) {
          const f32x4 o = *(const f32x4 LAS*)(src + (((j - 3) * 4 + g) * 64 + l) * 4);
          st16f(orow + 32 * j + (g >> 1) * 16 + (g & 1) * 4, acc[j][4 * g] + o[0], acc[j][4 * g + 1] + o[1], acc[j][4 * g + 2] + o[2], acc[j][4 * g + 3] + o[3]);
        }
    }
  }
  TS(2, 2);
}

__global__ __launch_bounds__(256) void reduce_kernel(const float* __restrict__ part, const bf16_t* __restrict__ x,
                                                     const bf16_t* __restrict__ b2, bf16_t* __restrict__ out) {
  const int i = blockIdx.x * 256 + threadIdx.x;
  const int e = i * 4;
  const int s = e / DM;
  f32x4 a = *(const f32x4*)(part + e);
#pragma unroll
  for (int sp = 1; sp < 4; ++sp) a += *(const f32x4*)(part + (size_t)sp * S_LEN * DM + e);
  const u32x2 xv = *(const u32x2*)(x + e);
  const float bb = bf2f(b2[s]);
  float r0 = __uint_as_float(xv[0] << 16) + a[0] + bb;
  float r1 = __uint_as_float(xv[0] & 0xffff0000u) + a[1] + bb;
  float r2 = __uint_as_float(xv[1] << 16) + a[2] + bb;
  float r3 = __uint_as_float(xv[1] & 0xffff0000u) + a[3] + bb;
  u32x2 o = {pack2(r0, r1), pack2(r2, r3)};
  *(u32x2*)(out + e) = o;
}

extern "C" void kernel_launch(void* const* d_in, const int* in_sizes, int n_in, void* d_out, int out_size, void* d_ws,
                              size_t ws_size, hipStream_t stream) {
  const bf16_t* x = (const bf16_t*)d_in[0];
  const bf16_t* ckv = (const bf16_t*)d_in[1];
  const bf16_t* wq = (const bf16_t*)d_in[2];
  const bf16_t* wuk = (const bf16_t*)d_in[3];
  const bf16_t* wuv = (const bf16_t*)d_in[4];
  const bf16_t* wo = (const bf16_t*)d_in[5];
  const bf16_t* w1 = (const bf16_t*)d_in[7];
  const bf16_t* b1 = (const bf16_t*)d_in[8];
  const bf16_t* w2 = (const bf16_t*)d_in[9];
  const bf16_t* b2 = (const bf16_t*)d_in[10];
  char* ws = (char*)d_ws;
  bf16_t* xn = (bf16_t*)(ws + WS_XN);
  bf16_t* h = (bf16_t*)(ws + WS_H);
  bf16_t* Q = (bf16_t*)(ws + WS_Q);
  bf16_t* K = (bf16_t*)(ws + WS_K);
  bf16_t* V = (bf16_t*)(ws + WS_V);
  bf16_t* U = (bf16_t*)(ws + WS_U);
  bf16_t* ctx = (bf16_t*)(ws + WS_CTX);
  float* part = (float*)(ws + WS_PART);
  float* up = (float*)(ws + WS_UP);
  ln_kernel<<<S_LEN / 4, 256, 0, stream>>>(x, xn, h);
  gemm1_kernel<<<256, 512, 0, stream>>>(xn, h, ckv, wq, wuk, wuv, w1, Q, K, V, up);
  attn_kernel<<<256, 512, 0, stream>>>(Q, K, V, ctx, up, b1, U);
  gemm2_kernel<<<256, 512, 0, stream>>>(ctx, wo, U, w2, part);
  reduce_kernel<<<S_LEN * DM / 4 / 256, 256, 0, stream>>>(part, x, b2, (bf16_t*)d_out);
}
```

```cpp
#include <hip/hip_runtime.h>
#include <hip/hip_bf16.h>
#include <stdint.h>

constexpr int S_LEN = 2048;
constexpr int DM = 640;
constexpr int NH = 32;
constexpr int HD = 128;
constexpr int DC = 512;
constexpr int FF = 1728;
constexpr int HDN = NH * HD;
constexpr int CTX_LD = HDN + 64;

typedef unsigned short bf16_t;
typedef short bf16x8 __attribute__((ext_vector_type(8)));
typedef short s16x4 __attribute__((ext_vector_type(4)));
typedef float f32x16 __attribute__((ext_vector_type(16)));
typedef float f32x4 __attribute__((ext_vector_type(4)));
typedef unsigned u32x4 __attribute__((ext_vector_type(4)));
typedef unsigned u32x2 __attribute__((ext_vector_type(2)));
typedef __bf16 bf16x2_t __attribute__((ext_vector_type(2)));
#define LAS __attribute__((address_space(3)))

__device__ __forceinline__ float bf2f(bf16_t v) { return __uint_as_float(((unsigned)v) << 16); }
__device__ __forceinline__ unsigned pack2(float a, float b) {
  bf16x2_t v = {(__bf16)a, (__bf16)b};
  return *reinterpret_cast<unsigned*>(&v);
}
__device__ __forceinline__ float rbf(float a) {
  __bf16 v = (__bf16)a;
  return __uint_as_float(((unsigned)*reinterpret_cast<unsigned short*>(&v)) << 16);
}

constexpr size_t WS_XN = 0;
constexpr size_t WS_H = WS_XN + (size_t)S_LEN * DM * 2;
constexpr size_t WS_Q = WS_H + (size_t)S_LEN * DM * 2;
constexpr size_t WS_K = WS_Q + (size_t)S_LEN * HDN * 2;
constexpr size_t WS_V = WS_K + (size_t)S_LEN * HDN * 2;
constexpr size_t WS_U = WS_V + (size_t)S_LEN * HDN * 2;
constexpr size_t WS_CTX = WS_U + (size_t)DM * FF * 2;
constexpr size_t WS_PART = WS_CTX + (size_t)S_LEN * CTX_LD * 2;
constexpr size_t WS_UP = WS_PART + (size_t)6 * S_LEN * DM * 4;

__device__ __forceinline__ float wave_sum(float v) {
#pragma unroll
  for (int o = 32; o > 0; o >>= 1) v += __shfl_xor(v, o);
  return v;
}
__device__ __forceinline__ void ln_row(float (&v)[10]) {
  float s = 0.f;
#pragma unroll
  for (int i = 0; i < 10; ++i) s += v[i];
  s = wave_sum(s);
  const float mean = s * (1.0f / DM);
  float q = 0.f;
#pragma unroll
  for (int i = 0; i < 10; ++i) { float d = v[i] - mean; q += d * d; }
  q = wave_sum(q);
  const float mb = rbf(mean);
  const float vb = rbf(q * (1.0f / DM));
  const float den = rbf(sqrtf(rbf(vb + 1e-5f)));
#pragma unroll
  for (int i = 0; i < 10; ++i) v[i] = rbf(rbf(v[i] - mb) / den);
}
__global__ __launch_bounds__(256) void ln_kernel(const bf16_t* __restrict__ x, bf16_t* __restrict__ xn,
                                                 bf16_t* __restrict__ h) {
  const int lane = threadIdx.x & 63, w = threadIdx.x >> 6;
  const int row = blockIdx.x * 4 + w;
  const unsigned* xr = reinterpret_cast<const unsigned*>(x + (size_t)row * DM);
  float v[10];
#pragma unroll
  for (int j = 0; j < 5; ++j) {
    unsigned u = xr[lane + 64 * j];
    v[2 * j] = __uint_as_float(u << 16);
    v[2 * j + 1] = __uint_as_float(u & 0xffff0000u);
  }
  ln_row(v);
  unsigned* xo = reinterpret_cast<unsigned*>(xn + (size_t)row * DM);
#pragma unroll
  for (int j = 0; j < 5; ++j) xo[lane + 64 * j] = pack2(v[2 * j], v[2 * j + 1]);
  ln_row(v);
  unsigned* ho = reinterpret_cast<unsigned*>(h + (size_t)row * DM);
#pragma unroll
  for (int j = 0; j < 5; ++j) ho[lane + 64 * j] = pack2(v[2 * j], v[2 * j + 1]);
}

constexpr int STAGE_BYTES = 49152;
constexpr int RBASE = 32768;

__device__ __forceinline__ int swapmid(int p) { return ((p & 1) << 1) | ((p >> 1) & 1); }
__device__ __forceinline__ int rperm(int r) {
  int g = r >> 2;
  int pg = (g & 4) | swapmid(g & 3);
  return pg * 4 + (r & 3);
}
__device__ __forceinline__ s16x4 tr_read(const char LAS* p) {
  return __builtin_amdgcn_ds_read_tr16_b64_v4i16((LAS s16x4*)p);
}
__device__ __forceinline__ bf16x8 cat8(s16x4 a, s16x4 b) {
  return (bf16x8){a[0], a[1], a[2], a[3], b[0], b[1], b[2], b[3]};
}

__device__ __forceinline__ void glds16(const void* sbase, int voff, unsigned lds_dst) {
  unsigned keep;
  asm volatile("s_mov_b32 %0, m0\n\ts_mov_b32 m0, %3\n\ts_nop 0\n\tglobal_load_lds_dwordx4 %1, %2\n\ts_mov_b32 m0, %0"
               : "=&s"(keep) : "v"(voff), "s"(sbase), "s"(lds_dst) : "memory");
}

#define ST_MODE 0
__device__ __forceinline__ void st16(void* p, u32x4 v) {
  *(u32x4*)p = v;
}
__device__ __forceinline__ void st16f(float* p, float a, float b, float c, float d) {
  st16(p, (u32x4){__float_as_uint(a), __float_as_uint(b), __float_as_uint(c), __float_as_uint(d)});
}

template <bool P_TR, bool R_TR, bool ROWS_P>
struct GemmCore {
  int pOff[4], rOff[2];
  int pAddr[4], rAddr[4];
  int w, l;
  __device__ __forceinline__ void init(int ldp, int ldr, int pcol_limit  ) {
    const int tid = threadIdx.x;
    l = tid & 63;
    w = __builtin_amdgcn_readfirstlane(tid >> 6);
    const int wp = w >> 1, wr = w & 1;
#pragma unroll
    for (int i = 0; i < 4; ++i) {
      const int j = w * 4 + i;
      if (!P_TR) {
        const int row = 8 * j + (l >> 3), slot = l & 7;
        const int chunk = slot ^ ((row >> 1) & 7);
        pOff[i] = row * ldp + chunk * 8;
      } else {
        const int half = j >> 4, jj = j & 15;
        const int krow = 4 * jj + (l >> 4), s16 = l & 15;
        const int c64 = (s16 >> 2) ^ (krow & 3);
        int col = half * 128 + c64 * 32 + (s16 & 3) * 8;
        if (col >= pcol_limit) col = pcol_limit - 8;
        pOff[i] = krow * ldp + col;
      }
    }
#pragma unroll
    for (int i = 0; i < 2; ++i) {
      const int j = w * 2 + i;
      if (!R_TR) {
        const int row = 8 * j + (l >> 3), slot = l & 7;
        const int chunk = slot ^ ((row >> 1) & 7);
        rOff[i] = row * ldr + chunk * 8;
      } else {
        const int krow = 4 * j + (l >> 4), s16 = l & 15;
        const int c64 = (s16 >> 2) ^ (krow & 3);
        const int col = c64 * 32 + (s16 & 3) * 8;
        rOff[i] = krow * ldr + col;
      }
    }
    const int r32 = l & 31, hi = l >> 5, blk = (l >> 4) & 1, q = (l & 15) >> 2, p = l & 3;
    if (!P_TR) {
      const int prow = 64 * wp + (ROWS_P ? rperm(r32) : r32);
      const int s = (prow >> 1) & 7;
#pragma unroll
      for (int kk = 0; kk < 4; ++kk) pAddr[kk] = prow * 128 + (((2 * kk + hi) ^ s) << 4);
    } else {
      const int half = wp >> 1, pp = ROWS_P ? swapmid(p) : p;
#pragma unroll
      for (int i = 0; i < 2; ++i) {
        const int c64 = 2 * (wp & 1) + i;
        pAddr[i] = half * 16384 + (8 * hi + q) * 256 + ((c64 ^ q) * 64) + blk * 32 + pp * 8;
      }
      pAddr[2] = pAddr[3] = 0;
    }
    if (!R_TR) {
      const int rrow = 64 * wr + (!ROWS_P ? rperm(r32) : r32);
      const int s = (rrow >> 1) & 7;
#pragma unroll
      for (int kk = 0; kk < 4; ++kk) rAddr[kk] = RBASE + rrow * 128 + (((2 * kk + hi) ^ s) << 4);
    } else {
      const int pp = !ROWS_P ? swapmid(p) : p;
#pragma unroll
      for (int i = 0; i < 2; ++i) {
        const int c64 = 2 * wr + i;
        rAddr[i] = RBASE + (8 * hi + q) * 256 + ((c64 ^ q) * 64) + blk * 32 + pp * 8;
      }
      rAddr[2] = rAddr[3] = 0;
    }
  }
  __device__ __forceinline__ void stage(char LAS* buf, const bf16_t* P, const bf16_t* R) {
    const unsigned b = (unsigned)(uintptr_t)buf;
#pragma unroll
    for (int i = 0; i < 4; ++i) glds16(P, pOff[i] * 2, b + (w * 4 + i) * 1024);
#pragma unroll
    for (int i = 0; i < 2; ++i) glds16(R, rOff[i] * 2, b + RBASE + (w * 2 + i) * 1024);
  }
  __device__ __forceinline__ void compute(const char LAS* buf, f32x16 (&acc)[2][2]) {
#pragma unroll
    for (int kk = 0; kk < 4; ++kk) {
      bf16x8 pf[2], rf[2];
#pragma unroll
      for (int i = 0; i < 2; ++i) {
        if (!P_TR) pf[i] = *(const LAS bf16x8*)(buf + pAddr[kk] + i * 4096);
        else pf[i] = cat8(tr_read(buf + pAddr[i] + kk * 4096), tr_read(buf + pAddr[i] + kk * 4096 + 1024));
        if (!R_TR) rf[i] = *(const LAS bf16x8*)(buf + rAddr[kk] + i * 4096);
        else rf[i] = cat8(tr_read(buf + rAddr[i] + kk * 4096), tr_read(buf + rAddr[i] + kk * 4096 + 1024));
      }
#pragma unroll
      for (int i = 0; i < 2; ++i)
#pragma unroll
        for (int j = 0; j < 2; ++j) {
          if (ROWS_P) acc[i][j] = __builtin_amdgcn_mfma_f32_32x32x16_bf16(pf[i], rf[j], acc[i][j], 0, 0, 0);
          else acc[i][j] = __builtin_amdgcn_mfma_f32_32x32x16_bf16(rf[j], pf[i], acc[i][j], 0, 0, 0);
        }
    }
  }
};

#define WAIT_VM0() asm volatile("s_waitcnt vmcnt(0)" ::: "memory")
#define WAIT_VM6() asm volatile("s_waitcnt vmcnt(6)" ::: "memory")
#define WAIT_VM16() asm volatile("s_waitcnt vmcnt(16)" ::: "memory")
#define BARRIER() __builtin_amdgcn_s_barrier()
#define TS(k, i)

__device__ __forceinline__ float gelu_tanh(float x) {
  const float y = 0.7978845608028654f * (x + 0.044715f * x * x * x);
  const float e = __expf(2.0f * y);
  const float t = 1.0f - 2.0f / (e + 1.0f);
  return 0.5f * x * (1.0f + t);
}

constexpr int STAGE256 = 32768;
#define NST256 4
struct Core256 {
  int pOff[2], rOff[2];
  int pAddr[2], rAddr[2];
  int w;
  __device__ __forceinline__ void init(int ldp, int ldr) {
    const int tid = threadIdx.x, l = tid & 63;
    w = __builtin_amdgcn_readfirstlane(tid >> 6);
    const int wp = w >> 2, wr = w & 3;
#pragma unroll
    for (int i = 0; i < 2; ++i) {
      const int j = w * 2 + i;
      const int row = 16 * j + (l >> 2), slot = l & 3;
      pOff[i] = (row * ldp + (slot ^ ((row >> 2) & 3)) * 8) * 2;
      const int half = j >> 3, jj = j & 7;
      const int krow = 4 * jj + (l >> 4), s16 = l & 15;
      const int c64 = (s16 >> 2) ^ (krow & 3);
      rOff[i] = (krow * ldr + half * 128 + c64 * 32 + (s16 & 3) * 8) * 2;
    }
    const int r32 = l & 31, hi = l >> 5, blk = (l >> 4) & 1, q = (l & 15) >> 2, p = l & 3;
    const int prow = 128 * wp + r32, s = (prow >> 2) & 3;
#pragma unroll
    for (int kk = 0; kk < 2; ++kk) pAddr[kk] = prow * 64 + (((2 * kk + hi) ^ s) << 4);
#pragma unroll
    for (int j = 0; j < 2; ++j) {
      const int c64 = 2 * (wr & 1) + j;
      rAddr[j] = 16384 + (wr >> 1) * 8192 + (8 * hi + q) * 256 + ((c64 ^ q) * 64) + blk * 32 + swapmid(p) * 8;
    }
  }
  __device__ __forceinline__ void stage(char LAS* buf, const bf16_t* P, const bf16_t* R) {
    const unsigned b = (unsigned)(uintptr_t)buf;
#pragma unroll
    for (int i = 0; i < 2; ++i) glds16(P, pOff[i], b + (w * 2 + i) * 1024);
#pragma unroll
    for (int i = 0; i < 2; ++i) glds16(R, rOff[i], b + 16384 + (w * 2 + i) * 1024);
  }
  __device__ __forceinline__ void compute(const char LAS* buf, f32x16 (&acc)[4][2]) {
#pragma unroll
    for (int kk = 0; kk < 2; ++kk) {
      bf16x8 pf[4], rf[2];
#pragma unroll
      for (int j = 0; j < 2; ++j) rf[j] = cat8(tr_read(buf + rAddr[j] + kk * 4096), tr_read(buf + rAddr[j] + kk * 4096 + 1024));
#pragma unroll
      for (int i = 0; i < 4; ++i) pf[i] = *(const LAS bf16x8*)(buf + pAddr[kk] + i * 2048);
#pragma unroll
      for (int i = 0; i < 4; ++i)
#pragma unroll
        for (int j = 0; j < 2; ++j) acc[i][j] = __builtin_amdgcn_mfma_f32_32x32x16_bf16(rf[j], pf[i], acc[i][j], 0, 0, 0);
    }
  }
  __device__ __forceinline__ void load_frags(const char LAS* buf, bf16x8 (&pf)[2][4], bf16x8 (&rf)[2][2]) {
#pragma unroll
    for (int kk = 0; kk < 2; ++kk) {
#pragma unroll
      for (int j = 0; j < 2; ++j) rf[kk][j] = cat8(tr_read(buf + rAddr[j] + kk * 4096), tr_read(buf + rAddr[j] + kk * 4096 + 1024));
#pragma unroll
      for (int i = 0; i < 4; ++i) pf[kk][i] = *(const LAS bf16x8*)(buf + pAddr[kk] + i * 2048);
    }
  }
  __device__ __forceinline__ void mma(const bf16x8 (&pf)[2][4], const bf16x8 (&rf)[2][2], f32x16 (&acc)[4][2]) {
#pragma unroll
    for (int kk = 0; kk < 2; ++kk)
#pragma unroll
      for (int i = 0; i < 4; ++i)
#pragma unroll
        for (int j = 0; j < 2; ++j) acc[i][j] = __builtin_amdgcn_mfma_f32_32x32x16_bf16(rf[kk][j], pf[kk][i], acc[i][j], 0, 0, 0);
  }
};
#define WAIT_LGKM0() asm volatile("s_waitcnt lgkmcnt(0)" ::: "memory")
#define SCHED0() __builtin_amdgcn_sched_barrier(0)
template <int N> __device__ __forceinline__ void wait_vm() { asm volatile("s_waitcnt vmcnt(%0)" ::"n"(N) : "memory"); }

__global__ __launch_bounds__(512, 2) void gemm1_kernel(const bf16_t* __restrict__ xn, const bf16_t* __restrict__ hbuf,
                                                       const bf16_t* __restrict__ ckv, const bf16_t* __restrict__ wq,
                                                       const bf16_t* __restrict__ wuk, const bf16_t* __restrict__ wuv,
                                                       const bf16_t* __restrict__ w1,
                                                       bf16_t* __restrict__ Qo, bf16_t* __restrict__ Ko,
                                                       bf16_t* __restrict__ Vo, float* __restrict__ Up) {
  __shared__ __attribute__((aligned(16))) char smem[NST256 * STAGE256 + 32768];
  char LAS* lds = (char LAS*)smem;
  const int xcd = blockIdx.x & 7, c = blockIdx.x >> 3;
  const int l = threadIdx.x & 63, w = __builtin_amdgcn_readfirstlane(threadIdx.x >> 6);
  const int r32 = l & 31, hi = l >> 5;
  TS(0, 0);
  const bool isQ = c >= 16;
  {
    int ntile, tb0, hp;
    const bf16_t* A; const bf16_t* W; bf16_t* O; int K;
    if (!isQ) {
      ntile = 2; tb0 = c >> 2; hp = xcd * 2 + (c & 1);
      A = ckv; K = DC;
      W = (c & 2) ? wuv : wuk; O = (c & 2) ? Vo : Ko;
    } else {
      ntile = 1; tb0 = (c - 16) >> 1; hp = xcd * 2 + (c & 1);
      A = xn; K = DM; W = wq; O = Qo;
    }
    const int nk = K / 32;
    Core256 g;
    g.init(K, HDN);
    const bf16_t* Wp = W + hp * 256;
    const int wp = w >> 2, wr = w & 3;
    const int total = ntile * nk;
    int pt = 0, pk = 0, pslot = 0, issued = 0;
    auto issue = [&]() {
      g.stage(lds + pslot * STAGE256, A + (size_t)(tb0 + 4 * pt) * 256 * K + pk * 32, Wp + (size_t)pk * 32 * HDN);
      if (++pk == nk) { pk = 0; ++pt; }
      if (++pslot == NST256) pslot = 0;
      ++issued;
    };
#pragma unroll
    for (int i = 0; i < NST256 - 1; ++i) issue();
    int buf = 0, step = 0;
    wait_vm<4 * (NST256 - 2)>();
    BARRIER();
    if (wp == 1) BARRIER();
    for (int t = 0; t < ntile; ++t) {
      const int tb = tb0 + 4 * t;
      f32x16 acc[4][2] = {};
      for (int kt = 0; kt < nk; ++kt) {
        bf16x8 pf[2][4], rf[2][2];
        if (issued < total) issue();
        g.load_frags(lds + buf * STAGE256, pf, rf);
        if (step + NST256 > total) WAIT_VM0();
        else if (t > 0 && kt < NST256 - 2) wait_vm<4 * (NST256 - 2) + 16>();
        else wait_vm<4 * (NST256 - 2)>();
        WAIT_LGKM0();
        SCHED0();
        BARRIER();
        SCHED0();
        __builtin_amdgcn_s_setprio(1);
        g.mma(pf, rf, acc);
        __builtin_amdgcn_s_setprio(0);
        SCHED0();
        BARRIER();
        SCHED0();
        if (++buf == NST256) buf = 0;
        ++step;
      }
      const int head = hp * 2 + (wr >> 1);
      char LAS* slab = lds + NST256 * STAGE256 + w * 4096;
      const int wa = r32 * 128, wsw = r32 & 7;
      const int rrow = l >> 3, ra = rrow * 128 + (((l & 7) ^ rrow) << 4);
      bf16_t* obase = O + ((size_t)head * S_LEN + tb * 256 + 128 * wp + rrow) * HD + 64 * (wr & 1) + (l & 7) * 8;
#pragma unroll
      for (int i = 0; i < 4; ++i) {
#pragma unroll
        for (int j = 0; j < 2; ++j) {
          const f32x16 a = acc[i][j];
          u32x4 v0 = {pack2(a[0], a[1]), pack2(a[2], a[3]), pack2(a[4], a[5]), pack2(a[6], a[7])};
          u32x4 v1 = {pack2(a[8], a[9]), pack2(a[10], a[11]), pack2(a[12], a[13]), pack2(a[14], a[15])};
          *(u32x4 LAS*)(slab + wa + (((4 * j + hi) ^ wsw) << 4)) = v0;
          *(u32x4 LAS*)(slab + wa + (((4 * j + 2 + hi) ^ wsw) << 4)) = v1;
        }
#pragma unroll
        for (int it = 0; it < 4; ++it) {
          const u32x4 v = *(const u32x4 LAS*)(slab + ra + it * 1024);
          st16(obase + (size_t)(32 * i + 8 * it) * HD, v);
        }
      }
    }
    if (wp == 0) BARRIER();
  }
  TS(0, 1);
  if (isQ) {
    const int e = c - 16, n4 = (xcd & 1) ? 11 : 12;
    int tile, part, k0, nk;
    if (e < n4) { tile = (xcd & 1) * 12 + e; part = xcd >> 1; k0 = 8 * part; nk = 8; }
    else {
      const int j = (xcd >> 1) * 9 + (xcd & 1) * 4 + (e - n4);
      part = j / 12; tile = 23 + j % 12; k0 = 11 * part; nk = part == 2 ? 10 : 11;
    }
    const int ft = tile / 5, dt = tile % 5;
    const int f0 = ft * 256, d0 = dt * 128;
    const int wp = w >> 1, wr = w & 1;
    const bf16_t* Pp = w1 + (size_t)k0 * 64 * FF + f0;
    const bf16_t* Rp = hbuf + (size_t)k0 * 64 * DM + d0;
    GemmCore<true, true, true> g;
    g.init(FF, DM, FF - f0);
    int buf = 0, pslot = 2;
    BARRIER();
    g.stage(lds, Pp, Rp);
    g.stage(lds + STAGE_BYTES, Pp + (size_t)64 * FF, Rp + (size_t)64 * DM);
    f32x16 acc[2][2] = {};
    for (int kt = 0; kt < nk; ++kt) {
      if (kt + 1 >= nk) WAIT_VM0(); else WAIT_VM6();
      BARRIER();
      if (kt + 2 < nk) g.stage(lds + pslot * STAGE_BYTES, Pp + (size_t)(kt + 2) * 64 * FF, Rp + (size_t)(kt + 2) * 64 * DM);
      g.compute(lds + buf * STAGE_BYTES, acc);
      if (++buf == 3) buf = 0;
      if (++pslot == 3) pslot = 0;
    }
#pragma unroll
    for (int i = 0; i < 2; ++i) {
      const int fb = f0 + 64 * wp + 32 * i;
      if (fb >= FF) continue;
#pragma unroll
      for (int j = 0; j < 2; ++j) {
        const int d = d0 + 64 * wr + 32 * j + r32;
        const f32x16 a = acc[i][j];
        float* orow = Up + ((size_t)part * DM + d) * FF + fb + 8 * hi;
        st16f(orow, a[0], a[1], a[2], a[3]);
        st16f(orow + 4, a[4], a[5], a[6], a[7]);
        st16f(orow + 16, a[8], a[9], a[10], a[11]);
        st16f(orow + 20, a[12], a[13], a[14], a[15]);
      }
    }
  }
  TS(0, 2);
}

constexpr int ATT_BUF = 65536;
__global__ __launch_bounds__(512, 2) void attn_kernel(const bf16_t* __restrict__ Q, const bf16_t* __restrict__ K,
                                                      const bf16_t* __restrict__ V, bf16_t* __restrict__ ctx,
                                                      const float* __restrict__ Up, const bf16_t* __restrict__ b1,
                                                      bf16_t* __restrict__ Uo) {
  __shared__ __attribute__((aligned(16))) char smem[2 * ATT_BUF + 4096];
  char LAS* lds = (char LAS*)smem;
  const int tid = threadIdx.x, l = tid & 63, w = __builtin_amdgcn_readfirstlane(tid >> 6);
  const int rg = w & 3, kh = w >> 2, r32 = l & 31, hi = l >> 5;
  const int xcd = blockIdx.x & 7, jj = blockIdx.x >> 3;
  const int head = xcd * 4 + (jj >> 3), pair = jj & 7;
  const bf16_t* Kh = K + (size_t)head * S_LEN * HD;
  const bf16_t* Vh = V + (size_t)head * S_LEN * HD;
  const bf16_t* Qh = Q + (size_t)head * S_LEN * HD;
  int kOff[4], vOff[4];
#pragma unroll
  for (int i = 0; i < 4; ++i) {
    const int j = w * 4 + i, row = 4 * j + (l >> 4), s16 = l & 15;
    kOff[i] = row * HD + ((s16 ^ (row & 15)) * 8);
    vOff[i] = row * HD + ((((s16 >> 2) ^ (row & 3)) * 4 + (s16 & 3)) * 8);
  }
  auto stage = [&](int b, int t) {
    const bf16_t* kp = Kh + (size_t)t * 128 * HD;
    const bf16_t* vp = Vh + (size_t)t * 128 * HD;
    char LAS* base = lds + b * ATT_BUF;
    const unsigned bb = (unsigned)(uintptr_t)base;
#pragma unroll
    for (int i = 0; i < 4; ++i) glds16(kp, kOff[i] * 2, bb + (w * 4 + i) * 1024);
#pragma unroll
    for (int i = 0; i < 4; ++i) glds16(vp, vOff[i] * 2, bb + 32768 + (w * 4 + i) * 1024);
  };
  const int krow = 64 * kh + r32;
  const int ksw = krow & 15;
  const int blk = (l >> 4) & 1, q4 = (l & 15) >> 2, p4 = l & 3;
  int vAddr[4];
#pragma unroll
  for (int db = 0; db < 4; ++db)
    vAddr[db] = 32768 + (64 * kh + 4 * hi + q4) * 256 + ((db ^ q4) * 64) + blk * 32 + swapmid(p4) * 8;
  constexpr float CS = 0.08838834764831845f * 1.4426950408889634f;

  TS(1, 0);
  int buf = 0;
  stage(0, 0);
  for (int g4 = tid; g4 < 1080; g4 += 512) {
    const int e = blockIdx.x * 4320 + g4 * 4;
    const int d = e / FF, f = e - d * FF;
    const int np = ((f >> 8) * 5 + (d >> 7)) >= 23 ? 3 : 4;
    f32x4 a = *(const f32x4*)(Up + e) + *(const f32x4*)(Up + (size_t)DM * FF + e) + *(const f32x4*)(Up + (size_t)2 * DM * FF + e);
    if (np == 4) a += *(const f32x4*)(Up + (size_t)3 * DM * FF + e);
    const u32x2 bv = *(const u32x2*)(b1 + f);
    const float r0 = gelu_tanh(rbf(rbf(a[0]) + __uint_as_float(bv[0] << 16)));
    const float r1 = gelu_tanh(rbf(rbf(a[1]) + __uint_as_float(bv[0] & 0xffff0000u)));
    const float r2 = gelu_tanh(rbf(rbf(a[2]) + __uint_as_float(bv[1] << 16)));
    const float r3 = gelu_tanh(rbf(rbf(a[3]) + __uint_as_float(bv[1] & 0xffff0000u)));
    *(u32x2*)(Uo + e) = (u32x2){pack2(r0, r1), pack2(r2, r3)};
  }
  for (int pass = 0; pass < 2; ++pass) {
    const int qb = pass == 0 ? (15 - pair) : pair;
    const int q0 = qb * 128 + 32 * rg;
    bf16x8 qf[8];
#pragma unroll
    for (int d0 = 0; d0 < 8; ++d0) qf[d0] = *(const bf16x8*)(Qh + (size_t)(q0 + r32) * HD + d0 * 16 + hi * 8);
    float m_run = -1e30f, l_run = 0.f;
    f32x16 o[4] = {};
    for (int t = 0; t <= qb; ++t) {
      WAIT_VM0(); BARRIER();
      if (t < qb) stage(buf ^ 1, t + 1);
      else if (pass == 0) stage(buf ^ 1, 0);
      const char LAS* base = lds + buf * ATT_BUF;
      const bool diag = (t == qb);
      const bool active = !(diag && (64 * kh > 32 * rg + 31));
      if (active) {
        f32x16 s0 = {}, s1 = {};
#pragma unroll
        for (int d0 = 0; d0 < 8; ++d0) {
          const int a = krow * 256 + (((2 * d0 + hi) ^ ksw) << 4);
          bf16x8 k0 = *(const LAS bf16x8*)(base + a);
          bf16x8 k1 = *(const LAS bf16x8*)(base + a + 32 * 256);
          s0 = __builtin_amdgcn_mfma_f32_32x32x16_bf16(k0, qf[d0], s0, 0, 0, 0);
          s1 = __builtin_amdgcn_mfma_f32_32x32x16_bf16(k1, qf[d0], s1, 0, 0, 0);
        }
        if (diag) {
          const int qi = 32 * rg + r32;
#pragma unroll
          for (int r = 0; r < 16; ++r) {
            const int key = 64 * kh + (r & 3) + 8 * (r >> 2) + 4 * hi;
            if (key > qi) s0[r] = -__builtin_inff();
            if (key + 32 > qi) s1[r] = -__builtin_inff();
          }
        }
        float pm = s0[0];
#pragma unroll
        for (int r = 1; r < 16; ++r) pm = fmaxf(pm, s0[r]);
#pragma unroll
        for (int r = 0; r < 16; ++r) pm = fmaxf(pm, s1[r]);
        pm = fmaxf(pm, __shfl_xor(pm, 32));
        const float mn = fmaxf(m_run, pm);
        const float alpha = __builtin_amdgcn_exp2f((m_run - mn) * CS);
        m_run = mn;
        const float mc = -mn * CS;
        float ps = 0.f;
#pragma unroll
        for (int r = 0; r < 16; ++r) { s0[r] = __builtin_amdgcn_exp2f(fmaf(s0[r], CS, mc)); ps += s0[r]; }
#pragma unroll
        for (int r = 0; r < 16; ++r) { s1[r] = __builtin_amdgcn_exp2f(fmaf(s1[r], CS, mc)); ps += s1[r]; }
        ps += __shfl_xor(ps, 32);
        l_run = l_run * alpha + ps;
        if (__any(alpha != 1.0f)) {
#pragma unroll
          for (int db = 0; db < 4; ++db)
#pragma unroll
            for (int r = 0; r < 16; ++r) o[db][r] *= alpha;
        }
        bf16x8 pf[4];
        {
          u32x4 a0 = {pack2(s0[0], s0[1]), pack2(s0[2], s0[3]), pack2(s0[4], s0[5]), pack2(s0[6], s0[7])};
          u32x4 a1 = {pack2(s0[8], s0[9]), pack2(s0[10], s0[11]), pack2(s0[12], s0[13]), pack2(s0[14], s0[15])};
          u32x4 a2 = {pack2(s1[0], s1[1]), pack2(s1[2], s1[3]), pack2(s1[4], s1[5]), pack2(s1[6], s1[7])};
          u32x4 a3 = {pack2(s1[8], s1[9]), pack2(s1[10], s1[11]), pack2(s1[12], s1[13]), pack2(s1[14], s1[15])};
          pf[0] = *(bf16x8*)&a0; pf[1] = *(bf16x8*)&a1; pf[2] = *(bf16x8*)&a2; pf[3] = *(bf16x8*)&a3;
        }
#pragma unroll
        for (int db = 0; db < 4; ++db) {
#pragma unroll
          for (int s = 0; s < 4; ++s) {
            bf16x8 vf = cat8(tr_read(base + vAddr[db] + s * 4096), tr_read(base + vAddr[db] + s * 4096 + 2048));
            o[db] = __builtin_amdgcn_mfma_f32_32x32x16_bf16(vf, pf[s], o[db], 0, 0, 0);
          }
        }
      }
      buf ^= 1;
    }
    BARRIER();
    {
      char LAS* xb = lds + (buf ^ 1) * ATT_BUF;
      float LAS* ml = (float LAS*)(lds + 2 * ATT_BUF);
      float LAS* dst = (float LAS*)(xb + w * 8192);
#pragma unroll
      for (int dd = 0; dd < 2; ++dd) {
        const f32x16 a = kh == 0 ? o[2 + dd] : o[dd];
#pragma unroll
        for (int g = 0; g < 4; ++g)
          *(f32x4 LAS*)(dst + ((dd * 4 + g) * 64 + l) * 4) = (f32x4){a[4 * g], a[4 * g + 1], a[4 * g + 2], a[4 * g + 3]};
      }
      ml[w * 128 + l] = m_run;
      ml[w * 128 + 64 + l] = l_run;
      asm volatile("s_waitcnt lgkmcnt(0)" ::: "memory");
      BARRIER();
      const int wo = w ^ 4;
      const float m2 = ml[wo * 128 + l], l2 = ml[wo * 128 + 64 + l];
      const float mt = fmaxf(m_run, m2);
      const float a1 = __builtin_amdgcn_exp2f((m_run - mt) * CS), a2 = __builtin_amdgcn_exp2f((m2 - mt) * CS);
      const float inv = 1.0f / (l_run * a1 + l2 * a2);
      const float f1 = a1 * inv, f2 = a2 * inv;
      const float LAS* src = (const float LAS*)(xb + wo * 8192);
      bf16_t* orow = ctx + (size_t)(q0 + r32) * CTX_LD + head * HD + 8 * hi;
#pragma unroll
      for (int dd = 0; dd < 2; ++dd) {
        const f32x16 a = kh == 0 ? o[dd] : o[2 + dd];
        float r[16];
#pragma unroll
        for (int g = 0; g < 4; ++g) {
          const f32x4 b = *(const f32x4 LAS*)(src + ((dd * 4 + g) * 64 + l) * 4);
#pragma unroll
          for (int e = 0; e < 4; ++e) r[4 * g + e] = a[4 * g + e] * f1 + b[e] * f2;
        }
        u32x4 v0 = {pack2(r[0], r[1]), pack2(r[2], r[3]), pack2(r[4], r[5]), pack2(r[6], r[7])};
        u32x4 v1 = {pack2(r[8], r[9]), pack2(r[10], r[11]), pack2(r[12], r[13]), pack2(r[14], r[15])};
        const int dcol = 32 * (2 * kh + dd);
        st16(orow + dcol, v0);
        st16(orow + dcol + 16, v1);
      }
      asm volatile("s_waitcnt lgkmcnt(0)" ::: "memory");
    }
    if (pass == 0) TS(1, 1);
  }
  TS(1, 2);
}

constexpr int G2_STAGE = 36864;
constexpr int G2_NST = 4;
__global__ __launch_bounds__(512, 2) void gemm2_kernel(const bf16_t* __restrict__ ctx, const bf16_t* __restrict__ wo,
                                                       const bf16_t* __restrict__ U, const bf16_t* __restrict__ w2,
                                                       float* __restrict__ part) {
  __shared__ __attribute__((aligned(16))) char smem[G2_NST * G2_STAGE];
  char LAS* lds = (char LAS*)smem;
  const int b = blockIdx.x, xcd = b & 7, c = b >> 3;
  const int ks = xcd >> 1;
  const int s0 = (xcd & 1) * 1024 + (c >> 2) * 128, m0 = (c & 3) * 160;
  const int l = threadIdx.x & 63, w = __builtin_amdgcn_readfirstlane(threadIdx.x >> 6);
  const int ws = w & 3, kh = w >> 2, r32 = l & 31, hi = l >> 5;
  const int blk = (l >> 4) & 1, q = (l & 15) >> 2, p = l & 3;
  TS(2, 0);
  int pOffA[2], pOffB[2], rOffA[3], rOffB[3];
#pragma unroll
  for (int i = 0; i < 2; ++i) {
    const int j = w * 2 + i;
    const int row = 8 * j + (l >> 3), slot = l & 7;
    pOffA[i] = (row * CTX_LD + (slot ^ ((row >> 1) & 7)) * 8) * 2;
    const int krow = 4 * j + (l >> 4), s16 = l & 15;
    pOffB[i] = (krow * S_LEN + ((s16 >> 2) ^ (krow & 3)) * 32 + (s16 & 3) * 8) * 2;
  }
#pragma unroll
  for (int i = 0; i < 3; ++i) {
    const int j = w + 8 * i;
    const int cb = j >> 2, kr = (j & 3) * 16 + (l >> 2);
    rOffA[i] = (kr * DM + cb * 32 + (l & 3) * 8) * 2;
    const int row = 8 * j + (l >> 3), slot = l & 7;
    rOffB[i] = (row * FF + (slot ^ ((row >> 1) & 7)) * 8) * 2;
  }
  const int f0 = ks * 7, nB = ks == 3 ? 6 : 7, total = 16 + nB;
  const bf16_t* PA = ctx + (size_t)s0 * CTX_LD + ks * 1024;
  const bf16_t* RA = wo + (size_t)ks * 1024 * DM + m0;
  const bf16_t* PB = w2 + (size_t)f0 * 64 * S_LEN + s0;
  const bf16_t* RB = U + (size_t)m0 * FF + f0 * 64;
  int pslot = 0, issued = 0;
  auto issue = [&]() {
    const unsigned bb = (unsigned)(uintptr_t)(lds + pslot * G2_STAGE);
    if (issued < 16) {
      const bf16_t* P = PA + issued * 64;
      const bf16_t* R = RA + (size_t)issued * 64 * DM;
      glds16(P, pOffA[0], bb + (w * 2) * 1024);
      glds16(P, pOffA[1], bb + (w * 2 + 1) * 1024);
      glds16(R, rOffA[0], bb + 16384 + w * 1024);
      glds16(R, rOffA[1], bb + 16384 + (w + 8) * 1024);
      if (w < 4) glds16(R, rOffA[2], bb + 16384 + (w + 16) * 1024);
    } else {
      const bf16_t* P = PB + (size_t)(issued - 16) * 64 * S_LEN;
      const bf16_t* R = RB + (issued - 16) * 64;
      glds16(P, pOffB[0], bb + (w * 2) * 1024);
      glds16(P, pOffB[1], bb + (w * 2 + 1) * 1024);
      glds16(R, rOffB[0], bb + 16384 + w * 1024);
      glds16(R, rOffB[1], bb + 16384 + (w + 8) * 1024);
      if (w < 4) glds16(R, rOffB[2], bb + 16384 + (w + 16) * 1024);
    }
    if (++pslot == G2_NST) pslot = 0;
    ++issued;
  };
  const int prow = 32 * ws + r32, psw = (prow >> 1) & 7;
  const int pA0 = prow * 128 + (((4 * kh + hi) ^ psw) << 4), pA1 = prow * 128 + (((4 * kh + 2 + hi) ^ psw) << 4);
  const int rA = 16384 + (32 * kh + 8 * hi + q) * 64 + blk * 32 + swapmid(p) * 8;
  const int pB = (32 * kh + 8 * hi + q) * 256 + ((ws ^ q) * 64) + blk * 32 + p * 8;
  const int rrow = rperm(r32), rsw = (rrow >> 1) & 7;
  const int rB0 = 16384 + rrow * 128 + (((4 * kh + hi) ^ rsw) << 4), rB1 = 16384 + rrow * 128 + (((4 * kh + 2 + hi) ^ rsw) << 4);
#pragma unroll
  for (int i = 0; i < G2_NST - 1; ++i) issue();
  f32x16 acc[5] = {};
  int buf = 0;
  for (int step = 0; step < 16; ++step) {
    if (w < 4) wait_vm<5 * (G2_NST - 2)>();
    else wait_vm<4 * (G2_NST - 2)>();
    BARRIER();
    issue();
    const char LAS* bp = lds + buf * G2_STAGE;
    bf16x8 pf[2], rf[2][5];
    pf[0] = *(const LAS bf16x8*)(bp + pA0);
#pragma unroll
    for (int j = 0; j < 5; ++j) rf[0][j] = cat8(tr_read(bp + rA + j * 4096), tr_read(bp + rA + j * 4096 + 256));
    pf[1] = *(const LAS bf16x8*)(bp + pA1);
#pragma unroll
    for (int j = 0; j < 5; ++j) rf[1][j] = cat8(tr_read(bp + rA + j * 4096 + 1024), tr_read(bp + rA + j * 4096 + 1024 + 256));
    __builtin_amdgcn_sched_barrier(0);
#pragma unroll
    for (int k2 = 0; k2 < 2; ++k2)
#pragma unroll
      for (int j = 0; j < 5; ++j) acc[j] = __builtin_amdgcn_mfma_f32_32x32x16_bf16(rf[k2][j], pf[k2], acc[j], 0, 0, 0);
    if (++buf == G2_NST) buf = 0;
  }
  for (int step = 16; step < total; ++step) {
    if (step + G2_NST - 1 > total) WAIT_VM0();
    else if (w < 4) wait_vm<5 * (G2_NST - 2)>();
    else wait_vm<4 * (G2_NST - 2)>();
    BARRIER();
    if (issued < total) issue();
    const char LAS* bp = lds + buf * G2_STAGE;
    bf16x8 pf[2], rf[2][5];
    pf[0] = cat8(tr_read(bp + pB), tr_read(bp + pB + 1024));
#pragma unroll
    for (int j = 0; j < 5; ++j) rf[0][j] = *(const LAS bf16x8*)(bp + rB0 + j * 4096);
    pf[1] = cat8(tr_read(bp + pB + 4096), tr_read(bp + pB + 4096 + 1024));
#pragma unroll
    for (int j = 0; j < 5; ++j) rf[1][j] = *(const LAS bf16x8*)(bp + rB1 + j * 4096);
    __builtin_amdgcn_sched_barrier(0);
#pragma unroll
    for (int k2 = 0; k2 < 2; ++k2)
#pragma unroll
      for (int j = 0; j < 5; ++j) acc[j] = __builtin_amdgcn_mfma_f32_32x32x16_bf16(rf[k2][j], pf[k2], acc[j], 0, 0, 0);
    if (++buf == G2_NST) buf = 0;
  }
  TS(2, 1);
  BARRIER();
  {
    float LAS* dst = (float LAS*)(lds + w * 12288);
    if (kh == 0) {
#pragma unroll
      for (int j = 3; j < 5; ++j)
#pragma unroll
        for (int g = 0; g < 4; ++g)
          *(f32x4 LAS*)(dst + (((j - 3) * 4 + g) * 64 + l) * 4) = (f32x4){acc[j][4 * g], acc[j][4 * g + 1], acc[j][4 * g + 2], acc[j][4 * g + 3]};
    } else {
#pragma unroll
      for (int j = 0; j < 3; ++j)
#pragma unroll
        for (int g = 0; g < 4; ++g)
          *(f32x4 LAS*)(dst + ((j * 4 + g) * 64 + l) * 4) = (f32x4){acc[j][4 * g], acc[j][4 * g + 1], acc[j][4 * g + 2], acc[j][4 * g + 3]};
    }
    asm volatile("s_waitcnt lgkmcnt(0)" ::: "memory");
    BARRIER();
    const float LAS* src = (const float LAS*)(lds + (w ^ 4) * 12288);
    float* orow = part + ((size_t)ks * S_LEN + s0 + 32 * ws + r32) * DM + m0 + 8 * hi;
    if (kh == 0) {
#pragma unroll
      for (int j = 0; j < 3; ++j)
#pragma unroll
        for (int g = 0; g < 4; ++g) {
          const f32x4 o = *(const f32x4 LAS*)(src + ((j * 4 + g) * 64 + l) * 4);
          st16f(orow + 32 * j + (g >> 1) * 16 + (g & 1) * 4, acc[j][4 * g] + o[0], acc[j][4 * g + 1] + o[1], acc[j][4 * g + 2] + o[2], acc[j][4 * g + 3] + o[3]);
        }
    } else {
#pragma unroll
      for (int j = 3; j < 5; ++j)
#pragma unroll
        for (int g = 0; g < 4; ++g) {
          const f32x4 o = *(const f32x4 LAS*)(src + (((j - 3) * 4 + g) * 64 + l) * 4);
          st16f(orow + 32 * j + (g >> 1) * 16 + (g & 1) * 4, acc[j][4 * g] + o[0], acc[j][4 * g + 1] + o[1], acc[j][4 * g + 2] + o[2], acc[j][4 * g + 3] + o[3]);
        }
    }
  }
  TS(2, 2);
}

__global__ __launch_bounds__(256) void reduce_kernel(const float* __restrict__ part, const bf16_t* __restrict__ x,
                                                     const bf16_t* __restrict__ b2, bf16_t* __restrict__ out) {
  const int i = blockIdx.x * 256 + threadIdx.x;
  const int e = i * 4;
  const int s = e / DM;
  f32x4 a = *(const f32x4*)(part + e);
#pragma unroll
  for (int sp = 1; sp < 4; ++sp) a += *(const f32x4*)(part + (size_t)sp * S_LEN * DM + e);
  const u32x2 xv = *(const u32x2*)(x + e);
  const float bb = bf2f(b2[s]);
  float r0 = __uint_as_float(xv[0] << 16) + a[0] + bb;
  float r1 = __uint_as_float(xv[0] & 0xffff0000u) + a[1] + bb;
  float r2 = __uint_as_float(xv[1] << 16) + a[2] + bb;
  float r3 = __uint_as_float(xv[1] & 0xffff0000u) + a[3] + bb;
  u32x2 o = {pack2(r0, r1), pack2(r2, r3)};
  *(u32x2*)(out + e) = o;
}

extern "C" void kernel_launch(void* const* d_in, const int* in_sizes, int n_in, void* d_out, int out_size, void* d_ws,
                              size_t ws_size, hipStream_t stream) {
  const bf16_t* x = (const bf16_t*)d_in[0];
  const bf16_t* ckv = (const bf16_t*)d_in[1];
  const bf16_t* wq = (const bf16_t*)d_in[2];
  const bf16_t* wuk = (const bf16_t*)d_in[3];
  const bf16_t* wuv = (const bf16_t*)d_in[4];
  const bf16_t* wo = (const bf16_t*)d_in[5];
  const bf16_t* w1 = (const bf16_t*)d_in[7];
  const bf16_t* b1 = (const bf16_t*)d_in[8];
  const bf16_t* w2 = (const bf16_t*)d_in[9];
  const bf16_t* b2 = (const bf16_t*)d_in[10];
  char* ws = (char*)d_ws;
  bf16_t* xn = (bf16_t*)(ws + WS_XN);
  bf16_t* h = (bf16_t*)(ws + WS_H);
  bf16_t* Q = (bf16_t*)(ws + WS_Q);
  bf16_t* K = (bf16_t*)(ws + WS_K);
  bf16_t* V = (bf16_t*)(ws + WS_V);
  bf16_t* U = (bf16_t*)(ws + WS_U);
  bf16_t* ctx = (bf16_t*)(ws + WS_CTX);
  float* part = (float*)(ws + WS_PART);
  float* up = (float*)(ws + WS_UP);
  ln_kernel<<<S_LEN / 4, 256, 0, stream>>>(x, xn, h);
  gemm1_kernel<<<256, 512, 0, stream>>>(xn, h, ckv, wq, wuk, wuv, w1, Q, K, V, up);
  attn_kernel<<<256, 512, 0, stream>>>(Q, K, V, ctx, up, b1, U);
  gemm2_kernel<<<256, 512, 0, stream>>>(ctx, wo, U, w2, part);
  reduce_kernel<<<S_LEN * DM / 4 / 256, 256, 0, stream>>>(part, x, b2, (bf16_t*)d_out);
}
```

```cpp
#include <hip/hip_runtime.h>
#include <hip/hip_bf16.h>
#include <stdint.h>

constexpr int S_LEN = 2048;
constexpr int DM = 640;
constexpr int NH = 32;
constexpr int HD = 128;
constexpr int DC = 512;
constexpr int FF = 1728;
constexpr int HDN = NH * HD;
constexpr int CTX_LD = HDN + 64;

typedef unsigned short bf16_t;
typedef short bf16x8 __attribute__((ext_vector_type(8)));
typedef short s16x4 __attribute__((ext_vector_type(4)));
typedef float f32x16 __attribute__((ext_vector_type(16)));
typedef float f32x4 __attribute__((ext_vector_type(4)));
typedef unsigned u32x4 __attribute__((ext_vector_type(4)));
typedef unsigned u32x2 __attribute__((ext_vector_type(2)));
typedef __bf16 bf16x2_t __attribute__((ext_vector_type(2)));
#define LAS __attribute__((address_space(3)))

__device__ __forceinline__ float bf2f(bf16_t v) { return __uint_as_float(((unsigned)v) << 16); }
__device__ __forceinline__ unsigned pack2(float a, float b) {
  bf16x2_t v = {(__bf16)a, (__bf16)b};
  return *reinterpret_cast<unsigned*>(&v);
}
__device__ __forceinline__ float rbf(float a) {
  __bf16 v = (__bf16)a;
  return __uint_as_float(((unsigned)*reinterpret_cast<unsigned short*>(&v)) << 16);
}

constexpr size_t WS_XN = 0;
constexpr size_t WS_H = WS_XN + (size_t)S_LEN * DM * 2;
constexpr size_t WS_Q = WS_H + (size_t)S_LEN * DM * 2;
constexpr size_t WS_K = WS_Q + (size_t)S_LEN * HDN * 2;
constexpr size_t WS_V = WS_K + (size_t)S_LEN * HDN * 2;
constexpr size_t WS_U = WS_V + (size_t)S_LEN * HDN * 2;
constexpr size_t WS_CTX = WS_U + (size_t)DM * FF * 2;
constexpr size_t WS_PART = WS_CTX + (size_t)S_LEN * CTX_LD * 2;
constexpr size_t WS_UP = WS_PART + (size_t)6 * S_LEN * DM * 4;

__device__ __forceinline__ float wave_sum(float v) {
#pragma unroll
  for (int o = 32; o > 0; o >>= 1) v += __shfl_xor(v, o);
  return v;
}
__device__ __forceinline__ void ln_row(float (&v)[10]) {
  float s = 0.f;
#pragma unroll
  for (int i = 0; i < 10; ++i) s += v[i];
  s = wave_sum(s);
  const float mean = s * (1.0f / DM);
  float q = 0.f;
#pragma unroll
  for (int i = 0; i < 10; ++i) { float d = v[i] - mean; q += d * d; }
  q = wave_sum(q);
  const float mb = rbf(mean);
  const float vb = rbf(q * (1.0f / DM));
  const float den = rbf(sqrtf(rbf(vb + 1e-5f)));
#pragma unroll
  for (int i = 0; i < 10; ++i) v[i] = rbf(rbf(v[i] - mb) / den);
}
__global__ __launch_bounds__(256) void ln_kernel(const bf16_t* __restrict__ x, bf16_t* __restrict__ xn,
                                                 bf16_t* __restrict__ h) {
  const int lane = threadIdx.x & 63, w = threadIdx.x >> 6;
  const int row = blockIdx.x * 4 + w;
  const unsigned* xr = reinterpret_cast<const unsigned*>(x + (size_t)row * DM);
  float v[10];
#pragma unroll
  for (int j = 0; j < 5; ++j) {
    unsigned u = xr[lane + 64 * j];
    v[2 * j] = __uint_as_float(u << 16);
    v[2 * j + 1] = __uint_as_float(u & 0xffff0000u);
  }
  ln_row(v);
  unsigned* xo = reinterpret_cast<unsigned*>(xn + (size_t)row * DM);
#pragma unroll
  for (int j = 0; j < 5; ++j) xo[lane + 64 * j] = pack2(v[2 * j], v[2 * j + 1]);
  ln_row(v);
  unsigned* ho = reinterpret_cast<unsigned*>(h + (size_t)row * DM);
#pragma unroll
  for (int j = 0; j < 5; ++j) ho[lane + 64 * j] = pack2(v[2 * j], v[2 * j + 1]);
}

constexpr int STAGE_BYTES = 49152;
constexpr int RBASE = 32768;

__device__ __forceinline__ int swapmid(int p) { return ((p & 1) << 1) | ((p >> 1) & 1); }
__device__ __forceinline__ int rperm(int r) {
  int g = r >> 2;
  int pg = (g & 4) | swapmid(g & 3);
  return pg * 4 + (r & 3);
}
__device__ __forceinline__ s16x4 tr_read(const char LAS* p) {
  return __builtin_amdgcn_ds_read_tr16_b64_v4i16((LAS s16x4*)p);
}
__device__ __forceinline__ bf16x8 cat8(s16x4 a, s16x4 b) {
  return (bf16x8){a[0], a[1], a[2], a[3], b[0], b[1], b[2], b[3]};
}

__device__ __forceinline__ void glds16(const void* sbase, int voff, unsigned lds_dst) {
  unsigned keep;
  asm volatile("s_mov_b32 %0, m0\n\ts_mov_b32 m0, %3\n\ts_nop 0\n\tglobal_load_lds_dwordx4 %1, %2\n\ts_mov_b32 m0, %0"
               : "=&s"(keep) : "v"(voff), "s"(sbase), "s"(lds_dst) : "memory");
}

#define ST_MODE 0
__device__ __forceinline__ void st16(void* p, u32x4 v) {
  *(u32x4*)p = v;
}
__device__ __forceinline__ void st16f(float* p, float a, float b, float c, float d) {
  st16(p, (u32x4){__float_as_uint(a), __float_as_uint(b), __float_as_uint(c), __float_as_uint(d)});
}

template <bool P_TR, bool R_TR, bool ROWS_P>
struct GemmCore {
  int pOff[4], rOff[2];
  int pAddr[4], rAddr[4];
  int w, l;
  __device__ __forceinline__ void init(int ldp, int ldr, int pcol_limit  ) {
    const int tid = threadIdx.x;
    l = tid & 63;
    w = __builtin_amdgcn_readfirstlane(tid >> 6);
    const int wp = w >> 1, wr = w & 1;
#pragma unroll
    for (int i = 0; i < 4; ++i) {
      const int j = w * 4 + i;
      if (!P_TR) {
        const int row = 8 * j + (l >> 3), slot = l & 7;
        const int chunk = slot ^ ((row >> 1) & 7);
        pOff[i] = row * ldp + chunk * 8;
      } else {
        const int half = j >> 4, jj = j & 15;
        const int krow = 4 * jj + (l >> 4), s16 = l & 15;
        const int c64 = (s16 >> 2) ^ (krow & 3);
        int col = half * 128 + c64 * 32 + (s16 & 3) * 8;
        if (col >= pcol_limit) col = pcol_limit - 8;
        pOff[i] = krow * ldp + col;
      }
    }
#pragma unroll
    for (int i = 0; i < 2; ++i) {
      const int j = w * 2 + i;
      if (!R_TR) {
        const int row = 8 * j + (l >> 3), slot = l & 7;
        const int chunk = slot ^ ((row >> 1) & 7);
        rOff[i] = row * ldr + chunk * 8;
      } else {
        const int krow = 4 * j + (l >> 4), s16 = l & 15;
        const int c64 = (s16 >> 2) ^ (krow & 3);
        const int col = c64 * 32 + (s16 & 3) * 8;
        rOff[i] = krow * ldr + col;
      }
    }
    const int r32 = l & 31, hi = l >> 5, blk = (l >> 4) & 1, q = (l & 15) >> 2, p = l & 3;
    if (!P_TR) {
      const int prow = 64 * wp + (ROWS_P ? rperm(r32) : r32);
      const int s = (prow >> 1) & 7;
#pragma unroll
      for (int kk = 0; kk < 4; ++kk) pAddr[kk] = prow * 128 + (((2 * kk + hi) ^ s) << 4);
    } else {
      const int half = wp >> 1, pp = ROWS_P ? swapmid(p) : p;
#pragma unroll
      for (int i = 0; i < 2; ++i) {
        const int c64 = 2 * (wp & 1) + i;
        pAddr[i] = half * 16384 + (8 * hi + q) * 256 + ((c64 ^ q) * 64) + blk * 32 + pp * 8;
      }
      pAddr[2] = pAddr[3] = 0;
    }
    if (!R_TR) {
      const int rrow = 64 * wr + (!ROWS_P ? rperm(r32) : r32);
      const int s = (rrow >> 1) & 7;
#pragma unroll
      for (int kk = 0; kk < 4; ++kk) rAddr[kk] = RBASE + rrow * 128 + (((2 * kk + hi) ^ s) << 4);
    } else {
      const int pp = !ROWS_P ? swapmid(p) : p;
#pragma unroll
      for (int i = 0; i < 2; ++i) {
        const int c64 = 2 * wr + i;
        rAddr[i] = RBASE + (8 * hi + q) * 256 + ((c64 ^ q) * 64) + blk * 32 + pp * 8;
      }
      rAddr[2] = rAddr[3] = 0;
    }
  }
  __device__ __forceinline__ void stage(char LAS* buf, const bf16_t* P, const bf16_t* R) {
    const unsigned b = (unsigned)(uintptr_t)buf;
#pragma unroll
    for (int i = 0; i < 4; ++i) glds16(P, pOff[i] * 2, b + (w * 4 + i) * 1024);
#pragma unroll
    for (int i = 0; i < 2; ++i) glds16(R, rOff[i] * 2, b + RBASE + (w * 2 + i) * 1024);
  }
  __device__ __forceinline__ void compute(const char LAS* buf, f32x16 (&acc)[2][2]) {
#pragma unroll
    for (int kk = 0; kk < 4; ++kk) {
      bf16x8 pf[2], rf[2];
#pragma unroll
      for (int i = 0; i < 2; ++i) {
        if (!P_TR) pf[i] = *(const LAS bf16x8*)(buf + pAddr[kk] + i * 4096);
        else pf[i] = cat8(tr_read(buf + pAddr[i] + kk * 4096), tr_read(buf + pAddr[i] + kk * 4096 + 1024));
        if (!R_TR) rf[i] = *(const LAS bf16x8*)(buf + rAddr[kk] + i * 4096);
        else rf[i] = cat8(tr_read(buf + rAddr[i] + kk * 4096), tr_read(buf + rAddr[i] + kk * 4096 + 1024));
      }
#pragma unroll
      for (int i = 0; i < 2; ++i)
#pragma unroll
        for (int j = 0; j < 2; ++j) {
          if (ROWS_P) acc[i][j] = __builtin_amdgcn_mfma_f32_32x32x16_bf16(pf[i], rf[j], acc[i][j], 0, 0, 0);
          else acc[i][j] = __builtin_amdgcn_mfma_f32_32x32x16_bf16(rf[j], pf[i], acc[i][j], 0, 0, 0);
        }
    }
  }
};

#define WAIT_VM0() asm volatile("s_waitcnt vmcnt(0)" ::: "memory")
#define WAIT_VM6() asm volatile("s_waitcnt vmcnt(6)" ::: "memory")
#define WAIT_VM16() asm volatile("s_waitcnt vmcnt(16)" ::: "memory")
#define BARRIER() __builtin_amdgcn_s_barrier()
#define TS(k, i)

__device__ __forceinline__ float gelu_tanh(float x) {
  const float y = 0.7978845608028654f * (x + 0.044715f * x * x * x);
  const float e = __expf(2.0f * y);
  const float t = 1.0f - 2.0f / (e + 1.0f);
  return 0.5f * x * (1.0f + t);
}

constexpr int STAGE256 = 32768;
#define NST256 4
struct Core256 {
  int pOff[2], rOff[2];
  int pAddr[2], rAddr[2];
  int w;
  __device__ __forceinline__ void init(int ldp, int ldr) {
    const int tid = threadIdx.x, l = tid & 63;
    w = __builtin_amdgcn_readfirstlane(tid >> 6);
    const int wp = w >> 2, wr = w & 3;
#pragma unroll
    for (int i = 0; i < 2; ++i) {
      const int j = w * 2 + i;
      const int row = 16 * j + (l >> 2), slot = l & 3;
      pOff[i] = (row * ldp + (slot ^ ((row >> 2) & 3)) * 8) * 2;
      const int half = j >> 3, jj = j & 7;
      const int krow = 4 * jj + (l >> 4), s16 = l & 15;
      const int c64 = (s16 >> 2) ^ (krow & 3);
      rOff[i] = (krow * ldr + half * 128 + c64 * 32 + (s16 & 3) * 8) * 2;
    }
    const int r32 = l & 31, hi = l >> 5, blk = (l >> 4) & 1, q = (l & 15) >> 2, p = l & 3;
    const int prow = 128 * wp + r32, s = (prow >> 2) & 3;
#pragma unroll
    for (int kk = 0; kk < 2; ++kk) pAddr[kk] = prow * 64 + (((2 * kk + hi) ^ s) << 4);
#pragma unroll
    for (int j = 0; j < 2; ++j) {
      const int c64 = 2 * (wr & 1) + j;
      rAddr[j] = 16384 + (wr >> 1) * 8192 + (8 * hi + q) * 256 + ((c64 ^ q) * 64) + blk * 32 + swapmid(p) * 8;
    }
  }
  __device__ __forceinline__ void stage(char LAS* buf, const bf16_t* P, const bf16_t* R) {
    const unsigned b = (unsigned)(uintptr_t)buf;
#pragma unroll
    for (int i = 0; i < 2; ++i) glds16(P, pOff[i], b + (w * 2 + i) * 1024);
#pragma unroll
    for (int i = 0; i < 2; ++i) glds16(R, rOff[i], b + 16384 + (w * 2 + i) * 1024);
  }
  __device__ __forceinline__ void compute(const char LAS* buf, f32x16 (&acc)[4][2]) {
#pragma unroll
    for (int kk = 0; kk < 2; ++kk) {
      bf16x8 pf[4], rf[2];
#pragma unroll
      for (int j = 0; j < 2; ++j) rf[j] = cat8(tr_read(buf + rAddr[j] + kk * 4096), tr_read(buf + rAddr[j] + kk * 4096 + 1024));
#pragma unroll
      for (int i = 0; i < 4; ++i) pf[i] = *(const LAS bf16x8*)(buf + pAddr[kk] + i * 2048);
#pragma unroll
      for (int i = 0; i < 4; ++i)
#pragma unroll
        for (int j = 0; j < 2; ++j) acc[i][j] = __builtin_amdgcn_mfma_f32_32x32x16_bf16(rf[j], pf[i], acc[i][j], 0, 0, 0);
    }
  }
  __device__ __forceinline__ void load_frags(const char LAS* buf, bf16x8 (&pf)[2][4], bf16x8 (&rf)[2][2]) {
#pragma unroll
    for (int kk = 0; kk < 2; ++kk) {
#pragma unroll
      for (int j = 0; j < 2; ++j) rf[kk][j] = cat8(tr_read(buf + rAddr[j] + kk * 4096), tr_read(buf + rAddr[j] + kk * 4096 + 1024));
#pragma unroll
      for (int i = 0; i < 4; ++i) pf[kk][i] = *(const LAS bf16x8*)(buf + pAddr[kk] + i * 2048);
    }
  }
  __device__ __forceinline__ void mma(const bf16x8 (&pf)[2][4], const bf16x8 (&rf)[2][2], f32x16 (&acc)[4][2]) {
#pragma unroll
    for (int kk = 0; kk < 2; ++kk)
#pragma unroll
      for (int i = 0; i < 4; ++i)
#pragma unroll
        for (int j = 0; j < 2; ++j) acc[i][j] = __builtin_amdgcn_mfma_f32_32x32x16_bf16(rf[kk][j], pf[kk][i], acc[i][j], 0, 0, 0);
  }
};
#define WAIT_LGKM0() asm volatile("s_waitcnt lgkmcnt(0)" ::: "memory")
#define SCHED0() __builtin_amdgcn_sched_barrier(0)
template <int N> __device__ __forceinline__ void wait_vm() { asm volatile("s_waitcnt vmcnt(%0)" ::"n"(N) : "memory"); }

__global__ __launch_bounds__(512, 2) void gemm1_kernel(const bf16_t* __restrict__ xn, const bf16_t* __restrict__ hbuf,
                                                       const bf16_t* __restrict__ ckv, const bf16_t* __restrict__ wq,
                                                       const bf16_t* __restrict__ wuk, const bf16_t* __restrict__ wuv,
                                                       const bf16_t* __restrict__ w1,
                                                       bf16_t* __restrict__ Qo, bf16_t* __restrict__ Ko,
                                                       bf16_t* __restrict__ Vo, float* __restrict__ Up) {
  __shared__ __attribute__((aligned(16))) char smem[NST256 * STAGE256 + 32768];
  char LAS* lds = (char LAS*)smem;
  const int xcd = blockIdx.x & 7, c = blockIdx.x >> 3;
  const int l = threadIdx.x & 63, w = __builtin_amdgcn_readfirstlane(threadIdx.x >> 6);
  const int r32 = l & 31, hi = l >> 5;
  TS(0, 0);
  const bool isQ = c >= 16;
  {
    int ntile, tb0, hp;
    const bf16_t* A; const bf16_t* W; bf16_t* O; int K;
    if (!isQ) {
      ntile = 2; tb0 = c >> 2; hp = xcd * 2 + (c & 1);
      A = ckv; K = DC;
      W = (c & 2) ? wuv : wuk; O = (c & 2) ? Vo : Ko;
    } else {
      ntile = 1; tb0 = (c - 16) >> 1; hp = xcd * 2 + (c & 1);
      A = xn; K = DM; W = wq; O = Qo;
    }
    const int nk = K / 32;
    Core256 g;
    g.init(K, HDN);
    const bf16_t* Wp = W + hp * 256;
    const int wp = w >> 2, wr = w & 3;
    const int total = ntile * nk;
    int pt = 0, pk = 0, pslot = 0, issued = 0;
    auto issue = [&]() {
      g.stage(lds + pslot * STAGE256, A + (size_t)(tb0 + 4 * pt) * 256 * K + pk * 32, Wp + (size_t)pk * 32 * HDN);
      if (++pk == nk) { pk = 0; ++pt; }
      if (++pslot == NST256) pslot = 0;
      ++issued;
    };
#pragma unroll
    for (int i = 0; i < NST256 - 1; ++i) issue();
    int buf = 0, step = 0;
    wait_vm<4 * (NST256 - 2)>();
    BARRIER();
    if (wp == 1) BARRIER();
    for (int t = 0; t < ntile; ++t) {
      const int tb = tb0 + 4 * t;
      f32x16 acc[4][2] = {};
      for (int kt = 0; kt < nk; ++kt) {
        bf16x8 pf[2][4], rf[2][2];
        if (issued < total) issue();
        g.load_frags(lds + buf * STAGE256, pf, rf);
        if (step + NST256 > total) WAIT_VM0();
        else if (t > 0 && kt < NST256 - 2) wait_vm<4 * (NST256 - 2) + 16>();
        else wait_vm<4 * (NST256 - 2)>();
        WAIT_LGKM0();
        SCHED0();
        BARRIER();
        SCHED0();
        __builtin_amdgcn_s_setprio(1);
        g.mma(pf, rf, acc);
        __builtin_amdgcn_s_setprio(0);
        SCHED0();
        BARRIER();
        SCHED0();
        if (++buf == NST256) buf = 0;
        ++step;
      }
      const int head = hp * 2 + (wr >> 1);
      char LAS* slab = lds + NST256 * STAGE256 + w * 4096;
      const int wa = r32 * 128, wsw = r32 & 7;
      const int rrow = l >> 3, ra = rrow * 128 + (((l & 7) ^ rrow) << 4);
      bf16_t* obase = O + ((size_t)head * S_LEN + tb * 256 + 128 * wp + rrow) * HD + 64 * (wr & 1) + (l & 7) * 8;
#pragma unroll
      for (int i = 0; i < 4; ++i) {
#pragma unroll
        for (int j = 0; j < 2; ++j) {
          const f32x16 a = acc[i][j];
          u32x4 v0 = {pack2(a[0], a[1]), pack2(a[2], a[3]), pack2(a[4], a[5]), pack2(a[6], a[7])};
          u32x4 v1 = {pack2(a[8], a[9]), pack2(a[10], a[11]), pack2(a[12], a[13]), pack2(a[14], a[15])};
          *(u32x4 LAS*)(slab + wa + (((4 * j + hi) ^ wsw) << 4)) = v0;
          *(u32x4 LAS*)(slab + wa + (((4 * j + 2 + hi) ^ wsw) << 4)) = v1;
        }
#pragma unroll
        for (int it = 0; it < 4; ++it) {
          const u32x4 v = *(const u32x4 LAS*)(slab + ra + it * 1024);
          st16(obase + (size_t)(32 * i + 8 * it) * HD, v);
        }
      }
    }
    if (wp == 0) BARRIER();
  }
  TS(0, 1);
  if (isQ) {
    const int e = c - 16, n4 = (xcd & 1) ? 11 : 12;
    int tile, part, k0, nk;
    if (e < n4) { tile = (xcd & 1) * 12 + e; part = xcd >> 1; k0 = 8 * part; nk = 8; }
    else {
      const int j = (xcd >> 1) * 9 + (xcd & 1) * 4 + (e - n4);
      part = j / 12; tile = 23 + j % 12; k0 = 11 * part; nk = part == 2 ? 10 : 11;
    }
    const int ft = tile / 5, dt = tile % 5;
    const int f0 = ft * 256, d0 = dt * 128;
    const int wp = w >> 1, wr = w & 1;
    const bf16_t* Pp = w1 + (size_t)k0 * 64 * FF + f0;
    const bf16_t* Rp = hbuf + (size_t)k0 * 64 * DM + d0;
    GemmCore<true, true, true> g;
    g.init(FF, DM, FF - f0);
    int buf = 0, pslot = 2;
    BARRIER();
    g.stage(lds, Pp, Rp);
    g.stage(lds + STAGE_BYTES, Pp + (size_t)64 * FF, Rp + (size_t)64 * DM);
    f32x16 acc[2][2] = {};
    for (int kt = 0; kt < nk; ++kt) {
      if (kt + 1 >= nk) WAIT_VM0(); else WAIT_VM6();
      BARRIER();
      if (kt + 2 < nk) g.stage(lds + pslot * STAGE_BYTES, Pp + (size_t)(kt + 2) * 64 * FF, Rp + (size_t)(kt + 2) * 64 * DM);
      g.compute(lds + buf * STAGE_BYTES, acc);
      if (++buf == 3) buf = 0;
      if (++pslot == 3) pslot = 0;
    }
#pragma unroll
    for (int i = 0; i < 2; ++i) {
      const int fb = f0 + 64 * wp + 32 * i;
      if (fb >= FF) continue;
#pragma unroll
      for (int j = 0; j < 2; ++j) {
        const int d = d0 + 64 * wr + 32 * j + r32;
        const f32x16 a = acc[i][j];
        float* orow = Up + ((size_t)part * DM + d) * FF + fb + 8 * hi;
        st16f(orow, a[0], a[1], a[2], a[3]);
        st16f(orow + 4, a[4], a[5], a[6], a[7]);
        st16f(orow + 16, a[8], a[9], a[10], a[11]);
        st16f(orow + 20, a[12], a[13], a[14], a[15]);
      }
    }
  }
  TS(0, 2);
}

constexpr int AT_HALF = 16384;
__global__ __launch_bounds__(512, 2) void attn_kernel(const bf16_t* __restrict__ Q, const bf16_t* __restrict__ K,
                                                      const bf16_t* __restrict__ V, bf16_t* __restrict__ ctx,
                                                      const float* __restrict__ Up, const bf16_t* __restrict__ b1,
                                                      bf16_t* __restrict__ Uo) {
  __shared__ __attribute__((aligned(16))) char smem[8 * AT_HALF + 4096];
  char LAS* lds = (char LAS*)smem;
  const int tid = threadIdx.x, l = tid & 63, w = __builtin_amdgcn_readfirstlane(tid >> 6);
  const int rg = w & 3, g = w >> 2, r32 = l & 31, hi = l >> 5;
  const int xcd = blockIdx.x & 7, jj = blockIdx.x >> 3;
  const int head = xcd * 4 + (jj >> 3), pair = jj & 7;
  const int LB = 15 - pair, nshort = 2 * pair + 2;
  const bf16_t* Kh = K + (size_t)head * S_LEN * HD;
  const bf16_t* Vh = V + (size_t)head * S_LEN * HD;
  const bf16_t* Qh = Q + (size_t)head * S_LEN * HD;
  char LAS* gl = lds + g * 4 * AT_HALF;
  int kOff[4], vOff[4];
#pragma unroll
  for (int i = 0; i < 4; ++i) {
    const int j = rg * 4 + i, row = 4 * j + (l >> 4), s16 = l & 15;
    kOff[i] = (row * HD + ((s16 ^ (row & 15)) * 8)) * 2;
    vOff[i] = (row * HD + ((((s16 >> 2) ^ (row & 3)) * 4 + (s16 & 3)) * 8)) * 2;
  }
  const unsigned glb = (unsigned)(uintptr_t)gl;
  auto htile = [&](int i) { return (g == 0 || i < nshort) ? i : i + 15 - 2 * pair; };
  auto stageK = [&](int i) {
    const bf16_t* kp = Kh + (size_t)htile(i) * 64 * HD;
    const unsigned bb = glb + (i & 1) * AT_HALF + rg * 4096;
#pragma unroll
    for (int j = 0; j < 4; ++j) glds16(kp, kOff[j], bb + j * 1024);
  };
  auto stageV = [&](int i) {
    const bf16_t* vp = Vh + (size_t)htile(i) * 64 * HD;
    const unsigned bb = glb + (2 + (i & 1)) * AT_HALF + rg * 4096;
#pragma unroll
    for (int j = 0; j < 4; ++j) glds16(vp, vOff[j], bb + j * 1024);
  };
  const int ksw = r32 & 15;
  const int blk = (l >> 4) & 1, q4 = (l & 15) >> 2, p4 = l & 3;
  int vAddr[4];
#pragma unroll
  for (int db = 0; db < 4; ++db)
    vAddr[db] = 2 * AT_HALF + (4 * hi + q4) * 256 + ((db ^ q4) * 64) + blk * 32 + swapmid(p4) * 8;
  constexpr float CS = 0.08838834764831845f * 1.4426950408889634f;

  TS(1, 0);
  stageK(0);
  stageV(0);
  stageK(1);
  int qb = g == 0 ? LB : pair;
  bf16x8 qf[8];
#pragma unroll
  for (int d0 = 0; d0 < 8; ++d0) qf[d0] = *(const bf16x8*)(Qh + (size_t)(qb * 128 + 32 * rg + r32) * HD + d0 * 16 + hi * 8);
  for (int g4 = tid; g4 < 1080; g4 += 512) {
    const int e = blockIdx.x * 4320 + g4 * 4;
    const int d = e / FF, f = e - d * FF;
    const int np = ((f >> 8) * 5 + (d >> 7)) >= 23 ? 3 : 4;
    f32x4 a = *(const f32x4*)(Up + e) + *(const f32x4*)(Up + (size_t)DM * FF + e) + *(const f32x4*)(Up + (size_t)2 * DM * FF + e);
    if (np == 4) a += *(const f32x4*)(Up + (size_t)3 * DM * FF + e);
    const u32x2 bv = *(const u32x2*)(b1 + f);
    const float r0 = gelu_tanh(rbf(rbf(a[0]) + __uint_as_float(bv[0] << 16)));
    const float r1 = gelu_tanh(rbf(rbf(a[1]) + __uint_as_float(bv[0] & 0xffff0000u)));
    const float r2 = gelu_tanh(rbf(rbf(a[2]) + __uint_as_float(bv[1] << 16)));
    const float r3 = gelu_tanh(rbf(rbf(a[3]) + __uint_as_float(bv[1] & 0xffff0000u)));
    *(u32x2*)(Uo + e) = (u32x2){pack2(r0, r1), pack2(r2, r3)};
  }
  float m_run = -1e30f, l_run = 0.f;
  f32x16 o[4] = {};
  f32x16 s0, s1;
  bf16x8 pf[4];
  auto qk = [&](int i) {
    const char LAS* base = gl + (i & 1) * AT_HALF;
    s0 = (f32x16){};
    s1 = (f32x16){};
#pragma unroll
    for (int d0 = 0; d0 < 8; ++d0) {
      const int a = r32 * 256 + (((2 * d0 + hi) ^ ksw) << 4);
      bf16x8 k0 = *(const LAS bf16x8*)(base + a);
      bf16x8 k1 = *(const LAS bf16x8*)(base + a + 32 * 256);
      s0 = __builtin_amdgcn_mfma_f32_32x32x16_bf16(k0, qf[d0], s0, 0, 0, 0);
      s1 = __builtin_amdgcn_mfma_f32_32x32x16_bf16(k1, qf[d0], s1, 0, 0, 0);
    }
  };
  auto pv = [&](int i) {
    const char LAS* base = gl + (i & 1) * AT_HALF;
#pragma unroll
    for (int db = 0; db < 4; ++db) {
#pragma unroll
      for (int s = 0; s < 4; ++s) {
        bf16x8 vf = cat8(tr_read(base + vAddr[db] + s * 4096), tr_read(base + vAddr[db] + s * 4096 + 2048));
        o[db] = __builtin_amdgcn_mfma_f32_32x32x16_bf16(vf, pf[s], o[db], 0, 0, 0);
      }
    }
  };
  auto softmax = [&](int i) {
    if (i + 2 < 17) stageK(i + 2);
    if (i + 1 < 17) stageV(i + 1);
    if (g == 1 && i == nshort) {
      const float inv = 1.0f / l_run;
      bf16_t* orow = ctx + (size_t)(pair * 128 + 32 * rg + r32) * CTX_LD + head * HD + 8 * hi;
#pragma unroll
      for (int db = 0; db < 4; ++db) {
        const f32x16 a = o[db];
        u32x4 v0 = {pack2(a[0] * inv, a[1] * inv), pack2(a[2] * inv, a[3] * inv), pack2(a[4] * inv, a[5] * inv), pack2(a[6] * inv, a[7] * inv)};
        u32x4 v1 = {pack2(a[8] * inv, a[9] * inv), pack2(a[10] * inv, a[11] * inv), pack2(a[12] * inv, a[13] * inv), pack2(a[14] * inv, a[15] * inv)};
        st16(orow + 32 * db, v0);
        st16(orow + 32 * db + 16, v1);
        o[db] = (f32x16){};
      }
      m_run = -1e30f;
      l_run = 0.f;
      qb = LB;
    }
    if (g == 1 && i == nshort - 1) {
#pragma unroll
      for (int d0 = 0; d0 < 8; ++d0) qf[d0] = *(const bf16x8*)(Qh + (size_t)(LB * 128 + 32 * rg + r32) * HD + d0 * 16 + hi * 8);
    }
    const int dh = htile(i) - 2 * qb;
    if (dh >= 0) {
      const int qi = 32 * rg + r32 - 64 * dh;
#pragma unroll
      for (int r = 0; r < 16; ++r) {
        const int key = (r & 3) + 8 * (r >> 2) + 4 * hi;
        if (key > qi) s0[r] = -__builtin_inff();
        if (key + 32 > qi) s1[r] = -__builtin_inff();
      }
    }
    float pm = s0[0];
#pragma unroll
    for (int r = 1; r < 16; ++r) pm = fmaxf(pm, s0[r]);
#pragma unroll
    for (int r = 0; r < 16; ++r) pm = fmaxf(pm, s1[r]);
    pm = fmaxf(pm, __shfl_xor(pm, 32));
    const float mn = fmaxf(m_run, pm);
    const float alpha = __builtin_amdgcn_exp2f((m_run - mn) * CS);
    m_run = mn;
    const float mc = -mn * CS;
    float ps = 0.f;
#pragma unroll
    for (int r = 0; r < 16; ++r) { s0[r] = __builtin_amdgcn_exp2f(fmaf(s0[r], CS, mc)); ps += s0[r]; }
#pragma unroll
    for (int r = 0; r < 16; ++r) { s1[r] = __builtin_amdgcn_exp2f(fmaf(s1[r], CS, mc)); ps += s1[r]; }
    ps += __shfl_xor(ps, 32);
    l_run = l_run * alpha + ps;
    if (__any(alpha != 1.0f)) {
#pragma unroll
      for (int db = 0; db < 4; ++db)
#pragma unroll
        for (int r = 0; r < 16; ++r) o[db][r] *= alpha;
    }
    u32x4 a0 = {pack2(s0[0], s0[1]), pack2(s0[2], s0[3]), pack2(s0[4], s0[5]), pack2(s0[6], s0[7])};
    u32x4 a1 = {pack2(s0[8], s0[9]), pack2(s0[10], s0[11]), pack2(s0[12], s0[13]), pack2(s0[14], s0[15])};
    u32x4 a2 = {pack2(s1[0], s1[1]), pack2(s1[2], s1[3]), pack2(s1[4], s1[5]), pack2(s1[6], s1[7])};
    u32x4 a3 = {pack2(s1[8], s1[9]), pack2(s1[10], s1[11]), pack2(s1[12], s1[13]), pack2(s1[14], s1[15])};
    pf[0] = *(bf16x8*)&a0; pf[1] = *(bf16x8*)&a1; pf[2] = *(bf16x8*)&a2; pf[3] = *(bf16x8*)&a3;
  };
  WAIT_VM0();
  BARRIER();
  if (g == 1) BARRIER();
  SCHED0();
  qk(0);
  SCHED0();
  BARRIER();
  for (int i = 0; i < 16; ++i) {
    SCHED0();
    softmax(i);
    SCHED0();
    BARRIER();
    SCHED0();
    qk(i + 1);
    pv(i);
    SCHED0();
    WAIT_VM0();
    BARRIER();
  }
  SCHED0();
  softmax(16);
  SCHED0();
  BARRIER();
  SCHED0();
  pv(16);
  SCHED0();
  BARRIER();
  if (g == 0) BARRIER();
  TS(1, 1);
  {
    const int kh = g;
    float LAS* ml = (float LAS*)(lds + 8 * AT_HALF);
    float LAS* dst = (float LAS*)(lds + w * 8192);
#pragma unroll
    for (int dd = 0; dd < 2; ++dd) {
      f32x16 a;
#pragma unroll
      for (int r = 0; r < 16; ++r) a[r] = kh == 0 ? o[2 + dd][r] : o[dd][r];
#pragma unroll
      for (int gg = 0; gg < 4; ++gg)
        *(f32x4 LAS*)(dst + ((dd * 4 + gg) * 64 + l) * 4) = (f32x4){a[4 * gg], a[4 * gg + 1], a[4 * gg + 2], a[4 * gg + 3]};
    }
    ml[w * 128 + l] = m_run;
    ml[w * 128 + 64 + l] = l_run;
    asm volatile("s_waitcnt lgkmcnt(0)" ::: "memory");
    BARRIER();
    const int wo = w ^ 4;
    const float m2 = ml[wo * 128 + l], l2 = ml[wo * 128 + 64 + l];
    const float mt = fmaxf(m_run, m2);
    const float a1 = __builtin_amdgcn_exp2f((m_run - mt) * CS), a2 = __builtin_amdgcn_exp2f((m2 - mt) * CS);
    const float inv = 1.0f / (l_run * a1 + l2 * a2);
    const float f1 = a1 * inv, f2 = a2 * inv;
    const float LAS* src = (const float LAS*)(lds + wo * 8192);
    bf16_t* orow = ctx + (size_t)(LB * 128 + 32 * rg + r32) * CTX_LD + head * HD + 8 * hi;
#pragma unroll
    for (int dd = 0; dd < 2; ++dd) {
      f32x16 a;
#pragma unroll
      for (int r = 0; r < 16; ++r) a[r] = kh == 0 ? o[dd][r] : o[2 + dd][r];
      float r[16];
#pragma unroll
      for (int gg = 0; gg < 4; ++gg) {
        const f32x4 b = *(const f32x4 LAS*)(src + ((dd * 4 + gg) * 64 + l) * 4);
#pragma unroll
        for (int e = 0; e < 4; ++e) r[4 * gg + e] = a[4 * gg + e] * f1 + b[e] * f2;
      }
      u32x4 v0 = {pack2(r[0], r[1]), pack2(r[2], r[3]), pack2(r[4], r[5]), pack2(r[6], r[7])};
      u32x4 v1 = {pack2(r[8], r[9]), pack2(r[10], r[11]), pack2(r[12], r[13]), pack2(r[14], r[15])};
      const int dcol = 32 * (2 * kh + dd);
      st16(orow + dcol, v0);
      st16(orow + dcol + 16, v1);
    }
  }
  TS(1, 2);
}

constexpr int G2_STAGE = 36864;
constexpr int G2_NST = 4;
__global__ __launch_bounds__(512, 2) void gemm2_kernel(const bf16_t* __restrict__ ctx, const bf16_t* __restrict__ wo,
                                                       const bf16_t* __restrict__ U, const bf16_t* __restrict__ w2,
                                                       float* __restrict__ part) {
  __shared__ __attribute__((aligned(16))) char smem[G2_NST * G2_STAGE];
  char LAS* lds = (char LAS*)smem;
  const int b = blockIdx.x, xcd = b & 7, c = b >> 3;
  const int ks = xcd >> 1;
  const int s0 = (xcd & 1) * 1024 + (c >> 2) * 128, m0 = (c & 3) * 160;
  const int l = threadIdx.x & 63, w = __builtin_amdgcn_readfirstlane(threadIdx.x >> 6);
  const int ws = w & 3, kh = w >> 2, r32 = l & 31, hi = l >> 5;
  const int blk = (l >> 4) & 1, q = (l & 15) >> 2, p = l & 3;
  TS(2, 0);
  int pOffA[2], pOffB[2], rOffA[3], rOffB[3];
#pragma unroll
  for (int i = 0; i < 2; ++i) {
    const int j = w * 2 + i;
    const int row = 8 * j + (l >> 3), slot = l & 7;
    pOffA[i] = (row * CTX_LD + (slot ^ ((row >> 1) & 7)) * 8) * 2;
    const int krow = 4 * j + (l >> 4), s16 = l & 15;
    pOffB[i] = (krow * S_LEN + ((s16 >> 2) ^ (krow & 3)) * 32 + (s16 & 3) * 8) * 2;
  }
#pragma unroll
  for (int i = 0; i < 3; ++i) {
    const int j = w + 8 * i;
    const int cb = j >> 2, kr = (j & 3) * 16 + (l >> 2);
    rOffA[i] = (kr * DM + cb * 32 + (l & 3) * 8) * 2;
    const int row = 8 * j + (l >> 3), slot = l & 7;
    rOffB[i] = (row * FF + (slot ^ ((row >> 1) & 7)) * 8) * 2;
  }
  const int f0 = ks * 7, nB = ks == 3 ? 6 : 7, total = 16 + nB;
  const bf16_t* PA = ctx + (size_t)s0 * CTX_LD + ks * 1024;
  const bf16_t* RA = wo + (size_t)ks * 1024 * DM + m0;
  const bf16_t* PB = w2 + (size_t)f0 * 64 * S_LEN + s0;
  const bf16_t* RB = U + (size_t)m0 * FF + f0 * 64;
  int pslot = 0, issued = 0;
  auto issue = [&]() {
    const unsigned bb = (unsigned)(uintptr_t)(lds + pslot * G2_STAGE);
    if (issued < 16) {
      const bf16_t* P = PA + issued * 64;
      const bf16_t* R = RA + (size_t)issued * 64 * DM;
      glds16(P, pOffA[0], bb + (w * 2) * 1024);
      glds16(P, pOffA[1], bb + (w * 2 + 1) * 1024);
      glds16(R, rOffA[0], bb + 16384 + w * 1024);
      glds16(R, rOffA[1], bb + 16384 + (w + 8) * 1024);
      if (w < 4) glds16(R, rOffA[2], bb + 16384 + (w + 16) * 1024);
    } else {
      const bf16_t* P = PB + (size_t)(issued - 16) * 64 * S_LEN;
      const bf16_t* R = RB + (issued - 16) * 64;
      glds16(P, pOffB[0], bb + (w * 2) * 1024);
      glds16(P, pOffB[1], bb + (w * 2 + 1) * 1024);
      glds16(R, rOffB[0], bb + 16384 + w * 1024);
      glds16(R, rOffB[1], bb + 16384 + (w + 8) * 1024);
      if (w < 4) glds16(R, rOffB[2], bb + 16384 + (w + 16) * 1024);
    }
    if (++pslot == G2_NST) pslot = 0;
    ++issued;
  };
  const int prow = 32 * ws + r32, psw = (prow >> 1) & 7;
  const int pA0 = prow * 128 + (((4 * kh + hi) ^ psw) << 4), pA1 = prow * 128 + (((4 * kh + 2 + hi) ^ psw) << 4);
  const int rA = 16384 + (32 * kh + 8 * hi + q) * 64 + blk * 32 + swapmid(p) * 8;
  const int pB = (32 * kh + 8 * hi + q) * 256 + ((ws ^ q) * 64) + blk * 32 + p * 8;
  const int rrow = rperm(r32), rsw = (rrow >> 1) & 7;
  const int rB0 = 16384 + rrow * 128 + (((4 * kh + hi) ^ rsw) << 4), rB1 = 16384 + rrow * 128 + (((4 * kh + 2 + hi) ^ rsw) << 4);
#pragma unroll
  for (int i = 0; i < G2_NST - 1; ++i) issue();
  f32x16 acc[5] = {};
  int buf = 0;
  for (int step = 0; step < 16; ++step) {
    if (w < 4) wait_vm<5 * (G2_NST - 2)>();
    else wait_vm<4 * (G2_NST - 2)>();
    BARRIER();
    issue();
    const char LAS* bp = lds + buf * G2_STAGE;
    bf16x8 pf[2], rf[2][5];
    pf[0] = *(const LAS bf16x8*)(bp + pA0);
#pragma unroll
    for (int j = 0; j < 5; ++j) rf[0][j] = cat8(tr_read(bp + rA + j * 4096), tr_read(bp + rA + j * 4096 + 256));
    pf[1] = *(const LAS bf16x8*)(bp + pA1);
#pragma unroll
    for (int j = 0; j < 5; ++j) rf[1][j] = cat8(tr_read(bp + rA + j * 4096 + 1024), tr_read(bp + rA + j * 4096 + 1024 + 256));
    __builtin_amdgcn_sched_barrier(0);
#pragma unroll
    for (int k2 = 0; k2 < 2; ++k2)
#pragma unroll
      for (int j = 0; j < 5; ++j) acc[j] = __builtin_amdgcn_mfma_f32_32x32x16_bf16(rf[k2][j], pf[k2], acc[j], 0, 0, 0);
    if (++buf == G2_NST) buf = 0;
  }
  for (int step = 16; step < total; ++step) {
    if (step + G2_NST - 1 > total) WAIT_VM0();
    else if (w < 4) wait_vm<5 * (G2_NST - 2)>();
    else wait_vm<4 * (G2_NST - 2)>();
    BARRIER();
    if (issued < total) issue();
    const char LAS* bp = lds + buf * G2_STAGE;
    bf16x8 pf[2], rf[2][5];
    pf[0] = cat8(tr_read(bp + pB), tr_read(bp + pB + 1024));
#pragma unroll
    for (int j = 0; j < 5; ++j) rf[0][j] = *(const LAS bf16x8*)(bp + rB0 + j * 4096);
    pf[1] = cat8(tr_read(bp + pB + 4096), tr_read(bp + pB + 4096 + 1024));
#pragma unroll
    for (int j = 0; j < 5; ++j) rf[1][j] = *(const LAS bf16x8*)(bp + rB1 + j * 4096);
    __builtin_amdgcn_sched_barrier(0);
#pragma unroll
    for (int k2 = 0; k2 < 2; ++k2)
#pragma unroll
      for (int j = 0; j < 5; ++j) acc[j] = __builtin_amdgcn_mfma_f32_32x32x16_bf16(rf[k2][j], pf[k2], acc[j], 0, 0, 0);
    if (++buf == G2_NST) buf = 0;
  }
  TS(2, 1);
  BARRIER();
  {
    float LAS* dst = (float LAS*)(lds + w * 12288);
    if (kh == 0) {
#pragma unroll
      for (int j = 3; j < 5; ++j)
#pragma unroll
        for (int g = 0; g < 4; ++g)
          *(f32x4 LAS*)(dst + (((j - 3) * 4 + g) * 64 + l) * 4) = (f32x4){acc[j][4 * g], acc[j][4 * g + 1], acc[j][4 * g + 2], acc[j][4 * g + 3]};
    } else {
#pragma unroll
      for (int j = 0; j < 3; ++j)
#pragma unroll
        for (int g = 0; g < 4; ++g)
          *(f32x4 LAS*)(dst + ((j * 4 + g) * 64 + l) * 4) = (f32x4){acc[j][4 * g], acc[j][4 * g + 1], acc[j][4 * g + 2], acc[j][4 * g + 3]};
    }
    asm volatile("s_waitcnt lgkmcnt(0)" ::: "memory");
    BARRIER();
    const float LAS* src = (const float LAS*)(lds + (w ^ 4) * 12288);
    float* orow = part + ((size_t)ks * S_LEN + s0 + 32 * ws + r32) * DM + m0 + 8 * hi;
    if (kh == 0) {
#pragma unroll
      for (int j = 0; j < 3; ++j)
#pragma unroll
        for (int g = 0; g < 4; ++g) {
          const f32x4 o = *(const f32x4 LAS*)(src + ((j * 4 + g) * 64 + l) * 4);
          st16f(orow + 32 * j + (g >> 1) * 16 + (g & 1) * 4, acc[j][4 * g] + o[0], acc[j][4 * g + 1] + o[1], acc[j][4 * g + 2] + o[2], acc[j][4 * g + 3] + o[3]);
        }
    } else {
#pragma unroll
      for (int j = 3; j < 5; ++j)
#pragma unroll
        for (int g = 0; g < 4; ++g) {
          const f32x4 o = *(const f32x4 LAS*)(src + (((j - 3) * 4 + g) * 64 + l) * 4);
          st16f(orow + 32 * j + (g >> 1) * 16 + (g & 1) * 4, acc[j][4 * g] + o[0], acc[j][4 * g + 1] + o[1], acc[j][4 * g + 2] + o[2], acc[j][4 * g + 3] + o[3]);
        }
    }
  }
  TS(2, 2);
}

__global__ __launch_bounds__(256) void reduce_kernel(const float* __restrict__ part, const bf16_t* __restrict__ x,
                                                     const bf16_t* __restrict__ b2, bf16_t* __restrict__ out) {
  const int i = blockIdx.x * 256 + threadIdx.x;
  const int e = i * 4;
  const int s = e / DM;
  f32x4 a = *(const f32x4*)(part + e);
#pragma unroll
  for (int sp = 1; sp < 4; ++sp) a += *(const f32x4*)(part + (size_t)sp * S_LEN * DM + e);
  const u32x2 xv = *(const u32x2*)(x + e);
  const float bb = bf2f(b2[s]);
  float r0 = __uint_as_float(xv[0] << 16) + a[0] + bb;
  float r1 = __uint_as_float(xv[0] & 0xffff0000u) + a[1] + bb;
  float r2 = __uint_as_float(xv[1] << 16) + a[2] + bb;
  float r3 = __uint_as_float(xv[1] & 0xffff0000u) + a[3] + bb;
  u32x2 o = {pack2(r0, r1), pack2(r2, r3)};
  *(u32x2*)(out + e) = o;
}

extern "C" void kernel_launch(void* const* d_in, const int* in_sizes, int n_in, void* d_out, int out_size, void* d_ws,
                              size_t ws_size, hipStream_t stream) {
  const bf16_t* x = (const bf16_t*)d_in[0];
  const bf16_t* ckv = (const bf16_t*)d_in[1];
  const bf16_t* wq = (const bf16_t*)d_in[2];
  const bf16_t* wuk = (const bf16_t*)d_in[3];
  const bf16_t* wuv = (const bf16_t*)d_in[4];
  const bf16_t* wo = (const bf16_t*)d_in[5];
  const bf16_t* w1 = (const bf16_t*)d_in[7];
  const bf16_t* b1 = (const bf16_t*)d_in[8];
  const bf16_t* w2 = (const bf16_t*)d_in[9];
  const bf16_t* b2 = (const bf16_t*)d_in[10];
  char* ws = (char*)d_ws;
  bf16_t* xn = (bf16_t*)(ws + WS_XN);
  bf16_t* h = (bf16_t*)(ws + WS_H);
  bf16_t* Q = (bf16_t*)(ws + WS_Q);
  bf16_t* K = (bf16_t*)(ws + WS_K);
  bf16_t* V = (bf16_t*)(ws + WS_V);
  bf16_t* U = (bf16_t*)(ws + WS_U);
  bf16_t* ctx = (bf16_t*)(ws + WS_CTX);
  float* part = (float*)(ws + WS_PART);
  float* up = (float*)(ws + WS_UP);
  ln_kernel<<<S_LEN / 4, 256, 0, stream>>>(x, xn, h);
  gemm1_kernel<<<256, 512, 0, stream>>>(xn, h, ckv, wq, wuk, wuv, w1, Q, K, V, up);
  attn_kernel<<<256, 512, 0, stream>>>(Q, K, V, ctx, up, b1, U);
  gemm2_kernel<<<256, 512, 0, stream>>>(ctx, wo, U, w2, part);
  reduce_kernel<<<S_LEN * DM / 4 / 256, 256, 0, stream>>>(part, x, b2, (bf16_t*)d_out);
}
```

```cpp
#include <hip/hip_runtime.h>
#include <hip/hip_bf16.h>
#include <stdint.h>

constexpr int S_LEN = 2048;
constexpr int DM = 640;
constexpr int NH = 32;
constexpr int HD = 128;
constexpr int DC = 512;
constexpr int FF = 1728;
constexpr int HDN = NH * HD;
constexpr int CTX_LD = HDN + 64;

typedef unsigned short bf16_t;
typedef short bf16x8 __attribute__((ext_vector_type(8)));
typedef short s16x4 __attribute__((ext_vector_type(4)));
typedef float f32x16 __attribute__((ext_vector_type(16)));
typedef float f32x4 __attribute__((ext_vector_type(4)));
typedef unsigned u32x4 __attribute__((ext_vector_type(4)));
typedef unsigned u32x2 __attribute__((ext_vector_type(2)));
typedef __bf16 bf16x2_t __attribute__((ext_vector_type(2)));
#define LAS __attribute__((address_space(3)))

__device__ __forceinline__ float bf2f(bf16_t v) { return __uint_as_float(((unsigned)v) << 16); }
__device__ __forceinline__ unsigned pack2(float a, float b) {
  bf16x2_t v = {(__bf16)a, (__bf16)b};
  return *reinterpret_cast<unsigned*>(&v);
}
__device__ __forceinline__ float rbf(float a) {
  __bf16 v = (__bf16)a;
  return __uint_as_float(((unsigned)*reinterpret_cast<unsigned short*>(&v)) << 16);
}

constexpr size_t WS_XN = 0;
constexpr size_t WS_H = WS_XN + (size_t)S_LEN * DM * 2;
constexpr size_t WS_Q = WS_H + (size_t)S_LEN * DM * 2;
constexpr size_t WS_K = WS_Q + (size_t)S_LEN * HDN * 2;
constexpr size_t WS_V = WS_K + (size_t)S_LEN * HDN * 2;
constexpr size_t WS_U = WS_V + (size_t)S_LEN * HDN * 2;
constexpr size_t WS_CTX = WS_U + (size_t)DM * FF * 2;
constexpr size_t WS_PART = WS_CTX + (size_t)S_LEN * CTX_LD * 2;
constexpr size_t WS_UP = WS_PART + (size_t)6 * S_LEN * DM * 4;

__device__ __forceinline__ float wave_sum(float v) {
#pragma unroll
  for (int o = 32; o > 0; o >>= 1) v += __shfl_xor(v, o);
  return v;
}
__device__ __forceinline__ void ln_row(float (&v)[10]) {
  float s = 0.f;
#pragma unroll
  for (int i = 0; i < 10; ++i) s += v[i];
  s = wave_sum(s);
  const float mean = s * (1.0f / DM);
  float q = 0.f;
#pragma unroll
  for (int i = 0; i < 10; ++i) { float d = v[i] - mean; q += d * d; }
  q = wave_sum(q);
  const float mb = rbf(mean);
  const float vb = rbf(q * (1.0f / DM));
  const float den = rbf(sqrtf(rbf(vb + 1e-5f)));
#pragma unroll
  for (int i = 0; i < 10; ++i) v[i] = rbf(rbf(v[i] - mb) / den);
}
__global__ __launch_bounds__(256) void ln_kernel(const bf16_t* __restrict__ x, bf16_t* __restrict__ xn,
                                                 bf16_t* __restrict__ h) {
  const int lane = threadIdx.x & 63, w = threadIdx.x >> 6;
  const int row = blockIdx.x * 4 + w;
  const unsigned* xr = reinterpret_cast<const unsigned*>(x + (size_t)row * DM);
  float v[10];
#pragma unroll
  for (int j = 0; j < 5; ++j) {
    unsigned u = xr[lane + 64 * j];
    v[2 * j] = __uint_as_float(u << 16);
    v[2 * j + 1] = __uint_as_float(u & 0xffff0000u);
  }
  ln_row(v);
  unsigned* xo = reinterpret_cast<unsigned*>(xn + (size_t)row * DM);
#pragma unroll
  for (int j = 0; j < 5; ++j) xo[lane + 64 * j] = pack2(v[2 * j], v[2 * j + 1]);
  ln_row(v);
  unsigned* ho = reinterpret_cast<unsigned*>(h + (size_t)row * DM);
#pragma unroll
  for (int j = 0; j < 5; ++j) ho[lane + 64 * j] = pack2(v[2 * j], v[2 * j + 1]);
}

constexpr int STAGE_BYTES = 49152;
constexpr int RBASE = 32768;

__device__ __forceinline__ int swapmid(int p) { return ((p & 1) << 1) | ((p >> 1) & 1); }
__device__ __forceinline__ int rperm(int r) {
  int g = r >> 2;
  int pg = (g & 4) | swapmid(g & 3);
  return pg * 4 + (r & 3);
}
__device__ __forceinline__ s16x4 tr_read(const char LAS* p) {
  return __builtin_amdgcn_ds_read_tr16_b64_v4i16((LAS s16x4*)p);
}
__device__ __forceinline__ bf16x8 cat8(s16x4 a, s16x4 b) {
  return (bf16x8){a[0], a[1], a[2], a[3], b[0], b[1], b[2], b[3]};
}

__device__ __forceinline__ void glds16(const void* sbase, int voff, unsigned lds_dst) {
  unsigned keep;
  asm volatile("s_mov_b32 %0, m0\n\ts_mov_b32 m0, %3\n\ts_nop 0\n\tglobal_load_lds_dwordx4 %1, %2\n\ts_mov_b32 m0, %0"
               : "=&s"(keep) : "v"(voff), "s"(sbase), "s"(lds_dst) : "memory");
}

__device__ __forceinline__ void glds16x4(const void* sbase, int v0, int v1, int v2, int v3, unsigned lds_dst) {
  unsigned keep;
  asm volatile("s_mov_b32 %0, m0\n\ts_mov_b32 m0, %6\n\ts_nop 0\n\tglobal_load_lds_dwordx4 %1, %5\n\t"
               "global_load_lds_dwordx4 %2, %5 offset:1024\n\tglobal_load_lds_dwordx4 %3, %5 offset:2048\n\t"
               "global_load_lds_dwordx4 %4, %5 offset:3072\n\ts_mov_b32 m0, %0"
               : "=&s"(keep) : "v"(v0), "v"(v1), "v"(v2), "v"(v3), "s"(sbase), "s"(lds_dst) : "memory");
}

#define ST_MODE 0
__device__ __forceinline__ void st16(void* p, u32x4 v) {
  *(u32x4*)p = v;
}
__device__ __forceinline__ void st16f(float* p, float a, float b, float c, float d) {
  st16(p, (u32x4){__float_as_uint(a), __float_as_uint(b), __float_as_uint(c), __float_as_uint(d)});
}

template <bool P_TR, bool R_TR, bool ROWS_P>
struct GemmCore {
  int pOff[4], rOff[2];
  int pAddr[4], rAddr[4];
  int w, l;
  __device__ __forceinline__ void init(int ldp, int ldr, int pcol_limit  ) {
    const int tid = threadIdx.x;
    l = tid & 63;
    w = __builtin_amdgcn_readfirstlane(tid >> 6);
    const int wp = w >> 1, wr = w & 1;
#pragma unroll
    for (int i = 0; i < 4; ++i) {
      const int j = w * 4 + i;
      if (!P_TR) {
        const int row = 8 * j + (l >> 3), slot = l & 7;
        const int chunk = slot ^ ((row >> 1) & 7);
        pOff[i] = row * ldp + chunk * 8;
      } else {
        const int half = j >> 4, jj = j & 15;
        const int krow = 4 * jj + (l >> 4), s16 = l & 15;
        const int c64 = (s16 >> 2) ^ (krow & 3);
        int col = half * 128 + c64 * 32 + (s16 & 3) * 8;
        if (col >= pcol_limit) col = pcol_limit - 8;
        pOff[i] = krow * ldp + col;
      }
    }
#pragma unroll
    for (int i = 0; i < 2; ++i) {
      const int j = w * 2 + i;
      if (!R_TR) {
        const int row = 8 * j + (l >> 3), slot = l & 7;
        const int chunk = slot ^ ((row >> 1) & 7);
        rOff[i] = row * ldr + chunk * 8;
      } else {
        const int krow = 4 * j + (l >> 4), s16 = l & 15;
        const int c64 = (s16 >> 2) ^ (krow & 3);
        const int col = c64 * 32 + (s16 & 3) * 8;
        rOff[i] = krow * ldr + col;
      }
    }
    const int r32 = l & 31, hi = l >> 5, blk = (l >> 4) & 1, q = (l & 15) >> 2, p = l & 3;
    if (!P_TR) {
      const int prow = 64 * wp + (ROWS_P ? rperm(r32) : r32);
      const int s = (prow >> 1) & 7;
#pragma unroll
      for (int kk = 0; kk < 4; ++kk) pAddr[kk] = prow * 128 + (((2 * kk + hi) ^ s) << 4);
    } else {
      const int half = wp >> 1, pp = ROWS_P ? swapmid(p) : p;
#pragma unroll
      for (int i = 0; i < 2; ++i) {
        const int c64 = 2 * (wp & 1) + i;
        pAddr[i] = half * 16384 + (8 * hi + q) * 256 + ((c64 ^ q) * 64) + blk * 32 + pp * 8;
      }
      pAddr[2] = pAddr[3] = 0;
    }
    if (!R_TR) {
      const int rrow = 64 * wr + (!ROWS_P ? rperm(r32) : r32);
      const int s = (rrow >> 1) & 7;
#pragma unroll
      for (int kk = 0; kk < 4; ++kk) rAddr[kk] = RBASE + rrow * 128 + (((2 * kk + hi) ^ s) << 4);
    } else {
      const int pp = !ROWS_P ? swapmid(p) : p;
#pragma unroll
      for (int i = 0; i < 2; ++i) {
        const int c64 = 2 * wr + i;
        rAddr[i] = RBASE + (8 * hi + q) * 256 + ((c64 ^ q) * 64) + blk * 32 + pp * 8;
      }
      rAddr[2] = rAddr[3] = 0;
    }
  }
  __device__ __forceinline__ void stage(char LAS* buf, const bf16_t* P, const bf16_t* R) {
    const unsigned b = (unsigned)(uintptr_t)buf;
#pragma unroll
    for (int i = 0; i < 4; ++i) glds16(P, pOff[i] * 2, b + (w * 4 + i) * 1024);
#pragma unroll
    for (int i = 0; i < 2; ++i) glds16(R, rOff[i] * 2, b + RBASE + (w * 2 + i) * 1024);
  }
  __device__ __forceinline__ void compute(const char LAS* buf, f32x16 (&acc)[2][2]) {
#pragma unroll
    for (int kk = 0; kk < 4; ++kk) {
      bf16x8 pf[2], rf[2];
#pragma unroll
      for (int i = 0; i < 2; ++i) {
        if (!P_TR) pf[i] = *(const LAS bf16x8*)(buf + pAddr[kk] + i * 4096);
        else pf[i] = cat8(tr_read(buf + pAddr[i] + kk * 4096), tr_read(buf + pAddr[i] + kk * 4096 + 1024));
        if (!R_TR) rf[i] = *(const LAS bf16x8*)(buf + rAddr[kk] + i * 4096);
        else rf[i] = cat8(tr_read(buf + rAddr[i] + kk * 4096), tr_read(buf + rAddr[i] + kk * 4096 + 1024));
      }
#pragma unroll
      for (int i = 0; i < 2; ++i)
#pragma unroll
        for (int j = 0; j < 2; ++j) {
          if (ROWS_P) acc[i][j] = __builtin_amdgcn_mfma_f32_32x32x16_bf16(pf[i], rf[j], acc[i][j], 0, 0, 0);
          else acc[i][j] = __builtin_amdgcn_mfma_f32_32x32x16_bf16(rf[j], pf[i], acc[i][j], 0, 0, 0);
        }
    }
  }
};

#define WAIT_VM0() asm volatile("s_waitcnt vmcnt(0)" ::: "memory")
#define WAIT_VM6() asm volatile("s_waitcnt vmcnt(6)" ::: "memory")
#define WAIT_VM16() asm volatile("s_waitcnt vmcnt(16)" ::: "memory")
#define BARRIER() __builtin_amdgcn_s_barrier()
#define TS(k, i)
#define PH(i, k)

__device__ __forceinline__ float gelu_tanh(float x) {
  const float y = 0.7978845608028654f * (x + 0.044715f * x * x * x);
  const float e = __expf(2.0f * y);
  const float t = 1.0f - 2.0f / (e + 1.0f);
  return 0.5f * x * (1.0f + t);
}

constexpr int STAGE256 = 32768;
#define NST256 4
struct Core256 {
  int pOff[2], rOff[2];
  int pAddr[2], rAddr[2];
  int w;
  __device__ __forceinline__ void init(int ldp, int ldr) {
    const int tid = threadIdx.x, l = tid & 63;
    w = __builtin_amdgcn_readfirstlane(tid >> 6);
    const int wp = w >> 2, wr = w & 3;
#pragma unroll
    for (int i = 0; i < 2; ++i) {
      const int j = w * 2 + i;
      const int row = 16 * j + (l >> 2), slot = l & 3;
      pOff[i] = (row * ldp + (slot ^ ((row >> 2) & 3)) * 8) * 2;
      const int half = j >> 3, jj = j & 7;
      const int krow = 4 * jj + (l >> 4), s16 = l & 15;
      const int c64 = (s16 >> 2) ^ (krow & 3);
      rOff[i] = (krow * ldr + half * 128 + c64 * 32 + (s16 & 3) * 8) * 2;
    }
    const int r32 = l & 31, hi = l >> 5, blk = (l >> 4) & 1, q = (l & 15) >> 2, p = l & 3;
    const int prow = 128 * wp + r32, s = (prow >> 2) & 3;
#pragma unroll
    for (int kk = 0; kk < 2; ++kk) pAddr[kk] = prow * 64 + (((2 * kk + hi) ^ s) << 4);
#pragma unroll
    for (int j = 0; j < 2; ++j) {
      const int c64 = 2 * (wr & 1) + j;
      rAddr[j] = 16384 + (wr >> 1) * 8192 + (8 * hi + q) * 256 + ((c64 ^ q) * 64) + blk * 32 + swapmid(p) * 8;
    }
  }
  __device__ __forceinline__ void stage(char LAS* buf, const bf16_t* P, const bf16_t* R) {
    const unsigned b = (unsigned)(uintptr_t)buf;
#pragma unroll
    for (int i = 0; i < 2; ++i) glds16(P, pOff[i], b + (w * 2 + i) * 1024);
#pragma unroll
    for (int i = 0; i < 2; ++i) glds16(R, rOff[i], b + 16384 + (w * 2 + i) * 1024);
  }
  __device__ __forceinline__ void compute(const char LAS* buf, f32x16 (&acc)[4][2]) {
#pragma unroll
    for (int kk = 0; kk < 2; ++kk) {
      bf16x8 pf[4], rf[2];
#pragma unroll
      for (int j = 0; j < 2; ++j) rf[j] = cat8(tr_read(buf + rAddr[j] + kk * 4096), tr_read(buf + rAddr[j] + kk * 4096 + 1024));
#pragma unroll
      for (int i = 0; i < 4; ++i) pf[i] = *(const LAS bf16x8*)(buf + pAddr[kk] + i * 2048);
#pragma unroll
      for (int i = 0; i < 4; ++i)
#pragma unroll
        for (int j = 0; j < 2; ++j) acc[i][j] = __builtin_amdgcn_mfma_f32_32x32x16_bf16(rf[j], pf[i], acc[i][j], 0, 0, 0);
    }
  }
  __device__ __forceinline__ void load_frags(const char LAS* buf, bf16x8 (&pf)[2][4], bf16x8 (&rf)[2][2]) {
#pragma unroll
    for (int kk = 0; kk < 2; ++kk) {
#pragma unroll
      for (int j = 0; j < 2; ++j) rf[kk][j] = cat8(tr_read(buf + rAddr[j] + kk * 4096), tr_read(buf + rAddr[j] + kk * 4096 + 1024));
#pragma unroll
      for (int i = 0; i < 4; ++i) pf[kk][i] = *(const LAS bf16x8*)(buf + pAddr[kk] + i * 2048);
    }
  }
  __device__ __forceinline__ void mma(const bf16x8 (&pf)[2][4], const bf16x8 (&rf)[2][2], f32x16 (&acc)[4][2]) {
#pragma unroll
    for (int kk = 0; kk < 2; ++kk)
#pragma unroll
      for (int i = 0; i < 4; ++i)
#pragma unroll
        for (int j = 0; j < 2; ++j) acc[i][j] = __builtin_amdgcn_mfma_f32_32x32x16_bf16(rf[kk][j], pf[kk][i], acc[i][j], 0, 0, 0);
  }
};
#define WAIT_LGKM0() asm volatile("s_waitcnt lgkmcnt(0)" ::: "memory")
#define SCHED0() __builtin_amdgcn_sched_barrier(0)
template <int N> __device__ __forceinline__ void wait_vm() { asm volatile("s_waitcnt vmcnt(%0)" ::"n"(N) : "memory"); }

__global__ __launch_bounds__(512, 2) void gemm1_kernel(const bf16_t* __restrict__ xn, const bf16_t* __restrict__ hbuf,
                                                       const bf16_t* __restrict__ ckv, const bf16_t* __restrict__ wq,
                                                       const bf16_t* __restrict__ wuk, const bf16_t* __restrict__ wuv,
                                                       const bf16_t* __restrict__ w1, const bf16_t* __restrict__ b1,
                                                       bf16_t* __restrict__ Qo, bf16_t* __restrict__ Ko,
                                                       bf16_t* __restrict__ Vo, bf16_t* __restrict__ Uo) {
  __shared__ __attribute__((aligned(16))) char smem[NST256 * STAGE256 + 32768];
  char LAS* lds = (char LAS*)smem;
  const int xcd = blockIdx.x & 7, c = blockIdx.x >> 3;
  const int l = threadIdx.x & 63, w = __builtin_amdgcn_readfirstlane(threadIdx.x >> 6);
  const int r32 = l & 31, hi = l >> 5;
  TS(0, 0);
  const bool isQ = c >= 16;
  {
    int ntile, tb0, hp;
    const bf16_t* A; const bf16_t* W; bf16_t* O; int K;
    if (!isQ) {
      ntile = 2; tb0 = c >> 2; hp = xcd * 2 + (c & 1);
      A = ckv; K = DC;
      W = (c & 2) ? wuv : wuk; O = (c & 2) ? Vo : Ko;
    } else {
      ntile = 1; tb0 = (c - 16) >> 1; hp = xcd * 2 + (c & 1);
      A = xn; K = DM; W = wq; O = Qo;
    }
    const int nk = K / 32;
    Core256 g;
    g.init(K, HDN);
    const bf16_t* Wp = W + hp * 256;
    const int wp = w >> 2, wr = w & 3;
    const int total = ntile * nk;
    int pt = 0, pk = 0, pslot = 0, issued = 0;
    auto issue = [&]() {
      g.stage(lds + pslot * STAGE256, A + (size_t)(tb0 + 4 * pt) * 256 * K + pk * 32, Wp + (size_t)pk * 32 * HDN);
      if (++pk == nk) { pk = 0; ++pt; }
      if (++pslot == NST256) pslot = 0;
      ++issued;
    };
#pragma unroll
    for (int i = 0; i < NST256 - 1; ++i) issue();
    int buf = 0, step = 0;
    wait_vm<4 * (NST256 - 2)>();
    BARRIER();
    if (wp == 1) BARRIER();
    for (int t = 0; t < ntile; ++t) {
      const int tb = tb0 + 4 * t;
      f32x16 acc[4][2] = {};
      for (int kt = 0; kt < nk; ++kt) {
        bf16x8 pf[2][4], rf[2][2];
        if (issued < total) issue();
        g.load_frags(lds + buf * STAGE256, pf, rf);
        if (step + NST256 > total) WAIT_VM0();
        else if (t > 0 && kt < NST256 - 2) wait_vm<4 * (NST256 - 2) + 16>();
        else wait_vm<4 * (NST256 - 2)>();
        WAIT_LGKM0();
        SCHED0();
        BARRIER();
        SCHED0();
        __builtin_amdgcn_s_setprio(1);
        g.mma(pf, rf, acc);
        __builtin_amdgcn_s_setprio(0);
        SCHED0();
        BARRIER();
        SCHED0();
        if (++buf == NST256) buf = 0;
        ++step;
      }
      const int head = hp * 2 + (wr >> 1);
      char LAS* slab = lds + NST256 * STAGE256 + w * 4096;
      const int wa = r32 * 128, wsw = r32 & 7;
      const int rrow = l >> 3, ra = rrow * 128 + (((l & 7) ^ rrow) << 4);
      bf16_t* obase = O + ((size_t)head * S_LEN + tb * 256 + 128 * wp + rrow) * HD + 64 * (wr & 1) + (l & 7) * 8;
#pragma unroll
      for (int i = 0; i < 4; ++i) {
#pragma unroll
        for (int j = 0; j < 2; ++j) {
          const f32x16 a = acc[i][j];
          u32x4 v0 = {pack2(a[0], a[1]), pack2(a[2], a[3]), pack2(a[4], a[5]), pack2(a[6], a[7])};
          u32x4 v1 = {pack2(a[8], a[9]), pack2(a[10], a[11]), pack2(a[12], a[13]), pack2(a[14], a[15])};
          *(u32x4 LAS*)(slab + wa + (((4 * j + hi) ^ wsw) << 4)) = v0;
          *(u32x4 LAS*)(slab + wa + (((4 * j + 2 + hi) ^ wsw) << 4)) = v1;
        }
#pragma unroll
        for (int it = 0; it < 4; ++it) {
          const u32x4 v = *(const u32x4 LAS*)(slab + ra + it * 1024);
          st16(obase + (size_t)(32 * i + 8 * it) * HD, v);
        }
      }
    }
    if (wp == 0) BARRIER();
  }
  TS(0, 1);
  const int mbase = (xcd * 90) >> 3, mcnt = (((xcd + 1) * 90) >> 3) - mbase;
  if (isQ && c - 16 < mcnt) {
    constexpr int MST = 28672;
    const int tile = mbase + (c - 16);
    const int d0 = (tile % 5) * 128, f0 = (tile / 5) * 96;
    const int ws = w & 3, kh = w >> 2;
    const int blk = (l >> 4) & 1, q = (l & 15) >> 2, p = l & 3;
    int pOff[2], rOff[2];
#pragma unroll
    for (int i = 0; i < 2; ++i) {
      const int j = w * 2 + i;
      const int krow = 4 * j + (l >> 4), s16 = l & 15;
      pOff[i] = (krow * DM + ((s16 >> 2) ^ (krow & 3)) * 32 + (s16 & 3) * 8) * 2;
      const int jr = w + 8 * i;
      const int cb = jr >> 2, kr = (jr & 3) * 16 + (l >> 2);
      rOff[i] = (kr * FF + cb * 32 + (l & 3) * 8) * 2;
    }
    const bf16_t* Pp = hbuf + d0;
    const bf16_t* Rp = w1 + f0;
    int pslot = 0, issued = 0;
    auto issue = [&]() {
      const unsigned bb = (unsigned)(uintptr_t)(lds + pslot * MST);
      const bf16_t* P = Pp + (size_t)issued * 64 * DM;
      const bf16_t* R = Rp + (size_t)issued * 64 * FF;
      glds16(P, pOff[0], bb + (w * 2) * 1024);
      glds16(P, pOff[1], bb + (w * 2 + 1) * 1024);
      glds16(R, rOff[0], bb + 16384 + w * 1024);
      if (w < 4) glds16(R, rOff[1], bb + 16384 + (w + 8) * 1024);
      if (++pslot == 4) pslot = 0;
      ++issued;
    };
    const int pB = (32 * kh + 8 * hi + q) * 256 + ((ws ^ q) * 64) + blk * 32 + p * 8;
    const int rA = 16384 + (32 * kh + 8 * hi + q) * 64 + blk * 32 + swapmid(p) * 8;
    BARRIER();
    issue(); issue(); issue();
    f32x16 acc[3] = {};
    int buf = 0;
    for (int step = 0; step < 32; ++step) {
      if (step + 3 > 32) WAIT_VM0();
      else if (w < 4) wait_vm<8>();
      else wait_vm<6>();
      BARRIER();
      if (issued < 32) issue();
      const char LAS* bp = lds + buf * MST;
      bf16x8 pf[2], rf[2][3];
      pf[0] = cat8(tr_read(bp + pB), tr_read(bp + pB + 1024));
#pragma unroll
      for (int j = 0; j < 3; ++j) rf[0][j] = cat8(tr_read(bp + rA + j * 4096), tr_read(bp + rA + j * 4096 + 256));
      pf[1] = cat8(tr_read(bp + pB + 4096), tr_read(bp + pB + 4096 + 1024));
#pragma unroll
      for (int j = 0; j < 3; ++j) rf[1][j] = cat8(tr_read(bp + rA + j * 4096 + 1024), tr_read(bp + rA + j * 4096 + 1024 + 256));
      SCHED0();
#pragma unroll
      for (int k2 = 0; k2 < 2; ++k2)
#pragma unroll
        for (int j = 0; j < 3; ++j) acc[j] = __builtin_amdgcn_mfma_f32_32x32x16_bf16(rf[k2][j], pf[k2], acc[j], 0, 0, 0);
      if (++buf == 4) buf = 0;
    }
    BARRIER();
    float LAS* dst = (float LAS*)(lds + w * 8192);
    const f32x16 sa = kh == 0 ? acc[2] : acc[0], ka = kh == 0 ? acc[0] : acc[2];
    const int so = kh == 0 ? 8 : 0;
#pragma unroll
    for (int g = 0; g < 4; ++g) *(f32x4 LAS*)(dst + (g * 64 + l) * 4) = (f32x4){sa[4 * g], sa[4 * g + 1], sa[4 * g + 2], sa[4 * g + 3]};
    float s1[8], k1[8];
#pragma unroll
    for (int e = 0; e < 8; ++e) { s1[e] = kh == 0 ? acc[1][8 + e] : acc[1][e]; k1[e] = kh == 0 ? acc[1][e] : acc[1][8 + e]; }
#pragma unroll
    for (int g = 0; g < 2; ++g) *(f32x4 LAS*)(dst + ((4 + g) * 64 + l) * 4) = (f32x4){s1[4 * g], s1[4 * g + 1], s1[4 * g + 2], s1[4 * g + 3]};
    (void)so;
    WAIT_LGKM0();
    BARRIER();
    const float LAS* src = (const float LAS*)(lds + (w ^ 4) * 8192);
    bf16_t* urow = Uo + (size_t)(d0 + 32 * ws + r32) * FF + f0 + 8 * hi;
    auto fin8 = [&](const float (&v)[8], int fo) {
      const u32x4 bv = *(const u32x4*)(b1 + f0 + 8 * hi + fo);
      float r[8];
#pragma unroll
      for (int e = 0; e < 4; ++e) {
        r[2 * e] = gelu_tanh(rbf(rbf(v[2 * e]) + __uint_as_float(bv[e] << 16)));
        r[2 * e + 1] = gelu_tanh(rbf(rbf(v[2 * e + 1]) + __uint_as_float(bv[e] & 0xffff0000u)));
      }
      st16(urow + fo, (u32x4){pack2(r[0], r[1]), pack2(r[2], r[3]), pack2(r[4], r[5]), pack2(r[6], r[7])});
    };
    const int jk = kh == 0 ? 0 : 2;
#pragma unroll
    for (int hh = 0; hh < 2; ++hh) {
      float v[8];
#pragma unroll
      for (int g = 0; g < 2; ++g) {
        const f32x4 o = *(const f32x4 LAS*)(src + ((2 * hh + g) * 64 + l) * 4);
#pragma unroll
        for (int e = 0; e < 4; ++e) v[4 * g + e] = ka[8 * hh + 4 * g + e] + o[e];
      }
      fin8(v, 32 * jk + 16 * hh);
    }
    {
      float v[8];
#pragma unroll
      for (int g = 0; g < 2; ++g) {
        const f32x4 o = *(const f32x4 LAS*)(src + ((4 + g) * 64 + l) * 4);
#pragma unroll
        for (int e = 0; e < 4; ++e) v[4 * g + e] = k1[4 * g + e] + o[e];
      }
      fin8(v, 32 + 16 * kh);
    }
  }
  TS(0, 2);
}

constexpr int AT_HALF = 16384;
__global__ __launch_bounds__(512, 2) void attn_kernel(const bf16_t* __restrict__ Q, const bf16_t* __restrict__ K,
                                                      const bf16_t* __restrict__ V, bf16_t* __restrict__ ctx) {
  __shared__ __attribute__((aligned(16))) char smem[8 * AT_HALF + 32768];
  char LAS* lds = (char LAS*)smem;
  const int tid = threadIdx.x, l = tid & 63, w = __builtin_amdgcn_readfirstlane(tid >> 6);
  const int rg = w & 3, g = w >> 2, r32 = l & 31, hi = l >> 5;
  const int xcd = blockIdx.x & 7, jj = blockIdx.x >> 3;
  const int head = xcd * 4 + (jj >> 3), pair = jj & 7;
  const int LB = 15 - pair, nshort = 2 * pair + 2;
  const bf16_t* Kh = K + (size_t)head * S_LEN * HD;
  const bf16_t* Vh = V + (size_t)head * S_LEN * HD;
  const bf16_t* Qh = Q + (size_t)head * S_LEN * HD;
  char LAS* gl = lds + g * 4 * AT_HALF;
  int kOff[4], vOff[4];
#pragma unroll
  for (int i = 0; i < 4; ++i) {
    const int j = rg * 4 + i, row = 4 * j + (l >> 4), s16 = l & 15;
    kOff[i] = (row * HD + ((s16 ^ (row & 15)) * 8)) * 2 - 1024 * i;
    vOff[i] = (row * HD + ((((s16 >> 2) ^ (row & 3)) * 4 + (s16 & 3)) * 8)) * 2 - 1024 * i;
  }
  const unsigned glb = (unsigned)(uintptr_t)gl;
  auto htile = [&](int i) { return (g == 0 || i < nshort) ? i : i + 15 - 2 * pair; };
  auto stageK = [&](int i) {
    const bf16_t* kp = Kh + (size_t)htile(i > 16 ? 16 : i) * 64 * HD;
    const unsigned bb = glb + (i & 1) * AT_HALF + rg * 4096;
    glds16x4(kp, kOff[0], kOff[1], kOff[2], kOff[3], bb);
  };
  auto stageV = [&](int i) {
    const bf16_t* vp = Vh + (size_t)htile(i) * 64 * HD;
    const unsigned bb = glb + (2 + (i & 1)) * AT_HALF + rg * 4096;
    glds16x4(vp, vOff[0], vOff[1], vOff[2], vOff[3], bb);
  };
  const int ksw = r32 & 15;
  const int blk = (l >> 4) & 1, q4 = (l & 15) >> 2, p4 = l & 3;
  int vAddr[4];
#pragma unroll
  for (int db = 0; db < 4; ++db)
    vAddr[db] = 2 * AT_HALF + (4 * hi + q4) * 256 + ((db ^ q4) * 64) + blk * 32 + swapmid(p4) * 8;
  constexpr float CS = 0.08838834764831845f * 1.4426950408889634f;

  TS(1, 0);
  stageK(0);
  stageV(0);
  stageK(1);
  int qb = g == 0 ? LB : pair;
  bf16x8 qf[8];
#pragma unroll
  for (int d0 = 0; d0 < 8; ++d0) qf[d0] = *(const bf16x8*)(Qh + (size_t)(qb * 128 + 32 * rg + r32) * HD + d0 * 16 + hi * 8);
  char LAS* qsave = lds + 8 * AT_HALF + rg * 8192 + l * 16;
  if (g == 0) {
#pragma unroll
    for (int d0 = 0; d0 < 8; ++d0) *(bf16x8 LAS*)(qsave + d0 * 1024) = qf[d0];
  }
  float m_run = -1e30f, l_run = 0.f, alpha = 1.f;
  f32x16 o[4] = {};
  f32x16 s0, s1;
  bf16x8 pf[4];
#define AT_W 6
  bf16x8 fr[32];
  const int kAddr0 = r32 * 256 + ((hi ^ ksw) << 4);
  auto rdK = [&](int k, int slot) {
    fr[k] = *(const LAS bf16x8*)(gl + slot * AT_HALF + (kAddr0 ^ ((k >> 1) << 5)) + (k & 1) * 8192);
  };
  auto rdV = [&](int k, int slot) {
    const char LAS* a = gl + slot * AT_HALF + vAddr[(k - 16) >> 2] + (k & 3) * 4096;
    fr[k] = cat8(tr_read(a), tr_read(a + 2048));
  };
  auto mm = [&](int k) {
    if (k < 16) {
      if (k & 1) s1 = __builtin_amdgcn_mfma_f32_32x32x16_bf16(fr[k], qf[k >> 1], k < 2 ? (f32x16){} : s1, 0, 0, 0);
      else s0 = __builtin_amdgcn_mfma_f32_32x32x16_bf16(fr[k], qf[k >> 1], k < 2 ? (f32x16){} : s0, 0, 0, 0);
    } else {
      o[(k - 16) >> 2] = __builtin_amdgcn_mfma_f32_32x32x16_bf16(fr[k], pf[k & 3], o[(k - 16) >> 2], 0, 0, 0);
    }
  };
  auto preK = [&](int slot) {
#pragma unroll
    for (int k = 0; k < AT_W; ++k) rdK(k, slot);
  };
  auto preV = [&](int slot) {
#pragma unroll
    for (int k = 16; k < 16 + AT_W; ++k) rdV(k, slot);
  };
  auto xhalf_max = [&](float v) {
    auto rr = __builtin_amdgcn_permlane32_swap(__float_as_uint(v), __float_as_uint(v), false, false);
    return fmaxf(__uint_as_float(rr[0]), __uint_as_float(rr[1]));
  };
  auto xhalf_sum = [&](float v) {
    auto rr = __builtin_amdgcn_permlane32_swap(__float_as_uint(v), __float_as_uint(v), false, false);
    return __uint_as_float(rr[0]) + __uint_as_float(rr[1]);
  };
  auto softmax = [&](int i) {
    if (g == 1 && i == nshort) {
      const float inv = 1.0f / xhalf_sum(l_run);
      bf16_t* orow = ctx + (size_t)(pair * 128 + 32 * rg + r32) * CTX_LD + head * HD + 8 * hi;
#pragma unroll
      for (int db = 0; db < 4; ++db) {
        const f32x16 a = o[db];
        u32x4 v0 = {pack2(a[0] * inv, a[1] * inv), pack2(a[2] * inv, a[3] * inv), pack2(a[4] * inv, a[5] * inv), pack2(a[6] * inv, a[7] * inv)};
        u32x4 v1 = {pack2(a[8] * inv, a[9] * inv), pack2(a[10] * inv, a[11] * inv), pack2(a[12] * inv, a[13] * inv), pack2(a[14] * inv, a[15] * inv)};
        st16(orow + 32 * db, v0);
        st16(orow + 32 * db + 16, v1);
        o[db] = (f32x16){};
      }
      m_run = -1e30f;
      l_run = 0.f;
      qb = LB;
    }
    if (g == 1 && i == nshort - 1) {
#pragma unroll
      for (int d0 = 0; d0 < 8; ++d0) qf[d0] = *(const bf16x8 LAS*)(qsave + d0 * 1024);
    }
    SCHED0();
    if (i < 16) stageK(i + 2);
    SCHED0(); PH(i, 5); SCHED0();
    const int dh = htile(i) - 2 * qb;
    if (dh >= 0) {
      const int qi = 32 * rg + r32 - 64 * dh;
#pragma unroll
      for (int r = 0; r < 16; ++r) {
        const int key = (r & 3) + 8 * (r >> 2) + 4 * hi;
        if (key > qi) s0[r] = -__builtin_inff();
        if (key + 32 > qi) s1[r] = -__builtin_inff();
      }
    }
    float pm = s0[0];
#pragma unroll
    for (int r = 1; r < 16; ++r) pm = fmaxf(pm, s0[r]);
#pragma unroll
    for (int r = 0; r < 16; ++r) pm = fmaxf(pm, s1[r]);
    pm = xhalf_max(pm);
    const float mn = fmaxf(m_run, pm);
    alpha = __builtin_amdgcn_exp2f((m_run - mn) * CS);
    m_run = mn;
    const float mc = -mn * CS;
    SCHED0(); PH(i, 6); SCHED0();
    float ps = 0.f;
#pragma unroll
    for (int r = 0; r < 16; ++r) { s0[r] = __builtin_amdgcn_exp2f(fmaf(s0[r], CS, mc)); ps += s0[r]; }
#pragma unroll
    for (int r = 0; r < 16; ++r) { s1[r] = __builtin_amdgcn_exp2f(fmaf(s1[r], CS, mc)); ps += s1[r]; }
    l_run = l_run * alpha + ps;
    SCHED0(); PH(i, 7); SCHED0();
    u32x4 a0 = {pack2(s0[0], s0[1]), pack2(s0[2], s0[3]), pack2(s0[4], s0[5]), pack2(s0[6], s0[7])};
    u32x4 a1 = {pack2(s0[8], s0[9]), pack2(s0[10], s0[11]), pack2(s0[12], s0[13]), pack2(s0[14], s0[15])};
    u32x4 a2 = {pack2(s1[0], s1[1]), pack2(s1[2], s1[3]), pack2(s1[4], s1[5]), pack2(s1[6], s1[7])};
    u32x4 a3 = {pack2(s1[8], s1[9]), pack2(s1[10], s1[11]), pack2(s1[12], s1[13]), pack2(s1[14], s1[15])};
    pf[0] = *(bf16x8*)&a0; pf[1] = *(bf16x8*)&a1; pf[2] = *(bf16x8*)&a2; pf[3] = *(bf16x8*)&a3;
    asm volatile("" : "+v"(pf[0]), "+v"(pf[1]), "+v"(pf[2]), "+v"(pf[3]), "+v"(l_run), "+v"(alpha));
  };
#define AT_DK0 16
  WAIT_VM0();
  WAIT_LGKM0();
  BARRIER();
  TS(1, 3);
  if (g == 1) BARRIER();
  SCHED0();
  preK(0);
#pragma unroll
  for (int k = 0; k < 16; ++k) {
    if (k + AT_W < 16) rdK(k + AT_W, 0);
    mm(k);
    SCHED0();
  }
  BARRIER();
  for (int i = 0; i < 16; ++i) {
    SCHED0();
    PH(i, 0);
    softmax(i);
    SCHED0();
    const int ks = (i + 1) & 1, vs = i & 1;
    preK(ks);
    SCHED0();
    wait_vm<4>();
    PH(i, 1);
    BARRIER();
    SCHED0();
    PH(i, 2);
#pragma unroll
    for (int k = 0; k < 32; ++k) {
      if (k + AT_W < 16) rdK(k + AT_W, ks);
      else if (k + AT_W < 32) rdV(k + AT_W, vs);
      mm(k);
      if (k < 16) {
#pragma unroll
        for (int e = 0; e < 4; ++e) {
          float t = o[k >> 2][4 * (k & 3) + e];
          asm volatile("v_mul_f32 %0, %1, %0" : "+v"(t) : "v"(alpha));
          o[k >> 2][4 * (k & 3) + e] = t;
        }
      }
      if (k == AT_DK0) stageV(i + 1);
      SCHED0();
    }
    PH(i, 3);
    wait_vm<4>();
    PH(i, 4);
    BARRIER();
  }
  SCHED0();
  softmax(16);
#pragma unroll
  for (int db = 0; db < 4; ++db)
#pragma unroll
    for (int r = 0; r < 16; ++r) o[db][r] *= alpha;
  SCHED0();
  WAIT_VM0();
  BARRIER();
  SCHED0();
  preV(0);
#pragma unroll
  for (int k = 16; k < 32; ++k) {
    if (k + AT_W < 32) rdV(k + AT_W, 0);
    mm(k);
    SCHED0();
  }
  BARRIER();
  if (g == 0) BARRIER();
  l_run = xhalf_sum(l_run);
  TS(1, 1);
  {
    const int kh = g;
    float LAS* ml = (float LAS*)(lds + 4 * AT_HALF);
    float LAS* dst = (float LAS*)(lds + w * 8192);
#pragma unroll
    for (int dd = 0; dd < 2; ++dd) {
      f32x16 a;
#pragma unroll
      for (int r = 0; r < 16; ++r) a[r] = kh == 0 ? o[2 + dd][r] : o[dd][r];
#pragma unroll
      for (int gg = 0; gg < 4; ++gg)
        *(f32x4 LAS*)(dst + ((dd * 4 + gg) * 64 + l) * 4) = (f32x4){a[4 * gg], a[4 * gg + 1], a[4 * gg + 2], a[4 * gg + 3]};
    }
    ml[w * 128 + l] = m_run;
    ml[w * 128 + 64 + l] = l_run;
    asm volatile("s_waitcnt lgkmcnt(0)" ::: "memory");
    BARRIER();
    const int wo = w ^ 4;
    const float m2 = ml[wo * 128 + l], l2 = ml[wo * 128 + 64 + l];
    const float mt = fmaxf(m_run, m2);
    const float a1 = __builtin_amdgcn_exp2f((m_run - mt) * CS), a2 = __builtin_amdgcn_exp2f((m2 - mt) * CS);
    const float inv = 1.0f / (l_run * a1 + l2 * a2);
    const float f1 = a1 * inv, f2 = a2 * inv;
    const float LAS* src = (const float LAS*)(lds + wo * 8192);
    bf16_t* orow = ctx + (size_t)(LB * 128 + 32 * rg + r32) * CTX_LD + head * HD + 8 * hi;
#pragma unroll
    for (int dd = 0; dd < 2; ++dd) {
      f32x16 a;
#pragma unroll
      for (int r = 0; r < 16; ++r) a[r] = kh == 0 ? o[dd][r] : o[2 + dd][r];
      float r[16];
#pragma unroll
      for (int gg = 0; gg < 4; ++gg) {
        const f32x4 b = *(const f32x4 LAS*)(src + ((dd * 4 + gg) * 64 + l) * 4);
#pragma unroll
        for (int e = 0; e < 4; ++e) r[4 * gg + e] = a[4 * gg + e] * f1 + b[e] * f2;
      }
      u32x4 v0 = {pack2(r[0], r[1]), pack2(r[2], r[3]), pack2(r[4], r[5]), pack2(r[6], r[7])};
      u32x4 v1 = {pack2(r[8], r[9]), pack2(r[10], r[11]), pack2(r[12], r[13]), pack2(r[14], r[15])};
      const int dcol = 32 * (2 * kh + dd);
      st16(orow + dcol, v0);
      st16(orow + dcol + 16, v1);
    }
  }
  TS(1, 2);
}

constexpr int G2_STAGE = 36864;
constexpr int G2_NST = 4;
__global__ __launch_bounds__(512, 2) void gemm2_kernel(const bf16_t* __restrict__ ctx, const bf16_t* __restrict__ wo,
                                                       const bf16_t* __restrict__ U, const bf16_t* __restrict__ w2,
                                                       float* __restrict__ part) {
  __shared__ __attribute__((aligned(16))) char smem[G2_NST * G2_STAGE];
  char LAS* lds = (char LAS*)smem;
  const int b = blockIdx.x, xcd = b & 7, c = b >> 3;
  const int ks = xcd >> 1;
  const int s0 = (xcd & 1) * 1024 + (c >> 2) * 128, m0 = (c & 3) * 160;
  const int l = threadIdx.x & 63, w = __builtin_amdgcn_readfirstlane(threadIdx.x >> 6);
  const int ws = w & 3, kh = w >> 2, r32 = l & 31, hi = l >> 5;
  const int blk = (l >> 4) & 1, q = (l & 15) >> 2, p = l & 3;
  TS(2, 0);
  int pOffA[2], pOffB[2], rOffA[3], rOffB[3];
#pragma unroll
  for (int i = 0; i < 2; ++i) {
    const int j = w * 2 + i;
    const int row = 8 * j + (l >> 3), slot = l & 7;
    pOffA[i] = (row * CTX_LD + (slot ^ ((row >> 1) & 7)) * 8) * 2;
    const int krow = 4 * j + (l >> 4), s16 = l & 15;
    pOffB[i] = (krow * S_LEN + ((s16 >> 2) ^ (krow & 3)) * 32 + (s16 & 3) * 8) * 2;
  }
#pragma unroll
  for (int i = 0; i < 3; ++i) {
    const int j = w + 8 * i;
    const int cb = j >> 2, kr = (j & 3) * 16 + (l >> 2);
    rOffA[i] = (kr * DM + cb * 32 + (l & 3) * 8) * 2;
    const int row = 8 * j + (l >> 3), slot = l & 7;
    rOffB[i] = (row * FF + (slot ^ ((row >> 1) & 7)) * 8) * 2;
  }
  const int f0 = ks * 7, nB = ks == 3 ? 6 : 7, total = 16 + nB;
  const bf16_t* PA = ctx + (size_t)s0 * CTX_LD + ks * 1024;
  const bf16_t* RA = wo + (size_t)ks * 1024 * DM + m0;
  const bf16_t* PB = w2 + (size_t)f0 * 64 * S_LEN + s0;
  const bf16_t* RB = U + (size_t)m0 * FF + f0 * 64;
  int pslot = 0, issued = 0;
  auto issue = [&]() {
    const unsigned bb = (unsigned)(uintptr_t)(lds + pslot * G2_STAGE);
    if (issued < 16) {
      const bf16_t* P = PA + issued * 64;
      const bf16_t* R = RA + (size_t)issued * 64 * DM;
      glds16(P, pOffA[0], bb + (w * 2) * 1024);
      glds16(P, pOffA[1], bb + (w * 2 + 1) * 1024);
      glds16(R, rOffA[0], bb + 16384 + w * 1024);
      glds16(R, rOffA[1], bb + 16384 + (w + 8) * 1024);
      if (w < 4) glds16(R, rOffA[2], bb + 16384 + (w + 16) * 1024);
    } else {
      const bf16_t* P = PB + (size_t)(issued - 16) * 64 * S_LEN;
      const bf16_t* R = RB + (issued - 16) * 64;
      glds16(P, pOffB[0], bb + (w * 2) * 1024);
      glds16(P, pOffB[1], bb + (w * 2 + 1) * 1024);
      glds16(R, rOffB[0], bb + 16384 + w * 1024);
      glds16(R, rOffB[1], bb + 16384 + (w + 8) * 1024);
      if (w < 4) glds16(R, rOffB[2], bb + 16384 + (w + 16) * 1024);
    }
    if (++pslot == G2_NST) pslot = 0;
    ++issued;
  };
  const int prow = 32 * ws + r32, psw = (prow >> 1) & 7;
  const int pA0 = prow * 128 + (((4 * kh + hi) ^ psw) << 4), pA1 = prow * 128 + (((4 * kh + 2 + hi) ^ psw) << 4);
  const int rA = 16384 + (32 * kh + 8 * hi + q) * 64 + blk * 32 + swapmid(p) * 8;
  const int pB = (32 * kh + 8 * hi + q) * 256 + ((ws ^ q) * 64) + blk * 32 + p * 8;
  const int rrow = rperm(r32), rsw = (rrow >> 1) & 7;
  const int rB0 = 16384 + rrow * 128 + (((4 * kh + hi) ^ rsw) << 4), rB1 = 16384 + rrow * 128 + (((4 * kh + 2 + hi) ^ rsw) << 4);
#pragma unroll
  for (int i = 0; i < G2_NST - 1; ++i) issue();
  f32x16 acc[5] = {};
  int buf = 0;
  for (int step = 0; step < 16; ++step) {
    if (w < 4) wait_vm<5 * (G2_NST - 2)>();
    else wait_vm<4 * (G2_NST - 2)>();
    BARRIER();
    issue();
    const char LAS* bp = lds + buf * G2_STAGE;
    bf16x8 pf[2], rf[2][5];
    pf[0] = *(const LAS bf16x8*)(bp + pA0);
#pragma unroll
    for (int j = 0; j < 5; ++j) rf[0][j] = cat8(tr_read(bp + rA + j * 4096), tr_read(bp + rA + j * 4096 + 256));
    pf[1] = *(const LAS bf16x8*)(bp + pA1);
#pragma unroll
    for (int j = 0; j < 5; ++j) rf[1][j] = cat8(tr_read(bp + rA + j * 4096 + 1024), tr_read(bp + rA + j * 4096 + 1024 + 256));
    __builtin_amdgcn_sched_barrier(0);
#pragma unroll
    for (int k2 = 0; k2 < 2; ++k2)
#pragma unroll
      for (int j = 0; j < 5; ++j) acc[j] = __builtin_amdgcn_mfma_f32_32x32x16_bf16(rf[k2][j], pf[k2], acc[j], 0, 0, 0);
    if (++buf == G2_NST) buf = 0;
  }
  for (int step = 16; step < total; ++step) {
    if (step + G2_NST - 1 > total) WAIT_VM0();
    else if (w < 4) wait_vm<5 * (G2_NST - 2)>();
    else wait_vm<4 * (G2_NST - 2)>();
    BARRIER();
    if (issued < total) issue();
    const char LAS* bp = lds + buf * G2_STAGE;
    bf16x8 pf[2], rf[2][5];
    pf[0] = cat8(tr_read(bp + pB), tr_read(bp + pB + 1024));
#pragma unroll
    for (int j = 0; j < 5; ++j) rf[0][j] = *(const LAS bf16x8*)(bp + rB0 + j * 4096);
    pf[1] = cat8(tr_read(bp + pB + 4096), tr_read(bp + pB + 4096 + 1024));
#pragma unroll
    for (int j = 0; j < 5; ++j) rf[1][j] = *(const LAS bf16x8*)(bp + rB1 + j * 4096);
    __builtin_amdgcn_sched_barrier(0);
#pragma unroll
    for (int k2 = 0; k2 < 2; ++k2)
#pragma unroll
      for (int j = 0; j < 5; ++j) acc[j] = __builtin_amdgcn_mfma_f32_32x32x16_bf16(rf[k2][j], pf[k2], acc[j], 0, 0, 0);
    if (++buf == G2_NST) buf = 0;
  }
  TS(2, 1);
  BARRIER();
  {
    float LAS* dst = (float LAS*)(lds + w * 12288);
    if (kh == 0) {
#pragma unroll
      for (int j = 3; j < 5; ++j)
#pragma unroll
        for (int g = 0; g < 4; ++g)
          *(f32x4 LAS*)(dst + (((j - 3) * 4 + g) * 64 + l) * 4) = (f32x4){acc[j][4 * g], acc[j][4 * g + 1], acc[j][4 * g + 2], acc[j][4 * g + 3]};
    } else {
#pragma unroll
      for (int j = 0; j < 3; ++j)
#pragma unroll
        for (int g = 0; g < 4; ++g)
          *(f32x4 LAS*)(dst + ((j * 4 + g) * 64 + l) * 4) = (f32x4){acc[j][4 * g], acc[j][4 * g + 1], acc[j][4 * g + 2], acc[j][4 * g + 3]};
    }
    asm volatile("s_waitcnt lgkmcnt(0)" ::: "memory");
    BARRIER();
    const float LAS* src = (const float LAS*)(lds + (w ^ 4) * 12288);
    float* orow = part + ((size_t)ks * S_LEN + s0 + 32 * ws + r32) * DM + m0 + 8 * hi;
    if (kh == 0) {
#pragma unroll
      for (int j = 0; j < 3; ++j)
#pragma unroll
        for (int g = 0; g < 4; ++g) {
          const f32x4 o = *(const f32x4 LAS*)(src + ((j * 4 + g) * 64 + l) * 4);
          st16f(orow + 32 * j + (g >> 1) * 16 + (g & 1) * 4, acc[j][4 * g] + o[0], acc[j][4 * g + 1] + o[1], acc[j][4 * g + 2] + o[2], acc[j][4 * g + 3] + o[3]);
        }
    } else {
#pragma unroll
      for (int j = 3; j < 5; ++j)
#pragma unroll
        for (int g = 0; g < 4; ++g) {
          const f32x4 o = *(const f32x4 LAS*)(src + (((j - 3) * 4 + g) * 64 + l) * 4);
          st16f(orow + 32 * j + (g >> 1) * 16 + (g & 1) * 4, acc[j][4 * g] + o[0], acc[j][4 * g + 1] + o[1], acc[j][4 * g + 2] + o[2], acc[j][4 * g + 3] + o[3]);
        }
    }
  }
  TS(2, 2);
}

__global__ __launch_bounds__(256) void reduce_kernel(const float* __restrict__ part, const bf16_t* __restrict__ x,
                                                     const bf16_t* __restrict__ b2, bf16_t* __restrict__ out) {
  const int i = blockIdx.x * 256 + threadIdx.x;
  const int e = i * 4;
  const int s = e / DM;
  f32x4 a = *(const f32x4*)(part + e);
#pragma unroll
  for (int sp = 1; sp < 4; ++sp) a += *(const f32x4*)(part + (size_t)sp * S_LEN * DM + e);
  const u32x2 xv = *(const u32x2*)(x + e);
  const float bb = bf2f(b2[s]);
  float r0 = __uint_as_float(xv[0] << 16) + a[0] + bb;
  float r1 = __uint_as_float(xv[0] & 0xffff0000u) + a[1] + bb;
  float r2 = __uint_as_float(xv[1] << 16) + a[2] + bb;
  float r3 = __uint_as_float(xv[1] & 0xffff0000u) + a[3] + bb;
  u32x2 o = {pack2(r0, r1), pack2(r2, r3)};
  *(u32x2*)(out + e) = o;
}

extern "C" void kernel_launch(void* const* d_in, const int* in_sizes, int n_in, void* d_out, int out_size, void* d_ws,
                              size_t ws_size, hipStream_t stream) {
  const bf16_t* x = (const bf16_t*)d_in[0];
  const bf16_t* ckv = (const bf16_t*)d_in[1];
  const bf16_t* wq = (const bf16_t*)d_in[2];
  const bf16_t* wuk = (const bf16_t*)d_in[3];
  const bf16_t* wuv = (const bf16_t*)d_in[4];
  const bf16_t* wo = (const bf16_t*)d_in[5];
  const bf16_t* w1 = (const bf16_t*)d_in[7];
  const bf16_t* b1 = (const bf16_t*)d_in[8];
  const bf16_t* w2 = (const bf16_t*)d_in[9];
  const bf16_t* b2 = (const bf16_t*)d_in[10];
  char* ws = (char*)d_ws;
  bf16_t* xn = (bf16_t*)(ws + WS_XN);
  bf16_t* h = (bf16_t*)(ws + WS_H);
  bf16_t* Q = (bf16_t*)(ws + WS_Q);
  bf16_t* K = (bf16_t*)(ws + WS_K);
  bf16_t* V = (bf16_t*)(ws + WS_V);
  bf16_t* U = (bf16_t*)(ws + WS_U);
  bf16_t* ctx = (bf16_t*)(ws + WS_CTX);
  float* part = (float*)(ws + WS_PART);
  ln_kernel<<<S_LEN / 4, 256, 0, stream>>>(x, xn, h);
  gemm1_kernel<<<256, 512, 0, stream>>>(xn, h, ckv, wq, wuk, wuv, w1, b1, Q, K, V, U);
  attn_kernel<<<256, 512, 0, stream>>>(Q, K, V, ctx);
  gemm2_kernel<<<256, 512, 0, stream>>>(ctx, wo, U, w2, part);
  reduce_kernel<<<S_LEN * DM / 4 / 256, 256, 0, stream>>>(part, x, b2, (bf16_t*)d_out);
}
```

```cpp
#include <hip/hip_runtime.h>
#include <hip/hip_bf16.h>
#include <stdint.h>

constexpr int S_LEN = 2048;
constexpr int DM = 640;
constexpr int NH = 32;
constexpr int HD = 128;
constexpr int DC = 512;
constexpr int FF = 1728;
constexpr int HDN = NH * HD;
constexpr int CTX_LD = HDN + 64;

typedef unsigned short bf16_t;
typedef short bf16x8 __attribute__((ext_vector_type(8)));
typedef short s16x4 __attribute__((ext_vector_type(4)));
typedef float f32x16 __attribute__((ext_vector_type(16)));
typedef float f32x4 __attribute__((ext_vector_type(4)));
typedef unsigned u32x4 __attribute__((ext_vector_type(4)));
typedef unsigned u32x2 __attribute__((ext_vector_type(2)));
typedef __bf16 bf16x2_t __attribute__((ext_vector_type(2)));
#define LAS __attribute__((address_space(3)))

__device__ __forceinline__ float bf2f(bf16_t v) { return __uint_as_float(((unsigned)v) << 16); }
__device__ __forceinline__ unsigned pack2(float a, float b) {
  bf16x2_t v = {(__bf16)a, (__bf16)b};
  return *reinterpret_cast<unsigned*>(&v);
}
__device__ __forceinline__ float rbf(float a) {
  __bf16 v = (__bf16)a;
  return __uint_as_float(((unsigned)*reinterpret_cast<unsigned short*>(&v)) << 16);
}

constexpr size_t WS_XN = 0;
constexpr size_t WS_H = WS_XN + (size_t)S_LEN * DM * 2;
constexpr size_t WS_Q = WS_H + (size_t)S_LEN * DM * 2;
constexpr size_t WS_K = WS_Q + (size_t)S_LEN * HDN * 2;
constexpr size_t WS_V = WS_K + (size_t)S_LEN * HDN * 2;
constexpr size_t WS_U = WS_V + (size_t)S_LEN * HDN * 2;
constexpr size_t WS_CTX = WS_U + (size_t)DM * FF * 2;
constexpr size_t WS_PART = WS_CTX + (size_t)S_LEN * CTX_LD * 2;
constexpr size_t WS_UP = WS_PART + (size_t)6 * S_LEN * DM * 4;

__device__ __forceinline__ float wave_sum(float v) {
#pragma unroll
  for (int o = 32; o > 0; o >>= 1) v += __shfl_xor(v, o);
  return v;
}
__device__ __forceinline__ void ln_row(float (&v)[10]) {
  float s = 0.f;
#pragma unroll
  for (int i = 0; i < 10; ++i) s += v[i];
  s = wave_sum(s);
  const float mean = s * (1.0f / DM);
  float q = 0.f;
#pragma unroll
  for (int i = 0; i < 10; ++i) { float d = v[i] - mean; q += d * d; }
  q = wave_sum(q);
  const float mb = rbf(mean);
  const float vb = rbf(q * (1.0f / DM));
  const float den = rbf(sqrtf(rbf(vb + 1e-5f)));
#pragma unroll
  for (int i = 0; i < 10; ++i) v[i] = rbf(rbf(v[i] - mb) / den);
}
__device__ __forceinline__ void touch64k(const void* base) {
  const char* p = (const char*)base + threadIdx.x * 64;
  unsigned a, b, c, d;
  asm volatile("global_load_dword %0, %4, off\n\tglobal_load_dword %1, %5, off\n\tglobal_load_dword %2, %6, off\n\t"
               "global_load_dword %3, %7, off\n\ts_waitcnt vmcnt(0)"
               : "=&v"(a), "=&v"(b), "=&v"(c), "=&v"(d)
               : "v"(p), "v"(p + 16384), "v"(p + 32768), "v"(p + 49152) : "memory");
}
#define PF_WO 0
constexpr int PF_BLOCKS = 32 + 80 + 64 + 64 + 108 + (PF_WO ? 80 + 108 : 0);
__global__ __launch_bounds__(256) void ln_kernel(const bf16_t* __restrict__ x, bf16_t* __restrict__ xn,
                                                 bf16_t* __restrict__ h, const bf16_t* __restrict__ ckv,
                                                 const bf16_t* __restrict__ wq, const bf16_t* __restrict__ wuk,
                                                 const bf16_t* __restrict__ wuv, const bf16_t* __restrict__ w1,
                                                 const bf16_t* __restrict__ wo, const bf16_t* __restrict__ w2) {
  if (blockIdx.x >= S_LEN / 4) {
    int b = blockIdx.x - S_LEN / 4;
    const bf16_t* src;
    if (b < 32) src = ckv;
    else if ((b -= 32) < 64) src = wuk;
    else if ((b -= 64) < 64) src = wuv;
    else if ((b -= 64) < 80) src = wq;
    else if ((b -= 80) < 108) src = w1;
    else if ((b -= 108) < 80) src = wo;
    else { b -= 80; src = w2; }
    touch64k((const char*)src + (size_t)b * 65536);
    return;
  }
  const int lane = threadIdx.x & 63, w = threadIdx.x >> 6;
  const int row = blockIdx.x * 4 + w;
  const unsigned* xr = reinterpret_cast<const unsigned*>(x + (size_t)row * DM);
  float v[10];
#pragma unroll
  for (int j = 0; j < 5; ++j) {
    unsigned u = xr[lane + 64 * j];
    v[2 * j] = __uint_as_float(u << 16);
    v[2 * j + 1] = __uint_as_float(u & 0xffff0000u);
  }
  ln_row(v);
  unsigned* xo = reinterpret_cast<unsigned*>(xn + (size_t)row * DM);
#pragma unroll
  for (int j = 0; j < 5; ++j) xo[lane + 64 * j] = pack2(v[2 * j], v[2 * j + 1]);
  ln_row(v);
  unsigned* ho = reinterpret_cast<unsigned*>(h + (size_t)row * DM);
#pragma unroll
  for (int j = 0; j < 5; ++j) ho[lane + 64 * j] = pack2(v[2 * j], v[2 * j + 1]);
}

constexpr int STAGE_BYTES = 49152;
constexpr int RBASE = 32768;

__device__ __forceinline__ int swapmid(int p) { return ((p & 1) << 1) | ((p >> 1) & 1); }
__device__ __forceinline__ int rperm(int r) {
  int g = r >> 2;
  int pg = (g & 4) | swapmid(g & 3);
  return pg * 4 + (r & 3);
}
__device__ __forceinline__ s16x4 tr_read(const char LAS* p) {
  return __builtin_amdgcn_ds_read_tr16_b64_v4i16((LAS s16x4*)p);
}
__device__ __forceinline__ bf16x8 cat8(s16x4 a, s16x4 b) {
  return (bf16x8){a[0], a[1], a[2], a[3], b[0], b[1], b[2], b[3]};
}

__device__ __forceinline__ void glds16(const void* sbase, int voff, unsigned lds_dst) {
  unsigned keep;
  asm volatile("s_mov_b32 %0, m0\n\ts_mov_b32 m0, %3\n\ts_nop 0\n\tglobal_load_lds_dwordx4 %1, %2\n\ts_mov_b32 m0, %0"
               : "=&s"(keep) : "v"(voff), "s"(sbase), "s"(lds_dst) : "memory");
}

__device__ __forceinline__ void glds16x4(const void* sbase, int v0, int v1, int v2, int v3, unsigned lds_dst) {
  unsigned keep;
  asm volatile("s_mov_b32 %0, m0\n\ts_mov_b32 m0, %6\n\ts_nop 0\n\tglobal_load_lds_dwordx4 %1, %5\n\t"
               "global_load_lds_dwordx4 %2, %5 offset:1024\n\tglobal_load_lds_dwordx4 %3, %5 offset:2048\n\t"
               "global_load_lds_dwordx4 %4, %5 offset:3072\n\ts_mov_b32 m0, %0"
               : "=&s"(keep) : "v"(v0), "v"(v1), "v"(v2), "v"(v3), "s"(sbase), "s"(lds_dst) : "memory");
}

#define ST_MODE 0
__device__ __forceinline__ void st16(void* p, u32x4 v) {
  *(u32x4*)p = v;
}
__device__ __forceinline__ void st16f(float* p, float a, float b, float c, float d) {
  st16(p, (u32x4){__float_as_uint(a), __float_as_uint(b), __float_as_uint(c), __float_as_uint(d)});
}

template <bool P_TR, bool R_TR, bool ROWS_P>
struct GemmCore {
  int pOff[4], rOff[2];
  int pAddr[4], rAddr[4];
  int w, l;
  __device__ __forceinline__ void init(int ldp, int ldr, int pcol_limit  ) {
    const int tid = threadIdx.x;
    l = tid & 63;
    w = __builtin_amdgcn_readfirstlane(tid >> 6);
    const int wp = w >> 1, wr = w & 1;
#pragma unroll
    for (int i = 0; i < 4; ++i) {
      const int j = w * 4 + i;
      if (!P_TR) {
        const int row = 8 * j + (l >> 3), slot = l & 7;
        const int chunk = slot ^ ((row >> 1) & 7);
        pOff[i] = row * ldp + chunk * 8;
      } else {
        const int half = j >> 4, jj = j & 15;
        const int krow = 4 * jj + (l >> 4), s16 = l & 15;
        const int c64 = (s16 >> 2) ^ (krow & 3);
        int col = half * 128 + c64 * 32 + (s16 & 3) * 8;
        if (col >= pcol_limit) col = pcol_limit - 8;
        pOff[i] = krow * ldp + col;
      }
    }
#pragma unroll
    for (int i = 0; i < 2; ++i) {
      const int j = w * 2 + i;
      if (!R_TR) {
        const int row = 8 * j + (l >> 3), slot = l & 7;
        const int chunk = slot ^ ((row >> 1) & 7);
        rOff[i] = row * ldr + chunk * 8;
      } else {
        const int krow = 4 * j + (l >> 4), s16 = l & 15;
        const int c64 = (s16 >> 2) ^ (krow & 3);
        const int col = c64 * 32 + (s16 & 3) * 8;
        rOff[i] = krow * ldr + col;
      }
    }
    const int r32 = l & 31, hi = l >> 5, blk = (l >> 4) & 1, q = (l & 15) >> 2, p = l & 3;
    if (!P_TR) {
      const int prow = 64 * wp + (ROWS_P ? rperm(r32) : r32);
      const int s = (prow >> 1) & 7;
#pragma unroll
      for (int kk = 0; kk < 4; ++kk) pAddr[kk] = prow * 128 + (((2 * kk + hi) ^ s) << 4);
    } else {
      const int half = wp >> 1, pp = ROWS_P ? swapmid(p) : p;
#pragma unroll
      for (int i = 0; i < 2; ++i) {
        const int c64 = 2 * (wp & 1) + i;
        pAddr[i] = half * 16384 + (8 * hi + q) * 256 + ((c64 ^ q) * 64) + blk * 32 + pp * 8;
      }
      pAddr[2] = pAddr[3] = 0;
    }
    if (!R_TR) {
      const int rrow = 64 * wr + (!ROWS_P ? rperm(r32) : r32);
      const int s = (rrow >> 1) & 7;
#pragma unroll
      for (int kk = 0; kk < 4; ++kk) rAddr[kk] = RBASE + rrow * 128 + (((2 * kk + hi) ^ s) << 4);
    } else {
      const int pp = !ROWS_P ? swapmid(p) : p;
#pragma unroll
      for (int i = 0; i < 2; ++i) {
        const int c64 = 2 * wr + i;
        rAddr[i] = RBASE + (8 * hi + q) * 256 + ((c64 ^ q) * 64) + blk * 32 + pp * 8;
      }
      rAddr[2] = rAddr[3] = 0;
    }
  }
  __device__ __forceinline__ void stage(char LAS* buf, const bf16_t* P, const bf16_t* R) {
    const unsigned b = (unsigned)(uintptr_t)buf;
#pragma unroll
    for (int i = 0; i < 4; ++i) glds16(P, pOff[i] * 2, b + (w * 4 + i) * 1024);
#pragma unroll
    for (int i = 0; i < 2; ++i) glds16(R, rOff[i] * 2, b + RBASE + (w * 2 + i) * 1024);
  }
  __device__ __forceinline__ void compute(const char LAS* buf, f32x16 (&acc)[2][2]) {
#pragma unroll
    for (int kk = 0; kk < 4; ++kk) {
      bf16x8 pf[2], rf[2];
#pragma unroll
      for (int i = 0; i < 2; ++i) {
        if (!P_TR) pf[i] = *(const LAS bf16x8*)(buf + pAddr[kk] + i * 4096);
        else pf[i] = cat8(tr_read(buf + pAddr[i] + kk * 4096), tr_read(buf + pAddr[i] + kk * 4096 + 1024));
        if (!R_TR) rf[i] = *(const LAS bf16x8*)(buf + rAddr[kk] + i * 4096);
        else rf[i] = cat8(tr_read(buf + rAddr[i] + kk * 4096), tr_read(buf + rAddr[i] + kk * 4096 + 1024));
      }
#pragma unroll
      for (int i = 0; i < 2; ++i)
#pragma unroll
        for (int j = 0; j < 2; ++j) {
          if (ROWS_P) acc[i][j] = __builtin_amdgcn_mfma_f32_32x32x16_bf16(pf[i], rf[j], acc[i][j], 0, 0, 0);
          else acc[i][j] = __builtin_amdgcn_mfma_f32_32x32x16_bf16(rf[j], pf[i], acc[i][j], 0, 0, 0);
        }
    }
  }
};

#define WAIT_VM0() asm volatile("s_waitcnt vmcnt(0)" ::: "memory")
#define WAIT_VM6() asm volatile("s_waitcnt vmcnt(6)" ::: "memory")
#define WAIT_VM16() asm volatile("s_waitcnt vmcnt(16)" ::: "memory")
#define BARRIER() __builtin_amdgcn_s_barrier()
#define TS(k, i)
#define PH(i, k)

__device__ __forceinline__ float gelu_tanh(float x) {
  const float y = 0.7978845608028654f * (x + 0.044715f * x * x * x);
  const float e = __expf(2.0f * y);
  const float t = 1.0f - 2.0f / (e + 1.0f);
  return 0.5f * x * (1.0f + t);
}

constexpr int STAGE256 = 32768;
#define NST256 4
struct Core256 {
  int pOff[2], rOff[2];
  int pAddr[2], rAddr[2];
  int w;
  __device__ __forceinline__ void init(int ldp, int ldr) {
    const int tid = threadIdx.x, l = tid & 63;
    w = __builtin_amdgcn_readfirstlane(tid >> 6);
    const int wp = w >> 2, wr = w & 3;
#pragma unroll
    for (int i = 0; i < 2; ++i) {
      const int j = w * 2 + i;
      const int row = 16 * j + (l >> 2), slot = l & 3;
      pOff[i] = (row * ldp + (slot ^ ((row >> 2) & 3)) * 8) * 2;
      const int half = j >> 3, jj = j & 7;
      const int krow = 4 * jj + (l >> 4), s16 = l & 15;
      const int c64 = (s16 >> 2) ^ (krow & 3);
      rOff[i] = (krow * ldr + half * 128 + c64 * 32 + (s16 & 3) * 8) * 2;
    }
    const int r32 = l & 31, hi = l >> 5, blk = (l >> 4) & 1, q = (l & 15) >> 2, p = l & 3;
    const int prow = 128 * wp + r32, s = (prow >> 2) & 3;
#pragma unroll
    for (int kk = 0; kk < 2; ++kk) pAddr[kk] = prow * 64 + (((2 * kk + hi) ^ s) << 4);
#pragma unroll
    for (int j = 0; j < 2; ++j) {
      const int c64 = 2 * (wr & 1) + j;
      rAddr[j] = 16384 + (wr >> 1) * 8192 + (8 * hi + q) * 256 + ((c64 ^ q) * 64) + blk * 32 + swapmid(p) * 8;
    }
  }
  __device__ __forceinline__ void stage(char LAS* buf, const bf16_t* P, const bf16_t* R) {
    const unsigned b = (unsigned)(uintptr_t)buf;
#pragma unroll
    for (int i = 0; i < 2; ++i) glds16(P, pOff[i], b + (w * 2 + i) * 1024);
#pragma unroll
    for (int i = 0; i < 2; ++i) glds16(R, rOff[i], b + 16384 + (w * 2 + i) * 1024);
  }
  __device__ __forceinline__ void compute(const char LAS* buf, f32x16 (&acc)[4][2]) {
#pragma unroll
    for (int kk = 0; kk < 2; ++kk) {
      bf16x8 pf[4], rf[2];
#pragma unroll
      for (int j = 0; j < 2; ++j) rf[j] = cat8(tr_read(buf + rAddr[j] + kk * 4096), tr_read(buf + rAddr[j] + kk * 4096 + 1024));
#pragma unroll
      for (int i = 0; i < 4; ++i) pf[i] = *(const LAS bf16x8*)(buf + pAddr[kk] + i * 2048);
#pragma unroll
      for (int i = 0; i < 4; ++i)
#pragma unroll
        for (int j = 0; j < 2; ++j) acc[i][j] = __builtin_amdgcn_mfma_f32_32x32x16_bf16(rf[j], pf[i], acc[i][j], 0, 0, 0);
    }
  }
  __device__ __forceinline__ void load_frags(const char LAS* buf, bf16x8 (&pf)[2][4], bf16x8 (&rf)[2][2]) {
#pragma unroll
    for (int kk = 0; kk < 2; ++kk) {
#pragma unroll
      for (int j = 0; j < 2; ++j) rf[kk][j] = cat8(tr_read(buf + rAddr[j] + kk * 4096), tr_read(buf + rAddr[j] + kk * 4096 + 1024));
#pragma unroll
      for (int i = 0; i < 4; ++i) pf[kk][i] = *(const LAS bf16x8*)(buf + pAddr[kk] + i * 2048);
    }
  }
  __device__ __forceinline__ void mma(const bf16x8 (&pf)[2][4], const bf16x8 (&rf)[2][2], f32x16 (&acc)[4][2]) {
#pragma unroll
    for (int kk = 0; kk < 2; ++kk)
#pragma unroll
      for (int i = 0; i < 4; ++i)
#pragma unroll
        for (int j = 0; j < 2; ++j) acc[i][j] = __builtin_amdgcn_mfma_f32_32x32x16_bf16(rf[kk][j], pf[kk][i], acc[i][j], 0, 0, 0);
  }
};
#define WAIT_LGKM0() asm volatile("s_waitcnt lgkmcnt(0)" ::: "memory")
#define SCHED0() __builtin_amdgcn_sched_barrier(0)
template <int N> __device__ __forceinline__ void wait_vm() { asm volatile("s_waitcnt vmcnt(%0)" ::"n"(N) : "memory"); }

__global__ __launch_bounds__(512, 2) void gemm1_kernel(const bf16_t* __restrict__ xn, const bf16_t* __restrict__ hbuf,
                                                       const bf16_t* __restrict__ ckv, const bf16_t* __restrict__ wq,
                                                       const bf16_t* __restrict__ wuk, const bf16_t* __restrict__ wuv,
                                                       const bf16_t* __restrict__ w1, const bf16_t* __restrict__ b1,
                                                       bf16_t* __restrict__ Qo, bf16_t* __restrict__ Ko,
                                                       bf16_t* __restrict__ Vo, bf16_t* __restrict__ Uo) {
  __shared__ __attribute__((aligned(16))) char smem[NST256 * STAGE256 + 32768];
  char LAS* lds = (char LAS*)smem;
  const int xcd = blockIdx.x & 7, c = blockIdx.x >> 3;
  const int l = threadIdx.x & 63, w = __builtin_amdgcn_readfirstlane(threadIdx.x >> 6);
  const int r32 = l & 31, hi = l >> 5;
  TS(0, 0);
  const bool isQ = c >= 16;
  {
    int ntile, tb0, hp;
    const bf16_t* A; const bf16_t* W; bf16_t* O; int K;
    if (!isQ) {
      ntile = 2; tb0 = c >> 2; hp = xcd * 2 + (c & 1);
      A = ckv; K = DC;
      W = (c & 2) ? wuv : wuk; O = (c & 2) ? Vo : Ko;
    } else {
      ntile = 1; tb0 = (c - 16) >> 1; hp = xcd * 2 + (c & 1);
      A = xn; K = DM; W = wq; O = Qo;
    }
    const int nk = K / 32;
    Core256 g;
    g.init(K, HDN);
    const bf16_t* Wp = W + hp * 256;
    const int wp = w >> 2, wr = w & 3;
    const int total = ntile * nk;
    int pt = 0, pk = 0, pslot = 0, issued = 0;
    auto issue = [&]() {
      g.stage(lds + pslot * STAGE256, A + (size_t)(tb0 + 4 * pt) * 256 * K + pk * 32, Wp + (size_t)pk * 32 * HDN);
      if (++pk == nk) { pk = 0; ++pt; }
      if (++pslot == NST256) pslot = 0;
      ++issued;
    };
#pragma unroll
    for (int i = 0; i < NST256 - 1; ++i) issue();
    int buf = 0, step = 0;
    wait_vm<4 * (NST256 - 2)>();
    BARRIER();
    if (wp == 1) BARRIER();
    for (int t = 0; t < ntile; ++t) {
      const int tb = tb0 + 4 * t;
      f32x16 acc[4][2] = {};
      for (int kt = 0; kt < nk; ++kt) {
        bf16x8 pf[2][4], rf[2][2];
        if (issued < total) issue();
        g.load_frags(lds + buf * STAGE256, pf, rf);
        if (step + NST256 > total) WAIT_VM0();
        else if (t > 0 && kt < NST256 - 2) wait_vm<4 * (NST256 - 2) + 16>();
        else wait_vm<4 * (NST256 - 2)>();
        WAIT_LGKM0();
        SCHED0();
        BARRIER();
        SCHED0();
        __builtin_amdgcn_s_setprio(1);
        g.mma(pf, rf, acc);
        __builtin_amdgcn_s_setprio(0);
        SCHED0();
        BARRIER();
        SCHED0();
        if (++buf == NST256) buf = 0;
        ++step;
      }
      const int head = hp * 2 + (wr >> 1);
      char LAS* slab = lds + NST256 * STAGE256 + w * 4096;
      const int wa = r32 * 128, wsw = r32 & 7;
      const int rrow = l >> 3, ra = rrow * 128 + (((l & 7) ^ rrow) << 4);
      bf16_t* obase = O + ((size_t)head * S_LEN + tb * 256 + 128 * wp + rrow) * HD + 64 * (wr & 1) + (l & 7) * 8;
#pragma unroll
      for (int i = 0; i < 4; ++i) {
#pragma unroll
        for (int j = 0; j < 2; ++j) {
          const f32x16 a = acc[i][j];
          u32x4 v0 = {pack2(a[0], a[1]), pack2(a[2], a[3]), pack2(a[4], a[5]), pack2(a[6], a[7])};
          u32x4 v1 = {pack2(a[8], a[9]), pack2(a[10], a[11]), pack2(a[12], a[13]), pack2(a[14], a[15])};
          *(u32x4 LAS*)(slab + wa + (((4 * j + hi) ^ wsw) << 4)) = v0;
          *(u32x4 LAS*)(slab + wa + (((4 * j + 2 + hi) ^ wsw) << 4)) = v1;
        }
#pragma unroll
        for (int it = 0; it < 4; ++it) {
          const u32x4 v = *(const u32x4 LAS*)(slab + ra + it * 1024);
          st16(obase + (size_t)(32 * i + 8 * it) * HD, v);
        }
      }
    }
    if (wp == 0) BARRIER();
  }
  TS(0, 1);
  const int mbase = (xcd * 90) >> 3, mcnt = (((xcd + 1) * 90) >> 3) - mbase;
  if (isQ && c - 16 < mcnt) {
    constexpr int MST = 28672;
    const int tile = mbase + (c - 16);
    const int d0 = (tile % 5) * 128, f0 = (tile / 5) * 96;
    const int ws = w & 3, kh = w >> 2;
    const int blk = (l >> 4) & 1, q = (l & 15) >> 2, p = l & 3;
    int pOff[2], rOff[2];
#pragma unroll
    for (int i = 0; i < 2; ++i) {
      const int j = w * 2 + i;
      const int krow = 4 * j + (l >> 4), s16 = l & 15;
      pOff[i] = (krow * DM + ((s16 >> 2) ^ (krow & 3)) * 32 + (s16 & 3) * 8) * 2;
      const int jr = w + 8 * i;
      const int cb = jr >> 2, kr = (jr & 3) * 16 + (l >> 2);
      rOff[i] = (kr * FF + cb * 32 + (l & 3) * 8) * 2;
    }
    const bf16_t* Pp = hbuf + d0;
    const bf16_t* Rp = w1 + f0;
    int pslot = 0, issued = 0;
    auto issue = [&]() {
      const unsigned bb = (unsigned)(uintptr_t)(lds + pslot * MST);
      const bf16_t* P = Pp + (size_t)issued * 64 * DM;
      const bf16_t* R = Rp + (size_t)issued * 64 * FF;
      glds16(P, pOff[0], bb + (w * 2) * 1024);
      glds16(P, pOff[1], bb + (w * 2 + 1) * 1024);
      glds16(R, rOff[0], bb + 16384 + w * 1024);
      if (w < 4) glds16(R, rOff[1], bb + 16384 + (w + 8) * 1024);
      if (++pslot == 4) pslot = 0;
      ++issued;
    };
    const int pB = (32 * kh + 8 * hi + q) * 256 + ((ws ^ q) * 64) + blk * 32 + p * 8;
    const int rA = 16384 + (32 * kh + 8 * hi + q) * 64 + blk * 32 + swapmid(p) * 8;
    BARRIER();
    issue(); issue(); issue();
    f32x16 acc[3] = {};
    int buf = 0;
    for (int step = 0; step < 32; ++step) {
      if (step + 3 > 32) WAIT_VM0();
      else if (w < 4) wait_vm<8>();
      else wait_vm<6>();
      BARRIER();
      if (issued < 32) issue();
      const char LAS* bp = lds + buf * MST;
      bf16x8 pf[2], rf[2][3];
      pf[0] = cat8(tr_read(bp + pB), tr_read(bp + pB + 1024));
#pragma unroll
      for (int j = 0; j < 3; ++j) rf[0][j] = cat8(tr_read(bp + rA + j * 4096), tr_read(bp + rA + j * 4096 + 256));
      pf[1] = cat8(tr_read(bp + pB + 4096), tr_read(bp + pB + 4096 + 1024));
#pragma unroll
      for (int j = 0; j < 3; ++j) rf[1][j] = cat8(tr_read(bp + rA + j * 4096 + 1024), tr_read(bp + rA + j * 4096 + 1024 + 256));
      SCHED0();
#pragma unroll
      for (int k2 = 0; k2 < 2; ++k2)
#pragma unroll
        for (int j = 0; j < 3; ++j) acc[j] = __builtin_amdgcn_mfma_f32_32x32x16_bf16(rf[k2][j], pf[k2], acc[j], 0, 0, 0);
      if (++buf == 4) buf = 0;
    }
    BARRIER();
    float LAS* dst = (float LAS*)(lds + w * 8192);
    const f32x16 sa = kh == 0 ? acc[2] : acc[0], ka = kh == 0 ? acc[0] : acc[2];
    const int so = kh == 0 ? 8 : 0;
#pragma unroll
    for (int g = 0; g < 4; ++g) *(f32x4 LAS*)(dst + (g * 64 + l) * 4) = (f32x4){sa[4 * g], sa[4 * g + 1], sa[4 * g + 2], sa[4 * g + 3]};
    float s1[8], k1[8];
#pragma unroll
    for (int e = 0; e < 8; ++e) { s1[e] = kh == 0 ? acc[1][8 + e] : acc[1][e]; k1[e] = kh == 0 ? acc[1][e] : acc[1][8 + e]; }
#pragma unroll
    for (int g = 0; g < 2; ++g) *(f32x4 LAS*)(dst + ((4 + g) * 64 + l) * 4) = (f32x4){s1[4 * g], s1[4 * g + 1], s1[4 * g + 2], s1[4 * g + 3]};
    (void)so;
    WAIT_LGKM0();
    BARRIER();
    const float LAS* src = (const float LAS*)(lds + (w ^ 4) * 8192);
    bf16_t* urow = Uo + (size_t)(d0 + 32 * ws + r32) * FF + f0 + 8 * hi;
    auto fin8 = [&](const float (&v)[8], int fo) {
      const u32x4 bv = *(const u32x4*)(b1 + f0 + 8 * hi + fo);
      float r[8];
#pragma unroll
      for (int e = 0; e < 4; ++e) {
        r[2 * e] = gelu_tanh(rbf(rbf(v[2 * e]) + __uint_as_float(bv[e] << 16)));
        r[2 * e + 1] = gelu_tanh(rbf(rbf(v[2 * e + 1]) + __uint_as_float(bv[e] & 0xffff0000u)));
      }
      st16(urow + fo, (u32x4){pack2(r[0], r[1]), pack2(r[2], r[3]), pack2(r[4], r[5]), pack2(r[6], r[7])});
    };
    const int jk = kh == 0 ? 0 : 2;
#pragma unroll
    for (int hh = 0; hh < 2; ++hh) {
      float v[8];
#pragma unroll
      for (int g = 0; g < 2; ++g) {
        const f32x4 o = *(const f32x4 LAS*)(src + ((2 * hh + g) * 64 + l) * 4);
#pragma unroll
        for (int e = 0; e < 4; ++e) v[4 * g + e] = ka[8 * hh + 4 * g + e] + o[e];
      }
      fin8(v, 32 * jk + 16 * hh);
    }
    {
      float v[8];
#pragma unroll
      for (int g = 0; g < 2; ++g) {
        const f32x4 o = *(const f32x4 LAS*)(src + ((4 + g) * 64 + l) * 4);
#pragma unroll
        for (int e = 0; e < 4; ++e) v[4 * g + e] = k1[4 * g + e] + o[e];
      }
      fin8(v, 32 + 16 * kh);
    }
  }
  TS(0, 2);
}

constexpr int AT_HALF = 16384;
__global__ __launch_bounds__(512, 2) void attn_kernel(const bf16_t* __restrict__ Q, const bf16_t* __restrict__ K,
                                                      const bf16_t* __restrict__ V, bf16_t* __restrict__ ctx,
                                                      const bf16_t* __restrict__ wo, const bf16_t* __restrict__ w2) {
  __shared__ __attribute__((aligned(16))) char smem[8 * AT_HALF + 32768];
  char LAS* lds = (char LAS*)smem;
  const int tid = threadIdx.x, l = tid & 63, w = __builtin_amdgcn_readfirstlane(tid >> 6);
  const int rg = w & 3, g = w >> 2, r32 = l & 31, hi = l >> 5;
  const int xcd = blockIdx.x & 7, jj = blockIdx.x >> 3;
  const int head = xcd * 4 + (jj >> 3), pair = jj & 7;
  const int LB = 15 - pair, nshort = 2 * pair + 2;
  const bf16_t* Kh = K + (size_t)head * S_LEN * HD;
  const bf16_t* Vh = V + (size_t)head * S_LEN * HD;
  const bf16_t* Qh = Q + (size_t)head * S_LEN * HD;
  char LAS* gl = lds + g * 4 * AT_HALF;
  int kOff[4], vOff[4];
#pragma unroll
  for (int i = 0; i < 4; ++i) {
    const int j = rg * 4 + i, row = 4 * j + (l >> 4), s16 = l & 15;
    kOff[i] = (row * HD + ((s16 ^ (row & 15)) * 8)) * 2 - 1024 * i;
    vOff[i] = (row * HD + ((((s16 >> 2) ^ (row & 3)) * 4 + (s16 & 3)) * 8)) * 2 - 1024 * i;
  }
  const unsigned glb = (unsigned)(uintptr_t)gl;
  auto htile = [&](int i) { return (g == 0 || i < nshort) ? i : i + 15 - 2 * pair; };
  auto stageK = [&](int i) {
    const bf16_t* kp = Kh + (size_t)htile(i > 16 ? 16 : i) * 64 * HD;
    const unsigned bb = glb + (i & 1) * AT_HALF + rg * 4096;
    glds16x4(kp, kOff[0], kOff[1], kOff[2], kOff[3], bb);
  };
  auto stageV = [&](int i) {
    const bf16_t* vp = Vh + (size_t)htile(i) * 64 * HD;
    const unsigned bb = glb + (2 + (i & 1)) * AT_HALF + rg * 4096;
    glds16x4(vp, vOff[0], vOff[1], vOff[2], vOff[3], bb);
  };
  const int ksw = r32 & 15;
  const int blk = (l >> 4) & 1, q4 = (l & 15) >> 2, p4 = l & 3;
  int vAddr[4];
#pragma unroll
  for (int db = 0; db < 4; ++db)
    vAddr[db] = 2 * AT_HALF + (4 * hi + q4) * 256 + ((db ^ q4) * 64) + blk * 32 + swapmid(p4) * 8;
  constexpr float CS = 0.08838834764831845f * 1.4426950408889634f;

  TS(1, 0);
  stageK(0);
  stageV(0);
  stageK(1);
  int qb = g == 0 ? LB : pair;
  bf16x8 qf[8];
#pragma unroll
  for (int d0 = 0; d0 < 8; ++d0) qf[d0] = *(const bf16x8*)(Qh + (size_t)(qb * 128 + 32 * rg + r32) * HD + d0 * 16 + hi * 8);
  char LAS* qsave = lds + 8 * AT_HALF + rg * 8192 + l * 16;
  if (g == 0) {
#pragma unroll
    for (int d0 = 0; d0 < 8; ++d0) *(bf16x8 LAS*)(qsave + d0 * 1024) = qf[d0];
  }
  float m_run = -1e30f, l_run = 0.f, alpha = 1.f;
  f32x16 o[4] = {};
  f32x16 s0, s1;
  bf16x8 pf[4];
  u32x2 pfd = {0u, 0u};
  constexpr int AT_VM = 5;
#define AT_W 6
  bf16x8 fr[32];
  const int kAddr0 = r32 * 256 + ((hi ^ ksw) << 4);
  auto rdK = [&](int k, int slot) {
    fr[k] = *(const LAS bf16x8*)(gl + slot * AT_HALF + (kAddr0 ^ ((k >> 1) << 5)) + (k & 1) * 8192);
  };
  auto rdV = [&](int k, int slot) {
    const char LAS* a = gl + slot * AT_HALF + vAddr[(k - 16) >> 2] + (k & 3) * 4096;
    fr[k] = cat8(tr_read(a), tr_read(a + 2048));
  };
  auto mm = [&](int k) {
    if (k < 16) {
      if (k & 1) s1 = __builtin_amdgcn_mfma_f32_32x32x16_bf16(fr[k], qf[k >> 1], k < 2 ? (f32x16){} : s1, 0, 0, 0);
      else s0 = __builtin_amdgcn_mfma_f32_32x32x16_bf16(fr[k], qf[k >> 1], k < 2 ? (f32x16){} : s0, 0, 0, 0);
    } else {
      o[(k - 16) >> 2] = __builtin_amdgcn_mfma_f32_32x32x16_bf16(fr[k], pf[k & 3], o[(k - 16) >> 2], 0, 0, 0);
    }
  };
  auto preK = [&](int slot) {
#pragma unroll
    for (int k = 0; k < AT_W; ++k) rdK(k, slot);
  };
  auto preV = [&](int slot) {
#pragma unroll
    for (int k = 16; k < 16 + AT_W; ++k) rdV(k, slot);
  };
  auto xhalf_max = [&](float v) {
    auto rr = __builtin_amdgcn_permlane32_swap(__float_as_uint(v), __float_as_uint(v), false, false);
    return fmaxf(__uint_as_float(rr[0]), __uint_as_float(rr[1]));
  };
  auto xhalf_sum = [&](float v) {
    auto rr = __builtin_amdgcn_permlane32_swap(__float_as_uint(v), __float_as_uint(v), false, false);
    return __uint_as_float(rr[0]) + __uint_as_float(rr[1]);
  };
  auto softmax = [&](int i) {
    if (g == 1 && i == nshort) {
      const float inv = 1.0f / xhalf_sum(l_run);
      bf16_t* orow = ctx + (size_t)(pair * 128 + 32 * rg + r32) * CTX_LD + head * HD + 8 * hi;
#pragma unroll
      for (int db = 0; db < 4; ++db) {
        const f32x16 a = o[db];
        u32x4 v0 = {pack2(a[0] * inv, a[1] * inv), pack2(a[2] * inv, a[3] * inv), pack2(a[4] * inv, a[5] * inv), pack2(a[6] * inv, a[7] * inv)};
        u32x4 v1 = {pack2(a[8] * inv, a[9] * inv), pack2(a[10] * inv, a[11] * inv), pack2(a[12] * inv, a[13] * inv), pack2(a[14] * inv, a[15] * inv)};
        st16(orow + 32 * db, v0);
        st16(orow + 32 * db + 16, v1);
        o[db] = (f32x16){};
      }
      m_run = -1e30f;
      l_run = 0.f;
      qb = LB;
    }
    if (g == 1 && i == nshort - 1) {
#pragma unroll
      for (int d0 = 0; d0 < 8; ++d0) qf[d0] = *(const bf16x8 LAS*)(qsave + d0 * 1024);
    }
    SCHED0();
    if (i < 16) stageK(i + 2);
    if (i < 16) {
      int idx = (i * 256 + (int)blockIdx.x) * 8 + w;
      if (idx >= 24064) idx -= 24064;
      const char* pb = idx < 10240 ? (const char*)wo + (size_t)idx * 512 : (const char*)w2 + (size_t)(idx - 10240) * 512;
      asm volatile("global_load_dwordx2 %0, %1, %2" : "+v"(pfd) : "v"(l * 8), "s"(pb) : "memory");
    }
    SCHED0(); PH(i, 5); SCHED0();
    const int dh = htile(i) - 2 * qb;
    if (dh >= 0) {
      const int qi = 32 * rg + r32 - 64 * dh;
#pragma unroll
      for (int r = 0; r < 16; ++r) {
        const int key = (r & 3) + 8 * (r >> 2) + 4 * hi;
        if (key > qi) s0[r] = -__builtin_inff();
        if (key + 32 > qi) s1[r] = -__builtin_inff();
      }
    }
    float pm = s0[0];
#pragma unroll
    for (int r = 1; r < 16; ++r) pm = fmaxf(pm, s0[r]);
#pragma unroll
    for (int r = 0; r < 16; ++r) pm = fmaxf(pm, s1[r]);
    pm = xhalf_max(pm);
    const float mn = fmaxf(m_run, pm);
    alpha = __builtin_amdgcn_exp2f((m_run - mn) * CS);
    m_run = mn;
    const float mc = -mn * CS;
    SCHED0(); PH(i, 6); SCHED0();
    float ps = 0.f;
#pragma unroll
    for (int r = 0; r < 16; ++r) { s0[r] = __builtin_amdgcn_exp2f(fmaf(s0[r], CS, mc)); ps += s0[r]; }
#pragma unroll
    for (int r = 0; r < 16; ++r) { s1[r] = __builtin_amdgcn_exp2f(fmaf(s1[r], CS, mc)); ps += s1[r]; }
    l_run = l_run * alpha + ps;
    SCHED0(); PH(i, 7); SCHED0();
    u32x4 a0 = {pack2(s0[0], s0[1]), pack2(s0[2], s0[3]), pack2(s0[4], s0[5]), pack2(s0[6], s0[7])};
    u32x4 a1 = {pack2(s0[8], s0[9]), pack2(s0[10], s0[11]), pack2(s0[12], s0[13]), pack2(s0[14], s0[15])};
    u32x4 a2 = {pack2(s1[0], s1[1]), pack2(s1[2], s1[3]), pack2(s1[4], s1[5]), pack2(s1[6], s1[7])};
    u32x4 a3 = {pack2(s1[8], s1[9]), pack2(s1[10], s1[11]), pack2(s1[12], s1[13]), pack2(s1[14], s1[15])};
    pf[0] = *(bf16x8*)&a0; pf[1] = *(bf16x8*)&a1; pf[2] = *(bf16x8*)&a2; pf[3] = *(bf16x8*)&a3;
    asm volatile("" : "+v"(pf[0]), "+v"(pf[1]), "+v"(pf[2]), "+v"(pf[3]), "+v"(l_run), "+v"(alpha));
  };
#define AT_DK0 16
  WAIT_VM0();
  WAIT_LGKM0();
  BARRIER();
  TS(1, 3);
  if (g == 1) BARRIER();
  SCHED0();
  preK(0);
#pragma unroll
  for (int k = 0; k < 16; ++k) {
    if (k + AT_W < 16) rdK(k + AT_W, 0);
    mm(k);
    SCHED0();
  }
  BARRIER();
  for (int i = 0; i < 16; ++i) {
    SCHED0();
    PH(i, 0);
    softmax(i);
    SCHED0();
    const int ks = (i + 1) & 1, vs = i & 1;
    preK(ks);
    SCHED0();
    wait_vm<AT_VM>();
    PH(i, 1);
    BARRIER();
    SCHED0();
    PH(i, 2);
    __builtin_amdgcn_s_setprio(1);
#pragma unroll
    for (int k = 0; k < 32; ++k) {
      if (k + AT_W < 16) rdK(k + AT_W, ks);
      else if (k + AT_W < 32) rdV(k + AT_W, vs);
      mm(k);
      if (k < 16) {
#pragma unroll
        for (int e = 0; e < 4; ++e) {
          float t = o[k >> 2][4 * (k & 3) + e];
          asm volatile("v_mul_f32 %0, %1, %0" : "+v"(t) : "v"(alpha));
          o[k >> 2][4 * (k & 3) + e] = t;
        }
      }
      if (k == AT_DK0) stageV(i + 1);
      SCHED0();
    }
    __builtin_amdgcn_s_setprio(0);
    PH(i, 3);
    wait_vm<AT_VM>();
    PH(i, 4);
    BARRIER();
  }
  SCHED0();
  softmax(16);
#pragma unroll
  for (int db = 0; db < 4; ++db)
#pragma unroll
    for (int r = 0; r < 16; ++r) o[db][r] *= alpha;
  SCHED0();
  WAIT_VM0();
  asm volatile("" ::"v"(pfd));
  BARRIER();
  SCHED0();
  preV(0);
#pragma unroll
  for (int k = 16; k < 32; ++k) {
    if (k + AT_W < 32) rdV(k + AT_W, 0);
    mm(k);
    SCHED0();
  }
  BARRIER();
  if (g == 0) BARRIER();
  l_run = xhalf_sum(l_run);
  TS(1, 1);
  {
    const int kh = g;
    float LAS* ml = (float LAS*)(lds + 4 * AT_HALF);
    float LAS* dst = (float LAS*)(lds + w * 8192);
#pragma unroll
    for (int dd = 0; dd < 2; ++dd) {
      f32x16 a;
#pragma unroll
      for (int r = 0; r < 16; ++r) a[r] = kh == 0 ? o[2 + dd][r] : o[dd][r];
#pragma unroll
      for (int gg = 0; gg < 4; ++gg)
        *(f32x4 LAS*)(dst + ((dd * 4 + gg) * 64 + l) * 4) = (f32x4){a[4 * gg], a[4 * gg + 1], a[4 * gg + 2], a[4 * gg + 3]};
    }
    ml[w * 128 + l] = m_run;
    ml[w * 128 + 64 + l] = l_run;
    asm volatile("s_waitcnt lgkmcnt(0)" ::: "memory");
    BARRIER();
    const int wo = w ^ 4;
    const float m2 = ml[wo * 128 + l], l2 = ml[wo * 128 + 64 + l];
    const float mt = fmaxf(m_run, m2);
    const float a1 = __builtin_amdgcn_exp2f((m_run - mt) * CS), a2 = __builtin_amdgcn_exp2f((m2 - mt) * CS);
    const float inv = 1.0f / (l_run * a1 + l2 * a2);
    const float f1 = a1 * inv, f2 = a2 * inv;
    const float LAS* src = (const float LAS*)(lds + wo * 8192);
    bf16_t* orow = ctx + (size_t)(LB * 128 + 32 * rg + r32) * CTX_LD + head * HD + 8 * hi;
#pragma unroll
    for (int dd = 0; dd < 2; ++dd) {
      f32x16 a;
#pragma unroll
      for (int r = 0; r < 16; ++r) a[r] = kh == 0 ? o[dd][r] : o[2 + dd][r];
      float r[16];
#pragma unroll
      for (int gg = 0; gg < 4; ++gg) {
        const f32x4 b = *(const f32x4 LAS*)(src + ((dd * 4 + gg) * 64 + l) * 4);
#pragma unroll
        for (int e = 0; e < 4; ++e) r[4 * gg + e] = a[4 * gg + e] * f1 + b[e] * f2;
      }
      u32x4 v0 = {pack2(r[0], r[1]), pack2(r[2], r[3]), pack2(r[4], r[5]), pack2(r[6], r[7])};
      u32x4 v1 = {pack2(r[8], r[9]), pack2(r[10], r[11]), pack2(r[12], r[13]), pack2(r[14], r[15])};
      const int dcol = 32 * (2 * kh + dd);
      st16(orow + dcol, v0);
      st16(orow + dcol + 16, v1);
    }
  }
  TS(1, 2);
}

constexpr int G2_STAGE = 36864;
constexpr int G2_NST = 4;
__global__ __launch_bounds__(512, 2) void gemm2_kernel(const bf16_t* __restrict__ ctx, const bf16_t* __restrict__ wo,
                                                       const bf16_t* __restrict__ U, const bf16_t* __restrict__ w2,
                                                       float* __restrict__ part) {
  __shared__ __attribute__((aligned(16))) char smem[G2_NST * G2_STAGE];
  char LAS* lds = (char LAS*)smem;
  const int b = blockIdx.x, xcd = b & 7, c = b >> 3;
  const int ks = xcd >> 1;
  const int s0 = (xcd & 1) * 1024 + (c >> 2) * 128, m0 = (c & 3) * 160;
  const int l = threadIdx.x & 63, w = __builtin_amdgcn_readfirstlane(threadIdx.x >> 6);
  const int ws = w & 3, kh = w >> 2, r32 = l & 31, hi = l >> 5;
  const int blk = (l >> 4) & 1, q = (l & 15) >> 2, p = l & 3;
  TS(2, 0);
  int pOffA[2], pOffB[2], rOffA[3], rOffB[3];
#pragma unroll
  for (int i = 0; i < 2; ++i) {
    const int j = w * 2 + i;
    const int row = 8 * j + (l >> 3), slot = l & 7;
    pOffA[i] = (row * CTX_LD + (slot ^ ((row >> 1) & 7)) * 8) * 2;
    const int krow = 4 * j + (l >> 4), s16 = l & 15;
    pOffB[i] = (krow * S_LEN + ((s16 >> 2) ^ (krow & 3)) * 32 + (s16 & 3) * 8) * 2;
  }
#pragma unroll
  for (int i = 0; i < 3; ++i) {
    const int j = w + 8 * i;
    const int cb = j >> 2, kr = (j & 3) * 16 + (l >> 2);
    rOffA[i] = (kr * DM + cb * 32 + (l & 3) * 8) * 2;
    const int row = 8 * j + (l >> 3), slot = l & 7;
    rOffB[i] = (row * FF + (slot ^ ((row >> 1) & 7)) * 8) * 2;
  }
  const int f0 = ks * 7, nB = ks == 3 ? 6 : 7, total = 16 + nB;
  const bf16_t* PA = ctx + (size_t)s0 * CTX_LD + ks * 1024;
  const bf16_t* RA = wo + (size_t)ks * 1024 * DM + m0;
  const bf16_t* PB = w2 + (size_t)f0 * 64 * S_LEN + s0;
  const bf16_t* RB = U + (size_t)m0 * FF + f0 * 64;
  int pslot = 0, issued = 0;
  auto issue = [&]() {
    const unsigned bb = (unsigned)(uintptr_t)(lds + pslot * G2_STAGE);
    if (issued < 16) {
      const bf16_t* P = PA + issued * 64;
      const bf16_t* R = RA + (size_t)issued * 64 * DM;
      glds16(P, pOffA[0], bb + (w * 2) * 1024);
      glds16(P, pOffA[1], bb + (w * 2 + 1) * 1024);
      glds16(R, rOffA[0], bb + 16384 + w * 1024);
      glds16(R, rOffA[1], bb + 16384 + (w + 8) * 1024);
      if (w < 4) glds16(R, rOffA[2], bb + 16384 + (w + 16) * 1024);
    } else {
      const bf16_t* P = PB + (size_t)(issued - 16) * 64 * S_LEN;
      const bf16_t* R = RB + (issued - 16) * 64;
      glds16(P, pOffB[0], bb + (w * 2) * 1024);
      glds16(P, pOffB[1], bb + (w * 2 + 1) * 1024);
      glds16(R, rOffB[0], bb + 16384 + w * 1024);
      glds16(R, rOffB[1], bb + 16384 + (w + 8) * 1024);
      if (w < 4) glds16(R, rOffB[2], bb + 16384 + (w + 16) * 1024);
    }
    if (++pslot == G2_NST) pslot = 0;
    ++issued;
  };
  const int prow = 32 * ws + r32, psw = (prow >> 1) & 7;
  const int pA0 = prow * 128 + (((4 * kh + hi) ^ psw) << 4), pA1 = prow * 128 + (((4 * kh + 2 + hi) ^ psw) << 4);
  const int rA = 16384 + (32 * kh + 8 * hi + q) * 64 + blk * 32 + swapmid(p) * 8;
  const int pB = (32 * kh + 8 * hi + q) * 256 + ((ws ^ q) * 64) + blk * 32 + p * 8;
  const int rrow = rperm(r32), rsw = (rrow >> 1) & 7;
  const int rB0 = 16384 + rrow * 128 + (((4 * kh + hi) ^ rsw) << 4), rB1 = 16384 + rrow * 128 + (((4 * kh + 2 + hi) ^ rsw) << 4);
#pragma unroll
  for (int i = 0; i < G2_NST - 1; ++i) issue();
  f32x16 acc[5] = {};
  int buf = 0;
  for (int step = 0; step < 16; ++step) {
    if (w < 4) wait_vm<5 * (G2_NST - 2)>();
    else wait_vm<4 * (G2_NST - 2)>();
    BARRIER();
    issue();
    const char LAS* bp = lds + buf * G2_STAGE;
    bf16x8 pf[2], rf[2][5];
    pf[0] = *(const LAS bf16x8*)(bp + pA0);
#pragma unroll
    for (int j = 0; j < 5; ++j) rf[0][j] = cat8(tr_read(bp + rA + j * 4096), tr_read(bp + rA + j * 4096 + 256));
    pf[1] = *(const LAS bf16x8*)(bp + pA1);
#pragma unroll
    for (int j = 0; j < 5; ++j) rf[1][j] = cat8(tr_read(bp + rA + j * 4096 + 1024), tr_read(bp + rA + j * 4096 + 1024 + 256));
    __builtin_amdgcn_sched_barrier(0);
#pragma unroll
    for (int k2 = 0; k2 < 2; ++k2)
#pragma unroll
      for (int j = 0; j < 5; ++j) acc[j] = __builtin_amdgcn_mfma_f32_32x32x16_bf16(rf[k2][j], pf[k2], acc[j], 0, 0, 0);
    if (++buf == G2_NST) buf = 0;
  }
  for (int step = 16; step < total; ++step) {
    if (step + G2_NST - 1 > total) WAIT_VM0();
    else if (w < 4) wait_vm<5 * (G2_NST - 2)>();
    else wait_vm<4 * (G2_NST - 2)>();
    BARRIER();
    if (issued < total) issue();
    const char LAS* bp = lds + buf * G2_STAGE;
    bf16x8 pf[2], rf[2][5];
    pf[0] = cat8(tr_read(bp + pB), tr_read(bp + pB + 1024));
#pragma unroll
    for (int j = 0; j < 5; ++j) rf[0][j] = *(const LAS bf16x8*)(bp + rB0 + j * 4096);
    pf[1] = cat8(tr_read(bp + pB + 4096), tr_read(bp + pB + 4096 + 1024));
#pragma unroll
    for (int j = 0; j < 5; ++j) rf[1][j] = *(const LAS bf16x8*)(bp + rB1 + j * 4096);
    __builtin_amdgcn_sched_barrier(0);
#pragma unroll
    for (int k2 = 0; k2 < 2; ++k2)
#pragma unroll
      for (int j = 0; j < 5; ++j) acc[j] = __builtin_amdgcn_mfma_f32_32x32x16_bf16(rf[k2][j], pf[k2], acc[j], 0, 0, 0);
    if (++buf == G2_NST) buf = 0;
  }
  TS(2, 1);
  BARRIER();
  {
    float LAS* dst = (float LAS*)(lds + w * 12288);
    if (kh == 0) {
#pragma unroll
      for (int j = 3; j < 5; ++j)
#pragma unroll
        for (int g = 0; g < 4; ++g)
          *(f32x4 LAS*)(dst + (((j - 3) * 4 + g) * 64 + l) * 4) = (f32x4){acc[j][4 * g], acc[j][4 * g + 1], acc[j][4 * g + 2], acc[j][4 * g + 3]};
    } else {
#pragma unroll
      for (int j = 0; j < 3; ++j)
#pragma unroll
        for (int g = 0; g < 4; ++g)
          *(f32x4 LAS*)(dst + ((j * 4 + g) * 64 + l) * 4) = (f32x4){acc[j][4 * g], acc[j][4 * g + 1], acc[j][4 * g + 2], acc[j][4 * g + 3]};
    }
    asm volatile("s_waitcnt lgkmcnt(0)" ::: "memory");
    BARRIER();
    const float LAS* src = (const float LAS*)(lds + (w ^ 4) * 12288);
    float* orow = part + ((size_t)ks * S_LEN + s0 + 32 * ws + r32) * DM + m0 + 8 * hi;
    if (kh == 0) {
#pragma unroll
      for (int j = 0; j < 3; ++j)
#pragma unroll
        for (int g = 0; g < 4; ++g) {
          const f32x4 o = *(const f32x4 LAS*)(src + ((j * 4 + g) * 64 + l) * 4);
          st16f(orow + 32 * j + (g >> 1) * 16 + (g & 1) * 4, acc[j][4 * g] + o[0], acc[j][4 * g + 1] + o[1], acc[j][4 * g + 2] + o[2], acc[j][4 * g + 3] + o[3]);
        }
    } else {
#pragma unroll
      for (int j = 3; j < 5; ++j)
#pragma unroll
        for (int g = 0; g < 4; ++g) {
          const f32x4 o = *(const f32x4 LAS*)(src + (((j - 3) * 4 + g) * 64 + l) * 4);
          st16f(orow + 32 * j + (g >> 1) * 16 + (g & 1) * 4, acc[j][4 * g] + o[0], acc[j][4 * g + 1] + o[1], acc[j][4 * g + 2] + o[2], acc[j][4 * g + 3] + o[3]);
        }
    }
  }
  TS(2, 2);
}

__global__ __launch_bounds__(256) void reduce_kernel(const float* __restrict__ part, const bf16_t* __restrict__ x,
                                                     const bf16_t* __restrict__ b2, bf16_t* __restrict__ out) {
  const int i = blockIdx.x * 256 + threadIdx.x;
  const int e = i * 4;
  const int s = e / DM;
  f32x4 a = *(const f32x4*)(part + e);
#pragma unroll
  for (int sp = 1; sp < 4; ++sp) a += *(const f32x4*)(part + (size_t)sp * S_LEN * DM + e);
  const u32x2 xv = *(const u32x2*)(x + e);
  const float bb = bf2f(b2[s]);
  float r0 = __uint_as_float(xv[0] << 16) + a[0] + bb;
  float r1 = __uint_as_float(xv[0] & 0xffff0000u) + a[1] + bb;
  float r2 = __uint_as_float(xv[1] << 16) + a[2] + bb;
  float r3 = __uint_as_float(xv[1] & 0xffff0000u) + a[3] + bb;
  u32x2 o = {pack2(r0, r1), pack2(r2, r3)};
  *(u32x2*)(out + e) = o;
}

extern "C" void kernel_launch(void* const* d_in, const int* in_sizes, int n_in, void* d_out, int out_size, void* d_ws,
                              size_t ws_size, hipStream_t stream) {
  const bf16_t* x = (const bf16_t*)d_in[0];
  const bf16_t* ckv = (const bf16_t*)d_in[1];
  const bf16_t* wq = (const bf16_t*)d_in[2];
  const bf16_t* wuk = (const bf16_t*)d_in[3];
  const bf16_t* wuv = (const bf16_t*)d_in[4];
  const bf16_t* wo = (const bf16_t*)d_in[5];
  const bf16_t* w1 = (const bf16_t*)d_in[7];
  const bf16_t* b1 = (const bf16_t*)d_in[8];
  const bf16_t* w2 = (const bf16_t*)d_in[9];
  const bf16_t* b2 = (const bf16_t*)d_in[10];
  char* ws = (char*)d_ws;
  bf16_t* xn = (bf16_t*)(ws + WS_XN);
  bf16_t* h = (bf16_t*)(ws + WS_H);
  bf16_t* Q = (bf16_t*)(ws + WS_Q);
  bf16_t* K = (bf16_t*)(ws + WS_K);
  bf16_t* V = (bf16_t*)(ws + WS_V);
  bf16_t* U = (bf16_t*)(ws + WS_U);
  bf16_t* ctx = (bf16_t*)(ws + WS_CTX);
  float* part = (float*)(ws + WS_PART);
  ln_kernel<<<S_LEN / 4 + PF_BLOCKS, 256, 0, stream>>>(x, xn, h, ckv, wq, wuk, wuv, w1, wo, w2);
  gemm1_kernel<<<256, 512, 0, stream>>>(xn, h, ckv, wq, wuk, wuv, w1, b1, Q, K, V, U);
  attn_kernel<<<256, 512, 0, stream>>>(Q, K, V, ctx, wo, w2);
  gemm2_kernel<<<256, 512, 0, stream>>>(ctx, wo, U, w2, part);
  reduce_kernel<<<S_LEN * DM / 4 / 256, 256, 0, stream>>>(part, x, b2, (bf16_t*)d_out);
}
```

```cpp
#include <hip/hip_runtime.h>
#include <hip/hip_bf16.h>
#include <stdint.h>
#include <type_traits>

constexpr int S_LEN = 2048;
constexpr int DM = 640;
constexpr int NH = 32;
constexpr int HD = 128;
constexpr int DC = 512;
constexpr int FF = 1728;
constexpr int HDN = NH * HD;
constexpr int CTX_LD = HDN + 64;

typedef unsigned short bf16_t;
typedef short bf16x8 __attribute__((ext_vector_type(8)));
typedef short s16x4 __attribute__((ext_vector_type(4)));
typedef float f32x16 __attribute__((ext_vector_type(16)));
typedef float f32x4 __attribute__((ext_vector_type(4)));
typedef unsigned u32x4 __attribute__((ext_vector_type(4)));
typedef unsigned u32x2 __attribute__((ext_vector_type(2)));
typedef __bf16 bf16x2_t __attribute__((ext_vector_type(2)));
#define LAS __attribute__((address_space(3)))

__device__ __forceinline__ float bf2f(bf16_t v) { return __uint_as_float(((unsigned)v) << 16); }
__device__ __forceinline__ unsigned pack2(float a, float b) {
  bf16x2_t v = {(__bf16)a, (__bf16)b};
  return *reinterpret_cast<unsigned*>(&v);
}
__device__ __forceinline__ float rbf(float a) {
  __bf16 v = (__bf16)a;
  return __uint_as_float(((unsigned)*reinterpret_cast<unsigned short*>(&v)) << 16);
}

constexpr size_t WS_XN = 0;
constexpr size_t WS_H = WS_XN + (size_t)S_LEN * DM * 2;
constexpr size_t WS_Q = WS_H + (size_t)S_LEN * DM * 2;
constexpr size_t WS_K = WS_Q + (size_t)S_LEN * HDN * 2;
constexpr size_t WS_V = WS_K + (size_t)S_LEN * HDN * 2;
constexpr size_t WS_U = WS_V + (size_t)S_LEN * HDN * 2;
constexpr size_t WS_CTX = WS_U + (size_t)DM * FF * 2;
constexpr size_t WS_PART = WS_CTX + (size_t)S_LEN * CTX_LD * 2;
constexpr size_t WS_UP = WS_PART + (size_t)6 * S_LEN * DM * 4;

__device__ __forceinline__ float wave_sum(float v) {
#pragma unroll
  for (int o = 32; o > 0; o >>= 1) v += __shfl_xor(v, o);
  return v;
}
__device__ __forceinline__ void ln_row(float (&v)[10]) {
  float s = 0.f;
#pragma unroll
  for (int i = 0; i < 10; ++i) s += v[i];
  s = wave_sum(s);
  const float mean = s * (1.0f / DM);
  float q = 0.f;
#pragma unroll
  for (int i = 0; i < 10; ++i) { float d = v[i] - mean; q += d * d; }
  q = wave_sum(q);
  const float mb = rbf(mean);
  const float vb = rbf(q * (1.0f / DM));
  const float den = rbf(sqrtf(rbf(vb + 1e-5f)));
#pragma unroll
  for (int i = 0; i < 10; ++i) v[i] = rbf(rbf(v[i] - mb) / den);
}
__device__ __forceinline__ void touch64k(const void* base) {
  const char* p = (const char*)base + threadIdx.x * 64;
  unsigned a, b, c, d;
  asm volatile("global_load_dword %0, %4, off\n\tglobal_load_dword %1, %5, off\n\tglobal_load_dword %2, %6, off\n\t"
               "global_load_dword %3, %7, off\n\ts_waitcnt vmcnt(0)"
               : "=&v"(a), "=&v"(b), "=&v"(c), "=&v"(d)
               : "v"(p), "v"(p + 16384), "v"(p + 32768), "v"(p + 49152) : "memory");
}
#define PF_WO 0
constexpr int PF_BLOCKS = 32 + 80 + 64 + 64 + 108 + (PF_WO ? 80 + 108 : 0);
__global__ __launch_bounds__(256) void ln_kernel(const bf16_t* __restrict__ x, bf16_t* __restrict__ xn,
                                                 bf16_t* __restrict__ h, const bf16_t* __restrict__ ckv,
                                                 const bf16_t* __restrict__ wq, const bf16_t* __restrict__ wuk,
                                                 const bf16_t* __restrict__ wuv, const bf16_t* __restrict__ w1,
                                                 const bf16_t* __restrict__ wo, const bf16_t* __restrict__ w2) {
  if (blockIdx.x >= S_LEN / 4) {
    int b = blockIdx.x - S_LEN / 4;
    const bf16_t* src;
    if (b < 32) src = ckv;
    else if ((b -= 32) < 64) src = wuk;
    else if ((b -= 64) < 64) src = wuv;
    else if ((b -= 64) < 80) src = wq;
    else if ((b -= 80) < 108) src = w1;
    else if ((b -= 108) < 80) src = wo;
    else { b -= 80; src = w2; }
    touch64k((const char*)src + (size_t)b * 65536);
    return;
  }
  const int lane = threadIdx.x & 63, w = threadIdx.x >> 6;
  const int row = blockIdx.x * 4 + w;
  const unsigned* xr = reinterpret_cast<const unsigned*>(x + (size_t)row * DM);
  float v[10];
#pragma unroll
  for (int j = 0; j < 5; ++j) {
    unsigned u = xr[lane + 64 * j];
    v[2 * j] = __uint_as_float(u << 16);
    v[2 * j + 1] = __uint_as_float(u & 0xffff0000u);
  }
  ln_row(v);
  unsigned* xo = reinterpret_cast<unsigned*>(xn + (size_t)row * DM);
#pragma unroll
  for (int j = 0; j < 5; ++j) xo[lane + 64 * j] = pack2(v[2 * j], v[2 * j + 1]);
  ln_row(v);
  unsigned* ho = reinterpret_cast<unsigned*>(h + (size_t)row * DM);
#pragma unroll
  for (int j = 0; j < 5; ++j) ho[lane + 64 * j] = pack2(v[2 * j], v[2 * j + 1]);
}

constexpr int STAGE_BYTES = 49152;
constexpr int RBASE = 32768;

__device__ __forceinline__ int swapmid(int p) { return ((p & 1) << 1) | ((p >> 1) & 1); }
__device__ __forceinline__ int rperm(int r) {
  int g = r >> 2;
  int pg = (g & 4) | swapmid(g & 3);
  return pg * 4 + (r & 3);
}
__device__ __forceinline__ s16x4 tr_read(const char LAS* p) {
  return __builtin_amdgcn_ds_read_tr16_b64_v4i16((LAS s16x4*)p);
}
__device__ __forceinline__ bf16x8 cat8(s16x4 a, s16x4 b) {
  return (bf16x8){a[0], a[1], a[2], a[3], b[0], b[1], b[2], b[3]};
}

__device__ __forceinline__ void glds16(const void* sbase, int voff, unsigned lds_dst) {
  unsigned keep;
  asm volatile("s_mov_b32 %0, m0\n\ts_mov_b32 m0, %3\n\ts_nop 0\n\tglobal_load_lds_dwordx4 %1, %2\n\ts_mov_b32 m0, %0"
               : "=&s"(keep) : "v"(voff), "s"(sbase), "s"(lds_dst) : "memory");
}

__device__ __forceinline__ void glds16x4(const void* sbase, int v0, int v1, int v2, int v3, unsigned lds_dst) {
  unsigned keep;
  asm volatile("s_mov_b32 %0, m0\n\ts_mov_b32 m0, %6\n\ts_nop 0\n\tglobal_load_lds_dwordx4 %1, %5\n\t"
               "global_load_lds_dwordx4 %2, %5 offset:1024\n\tglobal_load_lds_dwordx4 %3, %5 offset:2048\n\t"
               "global_load_lds_dwordx4 %4, %5 offset:3072\n\ts_mov_b32 m0, %0"
               : "=&s"(keep) : "v"(v0), "v"(v1), "v"(v2), "v"(v3), "s"(sbase), "s"(lds_dst) : "memory");
}

#define ST_MODE 0
__device__ __forceinline__ void st16(void* p, u32x4 v) {
  *(u32x4*)p = v;
}
__device__ __forceinline__ void st16f(float* p, float a, float b, float c, float d) {
  st16(p, (u32x4){__float_as_uint(a), __float_as_uint(b), __float_as_uint(c), __float_as_uint(d)});
}

template <bool P_TR, bool R_TR, bool ROWS_P>
struct GemmCore {
  int pOff[4], rOff[2];
  int pAddr[4], rAddr[4];
  int w, l;
  __device__ __forceinline__ void init(int ldp, int ldr, int pcol_limit  ) {
    const int tid = threadIdx.x;
    l = tid & 63;
    w = __builtin_amdgcn_readfirstlane(tid >> 6);
    const int wp = w >> 1, wr = w & 1;
#pragma unroll
    for (int i = 0; i < 4; ++i) {
      const int j = w * 4 + i;
      if (!P_TR) {
        const int row = 8 * j + (l >> 3), slot = l & 7;
        const int chunk = slot ^ ((row >> 1) & 7);
        pOff[i] = row * ldp + chunk * 8;
      } else {
        const int half = j >> 4, jj = j & 15;
        const int krow = 4 * jj + (l >> 4), s16 = l & 15;
        const int c64 = (s16 >> 2) ^ (krow & 3);
        int col = half * 128 + c64 * 32 + (s16 & 3) * 8;
        if (col >= pcol_limit) col = pcol_limit - 8;
        pOff[i] = krow * ldp + col;
      }
    }
#pragma unroll
    for (int i = 0; i < 2; ++i) {
      const int j = w * 2 + i;
      if (!R_TR) {
        const int row = 8 * j + (l >> 3), slot = l & 7;
        const int chunk = slot ^ ((row >> 1) & 7);
        rOff[i] = row * ldr + chunk * 8;
      } else {
        const int krow = 4 * j + (l >> 4), s16 = l & 15;
        const int c64 = (s16 >> 2) ^ (krow & 3);
        const int col = c64 * 32 + (s16 & 3) * 8;
        rOff[i] = krow * ldr + col;
      }
    }
    const int r32 = l & 31, hi = l >> 5, blk = (l >> 4) & 1, q = (l & 15) >> 2, p = l & 3;
    if (!P_TR) {
      const int prow = 64 * wp + (ROWS_P ? rperm(r32) : r32);
      const int s = (prow >> 1) & 7;
#pragma unroll
      for (int kk = 0; kk < 4; ++kk) pAddr[kk] = prow * 128 + (((2 * kk + hi) ^ s) << 4);
    } else {
      const int half = wp >> 1, pp = ROWS_P ? swapmid(p) : p;
#pragma unroll
      for (int i = 0; i < 2; ++i) {
        const int c64 = 2 * (wp & 1) + i;
        pAddr[i] = half * 16384 + (8 * hi + q) * 256 + ((c64 ^ q) * 64) + blk * 32 + pp * 8;
      }
      pAddr[2] = pAddr[3] = 0;
    }
    if (!R_TR) {
      const int rrow = 64 * wr + (!ROWS_P ? rperm(r32) : r32);
      const int s = (rrow >> 1) & 7;
#pragma unroll
      for (int kk = 0; kk < 4; ++kk) rAddr[kk] = RBASE + rrow * 128 + (((2 * kk + hi) ^ s) << 4);
    } else {
      const int pp = !ROWS_P ? swapmid(p) : p;
#pragma unroll
      for (int i = 0; i < 2; ++i) {
        const int c64 = 2 * wr + i;
        rAddr[i] = RBASE + (8 * hi + q) * 256 + ((c64 ^ q) * 64) + blk * 32 + pp * 8;
      }
      rAddr[2] = rAddr[3] = 0;
    }
  }
  __device__ __forceinline__ void stage(char LAS* buf, const bf16_t* P, const bf16_t* R) {
    const unsigned b = (unsigned)(uintptr_t)buf;
#pragma unroll
    for (int i = 0; i < 4; ++i) glds16(P, pOff[i] * 2, b + (w * 4 + i) * 1024);
#pragma unroll
    for (int i = 0; i < 2; ++i) glds16(R, rOff[i] * 2, b + RBASE + (w * 2 + i) * 1024);
  }
  __device__ __forceinline__ void compute(const char LAS* buf, f32x16 (&acc)[2][2]) {
#pragma unroll
    for (int kk = 0; kk < 4; ++kk) {
      bf16x8 pf[2], rf[2];
#pragma unroll
      for (int i = 0; i < 2; ++i) {
        if (!P_TR) pf[i] = *(const LAS bf16x8*)(buf + pAddr[kk] + i * 4096);
        else pf[i] = cat8(tr_read(buf + pAddr[i] + kk * 4096), tr_read(buf + pAddr[i] + kk * 4096 + 1024));
        if (!R_TR) rf[i] = *(const LAS bf16x8*)(buf + rAddr[kk] + i * 4096);
        else rf[i] = cat8(tr_read(buf + rAddr[i] + kk * 4096), tr_read(buf + rAddr[i] + kk * 4096 + 1024));
      }
#pragma unroll
      for (int i = 0; i < 2; ++i)
#pragma unroll
        for (int j = 0; j < 2; ++j) {
          if (ROWS_P) acc[i][j] = __builtin_amdgcn_mfma_f32_32x32x16_bf16(pf[i], rf[j], acc[i][j], 0, 0, 0);
          else acc[i][j] = __builtin_amdgcn_mfma_f32_32x32x16_bf16(rf[j], pf[i], acc[i][j], 0, 0, 0);
        }
    }
  }
};

#define WAIT_VM0() asm volatile("s_waitcnt vmcnt(0)" ::: "memory")
#define WAIT_VM6() asm volatile("s_waitcnt vmcnt(6)" ::: "memory")
#define WAIT_VM16() asm volatile("s_waitcnt vmcnt(16)" ::: "memory")
#define BARRIER() __builtin_amdgcn_s_barrier()
#define TS(k, i)
#define PH(i, k)

__device__ __forceinline__ float gelu_tanh(float x) {
  const float y = 0.7978845608028654f * (x + 0.044715f * x * x * x);
  const float e = __expf(2.0f * y);
  const float t = 1.0f - 2.0f / (e + 1.0f);
  return 0.5f * x * (1.0f + t);
}

constexpr int STAGE256 = 32768;
#define NST256 4
struct Core256 {
  int pOff[2], rOff[2];
  int pAddr[2], rAddr[2];
  int w;
  __device__ __forceinline__ void init(int ldp, int ldr) {
    const int tid = threadIdx.x, l = tid & 63;
    w = __builtin_amdgcn_readfirstlane(tid >> 6);
    const int wp = w >> 2, wr = w & 3;
#pragma unroll
    for (int i = 0; i < 2; ++i) {
      const int j = w * 2 + i;
      const int row = 16 * j + (l >> 2), slot = l & 3;
      pOff[i] = (row * ldp + (slot ^ ((row >> 2) & 3)) * 8) * 2;
      const int half = j >> 3, jj = j & 7;
      const int krow = 4 * jj + (l >> 4), s16 = l & 15;
      const int c64 = (s16 >> 2) ^ (krow & 3);
      rOff[i] = (krow * ldr + half * 128 + c64 * 32 + (s16 & 3) * 8) * 2;
    }
    const int r32 = l & 31, hi = l >> 5, blk = (l >> 4) & 1, q = (l & 15) >> 2, p = l & 3;
    const int prow = 128 * wp + r32, s = (prow >> 2) & 3;
#pragma unroll
    for (int kk = 0; kk < 2; ++kk) pAddr[kk] = prow * 64 + (((2 * kk + hi) ^ s) << 4);
#pragma unroll
    for (int j = 0; j < 2; ++j) {
      const int c64 = 2 * (wr & 1) + j;
      rAddr[j] = 16384 + (wr >> 1) * 8192 + (8 * hi + q) * 256 + ((c64 ^ q) * 64) + blk * 32 + swapmid(p) * 8;
    }
  }
  __device__ __forceinline__ void stage(char LAS* buf, const bf16_t* P, const bf16_t* R) {
    const unsigned b = (unsigned)(uintptr_t)buf;
#pragma unroll
    for (int i = 0; i < 2; ++i) glds16(P, pOff[i], b + (w * 2 + i) * 1024);
#pragma unroll
    for (int i = 0; i < 2; ++i) glds16(R, rOff[i], b + 16384 + (w * 2 + i) * 1024);
  }
  __device__ __forceinline__ void compute(const char LAS* buf, f32x16 (&acc)[4][2]) {
#pragma unroll
    for (int kk = 0; kk < 2; ++kk) {
      bf16x8 pf[4], rf[2];
#pragma unroll
      for (int j = 0; j < 2; ++j) rf[j] = cat8(tr_read(buf + rAddr[j] + kk * 4096), tr_read(buf + rAddr[j] + kk * 4096 + 1024));
#pragma unroll
      for (int i = 0; i < 4; ++i) pf[i] = *(const LAS bf16x8*)(buf + pAddr[kk] + i * 2048);
#pragma unroll
      for (int i = 0; i < 4; ++i)
#pragma unroll
        for (int j = 0; j < 2; ++j) acc[i][j] = __builtin_amdgcn_mfma_f32_32x32x16_bf16(rf[j], pf[i], acc[i][j], 0, 0, 0);
    }
  }
  __device__ __forceinline__ void load_frags(const char LAS* buf, bf16x8 (&pf)[2][4], bf16x8 (&rf)[2][2]) {
#pragma unroll
    for (int kk = 0; kk < 2; ++kk) {
#pragma unroll
      for (int j = 0; j < 2; ++j) rf[kk][j] = cat8(tr_read(buf + rAddr[j] + kk * 4096), tr_read(buf + rAddr[j] + kk * 4096 + 1024));
#pragma unroll
      for (int i = 0; i < 4; ++i) pf[kk][i] = *(const LAS bf16x8*)(buf + pAddr[kk] + i * 2048);
    }
  }
  __device__ __forceinline__ void mma(const bf16x8 (&pf)[2][4], const bf16x8 (&rf)[2][2], f32x16 (&acc)[4][2]) {
#pragma unroll
    for (int kk = 0; kk < 2; ++kk)
#pragma unroll
      for (int i = 0; i < 4; ++i)
#pragma unroll
        for (int j = 0; j < 2; ++j) acc[i][j] = __builtin_amdgcn_mfma_f32_32x32x16_bf16(rf[kk][j], pf[kk][i], acc[i][j], 0, 0, 0);
  }
};
#define WAIT_LGKM0() asm volatile("s_waitcnt lgkmcnt(0)" ::: "memory")
#define SCHED0() __builtin_amdgcn_sched_barrier(0)
template <int N> __device__ __forceinline__ void wait_vm() { asm volatile("s_waitcnt vmcnt(%0)" ::"n"(N) : "memory"); }

struct Frag { bf16x8 pf[4], rf[2]; };
__global__ __launch_bounds__(512, 2) void gemm1_kernel(const bf16_t* __restrict__ xn, const bf16_t* __restrict__ hbuf,
                                                       const bf16_t* __restrict__ ckv, const bf16_t* __restrict__ wq,
                                                       const bf16_t* __restrict__ wuk, const bf16_t* __restrict__ wuv,
                                                       const bf16_t* __restrict__ w1, const bf16_t* __restrict__ b1,
                                                       bf16_t* __restrict__ Qo, bf16_t* __restrict__ Ko,
                                                       bf16_t* __restrict__ Vo, bf16_t* __restrict__ Uo) {
  __shared__ __attribute__((aligned(16))) char smem[4 * STAGE256 + 32768];
  char LAS* lds = (char LAS*)smem;
  const unsigned ldsb = (unsigned)(uintptr_t)lds;
  const int xcd = blockIdx.x & 7, c = blockIdx.x >> 3;
  const int l = threadIdx.x & 63, w = __builtin_amdgcn_readfirstlane(threadIdx.x >> 6);
  const int r32 = l & 31, hi = l >> 5;
  TS(0, 0);
  const bool isQ = c >= 16;
  const int mbase = (xcd * 90) >> 3, mcnt = (((xcd + 1) * 90) >> 3) - mbase;
  const bool hasMix = isQ && c - 16 < mcnt;
  const int mtile = mbase + (c - 16);
  const int d0 = (mtile % 5) * 128, f0 = (mtile / 5) * 96;
  int mpOff[2], mrOff[2];
#pragma unroll
  for (int i = 0; i < 2; ++i) {
    const int j = w * 2 + i;
    const int krow = 4 * j + (l >> 4), s16 = l & 15;
    mpOff[i] = (krow * DM + ((s16 >> 2) ^ (krow & 3)) * 32 + (s16 & 3) * 8) * 2;
    const int jr = w + 8 * i;
    const int cb = jr >> 2, kr = (jr & 3) * 16 + (l >> 2);
    mrOff[i] = (kr * FF + cb * 32 + (l & 3) * 8) * 2;
  }
  const bf16_t* Mp = hbuf + d0;
  const bf16_t* Mr = w1 + f0;
  auto missue = [&](int m, int slot, int part) {
    const unsigned bb = ldsb + slot * STAGE256;
    if (part == 0) {
      const bf16_t* P = Mp + (size_t)m * 64 * DM;
      glds16(P, mpOff[0], bb + (w * 2) * 1024);
      glds16(P, mpOff[1], bb + (w * 2 + 1) * 1024);
    } else {
      const bf16_t* R = Mr + (size_t)m * 64 * FF;
      glds16(R, mrOff[0], bb + 16384 + w * 1024);
      if (w < 4) glds16(R, mrOff[1], bb + 16384 + (w + 8) * 1024);
    }
  };
  int total;
  {
    int ntile, tb0, hp;
    const bf16_t* A; const bf16_t* W; bf16_t* O; int K;
    if (!isQ) {
      ntile = 2; tb0 = c >> 2; hp = xcd * 2 + (c & 1);
      A = ckv; K = DC;
      W = (c & 2) ? wuv : wuk; O = (c & 2) ? Vo : Ko;
    } else {
      ntile = 1; tb0 = (c - 16) >> 1; hp = xcd * 2 + (c & 1);
      A = xn; K = DM; W = wq; O = Qo;
    }
    const int nk = K / 32;
    Core256 g;
    g.init(K, HDN);
    const bf16_t* Wp = W + hp * 256;
    const int wp = w >> 2, wr = w & 3;
    total = ntile * nk;
    auto issue = [&](int n, int part) {
      if (n < total) {
        const int pt = n >= nk ? 1 : 0, pk = n - pt * nk;
        const unsigned bb = ldsb + (n & 3) * STAGE256;
        if (part == 0) {
          const bf16_t* P = A + (size_t)(tb0 + 4 * pt) * 256 * K + pk * 32;
          glds16(P, g.pOff[0], bb + (w * 2) * 1024);
          glds16(P, g.pOff[1], bb + (w * 2 + 1) * 1024);
        } else {
          const bf16_t* R = Wp + (size_t)pk * 32 * HDN;
          glds16(R, g.rOff[0], bb + 16384 + (w * 2) * 1024);
          glds16(R, g.rOff[1], bb + 16384 + (w * 2 + 1) * 1024);
        }
      } else if (hasMix) {
        missue(n - total, n & 3, part);
      }
    };
    f32x16 acc[4][2];
    auto half = [&](auto first, const Frag& cur, Frag& nxt, const char LAS* nb, int nkk, int in, int ipart) {
      auto mm = [&](int i, int j) {
        acc[i][j] = __builtin_amdgcn_mfma_f32_32x32x16_bf16(cur.rf[j], cur.pf[i], first ? (f32x16){} : acc[i][j], 0, 0, 0);
        SCHED0();
      };
      auto rdR = [&](int j) {
        nxt.rf[j] = cat8(tr_read(nb + g.rAddr[j] + nkk * 4096), tr_read(nb + g.rAddr[j] + nkk * 4096 + 1024));
        SCHED0();
      };
      auto rdP = [&](int i) {
        nxt.pf[i] = *(const LAS bf16x8*)(nb + g.pAddr[nkk] + i * 2048);
        SCHED0();
      };
      mm(0, 0); rdR(0);
      mm(0, 1); rdP(0); rdR(1);
      mm(1, 0); rdP(1);
      mm(1, 1); rdP(2);
      mm(2, 0); rdP(3);
      mm(2, 1); issue(in, ipart); SCHED0();
      mm(3, 0);
      mm(3, 1);
    };
    issue(0, 0); issue(0, 1); issue(1, 0); issue(1, 1); issue(2, 0); issue(2, 1); issue(3, 0);
    wait_vm<10>();
    BARRIER();
    Frag F0, F1;
#pragma unroll
    for (int j = 0; j < 2; ++j) F0.rf[j] = cat8(tr_read(lds + g.rAddr[j]), tr_read(lds + g.rAddr[j] + 1024));
#pragma unroll
    for (int i = 0; i < 4; ++i) F0.pf[i] = *(const LAS bf16x8*)(lds + g.pAddr[0] + i * 2048);
    SCHED0();
    int step = 0;
    for (int t = 0; t < ntile; ++t) {
      const int tb = tb0 + 4 * t;
      for (int kt = 0; kt < nk; ++kt) {
        const char LAS* cb = lds + (step & 3) * STAGE256;
        const char LAS* nb = lds + ((step + 1) & 3) * STAGE256;
        if (kt == 0) half(std::true_type{}, F0, F1, cb, 1, step + 3, 1);
        else half(std::false_type{}, F0, F1, cb, 1, step + 3, 1);
        if (step + 3 >= total) {
          if (!hasMix) WAIT_VM0();
          else if (w < 4) wait_vm<8>();
          else wait_vm<6>();
        } else if (t > 0 && kt < 2) wait_vm<24>();
        else wait_vm<8>();
        WAIT_LGKM0();
        SCHED0();
        BARRIER();
        SCHED0();
        half(std::false_type{}, F1, F0, nb, 0, step + 4, 0);
        ++step;
      }
      if (hasMix) missue(3, (total + 3) & 3, 1);
      const int head = hp * 2 + (wr >> 1);
      char LAS* slab = lds + 4 * STAGE256 + w * 4096;
      const int wa = r32 * 128, wsw = r32 & 7;
      const int rrow = l >> 3, ra = rrow * 128 + (((l & 7) ^ rrow) << 4);
      bf16_t* obase = O + ((size_t)head * S_LEN + tb * 256 + 128 * wp + rrow) * HD + 64 * (wr & 1) + (l & 7) * 8;
#pragma unroll
      for (int i = 0; i < 4; ++i) {
#pragma unroll
        for (int j = 0; j < 2; ++j) {
          const f32x16 a = acc[i][j];
          u32x4 v0 = {pack2(a[0], a[1]), pack2(a[2], a[3]), pack2(a[4], a[5]), pack2(a[6], a[7])};
          u32x4 v1 = {pack2(a[8], a[9]), pack2(a[10], a[11]), pack2(a[12], a[13]), pack2(a[14], a[15])};
          *(u32x4 LAS*)(slab + wa + (((4 * j + hi) ^ wsw) << 4)) = v0;
          *(u32x4 LAS*)(slab + wa + (((4 * j + 2 + hi) ^ wsw) << 4)) = v1;
        }
#pragma unroll
        for (int it = 0; it < 4; ++it) {
          const u32x4 v = *(const u32x4 LAS*)(slab + ra + it * 1024);
          st16(obase + (size_t)(32 * i + 8 * it) * HD, v);
        }
      }
      SCHED0();
    }
  }
  TS(0, 1);
  if (hasMix) {
    const int ws = w & 3, kh = w >> 2;
    const int blk = (l >> 4) & 1, q = (l & 15) >> 2, p = l & 3;
    const int pB = (32 * kh + 8 * hi + q) * 256 + ((ws ^ q) * 64) + blk * 32 + p * 8;
    const int rA = 16384 + (32 * kh + 8 * hi + q) * 64 + blk * 32 + swapmid(p) * 8;
    f32x16 acc[3] = {};
    bf16x8 pa[2], ra[2][3], pb[2], rb[2][3];
    auto mstep = [&](const bf16x8 (&pc)[2], const bf16x8 (&rc)[2][3], bf16x8 (&pn)[2], bf16x8 (&rn)[2][3], int m) {
      if (m >= 29) WAIT_VM0();
      else if (m < 3) { if (w < 4) wait_vm<24>(); else wait_vm<22>(); }
      else { if (w < 4) wait_vm<8>(); else wait_vm<6>(); }
      WAIT_LGKM0();
      SCHED0();
      BARRIER();
      SCHED0();
      const char LAS* nb = lds + ((total + m + 1) & 3) * STAGE256;
      auto mm = [&](int k2, int j) {
        acc[j] = __builtin_amdgcn_mfma_f32_32x32x16_bf16(rc[k2][j], pc[k2], acc[j], 0, 0, 0);
        SCHED0();
      };
      auto rdP = [&](int k2) { pn[k2] = cat8(tr_read(nb + pB + k2 * 4096), tr_read(nb + pB + k2 * 4096 + 1024)); SCHED0(); };
      auto rdR = [&](int k2, int j) {
        rn[k2][j] = cat8(tr_read(nb + rA + j * 4096 + k2 * 1024), tr_read(nb + rA + j * 4096 + k2 * 1024 + 256));
        SCHED0();
      };
      mm(0, 0); rdP(0); rdR(0, 0);
      mm(0, 1); rdR(0, 1); rdR(0, 2);
      mm(0, 2); rdP(1); rdR(1, 0);
      mm(1, 0); rdR(1, 1); rdR(1, 2);
      mm(1, 1); if (m + 4 < 32) missue(m + 4, (total + m) & 3, 0); SCHED0();
      mm(1, 2); if (m + 4 < 32) missue(m + 4, (total + m) & 3, 1); SCHED0();
    };
    if (w < 4) wait_vm<28>(); else wait_vm<25>();
    BARRIER();
    {
      const char LAS* nb = lds + (total & 3) * STAGE256;
#pragma unroll
      for (int k2 = 0; k2 < 2; ++k2) {
        pa[k2] = cat8(tr_read(nb + pB + k2 * 4096), tr_read(nb + pB + k2 * 4096 + 1024));
#pragma unroll
        for (int j = 0; j < 3; ++j) ra[k2][j] = cat8(tr_read(nb + rA + j * 4096 + k2 * 1024), tr_read(nb + rA + j * 4096 + k2 * 1024 + 256));
      }
    }
    SCHED0();
    for (int m = 0; m < 32; m += 2) {
      mstep(pa, ra, pb, rb, m);
      mstep(pb, rb, pa, ra, m + 1);
    }
    WAIT_LGKM0();
    BARRIER();
    float LAS* dst = (float LAS*)(lds + w * 8192);
    const f32x16 sa = kh == 0 ? acc[2] : acc[0], ka = kh == 0 ? acc[0] : acc[2];
    const int so = kh == 0 ? 8 : 0;
#pragma unroll
    for (int g = 0; g < 4; ++g) *(f32x4 LAS*)(dst + (g * 64 + l) * 4) = (f32x4){sa[4 * g], sa[4 * g + 1], sa[4 * g + 2], sa[4 * g + 3]};
    float s1[8], k1[8];
#pragma unroll
    for (int e = 0; e < 8; ++e) { s1[e] = kh == 0 ? acc[1][8 + e] : acc[1][e]; k1[e] = kh == 0 ? acc[1][e] : acc[1][8 + e]; }
#pragma unroll
    for (int g = 0; g < 2; ++g) *(f32x4 LAS*)(dst + ((4 + g) * 64 + l) * 4) = (f32x4){s1[4 * g], s1[4 * g + 1], s1[4 * g + 2], s1[4 * g + 3]};
    (void)so;
    WAIT_LGKM0();
    BARRIER();
    const float LAS* src = (const float LAS*)(lds + (w ^ 4) * 8192);
    bf16_t* urow = Uo + (size_t)(d0 + 32 * ws + r32) * FF + f0 + 8 * hi;
    auto fin8 = [&](const float (&v)[8], int fo) {
      const u32x4 bv = *(const u32x4*)(b1 + f0 + 8 * hi + fo);
      float r[8];
#pragma unroll
      for (int e = 0; e < 4; ++e) {
        r[2 * e] = gelu_tanh(rbf(rbf(v[2 * e]) + __uint_as_float(bv[e] << 16)));
        r[2 * e + 1] = gelu_tanh(rbf(rbf(v[2 * e + 1]) + __uint_as_float(bv[e] & 0xffff0000u)));
      }
      st16(urow + fo, (u32x4){pack2(r[0], r[1]), pack2(r[2], r[3]), pack2(r[4], r[5]), pack2(r[6], r[7])});
    };
    const int jk = kh == 0 ? 0 : 2;
#pragma unroll
    for (int hh = 0; hh < 2; ++hh) {
      float v[8];
#pragma unroll
      for (int g = 0; g < 2; ++g) {
        const f32x4 o = *(const f32x4 LAS*)(src + ((2 * hh + g) * 64 + l) * 4);
#pragma unroll
        for (int e = 0; e < 4; ++e) v[4 * g + e] = ka[8 * hh + 4 * g + e] + o[e];
      }
      fin8(v, 32 * jk + 16 * hh);
    }
    {
      float v[8];
#pragma unroll
      for (int g = 0; g < 2; ++g) {
        const f32x4 o = *(const f32x4 LAS*)(src + ((4 + g) * 64 + l) * 4);
#pragma unroll
        for (int e = 0; e < 4; ++e) v[4 * g + e] = k1[4 * g + e] + o[e];
      }
      fin8(v, 32 + 16 * kh);
    }
  }
  TS(0, 2);
}

constexpr int AT_HALF = 16384;
__global__ __launch_bounds__(512, 2) void attn_kernel(const bf16_t* __restrict__ Q, const bf16_t* __restrict__ K,
                                                      const bf16_t* __restrict__ V, bf16_t* __restrict__ ctx,
                                                      const bf16_t* __restrict__ wo, const bf16_t* __restrict__ w2) {
  __shared__ __attribute__((aligned(16))) char smem[8 * AT_HALF + 32768];
  char LAS* lds = (char LAS*)smem;
  const int tid = threadIdx.x, l = tid & 63, w = __builtin_amdgcn_readfirstlane(tid >> 6);
  const int rg = w & 3, g = w >> 2, r32 = l & 31, hi = l >> 5;
  const int xcd = blockIdx.x & 7, jj = blockIdx.x >> 3;
  const int head = xcd * 4 + (jj >> 3), pair = jj & 7;
  const int LB = 15 - pair, nshort = 2 * pair + 2;
  const bf16_t* Kh = K + (size_t)head * S_LEN * HD;
  const bf16_t* Vh = V + (size_t)head * S_LEN * HD;
  const bf16_t* Qh = Q + (size_t)head * S_LEN * HD;
  char LAS* gl = lds + g * 4 * AT_HALF;
  int kOff[4], vOff[4];
#pragma unroll
  for (int i = 0; i < 4; ++i) {
    const int j = rg * 4 + i, row = 4 * j + (l >> 4), s16 = l & 15;
    kOff[i] = (row * HD + ((s16 ^ (row & 15)) * 8)) * 2 - 1024 * i;
    vOff[i] = (row * HD + ((((s16 >> 2) ^ (row & 3)) * 4 + (s16 & 3)) * 8)) * 2 - 1024 * i;
  }
  const unsigned glb = (unsigned)(uintptr_t)gl;
  auto htile = [&](int i) { return (g == 0 || i < nshort) ? i : i + 15 - 2 * pair; };
  auto stageK = [&](int i) {
    const bf16_t* kp = Kh + (size_t)htile(i > 16 ? 16 : i) * 64 * HD;
    const unsigned bb = glb + (i & 1) * AT_HALF + rg * 4096;
    glds16x4(kp, kOff[0], kOff[1], kOff[2], kOff[3], bb);
  };
  auto stageV = [&](int i) {
    const bf16_t* vp = Vh + (size_t)htile(i) * 64 * HD;
    const unsigned bb = glb + (2 + (i & 1)) * AT_HALF + rg * 4096;
    glds16x4(vp, vOff[0], vOff[1], vOff[2], vOff[3], bb);
  };
  const int ksw = r32 & 15;
  const int blk = (l >> 4) & 1, q4 = (l & 15) >> 2, p4 = l & 3;
  int vAddr[4];
#pragma unroll
  for (int db = 0; db < 4; ++db)
    vAddr[db] = 2 * AT_HALF + (4 * hi + q4) * 256 + ((db ^ q4) * 64) + blk * 32 + swapmid(p4) * 8;
  constexpr float CS = 0.08838834764831845f * 1.4426950408889634f;

  TS(1, 0);
  stageK(0);
  stageV(0);
  stageK(1);
  int qb = g == 0 ? LB : pair;
  bf16x8 qf[8];
#pragma unroll
  for (int d0 = 0; d0 < 8; ++d0) qf[d0] = *(const bf16x8*)(Qh + (size_t)(qb * 128 + 32 * rg + r32) * HD + d0 * 16 + hi * 8);
  char LAS* qsave = lds + 8 * AT_HALF + rg * 8192 + l * 16;
  if (g == 0) {
#pragma unroll
    for (int d0 = 0; d0 < 8; ++d0) *(bf16x8 LAS*)(qsave + d0 * 1024) = qf[d0];
  }
  float m_run = -1e30f, l_run = 0.f, alpha = 1.f;
  f32x16 o[4] = {};
  f32x16 s0, s1;
  bf16x8 pf[4];
  u32x2 pfd = {0u, 0u};
  constexpr int AT_VM = 5;
#define AT_W 6
  bf16x8 fr[32];
  const int kAddr0 = r32 * 256 + ((hi ^ ksw) << 4);
  auto rdK = [&](int k, int slot) {
    fr[k] = *(const LAS bf16x8*)(gl + slot * AT_HALF + (kAddr0 ^ ((k >> 1) << 5)) + (k & 1) * 8192);
  };
  auto rdV = [&](int k, int slot) {
    const char LAS* a = gl + slot * AT_HALF + vAddr[(k - 16) >> 2] + (k & 3) * 4096;
    fr[k] = cat8(tr_read(a), tr_read(a + 2048));
  };
  auto mm = [&](int k) {
    if (k < 16) {
      if (k & 1) s1 = __builtin_amdgcn_mfma_f32_32x32x16_bf16(fr[k], qf[k >> 1], k < 2 ? (f32x16){} : s1, 0, 0, 0);
      else s0 = __builtin_amdgcn_mfma_f32_32x32x16_bf16(fr[k], qf[k >> 1], k < 2 ? (f32x16){} : s0, 0, 0, 0);
    } else {
      o[(k - 16) >> 2] = __builtin_amdgcn_mfma_f32_32x32x16_bf16(fr[k], pf[k & 3], o[(k - 16) >> 2], 0, 0, 0);
    }
  };
  auto preK = [&](int slot) {
#pragma unroll
    for (int k = 0; k < AT_W; ++k) rdK(k, slot);
  };
  auto preV = [&](int slot) {
#pragma unroll
    for (int k = 16; k < 16 + AT_W; ++k) rdV(k, slot);
  };
  auto xhalf_max = [&](float v) {
    auto rr = __builtin_amdgcn_permlane32_swap(__float_as_uint(v), __float_as_uint(v), false, false);
    return fmaxf(__uint_as_float(rr[0]), __uint_as_float(rr[1]));
  };
  auto xhalf_sum = [&](float v) {
    auto rr = __builtin_amdgcn_permlane32_swap(__float_as_uint(v), __float_as_uint(v), false, false);
    return __uint_as_float(rr[0]) + __uint_as_float(rr[1]);
  };
  auto softmax = [&](int i) {
    if (g == 1 && i == nshort) {
      const float inv = 1.0f / xhalf_sum(l_run);
      bf16_t* orow = ctx + (size_t)(pair * 128 + 32 * rg + r32) * CTX_LD + head * HD + 8 * hi;
#pragma unroll
      for (int db = 0; db < 4; ++db) {
        const f32x16 a = o[db];
        u32x4 v0 = {pack2(a[0] * inv, a[1] * inv), pack2(a[2] * inv, a[3] * inv), pack2(a[4] * inv, a[5] * inv), pack2(a[6] * inv, a[7] * inv)};
        u32x4 v1 = {pack2(a[8] * inv, a[9] * inv), pack2(a[10] * inv, a[11] * inv), pack2(a[12] * inv, a[13] * inv), pack2(a[14] * inv, a[15] * inv)};
        st16(orow + 32 * db, v0);
        st16(orow + 32 * db + 16, v1);
        o[db] = (f32x16){};
      }
      m_run = -1e30f;
      l_run = 0.f;
      qb = LB;
    }
    if (g == 1 && i == nshort - 1) {
#pragma unroll
      for (int d0 = 0; d0 < 8; ++d0) qf[d0] = *(const bf16x8 LAS*)(qsave + d0 * 1024);
    }
    SCHED0();
    if (i < 16) stageK(i + 2);
    if (i < 16) {
      int idx = (i * 256 + (int)blockIdx.x) * 8 + w;
      if (idx >= 24064) idx -= 24064;
      const char* pb = idx < 10240 ? (const char*)wo + (size_t)idx * 512 : (const char*)w2 + (size_t)(idx - 10240) * 512;
      asm volatile("global_load_dwordx2 %0, %1, %2" : "+v"(pfd) : "v"(l * 8), "s"(pb) : "memory");
    }
    SCHED0(); PH(i, 5); SCHED0();
    const int dh = htile(i) - 2 * qb;
    if (dh >= 0) {
      const int qi = 32 * rg + r32 - 64 * dh;
#pragma unroll
      for (int r = 0; r < 16; ++r) {
        const int key = (r & 3) + 8 * (r >> 2) + 4 * hi;
        if (key > qi) s0[r] = -__builtin_inff();
        if (key + 32 > qi) s1[r] = -__builtin_inff();
      }
    }
    float pm = s0[0];
#pragma unroll
    for (int r = 1; r < 16; ++r) pm = fmaxf(pm, s0[r]);
#pragma unroll
    for (int r = 0; r < 16; ++r) pm = fmaxf(pm, s1[r]);
    pm = xhalf_max(pm);
    const float mn = fmaxf(m_run, pm);
    alpha = __builtin_amdgcn_exp2f((m_run - mn) * CS);
    m_run = mn;
    const float mc = -mn * CS;
    SCHED0(); PH(i, 6); SCHED0();
    float ps = 0.f;
#pragma unroll
    for (int r = 0; r < 16; ++r) { s0[r] = __builtin_amdgcn_exp2f(fmaf(s0[r], CS, mc)); ps += s0[r]; }
#pragma unroll
    for (int r = 0; r < 16; ++r) { s1[r] = __builtin_amdgcn_exp2f(fmaf(s1[r], CS, mc)); ps += s1[r]; }
    l_run = l_run * alpha + ps;
    SCHED0(); PH(i, 7); SCHED0();
    u32x4 a0 = {pack2(s0[0], s0[1]), pack2(s0[2], s0[3]), pack2(s0[4], s0[5]), pack2(s0[6], s0[7])};
    u32x4 a1 = {pack2(s0[8], s0[9]), pack2(s0[10], s0[11]), pack2(s0[12], s0[13]), pack2(s0[14], s0[15])};
    u32x4 a2 = {pack2(s1[0], s1[1]), pack2(s1[2], s1[3]), pack2(s1[4], s1[5]), pack2(s1[6], s1[7])};
    u32x4 a3 = {pack2(s1[8], s1[9]), pack2(s1[10], s1[11]), pack2(s1[12], s1[13]), pack2(s1[14], s1[15])};
    pf[0] = *(bf16x8*)&a0; pf[1] = *(bf16x8*)&a1; pf[2] = *(bf16x8*)&a2; pf[3] = *(bf16x8*)&a3;
    asm volatile("" : "+v"(pf[0]), "+v"(pf[1]), "+v"(pf[2]), "+v"(pf[3]), "+v"(l_run), "+v"(alpha));
  };
#define AT_DK0 16
  WAIT_VM0();
  WAIT_LGKM0();
  BARRIER();
  TS(1, 3);
  if (g == 1) BARRIER();
  SCHED0();
  preK(0);
#pragma unroll
  for (int k = 0; k < 16; ++k) {
    if (k + AT_W < 16) rdK(k + AT_W, 0);
    mm(k);
    SCHED0();
  }
  BARRIER();
  for (int i = 0; i < 16; ++i) {
    SCHED0();
    PH(i, 0);
    softmax(i);
    SCHED0();
    const int ks = (i + 1) & 1, vs = i & 1;
    preK(ks);
    SCHED0();
    wait_vm<AT_VM>();
    PH(i, 1);
    BARRIER();
    SCHED0();
    PH(i, 2);
    __builtin_amdgcn_s_setprio(1);
#pragma unroll
    for (int k = 0; k < 32; ++k) {
      if (k + AT_W < 16) rdK(k + AT_W, ks);
      else if (k + AT_W < 32) rdV(k + AT_W, vs);
      mm(k);
      if (k < 16) {
#pragma unroll
        for (int e = 0; e < 4; ++e) {
          float t = o[k >> 2][4 * (k & 3) + e];
          asm volatile("v_mul_f32 %0, %1, %0" : "+v"(t) : "v"(alpha));
          o[k >> 2][4 * (k & 3) + e] = t;
        }
      }
      if (k == AT_DK0) stageV(i + 1);
      SCHED0();
    }
    __builtin_amdgcn_s_setprio(0);
    PH(i, 3);
    wait_vm<AT_VM>();
    PH(i, 4);
    BARRIER();
  }
  SCHED0();
  softmax(16);
#pragma unroll
  for (int db = 0; db < 4; ++db)
#pragma unroll
    for (int r = 0; r < 16; ++r) o[db][r] *= alpha;
  SCHED0();
  WAIT_VM0();
  asm volatile("" ::"v"(pfd));
  BARRIER();
  SCHED0();
  preV(0);
#pragma unroll
  for (int k = 16; k < 32; ++k) {
    if (k + AT_W < 32) rdV(k + AT_W, 0);
    mm(k);
    SCHED0();
  }
  BARRIER();
  if (g == 0) BARRIER();
  l_run = xhalf_sum(l_run);
  TS(1, 1);
  {
    const int kh = g;
    float LAS* ml = (float LAS*)(lds + 4 * AT_HALF);
    float LAS* dst = (float LAS*)(lds + w * 8192);
#pragma unroll
    for (int dd = 0; dd < 2; ++dd) {
      f32x16 a;
#pragma unroll
      for (int r = 0; r < 16; ++r) a[r] = kh == 0 ? o[2 + dd][r] : o[dd][r];
#pragma unroll
      for (int gg = 0; gg < 4; ++gg)
        *(f32x4 LAS*)(dst + ((dd * 4 + gg) * 64 + l) * 4) = (f32x4){a[4 * gg], a[4 * gg + 1], a[4 * gg + 2], a[4 * gg + 3]};
    }
    ml[w * 128 + l] = m_run;
    ml[w * 128 + 64 + l] = l_run;
    asm volatile("s_waitcnt lgkmcnt(0)" ::: "memory");
    BARRIER();
    const int wo = w ^ 4;
    const float m2 = ml[wo * 128 + l], l2 = ml[wo * 128 + 64 + l];
    const float mt = fmaxf(m_run, m2);
    const float a1 = __builtin_amdgcn_exp2f((m_run - mt) * CS), a2 = __builtin_amdgcn_exp2f((m2 - mt) * CS);
    const float inv = 1.0f / (l_run * a1 + l2 * a2);
    const float f1 = a1 * inv, f2 = a2 * inv;
    const float LAS* src = (const float LAS*)(lds + wo * 8192);
    bf16_t* orow = ctx + (size_t)(LB * 128 + 32 * rg + r32) * CTX_LD + head * HD + 8 * hi;
#pragma unroll
    for (int dd = 0; dd < 2; ++dd) {
      f32x16 a;
#pragma unroll
      for (int r = 0; r < 16; ++r) a[r] = kh == 0 ? o[dd][r] : o[2 + dd][r];
      float r[16];
#pragma unroll
      for (int gg = 0; gg < 4; ++gg) {
        const f32x4 b = *(const f32x4 LAS*)(src + ((dd * 4 + gg) * 64 + l) * 4);
#pragma unroll
        for (int e = 0; e < 4; ++e) r[4 * gg + e] = a[4 * gg + e] * f1 + b[e] * f2;
      }
      u32x4 v0 = {pack2(r[0], r[1]), pack2(r[2], r[3]), pack2(r[4], r[5]), pack2(r[6], r[7])};
      u32x4 v1 = {pack2(r[8], r[9]), pack2(r[10], r[11]), pack2(r[12], r[13]), pack2(r[14], r[15])};
      const int dcol = 32 * (2 * kh + dd);
      st16(orow + dcol, v0);
      st16(orow + dcol + 16, v1);
    }
  }
  TS(1, 2);
}

constexpr int G2_STAGE = 36864;
constexpr int G2_NST = 4;
__global__ __launch_bounds__(512, 2) void gemm2_kernel(const bf16_t* __restrict__ ctx, const bf16_t* __restrict__ wo,
                                                       const bf16_t* __restrict__ U, const bf16_t* __restrict__ w2,
                                                       float* __restrict__ part) {
  __shared__ __attribute__((aligned(16))) char smem[G2_NST * G2_STAGE];
  char LAS* lds = (char LAS*)smem;
  const int b = blockIdx.x, xcd = b & 7, c = b >> 3;
  const int ks = xcd >> 1;
  const int s0 = (xcd & 1) * 1024 + (c >> 2) * 128, m0 = (c & 3) * 160;
  const int l = threadIdx.x & 63, w = __builtin_amdgcn_readfirstlane(threadIdx.x >> 6);
  const int ws = w & 3, kh = w >> 2, r32 = l & 31, hi = l >> 5;
  const int blk = (l >> 4) & 1, q = (l & 15) >> 2, p = l & 3;
  TS(2, 0);
  int pOffA[2], pOffB[2], rOffA[3], rOffB[3];
#pragma unroll
  for (int i = 0; i < 2; ++i) {
    const int j = w * 2 + i;
    const int row = 8 * j + (l >> 3), slot = l & 7;
    pOffA[i] = (row * CTX_LD + (slot ^ ((row >> 1) & 7)) * 8) * 2;
    const int krow = 4 * j + (l >> 4), s16 = l & 15;
    pOffB[i] = (krow * S_LEN + ((s16 >> 2) ^ (krow & 3)) * 32 + (s16 & 3) * 8) * 2;
  }
#pragma unroll
  for (int i = 0; i < 3; ++i) {
    const int j = w + 8 * i;
    const int cb = j >> 2, kr = (j & 3) * 16 + (l >> 2);
    rOffA[i] = (kr * DM + cb * 32 + (l & 3) * 8) * 2;
    const int row = 8 * j + (l >> 3), slot = l & 7;
    rOffB[i] = (row * FF + (slot ^ ((row >> 1) & 7)) * 8) * 2;
  }
  const int f0 = ks * 7, nB = ks == 3 ? 6 : 7, total = 16 + nB;
  const bf16_t* PA = ctx + (size_t)s0 * CTX_LD + ks * 1024;
  const bf16_t* RA = wo + (size_t)ks * 1024 * DM + m0;
  const bf16_t* PB = w2 + (size_t)f0 * 64 * S_LEN + s0;
  const bf16_t* RB = U + (size_t)m0 * FF + f0 * 64;
  int pslot = 0, issued = 0;
  auto issue = [&]() {
    const unsigned bb = (unsigned)(uintptr_t)(lds + pslot * G2_STAGE);
    if (issued < 16) {
      const bf16_t* P = PA + issued * 64;
      const bf16_t* R = RA + (size_t)issued * 64 * DM;
      glds16(P, pOffA[0], bb + (w * 2) * 1024);
      glds16(P, pOffA[1], bb + (w * 2 + 1) * 1024);
      glds16(R, rOffA[0], bb + 16384 + w * 1024);
      glds16(R, rOffA[1], bb + 16384 + (w + 8) * 1024);
      if (w < 4) glds16(R, rOffA[2], bb + 16384 + (w + 16) * 1024);
    } else {
      const bf16_t* P = PB + (size_t)(issued - 16) * 64 * S_LEN;
      const bf16_t* R = RB + (issued - 16) * 64;
      glds16(P, pOffB[0], bb + (w * 2) * 1024);
      glds16(P, pOffB[1], bb + (w * 2 + 1) * 1024);
      glds16(R, rOffB[0], bb + 16384 + w * 1024);
      glds16(R, rOffB[1], bb + 16384 + (w + 8) * 1024);
      if (w < 4) glds16(R, rOffB[2], bb + 16384 + (w + 16) * 1024);
    }
    if (++pslot == G2_NST) pslot = 0;
    ++issued;
  };
  const int prow = 32 * ws + r32, psw = (prow >> 1) & 7;
  const int pA0 = prow * 128 + (((4 * kh + hi) ^ psw) << 4), pA1 = prow * 128 + (((4 * kh + 2 + hi) ^ psw) << 4);
  const int rA = 16384 + (32 * kh + 8 * hi + q) * 64 + blk * 32 + swapmid(p) * 8;
  const int pB = (32 * kh + 8 * hi + q) * 256 + ((ws ^ q) * 64) + blk * 32 + p * 8;
  const int rrow = rperm(r32), rsw = (rrow >> 1) & 7;
  const int rB0 = 16384 + rrow * 128 + (((4 * kh + hi) ^ rsw) << 4), rB1 = 16384 + rrow * 128 + (((4 * kh + 2 + hi) ^ rsw) << 4);
#pragma unroll
  for (int i = 0; i < G2_NST - 1; ++i) issue();
  f32x16 acc[5] = {};
  int buf = 0;
  for (int step = 0; step < 16; ++step) {
    if (w < 4) wait_vm<5 * (G2_NST - 2)>();
    else wait_vm<4 * (G2_NST - 2)>();
    BARRIER();
    issue();
    const char LAS* bp = lds + buf * G2_STAGE;
    bf16x8 pf[2], rf[2][5];
    pf[0] = *(const LAS bf16x8*)(bp + pA0);
#pragma unroll
    for (int j = 0; j < 5; ++j) rf[0][j] = cat8(tr_read(bp + rA + j * 4096), tr_read(bp + rA + j * 4096 + 256));
    pf[1] = *(const LAS bf16x8*)(bp + pA1);
#pragma unroll
    for (int j = 0; j < 5; ++j) rf[1][j] = cat8(tr_read(bp + rA + j * 4096 + 1024), tr_read(bp + rA + j * 4096 + 1024 + 256));
    __builtin_amdgcn_sched_barrier(0);
#pragma unroll
    for (int k2 = 0; k2 < 2; ++k2)
#pragma unroll
      for (int j = 0; j < 5; ++j) acc[j] = __builtin_amdgcn_mfma_f32_32x32x16_bf16(rf[k2][j], pf[k2], acc[j], 0, 0, 0);
    if (++buf == G2_NST) buf = 0;
  }
  for (int step = 16; step < total; ++step) {
    if (step + G2_NST - 1 > total) WAIT_VM0();
    else if (w < 4) wait_vm<5 * (G2_NST - 2)>();
    else wait_vm<4 * (G2_NST - 2)>();
    BARRIER();
    if (issued < total) issue();
    const char LAS* bp = lds + buf * G2_STAGE;
    bf16x8 pf[2], rf[2][5];
    pf[0] = cat8(tr_read(bp + pB), tr_read(bp + pB + 1024));
#pragma unroll
    for (int j = 0; j < 5; ++j) rf[0][j] = *(const LAS bf16x8*)(bp + rB0 + j * 4096);
    pf[1] = cat8(tr_read(bp + pB + 4096), tr_read(bp + pB + 4096 + 1024));
#pragma unroll
    for (int j = 0; j < 5; ++j) rf[1][j] = *(const LAS bf16x8*)(bp + rB1 + j * 4096);
    __builtin_amdgcn_sched_barrier(0);
#pragma unroll
    for (int k2 = 0; k2 < 2; ++k2)
#pragma unroll
      for (int j = 0; j < 5; ++j) acc[j] = __builtin_amdgcn_mfma_f32_32x32x16_bf16(rf[k2][j], pf[k2], acc[j], 0, 0, 0);
    if (++buf == G2_NST) buf = 0;
  }
  TS(2, 1);
  BARRIER();
  {
    float LAS* dst = (float LAS*)(lds + w * 12288);
    if (kh == 0) {
#pragma unroll
      for (int j = 3; j < 5; ++j)
#pragma unroll
        for (int g = 0; g < 4; ++g)
          *(f32x4 LAS*)(dst + (((j - 3) * 4 + g) * 64 + l) * 4) = (f32x4){acc[j][4 * g], acc[j][4 * g + 1], acc[j][4 * g + 2], acc[j][4 * g + 3]};
    } else {
#pragma unroll
      for (int j = 0; j < 3; ++j)
#pragma unroll
        for (int g = 0; g < 4; ++g)
          *(f32x4 LAS*)(dst + ((j * 4 + g) * 64 + l) * 4) = (f32x4){acc[j][4 * g], acc[j][4 * g + 1], acc[j][4 * g + 2], acc[j][4 * g + 3]};
    }
    asm volatile("s_waitcnt lgkmcnt(0)" ::: "memory");
    BARRIER();
    const float LAS* src = (const float LAS*)(lds + (w ^ 4) * 12288);
    float* orow = part + ((size_t)ks * S_LEN + s0 + 32 * ws + r32) * DM + m0 + 8 * hi;
    if (kh == 0) {
#pragma unroll
      for (int j = 0; j < 3; ++j)
#pragma unroll
        for (int g = 0; g < 4; ++g) {
          const f32x4 o = *(const f32x4 LAS*)(src + ((j * 4 + g) * 64 + l) * 4);
          st16f(orow + 32 * j + (g >> 1) * 16 + (g & 1) * 4, acc[j][4 * g] + o[0], acc[j][4 * g + 1] + o[1], acc[j][4 * g + 2] + o[2], acc[j][4 * g + 3] + o[3]);
        }
    } else {
#pragma unroll
      for (int j = 3; j < 5; ++j)
#pragma unroll
        for (int g = 0; g < 4; ++g) {
          const f32x4 o = *(const f32x4 LAS*)(src + (((j - 3) * 4 + g) * 64 + l) * 4);
          st16f(orow + 32 * j + (g >> 1) * 16 + (g & 1) * 4, acc[j][4 * g] + o[0], acc[j][4 * g + 1] + o[1], acc[j][4 * g + 2] + o[2], acc[j][4 * g + 3] + o[3]);
        }
    }
  }
  TS(2, 2);
}

__global__ __launch_bounds__(256) void reduce_kernel(const float* __restrict__ part, const bf16_t* __restrict__ x,
                                                     const bf16_t* __restrict__ b2, bf16_t* __restrict__ out) {
  const int i = blockIdx.x * 256 + threadIdx.x;
  const int e = i * 4;
  const int s = e / DM;
  f32x4 a = *(const f32x4*)(part + e);
#pragma unroll
  for (int sp = 1; sp < 4; ++sp) a += *(const f32x4*)(part + (size_t)sp * S_LEN * DM + e);
  const u32x2 xv = *(const u32x2*)(x + e);
  const float bb = bf2f(b2[s]);
  float r0 = __uint_as_float(xv[0] << 16) + a[0] + bb;
  float r1 = __uint_as_float(xv[0] & 0xffff0000u) + a[1] + bb;
  float r2 = __uint_as_float(xv[1] << 16) + a[2] + bb;
  float r3 = __uint_as_float(xv[1] & 0xffff0000u) + a[3] + bb;
  u32x2 o = {pack2(r0, r1), pack2(r2, r3)};
  *(u32x2*)(out + e) = o;
}

extern "C" void kernel_launch(void* const* d_in, const int* in_sizes, int n_in, void* d_out, int out_size, void* d_ws,
                              size_t ws_size, hipStream_t stream) {
  const bf16_t* x = (const bf16_t*)d_in[0];
  const bf16_t* ckv = (const bf16_t*)d_in[1];
  const bf16_t* wq = (const bf16_t*)d_in[2];
  const bf16_t* wuk = (const bf16_t*)d_in[3];
  const bf16_t* wuv = (const bf16_t*)d_in[4];
  const bf16_t* wo = (const bf16_t*)d_in[5];
  const bf16_t* w1 = (const bf16_t*)d_in[7];
  const bf16_t* b1 = (const bf16_t*)d_in[8];
  const bf16_t* w2 = (const bf16_t*)d_in[9];
  const bf16_t* b2 = (const bf16_t*)d_in[10];
  char* ws = (char*)d_ws;
  bf16_t* xn = (bf16_t*)(ws + WS_XN);
  bf16_t* h = (bf16_t*)(ws + WS_H);
  bf16_t* Q = (bf16_t*)(ws + WS_Q);
  bf16_t* K = (bf16_t*)(ws + WS_K);
  bf16_t* V = (bf16_t*)(ws + WS_V);
  bf16_t* U = (bf16_t*)(ws + WS_U);
  bf16_t* ctx = (bf16_t*)(ws + WS_CTX);
  float* part = (float*)(ws + WS_PART);
  ln_kernel<<<S_LEN / 4 + PF_BLOCKS, 256, 0, stream>>>(x, xn, h, ckv, wq, wuk, wuv, w1, wo, w2);
  gemm1_kernel<<<256, 512, 0, stream>>>(xn, h, ckv, wq, wuk, wuv, w1, b1, Q, K, V, U);
  attn_kernel<<<256, 512, 0, stream>>>(Q, K, V, ctx, wo, w2);
  gemm2_kernel<<<256, 512, 0, stream>>>(ctx, wo, U, w2, part);
  reduce_kernel<<<S_LEN * DM / 4 / 256, 256, 0, stream>>>(part, x, b2, (bf16_t*)d_out);
}
```

```cpp
#include <hip/hip_runtime.h>
#include <hip/hip_bf16.h>
#include <stdint.h>
#include <type_traits>

constexpr int S_LEN = 2048;
constexpr int DM = 640;
constexpr int NH = 32;
constexpr int HD = 128;
constexpr int DC = 512;
constexpr int FF = 1728;
constexpr int HDN = NH * HD;
constexpr int CTX_LD = HDN + 64;

typedef unsigned short bf16_t;
typedef short bf16x8 __attribute__((ext_vector_type(8)));
typedef short s16x4 __attribute__((ext_vector_type(4)));
typedef float f32x16 __attribute__((ext_vector_type(16)));
typedef float f32x4 __attribute__((ext_vector_type(4)));
typedef unsigned u32x4 __attribute__((ext_vector_type(4)));
typedef unsigned u32x2 __attribute__((ext_vector_type(2)));
typedef __bf16 bf16x2_t __attribute__((ext_vector_type(2)));
#define LAS __attribute__((address_space(3)))

__device__ __forceinline__ float bf2f(bf16_t v) { return __uint_as_float(((unsigned)v) << 16); }
__device__ __forceinline__ unsigned pack2(float a, float b) {
  bf16x2_t v = {(__bf16)a, (__bf16)b};
  return *reinterpret_cast<unsigned*>(&v);
}
__device__ __forceinline__ float rbf(float a) {
  __bf16 v = (__bf16)a;
  return __uint_as_float(((unsigned)*reinterpret_cast<unsigned short*>(&v)) << 16);
}

constexpr size_t WS_XN = 0;
constexpr size_t WS_H = WS_XN + (size_t)S_LEN * DM * 2;
constexpr size_t WS_Q = WS_H + (size_t)S_LEN * DM * 2;
constexpr size_t WS_K = WS_Q + (size_t)S_LEN * HDN * 2;
constexpr size_t WS_V = WS_K + (size_t)S_LEN * HDN * 2;
constexpr size_t WS_U = WS_V + (size_t)S_LEN * HDN * 2;
constexpr size_t WS_CTX = WS_U + (size_t)DM * FF * 2;
constexpr size_t WS_PART = WS_CTX + (size_t)S_LEN * CTX_LD * 2;
constexpr size_t WS_UP = WS_PART + (size_t)6 * S_LEN * DM * 4;

__device__ __forceinline__ float wave_sum(float v) {
#pragma unroll
  for (int o = 32; o > 0; o >>= 1) v += __shfl_xor(v, o);
  return v;
}
__device__ __forceinline__ void ln_row(float (&v)[10]) {
  float s = 0.f;
#pragma unroll
  for (int i = 0; i < 10; ++i) s += v[i];
  s = wave_sum(s);
  const float mean = s * (1.0f / DM);
  float q = 0.f;
#pragma unroll
  for (int i = 0; i < 10; ++i) { float d = v[i] - mean; q += d * d; }
  q = wave_sum(q);
  const float mb = rbf(mean);
  const float vb = rbf(q * (1.0f / DM));
  const float den = rbf(sqrtf(rbf(vb + 1e-5f)));
#pragma unroll
  for (int i = 0; i < 10; ++i) v[i] = rbf(rbf(v[i] - mb) / den);
}
__device__ __forceinline__ void touch64k(const void* base) {
  const char* p = (const char*)base + threadIdx.x * 64;
  unsigned a, b, c, d;
  asm volatile("global_load_dword %0, %4, off\n\tglobal_load_dword %1, %5, off\n\tglobal_load_dword %2, %6, off\n\t"
               "global_load_dword %3, %7, off\n\ts_waitcnt vmcnt(0)"
               : "=&v"(a), "=&v"(b), "=&v"(c), "=&v"(d)
               : "v"(p), "v"(p + 16384), "v"(p + 32768), "v"(p + 49152) : "memory");
}
#define PF_WO 0
constexpr int PF_BLOCKS = 32 + 80 + 64 + 64 + 108 + (PF_WO ? 80 + 108 : 0);
__global__ __launch_bounds__(256) void ln_kernel(const bf16_t* __restrict__ x, bf16_t* __restrict__ xn,
                                                 bf16_t* __restrict__ h, const bf16_t* __restrict__ ckv,
                                                 const bf16_t* __restrict__ wq, const bf16_t* __restrict__ wuk,
                                                 const bf16_t* __restrict__ wuv, const bf16_t* __restrict__ w1,
                                                 const bf16_t* __restrict__ wo, const bf16_t* __restrict__ w2) {
  if (blockIdx.x >= S_LEN / 4) {
    int b = blockIdx.x - S_LEN / 4;
    const bf16_t* src;
    if (b < 32) src = ckv;
    else if ((b -= 32) < 64) src = wuk;
    else if ((b -= 64) < 64) src = wuv;
    else if ((b -= 64) < 80) src = wq;
    else if ((b -= 80) < 108) src = w1;
    else if ((b -= 108) < 80) src = wo;
    else { b -= 80; src = w2; }
    touch64k((const char*)src + (size_t)b * 65536);
    return;
  }
  const int lane = threadIdx.x & 63, w = threadIdx.x >> 6;
  const int row = blockIdx.x * 4 + w;
  const unsigned* xr = reinterpret_cast<const unsigned*>(x + (size_t)row * DM);
  float v[10];
#pragma unroll
  for (int j = 0; j < 5; ++j) {
    unsigned u = xr[lane + 64 * j];
    v[2 * j] = __uint_as_float(u << 16);
    v[2 * j + 1] = __uint_as_float(u & 0xffff0000u);
  }
  ln_row(v);
  unsigned* xo = reinterpret_cast<unsigned*>(xn + (size_t)row * DM);
#pragma unroll
  for (int j = 0; j < 5; ++j) xo[lane + 64 * j] = pack2(v[2 * j], v[2 * j + 1]);
  ln_row(v);
  unsigned* ho = reinterpret_cast<unsigned*>(h + (size_t)row * DM);
#pragma unroll
  for (int j = 0; j < 5; ++j) ho[lane + 64 * j] = pack2(v[2 * j], v[2 * j + 1]);
}

constexpr int STAGE_BYTES = 49152;
constexpr int RBASE = 32768;

__device__ __forceinline__ int swapmid(int p) { return ((p & 1) << 1) | ((p >> 1) & 1); }
__device__ __forceinline__ int rperm(int r) {
  int g = r >> 2;
  int pg = (g & 4) | swapmid(g & 3);
  return pg * 4 + (r & 3);
}
__device__ __forceinline__ s16x4 tr_read(const char LAS* p) {
  return __builtin_amdgcn_ds_read_tr16_b64_v4i16((LAS s16x4*)p);
}
__device__ __forceinline__ bf16x8 cat8(s16x4 a, s16x4 b) {
  return (bf16x8){a[0], a[1], a[2], a[3], b[0], b[1], b[2], b[3]};
}

__device__ __forceinline__ void glds16(const void* sbase, int voff, unsigned lds_dst) {
  unsigned keep;
  asm volatile("s_mov_b32 %0, m0\n\ts_mov_b32 m0, %3\n\ts_nop 0\n\tglobal_load_lds_dwordx4 %1, %2\n\ts_mov_b32 m0, %0"
               : "=&s"(keep) : "v"(voff), "s"(sbase), "s"(lds_dst) : "memory");
}

__device__ __forceinline__ void glds16x4(const void* sbase, int v0, int v1, int v2, int v3, unsigned lds_dst) {
  unsigned keep;
  asm volatile("s_mov_b32 %0, m0\n\ts_mov_b32 m0, %6\n\ts_nop 0\n\tglobal_load_lds_dwordx4 %1, %5\n\t"
               "global_load_lds_dwordx4 %2, %5 offset:1024\n\tglobal_load_lds_dwordx4 %3, %5 offset:2048\n\t"
               "global_load_lds_dwordx4 %4, %5 offset:3072\n\ts_mov_b32 m0, %0"
               : "=&s"(keep) : "v"(v0), "v"(v1), "v"(v2), "v"(v3), "s"(sbase), "s"(lds_dst) : "memory");
}

#define ST_MODE 0
__device__ __forceinline__ void st16(void* p, u32x4 v) {
  *(u32x4*)p = v;
}
__device__ __forceinline__ void st16f(float* p, float a, float b, float c, float d) {
  st16(p, (u32x4){__float_as_uint(a), __float_as_uint(b), __float_as_uint(c), __float_as_uint(d)});
}

template <bool P_TR, bool R_TR, bool ROWS_P>
struct GemmCore {
  int pOff[4], rOff[2];
  int pAddr[4], rAddr[4];
  int w, l;
  __device__ __forceinline__ void init(int ldp, int ldr, int pcol_limit  ) {
    const int tid = threadIdx.x;
    l = tid & 63;
    w = __builtin_amdgcn_readfirstlane(tid >> 6);
    const int wp = w >> 1, wr = w & 1;
#pragma unroll
    for (int i = 0; i < 4; ++i) {
      const int j = w * 4 + i;
      if (!P_TR) {
        const int row = 8 * j + (l >> 3), slot = l & 7;
        const int chunk = slot ^ ((row >> 1) & 7);
        pOff[i] = row * ldp + chunk * 8;
      } else {
        const int half = j >> 4, jj = j & 15;
        const int krow = 4 * jj + (l >> 4), s16 = l & 15;
        const int c64 = (s16 >> 2) ^ (krow & 3);
        int col = half * 128 + c64 * 32 + (s16 & 3) * 8;
        if (col >= pcol_limit) col = pcol_limit - 8;
        pOff[i] = krow * ldp + col;
      }
    }
#pragma unroll
    for (int i = 0; i < 2; ++i) {
      const int j = w * 2 + i;
      if (!R_TR) {
        const int row = 8 * j + (l >> 3), slot = l & 7;
        const int chunk = slot ^ ((row >> 1) & 7);
        rOff[i] = row * ldr + chunk * 8;
      } else {
        const int krow = 4 * j + (l >> 4), s16 = l & 15;
        const int c64 = (s16 >> 2) ^ (krow & 3);
        const int col = c64 * 32 + (s16 & 3) * 8;
        rOff[i] = krow * ldr + col;
      }
    }
    const int r32 = l & 31, hi = l >> 5, blk = (l >> 4) & 1, q = (l & 15) >> 2, p = l & 3;
    if (!P_TR) {
      const int prow = 64 * wp + (ROWS_P ? rperm(r32) : r32);
      const int s = (prow >> 1) & 7;
#pragma unroll
      for (int kk = 0; kk < 4; ++kk) pAddr[kk] = prow * 128 + (((2 * kk + hi) ^ s) << 4);
    } else {
      const int half = wp >> 1, pp = ROWS_P ? swapmid(p) : p;
#pragma unroll
      for (int i = 0; i < 2; ++i) {
        const int c64 = 2 * (wp & 1) + i;
        pAddr[i] = half * 16384 + (8 * hi + q) * 256 + ((c64 ^ q) * 64) + blk * 32 + pp * 8;
      }
      pAddr[2] = pAddr[3] = 0;
    }
    if (!R_TR) {
      const int rrow = 64 * wr + (!ROWS_P ? rperm(r32) : r32);
      const int s = (rrow >> 1) & 7;
#pragma unroll
      for (int kk = 0; kk < 4; ++kk) rAddr[kk] = RBASE + rrow * 128 + (((2 * kk + hi) ^ s) << 4);
    } else {
      const int pp = !ROWS_P ? swapmid(p) : p;
#pragma unroll
      for (int i = 0; i < 2; ++i) {
        const int c64 = 2 * wr + i;
        rAddr[i] = RBASE + (8 * hi + q) * 256 + ((c64 ^ q) * 64) + blk * 32 + pp * 8;
      }
      rAddr[2] = rAddr[3] = 0;
    }
  }
  __device__ __forceinline__ void stage(char LAS* buf, const bf16_t* P, const bf16_t* R) {
    const unsigned b = (unsigned)(uintptr_t)buf;
#pragma unroll
    for (int i = 0; i < 4; ++i) glds16(P, pOff[i] * 2, b + (w * 4 + i) * 1024);
#pragma unroll
    for (int i = 0; i < 2; ++i) glds16(R, rOff[i] * 2, b + RBASE + (w * 2 + i) * 1024);
  }
  __device__ __forceinline__ void compute(const char LAS* buf, f32x16 (&acc)[2][2]) {
#pragma unroll
    for (int kk = 0; kk < 4; ++kk) {
      bf16x8 pf[2], rf[2];
#pragma unroll
      for (int i = 0; i < 2; ++i) {
        if (!P_TR) pf[i] = *(const LAS bf16x8*)(buf + pAddr[kk] + i * 4096);
        else pf[i] = cat8(tr_read(buf + pAddr[i] + kk * 4096), tr_read(buf + pAddr[i] + kk * 4096 + 1024));
        if (!R_TR) rf[i] = *(const LAS bf16x8*)(buf + rAddr[kk] + i * 4096);
        else rf[i] = cat8(tr_read(buf + rAddr[i] + kk * 4096), tr_read(buf + rAddr[i] + kk * 4096 + 1024));
      }
#pragma unroll
      for (int i = 0; i < 2; ++i)
#pragma unroll
        for (int j = 0; j < 2; ++j) {
          if (ROWS_P) acc[i][j] = __builtin_amdgcn_mfma_f32_32x32x16_bf16(pf[i], rf[j], acc[i][j], 0, 0, 0);
          else acc[i][j] = __builtin_amdgcn_mfma_f32_32x32x16_bf16(rf[j], pf[i], acc[i][j], 0, 0, 0);
        }
    }
  }
};

#define WAIT_VM0() asm volatile("s_waitcnt vmcnt(0)" ::: "memory")
#define WAIT_VM6() asm volatile("s_waitcnt vmcnt(6)" ::: "memory")
#define WAIT_VM16() asm volatile("s_waitcnt vmcnt(16)" ::: "memory")
#define BARRIER() __builtin_amdgcn_s_barrier()
#define TS(k, i)
#define PH(i, k)

__device__ __forceinline__ float gelu_tanh(float x) {
  const float y = 0.7978845608028654f * (x + 0.044715f * x * x * x);
  const float e = __expf(2.0f * y);
  const float t = 1.0f - 2.0f / (e + 1.0f);
  return 0.5f * x * (1.0f + t);
}

constexpr int STAGE256 = 32768;
#define NST256 4
struct Core256 {
  int pOff[2], rOff[2];
  int pAddr[2], rAddr[2];
  int w;
  __device__ __forceinline__ void init(int ldp, int ldr) {
    const int tid = threadIdx.x, l = tid & 63;
    w = __builtin_amdgcn_readfirstlane(tid >> 6);
    const int wp = w >> 2, wr = w & 3;
#pragma unroll
    for (int i = 0; i < 2; ++i) {
      const int j = w * 2 + i;
      const int row = 16 * j + (l >> 2), slot = l & 3;
      pOff[i] = (row * ldp + (slot ^ ((row >> 2) & 3)) * 8) * 2;
      const int half = j >> 3, jj = j & 7;
      const int krow = 4 * jj + (l >> 4), s16 = l & 15;
      const int c64 = (s16 >> 2) ^ (krow & 3);
      rOff[i] = (krow * ldr + half * 128 + c64 * 32 + (s16 & 3) * 8) * 2;
    }
    const int r32 = l & 31, hi = l >> 5, blk = (l >> 4) & 1, q = (l & 15) >> 2, p = l & 3;
    const int prow = 128 * wp + r32, s = (prow >> 2) & 3;
#pragma unroll
    for (int kk = 0; kk < 2; ++kk) pAddr[kk] = prow * 64 + (((2 * kk + hi) ^ s) << 4);
#pragma unroll
    for (int j = 0; j < 2; ++j) {
      const int c64 = 2 * (wr & 1) + j;
      rAddr[j] = 16384 + (wr >> 1) * 8192 + (8 * hi + q) * 256 + ((c64 ^ q) * 64) + blk * 32 + swapmid(p) * 8;
    }
  }
  __device__ __forceinline__ void stage(char LAS* buf, const bf16_t* P, const bf16_t* R) {
    const unsigned b = (unsigned)(uintptr_t)buf;
#pragma unroll
    for (int i = 0; i < 2; ++i) glds16(P, pOff[i], b + (w * 2 + i) * 1024);
#pragma unroll
    for (int i = 0; i < 2; ++i) glds16(R, rOff[i], b + 16384 + (w * 2 + i) * 1024);
  }
  __device__ __forceinline__ void compute(const char LAS* buf, f32x16 (&acc)[4][2]) {
#pragma unroll
    for (int kk = 0; kk < 2; ++kk) {
      bf16x8 pf[4], rf[2];
#pragma unroll
      for (int j = 0; j < 2; ++j) rf[j] = cat8(tr_read(buf + rAddr[j] + kk * 4096), tr_read(buf + rAddr[j] + kk * 4096 + 1024));
#pragma unroll
      for (int i = 0; i < 4; ++i) pf[i] = *(const LAS bf16x8*)(buf + pAddr[kk] + i * 2048);
#pragma unroll
      for (int i = 0; i < 4; ++i)
#pragma unroll
        for (int j = 0; j < 2; ++j) acc[i][j] = __builtin_amdgcn_mfma_f32_32x32x16_bf16(rf[j], pf[i], acc[i][j], 0, 0, 0);
    }
  }
  __device__ __forceinline__ void load_frags(const char LAS* buf, bf16x8 (&pf)[2][4], bf16x8 (&rf)[2][2]) {
#pragma unroll
    for (int kk = 0; kk < 2; ++kk) {
#pragma unroll
      for (int j = 0; j < 2; ++j) rf[kk][j] = cat8(tr_read(buf + rAddr[j] + kk * 4096), tr_read(buf + rAddr[j] + kk * 4096 + 1024));
#pragma unroll
      for (int i = 0; i < 4; ++i) pf[kk][i] = *(const LAS bf16x8*)(buf + pAddr[kk] + i * 2048);
    }
  }
  __device__ __forceinline__ void mma(const bf16x8 (&pf)[2][4], const bf16x8 (&rf)[2][2], f32x16 (&acc)[4][2]) {
#pragma unroll
    for (int kk = 0; kk < 2; ++kk)
#pragma unroll
      for (int i = 0; i < 4; ++i)
#pragma unroll
        for (int j = 0; j < 2; ++j) acc[i][j] = __builtin_amdgcn_mfma_f32_32x32x16_bf16(rf[kk][j], pf[kk][i], acc[i][j], 0, 0, 0);
  }
};
#define WAIT_LGKM0() asm volatile("s_waitcnt lgkmcnt(0)" ::: "memory")
#define SCHED0() __builtin_amdgcn_sched_barrier(0)
template <int N> __device__ __forceinline__ void wait_vm() { asm volatile("s_waitcnt vmcnt(%0)" ::"n"(N) : "memory"); }

struct Frag { bf16x8 pf[4], rf[2]; };
struct Desc { const char* p; unsigned stride; int v0, v1; };
template <int IMM> __device__ __forceinline__ void setm0(unsigned base) {
  asm volatile("s_add_u32 m0, %0, %1" ::"s"(base), "n"(IMM) : "memory", "scc");
}
template <int OFF> __device__ __forceinline__ void gl(const void* sbase, int voff) {
  asm volatile("global_load_lds_dwordx4 %0, %1 offset:%2" ::"v"(voff), "s"(sbase), "n"(OFF) : "memory");
}
template <int V> using IC = std::integral_constant<int, V>;
__global__ __launch_bounds__(512, 2) void gemm1_kernel(const bf16_t* __restrict__ xn, const bf16_t* __restrict__ hbuf,
                                                       const bf16_t* __restrict__ ckv, const bf16_t* __restrict__ wq,
                                                       const bf16_t* __restrict__ wuk, const bf16_t* __restrict__ wuv,
                                                       const bf16_t* __restrict__ w1, const bf16_t* __restrict__ b1,
                                                       bf16_t* __restrict__ Qo, bf16_t* __restrict__ Ko,
                                                       bf16_t* __restrict__ Vo, bf16_t* __restrict__ Uo) {
  __shared__ __attribute__((aligned(16))) char smem[4 * STAGE256 + 32768];
  char LAS* lds = (char LAS*)smem;
  const int xcd = blockIdx.x & 7, c = blockIdx.x >> 3;
  const int l = threadIdx.x & 63, w = __builtin_amdgcn_readfirstlane(threadIdx.x >> 6);
  const unsigned sW = (unsigned)(uintptr_t)lds + w * 2048;
  const int r32 = l & 31, hi = l >> 5;
  TS(0, 0);
  const bool isQ = c >= 16;
  const int mbase = (xcd * 90) >> 3, mcnt = (((xcd + 1) * 90) >> 3) - mbase;
  const bool hasMix = isQ && c - 16 < mcnt;
  const int mtile = mbase + (c - 16);
  const int d0 = (mtile % 5) * 128, f0 = (mtile / 5) * 96;
  Desc MP, MR;
  {
    int po[2], ro[2];
#pragma unroll
    for (int i = 0; i < 2; ++i) {
      const int j = w * 2 + i;
      const int krow = 4 * j + (l >> 4), s16 = l & 15;
      po[i] = (krow * DM + ((s16 >> 2) ^ (krow & 3)) * 32 + (s16 & 3) * 8) * 2 - 1024 * i;
      const int jr = j < 12 ? j : j - 4;
      const int cb = jr >> 2, kr = (jr & 3) * 16 + (l >> 2);
      ro[i] = (kr * FF + cb * 32 + (l & 3) * 8) * 2 - 1024 * i;
    }
    MP = Desc{(const char*)(hbuf + d0), 64u * DM * 2, po[0], po[1]};
    MR = Desc{(const char*)(w1 + f0), 64u * FF * 2, ro[0], ro[1]};
  }
  {
    int ntile, tb0, hp;
    const bf16_t* A; const bf16_t* W; bf16_t* O; int K;
    if (!isQ) {
      ntile = 2; tb0 = c >> 2; hp = xcd * 2 + (c & 1);
      A = ckv; K = DC;
      W = (c & 2) ? wuv : wuk; O = (c & 2) ? Vo : Ko;
    } else {
      ntile = 1; tb0 = (c - 16) >> 1; hp = xcd * 2 + (c & 1);
      A = xn; K = DM; W = wq; O = Qo;
    }
    const int ngt = K / 128;
    Core256 g;
    g.init(K, HDN);
    const char* Ap = (const char*)(A + (size_t)tb0 * 256 * K);
    const char* Wp = (const char*)(W + hp * 256);
    const int wp = w >> 2, wr = w & 3;
    constexpr unsigned RSTR = 32u * HDN * 2;
    f32x16 acc[4][2];
    Frag F0, F1;
    auto half = [&](auto first, auto dst, const Frag& cur, Frag& nxt, const char LAS* nb, int nkk, const char* sp, int v0, int v1) {
      auto mm = [&](int i, int j) {
        acc[i][j] = __builtin_amdgcn_mfma_f32_32x32x16_bf16(cur.rf[j], cur.pf[i], first ? (f32x16){} : acc[i][j], 0, 0, 0);
        SCHED0();
      };
      auto rdR = [&](int j) {
        nxt.rf[j] = cat8(tr_read(nb + g.rAddr[j] + nkk * 4096), tr_read(nb + g.rAddr[j] + nkk * 4096 + 1024));
        SCHED0();
      };
      auto rdP = [&](int i) {
        nxt.pf[i] = *(const LAS bf16x8*)(nb + g.pAddr[nkk] + i * 2048);
        SCHED0();
      };
      mm(0, 0); rdR(0);
      mm(0, 1); rdP(0); rdR(1);
      mm(1, 0); rdP(1);
      mm(1, 1); rdP(2);
      mm(2, 0); rdP(3); if (dst >= 0) setm0<(dst >= 0 ? (int)dst : 0)>(sW); SCHED0();
      mm(2, 1); if (dst >= 0) gl<0>(sp, v0); SCHED0();
      mm(3, 0); if (dst >= 0) gl<1024>(sp, v1); SCHED0();
      mm(3, 1);
    };
    auto group = [&](auto VAR, bool second, const Desc& Rc, const Desc& Pn, const Desc& Rn) {
      auto stp = [&](auto J) {
        constexpr int j = J, var = VAR;
        constexpr int dA = (var == 2 && j > 0) ? -1 : ((j + 3) & 3) * STAGE256 + 16384;
        constexpr int dB = var == 2 ? -1 : j * STAGE256;
        const char LAS* cb = lds + j * STAGE256;
        const char LAS* nb = lds + ((j + 1) & 3) * STAGE256;
        if (j == 0) half(std::integral_constant<bool, var == 0>{}, IC<dA>{}, F0, F1, cb, 1, Rc.p, Rc.v0, Rc.v1);
        else half(std::false_type{}, IC<dA>{}, F0, F1, cb, 1, Rn.p + (j - 1) * Rn.stride, Rn.v0, Rn.v1);
        if (var == 2) { if (j == 0) wait_vm<8>(); else if (j == 1) wait_vm<4>(); else if (j == 2) wait_vm<0>(); }
        else if (var == 0 && j < 2) { if (second) wait_vm<24>(); else wait_vm<8>(); }
        else wait_vm<8>();
        WAIT_LGKM0();
        SCHED0();
        BARRIER();
        SCHED0();
        half(std::false_type{}, IC<dB>{}, F1, F0, nb, 0, Pn.p + j * Pn.stride, Pn.v0, Pn.v1);
      };
      stp(IC<0>{}); stp(IC<1>{}); stp(IC<2>{}); stp(IC<3>{});
    };
    const int pv0 = g.pOff[0], pv1 = g.pOff[1] - 1024, rv0 = g.rOff[0], rv1 = g.rOff[1] - 1024;
    setm0<0>(sW); SCHED0(); asm volatile("s_nop 0"); gl<0>(Ap, pv0); gl<1024>(Ap, pv1);
    setm0<16384>(sW); SCHED0(); asm volatile("s_nop 0"); gl<0>(Wp, rv0); gl<1024>(Wp, rv1);
    setm0<STAGE256>(sW); SCHED0(); asm volatile("s_nop 0"); gl<0>(Ap + 64, pv0); gl<1024>(Ap + 64, pv1);
    setm0<STAGE256 + 16384>(sW); SCHED0(); asm volatile("s_nop 0"); gl<0>(Wp + RSTR, rv0); gl<1024>(Wp + RSTR, rv1);
    setm0<2 * STAGE256>(sW); SCHED0(); asm volatile("s_nop 0"); gl<0>(Ap + 128, pv0); gl<1024>(Ap + 128, pv1);
    setm0<2 * STAGE256 + 16384>(sW); SCHED0(); asm volatile("s_nop 0"); gl<0>(Wp + 2 * RSTR, rv0); gl<1024>(Wp + 2 * RSTR, rv1);
    setm0<3 * STAGE256>(sW); SCHED0(); asm volatile("s_nop 0"); gl<0>(Ap + 192, pv0); gl<1024>(Ap + 192, pv1);
    wait_vm<10>();
    BARRIER();
#pragma unroll
    for (int j = 0; j < 2; ++j) F0.rf[j] = cat8(tr_read(lds + g.rAddr[j]), tr_read(lds + g.rAddr[j] + 1024));
#pragma unroll
    for (int i = 0; i < 4; ++i) F0.pf[i] = *(const LAS bf16x8*)(lds + g.pAddr[0] + i * 2048);
    SCHED0();
    for (int t = 0; t < ntile; ++t) {
      const int tb = tb0 + 4 * t;
      auto run = [&](auto VAR, int gi) {
        const Desc Rc{Wp + (size_t)(4 * gi + 3) * RSTR, RSTR, rv0, rv1};
        const bool tl = gi + 1 == ngt;
        const bool sl = tl && t + 1 == ntile;
        const int gn = tl ? 0 : gi + 1;
        Desc Pn{Ap + (size_t)(tl && !sl ? t + 1 : t) * 1024 * K * 2 + gn * 256, 64u, pv0, pv1};
        Desc Rn{Wp + (size_t)(4 * gn) * RSTR, RSTR, rv0, rv1};
        if (sl && hasMix) { Pn = MP; Rn = MR; }
        SCHED0();
        group(VAR, t > 0, Rc, Pn, Rn);
      };
      run(IC<0>{}, 0);
      for (int gi = 1; gi < ngt; ++gi) run(IC<1>{}, gi);
      if (hasMix) {
        setm0<3 * STAGE256 + 16384>(sW); SCHED0(); asm volatile("s_nop 0");
        gl<0>(MR.p + 3 * MR.stride, MR.v0); gl<1024>(MR.p + 3 * MR.stride, MR.v1);
      }
      const int head = hp * 2 + (wr >> 1);
      char LAS* slab = lds + 4 * STAGE256 + w * 4096;
      const int wa = r32 * 128, wsw = r32 & 7;
      const int rrow = l >> 3, ra = rrow * 128 + (((l & 7) ^ rrow) << 4);
      bf16_t* obase = O + ((size_t)head * S_LEN + tb * 256 + 128 * wp + rrow) * HD + 64 * (wr & 1) + (l & 7) * 8;
#pragma unroll
      for (int i = 0; i < 4; ++i) {
#pragma unroll
        for (int j = 0; j < 2; ++j) {
          const f32x16 a = acc[i][j];
          u32x4 v0 = {pack2(a[0], a[1]), pack2(a[2], a[3]), pack2(a[4], a[5]), pack2(a[6], a[7])};
          u32x4 v1 = {pack2(a[8], a[9]), pack2(a[10], a[11]), pack2(a[12], a[13]), pack2(a[14], a[15])};
          *(u32x4 LAS*)(slab + wa + (((4 * j + hi) ^ wsw) << 4)) = v0;
          *(u32x4 LAS*)(slab + wa + (((4 * j + 2 + hi) ^ wsw) << 4)) = v1;
        }
#pragma unroll
        for (int it = 0; it < 4; ++it) {
          const u32x4 v = *(const u32x4 LAS*)(slab + ra + it * 1024);
          st16(obase + (size_t)(32 * i + 8 * it) * HD, v);
        }
      }
      SCHED0();
    }
  }
  TS(0, 1);
  if (!hasMix) wait_vm<(ST_MODE == 9 ? 0 : 16)>();
  if (hasMix) {
    const int ws = w & 3, kh = w >> 2;
    const int blk = (l >> 4) & 1, q = (l & 15) >> 2, p = l & 3;
    const int pB = (32 * kh + 8 * hi + q) * 256 + ((ws ^ q) * 64) + blk * 32 + p * 8;
    const int rA = 16384 + (32 * kh + 8 * hi + q) * 64 + blk * 32 + swapmid(p) * 8;
    f32x16 acc[3] = {};
    bf16x8 pa[2], ra[2][3], pb[2], rb[2][3];
    auto mstep = [&](auto VAR, auto J, const bf16x8 (&pc)[2], const bf16x8 (&rc)[2][3], bf16x8 (&pn)[2], bf16x8 (&rn)[2][3],
                     const char* sp, const char* sr) {
      constexpr int j = J, var = VAR;
      if (var == 0) { if (j < 3) wait_vm<24>(); else wait_vm<8>(); }
      else if (var == 1) wait_vm<8>();
      else { if (j == 0) wait_vm<8>(); else if (j == 1) wait_vm<4>(); else if (j == 2) wait_vm<0>(); }
      WAIT_LGKM0();
      SCHED0();
      BARRIER();
      SCHED0();
      const char LAS* nb = lds + ((j + 1) & 3) * STAGE256;
      auto mm = [&](int k2, int jj) {
        acc[jj] = __builtin_amdgcn_mfma_f32_32x32x16_bf16(rc[k2][jj], pc[k2], acc[jj], 0, 0, 0);
        SCHED0();
      };
      auto rdP = [&](int k2) { pn[k2] = cat8(tr_read(nb + pB + k2 * 4096), tr_read(nb + pB + k2 * 4096 + 1024)); SCHED0(); };
      auto rdR = [&](int k2, int jj) {
        rn[k2][jj] = cat8(tr_read(nb + rA + jj * 4096 + k2 * 1024), tr_read(nb + rA + jj * 4096 + k2 * 1024 + 256));
        SCHED0();
      };
      mm(0, 0); rdP(0); rdR(0, 0); if (var != 2) setm0<j * STAGE256>(sW); SCHED0();
      mm(0, 1); rdR(0, 1); if (var != 2) gl<0>(sp, MP.v0); SCHED0();
      mm(0, 2); rdR(0, 2); if (var != 2) gl<1024>(sp, MP.v1); SCHED0();
      mm(1, 0); rdP(1); rdR(1, 0); if (var != 2) setm0<j * STAGE256 + 16384>(sW); SCHED0();
      mm(1, 1); rdR(1, 1); if (var != 2) gl<0>(sr, MR.v0); SCHED0();
      mm(1, 2); rdR(1, 2); if (var != 2) gl<1024>(sr, MR.v1); SCHED0();
    };
    auto mgroup = [&](auto VAR, int m) {
      const char* sp = MP.p + (size_t)(m + 4) * MP.stride;
      const char* sr = MR.p + (size_t)(m + 4) * MR.stride;
      mstep(VAR, IC<0>{}, pa, ra, pb, rb, sp, sr);
      mstep(VAR, IC<1>{}, pb, rb, pa, ra, sp + MP.stride, sr + MR.stride);
      mstep(VAR, IC<2>{}, pa, ra, pb, rb, sp + 2 * MP.stride, sr + 2 * MR.stride);
      mstep(VAR, IC<3>{}, pb, rb, pa, ra, sp + 3 * MP.stride, sr + 3 * MR.stride);
    };
    wait_vm<28>();
    BARRIER();
#pragma unroll
    for (int k2 = 0; k2 < 2; ++k2) {
      pa[k2] = cat8(tr_read(lds + pB + k2 * 4096), tr_read(lds + pB + k2 * 4096 + 1024));
#pragma unroll
      for (int j = 0; j < 3; ++j) ra[k2][j] = cat8(tr_read(lds + rA + j * 4096 + k2 * 1024), tr_read(lds + rA + j * 4096 + k2 * 1024 + 256));
    }
    SCHED0();
    mgroup(IC<0>{}, 0);
    for (int m = 4; m < 28; m += 4) mgroup(IC<1>{}, m);
    mgroup(IC<2>{}, 28);
    WAIT_LGKM0();
    BARRIER();
    float LAS* dst = (float LAS*)(lds + w * 8192);
    const f32x16 sa = kh == 0 ? acc[2] : acc[0], ka = kh == 0 ? acc[0] : acc[2];
    const int so = kh == 0 ? 8 : 0;
#pragma unroll
    for (int g = 0; g < 4; ++g) *(f32x4 LAS*)(dst + (g * 64 + l) * 4) = (f32x4){sa[4 * g], sa[4 * g + 1], sa[4 * g + 2], sa[4 * g + 3]};
    float s1[8], k1[8];
#pragma unroll
    for (int e = 0; e < 8; ++e) { s1[e] = kh == 0 ? acc[1][8 + e] : acc[1][e]; k1[e] = kh == 0 ? acc[1][e] : acc[1][8 + e]; }
#pragma unroll
    for (int g = 0; g < 2; ++g) *(f32x4 LAS*)(dst + ((4 + g) * 64 + l) * 4) = (f32x4){s1[4 * g], s1[4 * g + 1], s1[4 * g + 2], s1[4 * g + 3]};
    (void)so;
    WAIT_LGKM0();
    BARRIER();
    const float LAS* src = (const float LAS*)(lds + (w ^ 4) * 8192);
    bf16_t* urow = Uo + (size_t)(d0 + 32 * ws + r32) * FF + f0 + 8 * hi;
    auto fin8 = [&](const float (&v)[8], int fo) {
      const u32x4 bv = *(const u32x4*)(b1 + f0 + 8 * hi + fo);
      float r[8];
#pragma unroll
      for (int e = 0; e < 4; ++e) {
        r[2 * e] = gelu_tanh(rbf(rbf(v[2 * e]) + __uint_as_float(bv[e] << 16)));
        r[2 * e + 1] = gelu_tanh(rbf(rbf(v[2 * e + 1]) + __uint_as_float(bv[e] & 0xffff0000u)));
      }
      st16(urow + fo, (u32x4){pack2(r[0], r[1]), pack2(r[2], r[3]), pack2(r[4], r[5]), pack2(r[6], r[7])});
    };
    const int jk = kh == 0 ? 0 : 2;
#pragma unroll
    for (int hh = 0; hh < 2; ++hh) {
      float v[8];
#pragma unroll
      for (int g = 0; g < 2; ++g) {
        const f32x4 o = *(const f32x4 LAS*)(src + ((2 * hh + g) * 64 + l) * 4);
#pragma unroll
        for (int e = 0; e < 4; ++e) v[4 * g + e] = ka[8 * hh + 4 * g + e] + o[e];
      }
      fin8(v, 32 * jk + 16 * hh);
    }
    {
      float v[8];
#pragma unroll
      for (int g = 0; g < 2; ++g) {
        const f32x4 o = *(const f32x4 LAS*)(src + ((4 + g) * 64 + l) * 4);
#pragma unroll
        for (int e = 0; e < 4; ++e) v[4 * g + e] = k1[4 * g + e] + o[e];
      }
      fin8(v, 32 + 16 * kh);
    }
  }
  TS(0, 2);
}

constexpr int AT_HALF = 16384;
__global__ __launch_bounds__(512, 2) void attn_kernel(const bf16_t* __restrict__ Q, const bf16_t* __restrict__ K,
                                                      const bf16_t* __restrict__ V, bf16_t* __restrict__ ctx,
                                                      const bf16_t* __restrict__ wo, const bf16_t* __restrict__ w2) {
  __shared__ __attribute__((aligned(16))) char smem[8 * AT_HALF + 32768];
  char LAS* lds = (char LAS*)smem;
  const int tid = threadIdx.x, l = tid & 63, w = __builtin_amdgcn_readfirstlane(tid >> 6);
  const int rg = w & 3, g = w >> 2, r32 = l & 31, hi = l >> 5;
  const int xcd = blockIdx.x & 7, jj = blockIdx.x >> 3;
  const int head = xcd * 4 + (jj >> 3), pair = jj & 7;
  const int LB = 15 - pair, nshort = 2 * pair + 2;
  const bf16_t* Kh = K + (size_t)head * S_LEN * HD;
  const bf16_t* Vh = V + (size_t)head * S_LEN * HD;
  const bf16_t* Qh = Q + (size_t)head * S_LEN * HD;
  char LAS* gl = lds + g * 4 * AT_HALF;
  int kOff[4], vOff[4];
#pragma unroll
  for (int i = 0; i < 4; ++i) {
    const int j = rg * 4 + i, row = 4 * j + (l >> 4), s16 = l & 15;
    kOff[i] = (row * HD + ((s16 ^ (row & 15)) * 8)) * 2 - 1024 * i;
    vOff[i] = (row * HD + ((((s16 >> 2) ^ (row & 3)) * 4 + (s16 & 3)) * 8)) * 2 - 1024 * i;
  }
  const unsigned glb = (unsigned)(uintptr_t)gl;
  auto htile = [&](int i) { return (g == 0 || i < nshort) ? i : i + 15 - 2 * pair; };
  auto stageK = [&](int i) {
    const bf16_t* kp = Kh + (size_t)htile(i > 16 ? 16 : i) * 64 * HD;
    const unsigned bb = glb + (i & 1) * AT_HALF + rg * 4096;
    glds16x4(kp, kOff[0], kOff[1], kOff[2], kOff[3], bb);
  };
  auto stageV = [&](int i) {
    const bf16_t* vp = Vh + (size_t)htile(i) * 64 * HD;
    const unsigned bb = glb + (2 + (i & 1)) * AT_HALF + rg * 4096;
    glds16x4(vp, vOff[0], vOff[1], vOff[2], vOff[3], bb);
  };
  const int ksw = r32 & 15;
  const int blk = (l >> 4) & 1, q4 = (l & 15) >> 2, p4 = l & 3;
  int vAddr[4];
#pragma unroll
  for (int db = 0; db < 4; ++db)
    vAddr[db] = 2 * AT_HALF + (4 * hi + q4) * 256 + ((db ^ q4) * 64) + blk * 32 + swapmid(p4) * 8;
  constexpr float CS = 0.08838834764831845f * 1.4426950408889634f;

  TS(1, 0);
  stageK(0);
  stageV(0);
  stageK(1);
  int qb = g == 0 ? LB : pair;
  bf16x8 qf[8];
#pragma unroll
  for (int d0 = 0; d0 < 8; ++d0) qf[d0] = *(const bf16x8*)(Qh + (size_t)(qb * 128 + 32 * rg + r32) * HD + d0 * 16 + hi * 8);
  char LAS* qsave = lds + 8 * AT_HALF + rg * 8192 + l * 16;
  if (g == 0) {
#pragma unroll
    for (int d0 = 0; d0 < 8; ++d0) *(bf16x8 LAS*)(qsave + d0 * 1024) = qf[d0];
  }
  float m_run = -1e30f, l_run = 0.f, alpha = 1.f;
  f32x16 o[4] = {};
  f32x16 s0, s1;
  bf16x8 pf[4];
  u32x2 pfd = {0u, 0u};
  constexpr int AT_VM = 5;
#define AT_W 6
  bf16x8 fr[32];
  const int kAddr0 = r32 * 256 + ((hi ^ ksw) << 4);
  auto rdK = [&](int k, int slot) {
    fr[k] = *(const LAS bf16x8*)(gl + slot * AT_HALF + (kAddr0 ^ ((k >> 1) << 5)) + (k & 1) * 8192);
  };
  auto rdV = [&](int k, int slot) {
    const char LAS* a = gl + slot * AT_HALF + vAddr[(k - 16) >> 2] + (k & 3) * 4096;
    fr[k] = cat8(tr_read(a), tr_read(a + 2048));
  };
  auto mm = [&](int k) {
    if (k < 16) {
      if (k & 1) s1 = __builtin_amdgcn_mfma_f32_32x32x16_bf16(fr[k], qf[k >> 1], k < 2 ? (f32x16){} : s1, 0, 0, 0);
      else s0 = __builtin_amdgcn_mfma_f32_32x32x16_bf16(fr[k], qf[k >> 1], k < 2 ? (f32x16){} : s0, 0, 0, 0);
    } else {
      o[(k - 16) >> 2] = __builtin_amdgcn_mfma_f32_32x32x16_bf16(fr[k], pf[k & 3], o[(k - 16) >> 2], 0, 0, 0);
    }
  };
  auto preK = [&](int slot) {
#pragma unroll
    for (int k = 0; k < AT_W; ++k) rdK(k, slot);
  };
  auto preV = [&](int slot) {
#pragma unroll
    for (int k = 16; k < 16 + AT_W; ++k) rdV(k, slot);
  };
  auto xhalf_max = [&](float v) {
    auto rr = __builtin_amdgcn_permlane32_swap(__float_as_uint(v), __float_as_uint(v), false, false);
    return fmaxf(__uint_as_float(rr[0]), __uint_as_float(rr[1]));
  };
  auto xhalf_sum = [&](float v) {
    auto rr = __builtin_amdgcn_permlane32_swap(__float_as_uint(v), __float_as_uint(v), false, false);
    return __uint_as_float(rr[0]) + __uint_as_float(rr[1]);
  };
  auto softmax = [&](int i) {
    if (g == 1 && i == nshort) {
      const float inv = 1.0f / xhalf_sum(l_run);
      bf16_t* orow = ctx + (size_t)(pair * 128 + 32 * rg + r32) * CTX_LD + head * HD + 8 * hi;
#pragma unroll
      for (int db = 0; db < 4; ++db) {
        const f32x16 a = o[db];
        u32x4 v0 = {pack2(a[0] * inv, a[1] * inv), pack2(a[2] * inv, a[3] * inv), pack2(a[4] * inv, a[5] * inv), pack2(a[6] * inv, a[7] * inv)};
        u32x4 v1 = {pack2(a[8] * inv, a[9] * inv), pack2(a[10] * inv, a[11] * inv), pack2(a[12] * inv, a[13] * inv), pack2(a[14] * inv, a[15] * inv)};
        st16(orow + 32 * db, v0);
        st16(orow + 32 * db + 16, v1);
        o[db] = (f32x16){};
      }
      m_run = -1e30f;
      l_run = 0.f;
      qb = LB;
    }
    if (g == 1 && i == nshort - 1) {
#pragma unroll
      for (int d0 = 0; d0 < 8; ++d0) qf[d0] = *(const bf16x8 LAS*)(qsave + d0 * 1024);
    }
    SCHED0();
    if (i < 16) stageK(i + 2);
    if (i < 16) {
      int idx = (i * 256 + (int)blockIdx.x) * 8 + w;
      if (idx >= 24064) idx -= 24064;
      const char* pb = idx < 10240 ? (const char*)wo + (size_t)idx * 512 : (const char*)w2 + (size_t)(idx - 10240) * 512;
      asm volatile("global_load_dwordx2 %0, %1, %2" : "+v"(pfd) : "v"(l * 8), "s"(pb) : "memory");
    }
    SCHED0(); PH(i, 5); SCHED0();
    const int dh = htile(i) - 2 * qb;
    if (dh >= 0) {
      const int qi = 32 * rg + r32 - 64 * dh;
#pragma unroll
      for (int r = 0; r < 16; ++r) {
        const int key = (r & 3) + 8 * (r >> 2) + 4 * hi;
        if (key > qi) s0[r] = -__builtin_inff();
        if (key + 32 > qi) s1[r] = -__builtin_inff();
      }
    }
    float pm = s0[0];
#pragma unroll
    for (int r = 1; r < 16; ++r) pm = fmaxf(pm, s0[r]);
#pragma unroll
    for (int r = 0; r < 16; ++r) pm = fmaxf(pm, s1[r]);
    pm = xhalf_max(pm);
    const float mn = fmaxf(m_run, pm);
    alpha = __builtin_amdgcn_exp2f((m_run - mn) * CS);
    m_run = mn;
    const float mc = -mn * CS;
    SCHED0(); PH(i, 6); SCHED0();
    float ps = 0.f;
#pragma unroll
    for (int r = 0; r < 16; ++r) { s0[r] = __builtin_amdgcn_exp2f(fmaf(s0[r], CS, mc)); ps += s0[r]; }
#pragma unroll
    for (int r = 0; r < 16; ++r) { s1[r] = __builtin_amdgcn_exp2f(fmaf(s1[r], CS, mc)); ps += s1[r]; }
    l_run = l_run * alpha + ps;
    SCHED0(); PH(i, 7); SCHED0();
    u32x4 a0 = {pack2(s0[0], s0[1]), pack2(s0[2], s0[3]), pack2(s0[4], s0[5]), pack2(s0[6], s0[7])};
    u32x4 a1 = {pack2(s0[8], s0[9]), pack2(s0[10], s0[11]), pack2(s0[12], s0[13]), pack2(s0[14], s0[15])};
    u32x4 a2 = {pack2(s1[0], s1[1]), pack2(s1[2], s1[3]), pack2(s1[4], s1[5]), pack2(s1[6], s1[7])};
    u32x4 a3 = {pack2(s1[8], s1[9]), pack2(s1[10], s1[11]), pack2(s1[12], s1[13]), pack2(s1[14], s1[15])};
    pf[0] = *(bf16x8*)&a0; pf[1] = *(bf16x8*)&a1; pf[2] = *(bf16x8*)&a2; pf[3] = *(bf16x8*)&a3;
    asm volatile("" : "+v"(pf[0]), "+v"(pf[1]), "+v"(pf[2]), "+v"(pf[3]), "+v"(l_run), "+v"(alpha));
  };
#define AT_DK0 16
  WAIT_VM0();
  WAIT_LGKM0();
  BARRIER();
  TS(1, 3);
  if (g == 1) BARRIER();
  SCHED0();
  preK(0);
#pragma unroll
  for (int k = 0; k < 16; ++k) {
    if (k + AT_W < 16) rdK(k + AT_W, 0);
    mm(k);
    SCHED0();
  }
  BARRIER();
  for (int i = 0; i < 16; ++i) {
    SCHED0();
    PH(i, 0);
    softmax(i);
    SCHED0();
    const int ks = (i + 1) & 1, vs = i & 1;
    preK(ks);
    SCHED0();
    wait_vm<AT_VM>();
    PH(i, 1);
    BARRIER();
    SCHED0();
    PH(i, 2);
    __builtin_amdgcn_s_setprio(1);
#pragma unroll
    for (int k = 0; k < 32; ++k) {
      if (k + AT_W < 16) rdK(k + AT_W, ks);
      else if (k + AT_W < 32) rdV(k + AT_W, vs);
      mm(k);
      if (k < 16) {
#pragma unroll
        for (int e = 0; e < 4; ++e) {
          float t = o[k >> 2][4 * (k & 3) + e];
          asm volatile("v_mul_f32 %0, %1, %0" : "+v"(t) : "v"(alpha));
          o[k >> 2][4 * (k & 3) + e] = t;
        }
      }
      if (k == AT_DK0) stageV(i + 1);
      SCHED0();
    }
    __builtin_amdgcn_s_setprio(0);
    PH(i, 3);
    wait_vm<AT_VM>();
    PH(i, 4);
    BARRIER();
  }
  SCHED0();
  softmax(16);
#pragma unroll
  for (int db = 0; db < 4; ++db)
#pragma unroll
    for (int r = 0; r < 16; ++r) o[db][r] *= alpha;
  SCHED0();
  WAIT_VM0();
  asm volatile("" ::"v"(pfd));
  BARRIER();
  SCHED0();
  preV(0);
#pragma unroll
  for (int k = 16; k < 32; ++k) {
    if (k + AT_W < 32) rdV(k + AT_W, 0);
    mm(k);
    SCHED0();
  }
  BARRIER();
  if (g == 0) BARRIER();
  l_run = xhalf_sum(l_run);
  TS(1, 1);
  {
    const int kh = g;
    float LAS* ml = (float LAS*)(lds + 4 * AT_HALF);
    float LAS* dst = (float LAS*)(lds + w * 8192);
#pragma unroll
    for (int dd = 0; dd < 2; ++dd) {
      f32x16 a;
#pragma unroll
      for (int r = 0; r < 16; ++r) a[r] = kh == 0 ? o[2 + dd][r] : o[dd][r];
#pragma unroll
      for (int gg = 0; gg < 4; ++gg)
        *(f32x4 LAS*)(dst + ((dd * 4 + gg) * 64 + l) * 4) = (f32x4){a[4 * gg], a[4 * gg + 1], a[4 * gg + 2], a[4 * gg + 3]};
    }
    ml[w * 128 + l] = m_run;
    ml[w * 128 + 64 + l] = l_run;
    asm volatile("s_waitcnt lgkmcnt(0)" ::: "memory");
    BARRIER();
    const int wo = w ^ 4;
    const float m2 = ml[wo * 128 + l], l2 = ml[wo * 128 + 64 + l];
    const float mt = fmaxf(m_run, m2);
    const float a1 = __builtin_amdgcn_exp2f((m_run - mt) * CS), a2 = __builtin_amdgcn_exp2f((m2 - mt) * CS);
    const float inv = 1.0f / (l_run * a1 + l2 * a2);
    const float f1 = a1 * inv, f2 = a2 * inv;
    const float LAS* src = (const float LAS*)(lds + wo * 8192);
    bf16_t* orow = ctx + (size_t)(LB * 128 + 32 * rg + r32) * CTX_LD + head * HD + 8 * hi;
#pragma unroll
    for (int dd = 0; dd < 2; ++dd) {
      f32x16 a;
#pragma unroll
      for (int r = 0; r < 16; ++r) a[r] = kh == 0 ? o[dd][r] : o[2 + dd][r];
      float r[16];
#pragma unroll
      for (int gg = 0; gg < 4; ++gg) {
        const f32x4 b = *(const f32x4 LAS*)(src + ((dd * 4 + gg) * 64 + l) * 4);
#pragma unroll
        for (int e = 0; e < 4; ++e) r[4 * gg + e] = a[4 * gg + e] * f1 + b[e] * f2;
      }
      u32x4 v0 = {pack2(r[0], r[1]), pack2(r[2], r[3]), pack2(r[4], r[5]), pack2(r[6], r[7])};
      u32x4 v1 = {pack2(r[8], r[9]), pack2(r[10], r[11]), pack2(r[12], r[13]), pack2(r[14], r[15])};
      const int dcol = 32 * (2 * kh + dd);
      st16(orow + dcol, v0);
      st16(orow + dcol + 16, v1);
    }
  }
  TS(1, 2);
}

constexpr int G2_STAGE = 36864;
constexpr int G2_NST = 4;
__global__ __launch_bounds__(512, 2) void gemm2_kernel(const bf16_t* __restrict__ ctx, const bf16_t* __restrict__ wo,
                                                       const bf16_t* __restrict__ U, const bf16_t* __restrict__ w2,
                                                       float* __restrict__ part) {
  __shared__ __attribute__((aligned(16))) char smem[G2_NST * G2_STAGE];
  char LAS* lds = (char LAS*)smem;
  const int b = blockIdx.x, xcd = b & 7, c = b >> 3;
  const int ks = xcd >> 1;
  const int s0 = (xcd & 1) * 1024 + (c >> 2) * 128, m0 = (c & 3) * 160;
  const int l = threadIdx.x & 63, w = __builtin_amdgcn_readfirstlane(threadIdx.x >> 6);
  const int ws = w & 3, kh = w >> 2, r32 = l & 31, hi = l >> 5;
  const int blk = (l >> 4) & 1, q = (l & 15) >> 2, p = l & 3;
  TS(2, 0);
  int pOffA[2], pOffB[2], rOffA[3], rOffB[3];
#pragma unroll
  for (int i = 0; i < 2; ++i) {
    const int j = w * 2 + i;
    const int row = 8 * j + (l >> 3), slot = l & 7;
    pOffA[i] = (row * CTX_LD + (slot ^ ((row >> 1) & 7)) * 8) * 2;
    const int krow = 4 * j + (l >> 4), s16 = l & 15;
    pOffB[i] = (krow * S_LEN + ((s16 >> 2) ^ (krow & 3)) * 32 + (s16 & 3) * 8) * 2;
  }
#pragma unroll
  for (int i = 0; i < 3; ++i) {
    const int j = w + 8 * i;
    const int cb = j >> 2, kr = (j & 3) * 16 + (l >> 2);
    rOffA[i] = (kr * DM + cb * 32 + (l & 3) * 8) * 2;
    const int row = 8 * j + (l >> 3), slot = l & 7;
    rOffB[i] = (row * FF + (slot ^ ((row >> 1) & 7)) * 8) * 2;
  }
  const int f0 = ks * 7, nB = ks == 3 ? 6 : 7, total = 16 + nB;
  const bf16_t* PA = ctx + (size_t)s0 * CTX_LD + ks * 1024;
  const bf16_t* RA = wo + (size_t)ks * 1024 * DM + m0;
  const bf16_t* PB = w2 + (size_t)f0 * 64 * S_LEN + s0;
  const bf16_t* RB = U + (size_t)m0 * FF + f0 * 64;
  int pslot = 0, issued = 0;
  auto issue = [&]() {
    const unsigned bb = (unsigned)(uintptr_t)(lds + pslot * G2_STAGE);
    if (issued < 16) {
      const bf16_t* P = PA + issued * 64;
      const bf16_t* R = RA + (size_t)issued * 64 * DM;
      glds16(P, pOffA[0], bb + (w * 2) * 1024);
      glds16(P, pOffA[1], bb + (w * 2 + 1) * 1024);
      glds16(R, rOffA[0], bb + 16384 + w * 1024);
      glds16(R, rOffA[1], bb + 16384 + (w + 8) * 1024);
      if (w < 4) glds16(R, rOffA[2], bb + 16384 + (w + 16) * 1024);
    } else {
      const bf16_t* P = PB + (size_t)(issued - 16) * 64 * S_LEN;
      const bf16_t* R = RB + (issued - 16) * 64;
      glds16(P, pOffB[0], bb + (w * 2) * 1024);
      glds16(P, pOffB[1], bb + (w * 2 + 1) * 1024);
      glds16(R, rOffB[0], bb + 16384 + w * 1024);
      glds16(R, rOffB[1], bb + 16384 + (w + 8) * 1024);
      if (w < 4) glds16(R, rOffB[2], bb + 16384 + (w + 16) * 1024);
    }
    if (++pslot == G2_NST) pslot = 0;
    ++issued;
  };
  const int prow = 32 * ws + r32, psw = (prow >> 1) & 7;
  const int pA0 = prow * 128 + (((4 * kh + hi) ^ psw) << 4), pA1 = prow * 128 + (((4 * kh + 2 + hi) ^ psw) << 4);
  const int rA = 16384 + (32 * kh + 8 * hi + q) * 64 + blk * 32 + swapmid(p) * 8;
  const int pB = (32 * kh + 8 * hi + q) * 256 + ((ws ^ q) * 64) + blk * 32 + p * 8;
  const int rrow = rperm(r32), rsw = (rrow >> 1) & 7;
  const int rB0 = 16384 + rrow * 128 + (((4 * kh + hi) ^ rsw) << 4), rB1 = 16384 + rrow * 128 + (((4 * kh + 2 + hi) ^ rsw) << 4);
#pragma unroll
  for (int i = 0; i < G2_NST - 1; ++i) issue();
  f32x16 acc[5] = {};
  int buf = 0;
  for (int step = 0; step < 16; ++step) {
    if (w < 4) wait_vm<5 * (G2_NST - 2)>();
    else wait_vm<4 * (G2_NST - 2)>();
    BARRIER();
    issue();
    const char LAS* bp = lds + buf * G2_STAGE;
    bf16x8 pf[2], rf[2][5];
    pf[0] = *(const LAS bf16x8*)(bp + pA0);
#pragma unroll
    for (int j = 0; j < 5; ++j) rf[0][j] = cat8(tr_read(bp + rA + j * 4096), tr_read(bp + rA + j * 4096 + 256));
    pf[1] = *(const LAS bf16x8*)(bp + pA1);
#pragma unroll
    for (int j = 0; j < 5; ++j) rf[1][j] = cat8(tr_read(bp + rA + j * 4096 + 1024), tr_read(bp + rA + j * 4096 + 1024 + 256));
    __builtin_amdgcn_sched_barrier(0);
#pragma unroll
    for (int k2 = 0; k2 < 2; ++k2)
#pragma unroll
      for (int j = 0; j < 5; ++j) acc[j] = __builtin_amdgcn_mfma_f32_32x32x16_bf16(rf[k2][j], pf[k2], acc[j], 0, 0, 0);
    if (++buf == G2_NST) buf = 0;
  }
  for (int step = 16; step < total; ++step) {
    if (step + G2_NST - 1 > total) WAIT_VM0();
    else if (w < 4) wait_vm<5 * (G2_NST - 2)>();
    else wait_vm<4 * (G2_NST - 2)>();
    BARRIER();
    if (issued < total) issue();
    const char LAS* bp = lds + buf * G2_STAGE;
    bf16x8 pf[2], rf[2][5];
    pf[0] = cat8(tr_read(bp + pB), tr_read(bp + pB + 1024));
#pragma unroll
    for (int j = 0; j < 5; ++j) rf[0][j] = *(const LAS bf16x8*)(bp + rB0 + j * 4096);
    pf[1] = cat8(tr_read(bp + pB + 4096), tr_read(bp + pB + 4096 + 1024));
#pragma unroll
    for (int j = 0; j < 5; ++j) rf[1][j] = *(const LAS bf16x8*)(bp + rB1 + j * 4096);
    __builtin_amdgcn_sched_barrier(0);
#pragma unroll
    for (int k2 = 0; k2 < 2; ++k2)
#pragma unroll
      for (int j = 0; j < 5; ++j) acc[j] = __builtin_amdgcn_mfma_f32_32x32x16_bf16(rf[k2][j], pf[k2], acc[j], 0, 0, 0);
    if (++buf == G2_NST) buf = 0;
  }
  TS(2, 1);
  BARRIER();
  {
    float LAS* dst = (float LAS*)(lds + w * 12288);
    if (kh == 0) {
#pragma unroll
      for (int j = 3; j < 5; ++j)
#pragma unroll
        for (int g = 0; g < 4; ++g)
          *(f32x4 LAS*)(dst + (((j - 3) * 4 + g) * 64 + l) * 4) = (f32x4){acc[j][4 * g], acc[j][4 * g + 1], acc[j][4 * g + 2], acc[j][4 * g + 3]};
    } else {
#pragma unroll
      for (int j = 0; j < 3; ++j)
#pragma unroll
        for (int g = 0; g < 4; ++g)
          *(f32x4 LAS*)(dst + ((j * 4 + g) * 64 + l) * 4) = (f32x4){acc[j][4 * g], acc[j][4 * g + 1], acc[j][4 * g + 2], acc[j][4 * g + 3]};
    }
    asm volatile("s_waitcnt lgkmcnt(0)" ::: "memory");
    BARRIER();
    const float LAS* src = (const float LAS*)(lds + (w ^ 4) * 12288);
    float* orow = part + ((size_t)ks * S_LEN + s0 + 32 * ws + r32) * DM + m0 + 8 * hi;
    if (kh == 0) {
#pragma unroll
      for (int j = 0; j < 3; ++j)
#pragma unroll
        for (int g = 0; g < 4; ++g) {
          const f32x4 o = *(const f32x4 LAS*)(src + ((j * 4 + g) * 64 + l) * 4);
          st16f(orow + 32 * j + (g >> 1) * 16 + (g & 1) * 4, acc[j][4 * g] + o[0], acc[j][4 * g + 1] + o[1], acc[j][4 * g + 2] + o[2], acc[j][4 * g + 3] + o[3]);
        }
    } else {
#pragma unroll
      for (int j = 3; j < 5; ++j)
#pragma unroll
        for (int g = 0; g < 4; ++g) {
          const f32x4 o = *(const f32x4 LAS*)(src + (((j - 3) * 4 + g) * 64 + l) * 4);
          st16f(orow + 32 * j + (g >> 1) * 16 + (g & 1) * 4, acc[j][4 * g] + o[0], acc[j][4 * g + 1] + o[1], acc[j][4 * g + 2] + o[2], acc[j][4 * g + 3] + o[3]);
        }
    }
  }
  TS(2, 2);
}

__global__ __launch_bounds__(256) void reduce_kernel(const float* __restrict__ part, const bf16_t* __restrict__ x,
                                                     const bf16_t* __restrict__ b2, bf16_t* __restrict__ out) {
  const int i = blockIdx.x * 256 + threadIdx.x;
  const int e = i * 4;
  const int s = e / DM;
  f32x4 a = *(const f32x4*)(part + e);
#pragma unroll
  for (int sp = 1; sp < 4; ++sp) a += *(const f32x4*)(part + (size_t)sp * S_LEN * DM + e);
  const u32x2 xv = *(const u32x2*)(x + e);
  const float bb = bf2f(b2[s]);
  float r0 = __uint_as_float(xv[0] << 16) + a[0] + bb;
  float r1 = __uint_as_float(xv[0] & 0xffff0000u) + a[1] + bb;
  float r2 = __uint_as_float(xv[1] << 16) + a[2] + bb;
  float r3 = __uint_as_float(xv[1] & 0xffff0000u) + a[3] + bb;
  u32x2 o = {pack2(r0, r1), pack2(r2, r3)};
  *(u32x2*)(out + e) = o;
}

extern "C" void kernel_launch(void* const* d_in, const int* in_sizes, int n_in, void* d_out, int out_size, void* d_ws,
                              size_t ws_size, hipStream_t stream) {
  const bf16_t* x = (const bf16_t*)d_in[0];
  const bf16_t* ckv = (const bf16_t*)d_in[1];
  const bf16_t* wq = (const bf16_t*)d_in[2];
  const bf16_t* wuk = (const bf16_t*)d_in[3];
  const bf16_t* wuv = (const bf16_t*)d_in[4];
  const bf16_t* wo = (const bf16_t*)d_in[5];
  const bf16_t* w1 = (const bf16_t*)d_in[7];
  const bf16_t* b1 = (const bf16_t*)d_in[8];
  const bf16_t* w2 = (const bf16_t*)d_in[9];
  const bf16_t* b2 = (const bf16_t*)d_in[10];
  char* ws = (char*)d_ws;
  bf16_t* xn = (bf16_t*)(ws + WS_XN);
  bf16_t* h = (bf16_t*)(ws + WS_H);
  bf16_t* Q = (bf16_t*)(ws + WS_Q);
  bf16_t* K = (bf16_t*)(ws + WS_K);
  bf16_t* V = (bf16_t*)(ws + WS_V);
  bf16_t* U = (bf16_t*)(ws + WS_U);
  bf16_t* ctx = (bf16_t*)(ws + WS_CTX);
  float* part = (float*)(ws + WS_PART);
  ln_kernel<<<S_LEN / 4 + PF_BLOCKS, 256, 0, stream>>>(x, xn, h, ckv, wq, wuk, wuv, w1, wo, w2);
  gemm1_kernel<<<256, 512, 0, stream>>>(xn, h, ckv, wq, wuk, wuv, w1, b1, Q, K, V, U);
  attn_kernel<<<256, 512, 0, stream>>>(Q, K, V, ctx, wo, w2);
  gemm2_kernel<<<256, 512, 0, stream>>>(ctx, wo, U, w2, part);
  reduce_kernel<<<S_LEN * DM / 4 / 256, 256, 0, stream>>>(part, x, b2, (bf16_t*)d_out);
}
```

```cpp
#include <hip/hip_runtime.h>
#include <hip/hip_bf16.h>
#include <stdint.h>
#include <type_traits>

constexpr int S_LEN = 2048;
constexpr int DM = 640;
constexpr int NH = 32;
constexpr int HD = 128;
constexpr int DC = 512;
constexpr int FF = 1728;
constexpr int HDN = NH * HD;
constexpr int CTX_LD = HDN + 64;

typedef unsigned short bf16_t;
typedef short bf16x8 __attribute__((ext_vector_type(8)));
typedef short s16x4 __attribute__((ext_vector_type(4)));
typedef float f32x16 __attribute__((ext_vector_type(16)));
typedef float f32x4 __attribute__((ext_vector_type(4)));
typedef unsigned u32x4 __attribute__((ext_vector_type(4)));
typedef unsigned u32x2 __attribute__((ext_vector_type(2)));
typedef __bf16 bf16x2_t __attribute__((ext_vector_type(2)));
#define LAS __attribute__((address_space(3)))

__device__ __forceinline__ float bf2f(bf16_t v) { return __uint_as_float(((unsigned)v) << 16); }
__device__ __forceinline__ unsigned pack2(float a, float b) {
  bf16x2_t v = {(__bf16)a, (__bf16)b};
  return *reinterpret_cast<unsigned*>(&v);
}
__device__ __forceinline__ float rbf(float a) {
  __bf16 v = (__bf16)a;
  return __uint_as_float(((unsigned)*reinterpret_cast<unsigned short*>(&v)) << 16);
}

constexpr size_t WS_XN = 0;
constexpr size_t WS_H = WS_XN + (size_t)S_LEN * DM * 2;
constexpr size_t WS_Q = WS_H + (size_t)S_LEN * DM * 2;
constexpr size_t WS_K = WS_Q + (size_t)S_LEN * HDN * 2;
constexpr size_t WS_V = WS_K + (size_t)S_LEN * HDN * 2;
constexpr size_t WS_U = WS_V + (size_t)S_LEN * HDN * 2;
constexpr size_t WS_CTX = WS_U + (size_t)DM * FF * 2;
constexpr size_t WS_PART = WS_CTX + (size_t)S_LEN * CTX_LD * 2;
constexpr size_t WS_UP = WS_PART + (size_t)6 * S_LEN * DM * 4;

__device__ __forceinline__ float wave_sum(float v) {
#pragma unroll
  for (int o = 32; o > 0; o >>= 1) v += __shfl_xor(v, o);
  return v;
}
__device__ __forceinline__ void ln_row(float (&v)[10]) {
  float s = 0.f;
#pragma unroll
  for (int i = 0; i < 10; ++i) s += v[i];
  s = wave_sum(s);
  const float mean = s * (1.0f / DM);
  float q = 0.f;
#pragma unroll
  for (int i = 0; i < 10; ++i) { float d = v[i] - mean; q += d * d; }
  q = wave_sum(q);
  const float mb = rbf(mean);
  const float vb = rbf(q * (1.0f / DM));
  const float den = rbf(sqrtf(rbf(vb + 1e-5f)));
#pragma unroll
  for (int i = 0; i < 10; ++i) v[i] = rbf(rbf(v[i] - mb) / den);
}
constexpr int STAGE_BYTES = 49152;
constexpr int RBASE = 32768;

__device__ __forceinline__ int swapmid(int p) { return ((p & 1) << 1) | ((p >> 1) & 1); }
__device__ __forceinline__ int rperm(int r) {
  int g = r >> 2;
  int pg = (g & 4) | swapmid(g & 3);
  return pg * 4 + (r & 3);
}
__device__ __forceinline__ s16x4 tr_read(const char LAS* p) {
  return __builtin_amdgcn_ds_read_tr16_b64_v4i16((LAS s16x4*)p);
}
__device__ __forceinline__ bf16x8 cat8(s16x4 a, s16x4 b) {
  return (bf16x8){a[0], a[1], a[2], a[3], b[0], b[1], b[2], b[3]};
}

__device__ __forceinline__ void glds16(const void* sbase, int voff, unsigned lds_dst) {
  unsigned keep;
  asm volatile("s_mov_b32 %0, m0\n\ts_mov_b32 m0, %3\n\ts_nop 0\n\tglobal_load_lds_dwordx4 %1, %2\n\ts_mov_b32 m0, %0"
               : "=&s"(keep) : "v"(voff), "s"(sbase), "s"(lds_dst) : "memory");
}

__device__ __forceinline__ void glds16x4(const void* sbase, int v0, int v1, int v2, int v3, unsigned lds_dst) {
  unsigned keep;
  asm volatile("s_mov_b32 %0, m0\n\ts_mov_b32 m0, %6\n\ts_nop 0\n\tglobal_load_lds_dwordx4 %1, %5\n\t"
               "global_load_lds_dwordx4 %2, %5 offset:1024\n\tglobal_load_lds_dwordx4 %3, %5 offset:2048\n\t"
               "global_load_lds_dwordx4 %4, %5 offset:3072\n\ts_mov_b32 m0, %0"
               : "=&s"(keep) : "v"(v0), "v"(v1), "v"(v2), "v"(v3), "s"(sbase), "s"(lds_dst) : "memory");
}

#define ST_MODE 0
__device__ __forceinline__ void st16(void* p, u32x4 v) {
  *(u32x4*)p = v;
}
__device__ __forceinline__ void st16f(float* p, float a, float b, float c, float d) {
  st16(p, (u32x4){__float_as_uint(a), __float_as_uint(b), __float_as_uint(c), __float_as_uint(d)});
}

template <bool P_TR, bool R_TR, bool ROWS_P>
struct GemmCore {
  int pOff[4], rOff[2];
  int pAddr[4], rAddr[4];
  int w, l;
  __device__ __forceinline__ void init(int ldp, int ldr, int pcol_limit  ) {
    const int tid = threadIdx.x;
    l = tid & 63;
    w = __builtin_amdgcn_readfirstlane(tid >> 6);
    const int wp = w >> 1, wr = w & 1;
#pragma unroll
    for (int i = 0; i < 4; ++i) {
      const int j = w * 4 + i;
      if (!P_TR) {
        const int row = 8 * j + (l >> 3), slot = l & 7;
        const int chunk = slot ^ ((row >> 1) & 7);
        pOff[i] = row * ldp + chunk * 8;
      } else {
        const int half = j >> 4, jj = j & 15;
        const int krow = 4 * jj + (l >> 4), s16 = l & 15;
        const int c64 = (s16 >> 2) ^ (krow & 3);
        int col = half * 128 + c64 * 32 + (s16 & 3) * 8;
        if (col >= pcol_limit) col = pcol_limit - 8;
        pOff[i] = krow * ldp + col;
      }
    }
#pragma unroll
    for (int i = 0; i < 2; ++i) {
      const int j = w * 2 + i;
      if (!R_TR) {
        const int row = 8 * j + (l >> 3), slot = l & 7;
        const int chunk = slot ^ ((row >> 1) & 7);
        rOff[i] = row * ldr + chunk * 8;
      } else {
        const int krow = 4 * j + (l >> 4), s16 = l & 15;
        const int c64 = (s16 >> 2) ^ (krow & 3);
        const int col = c64 * 32 + (s16 & 3) * 8;
        rOff[i] = krow * ldr + col;
      }
    }
    const int r32 = l & 31, hi = l >> 5, blk = (l >> 4) & 1, q = (l & 15) >> 2, p = l & 3;
    if (!P_TR) {
      const int prow = 64 * wp + (ROWS_P ? rperm(r32) : r32);
      const int s = (prow >> 1) & 7;
#pragma unroll
      for (int kk = 0; kk < 4; ++kk) pAddr[kk] = prow * 128 + (((2 * kk + hi) ^ s) << 4);
    } else {
      const int half = wp >> 1, pp = ROWS_P ? swapmid(p) : p;
#pragma unroll
      for (int i = 0; i < 2; ++i) {
        const int c64 = 2 * (wp & 1) + i;
        pAddr[i] = half * 16384 + (8 * hi + q) * 256 + ((c64 ^ q) * 64) + blk * 32 + pp * 8;
      }
      pAddr[2] = pAddr[3] = 0;
    }
    if (!R_TR) {
      const int rrow = 64 * wr + (!ROWS_P ? rperm(r32) : r32);
      const int s = (rrow >> 1) & 7;
#pragma unroll
      for (int kk = 0; kk < 4; ++kk) rAddr[kk] = RBASE + rrow * 128 + (((2 * kk + hi) ^ s) << 4);
    } else {
      const int pp = !ROWS_P ? swapmid(p) : p;
#pragma unroll
      for (int i = 0; i < 2; ++i) {
        const int c64 = 2 * wr + i;
        rAddr[i] = RBASE + (8 * hi + q) * 256 + ((c64 ^ q) * 64) + blk * 32 + pp * 8;
      }
      rAddr[2] = rAddr[3] = 0;
    }
  }
  __device__ __forceinline__ void stage(char LAS* buf, const bf16_t* P, const bf16_t* R) {
    const unsigned b = (unsigned)(uintptr_t)buf;
#pragma unroll
    for (int i = 0; i < 4; ++i) glds16(P, pOff[i] * 2, b + (w * 4 + i) * 1024);
#pragma unroll
    for (int i = 0; i < 2; ++i) glds16(R, rOff[i] * 2, b + RBASE + (w * 2 + i) * 1024);
  }
  __device__ __forceinline__ void compute(const char LAS* buf, f32x16 (&acc)[2][2]) {
#pragma unroll
    for (int kk = 0; kk < 4; ++kk) {
      bf16x8 pf[2], rf[2];
#pragma unroll
      for (int i = 0; i < 2; ++i) {
        if (!P_TR) pf[i] = *(const LAS bf16x8*)(buf + pAddr[kk] + i * 4096);
        else pf[i] = cat8(tr_read(buf + pAddr[i] + kk * 4096), tr_read(buf + pAddr[i] + kk * 4096 + 1024));
        if (!R_TR) rf[i] = *(const LAS bf16x8*)(buf + rAddr[kk] + i * 4096);
        else rf[i] = cat8(tr_read(buf + rAddr[i] + kk * 4096), tr_read(buf + rAddr[i] + kk * 4096 + 1024));
      }
#pragma unroll
      for (int i = 0; i < 2; ++i)
#pragma unroll
        for (int j = 0; j < 2; ++j) {
          if (ROWS_P) acc[i][j] = __builtin_amdgcn_mfma_f32_32x32x16_bf16(pf[i], rf[j], acc[i][j], 0, 0, 0);
          else acc[i][j] = __builtin_amdgcn_mfma_f32_32x32x16_bf16(rf[j], pf[i], acc[i][j], 0, 0, 0);
        }
    }
  }
};

#define WAIT_VM0() asm volatile("s_waitcnt vmcnt(0)" ::: "memory")
#define WAIT_VM6() asm volatile("s_waitcnt vmcnt(6)" ::: "memory")
#define WAIT_VM16() asm volatile("s_waitcnt vmcnt(16)" ::: "memory")
#define BARRIER() __builtin_amdgcn_s_barrier()
#define TS(k, i)
#define PH(i, k)

__device__ __forceinline__ float gelu_tanh(float x) {
  const float y = 0.7978845608028654f * (x + 0.044715f * x * x * x);
  const float e = __expf(2.0f * y);
  const float t = 1.0f - 2.0f / (e + 1.0f);
  return 0.5f * x * (1.0f + t);
}

constexpr int STAGE256 = 32768;
#define NST256 4
struct Core256 {
  int pOff[2], rOff[2];
  int pAddr[2], rAddr[2];
  int w;
  __device__ __forceinline__ void init(int ldp, int ldr) {
    const int tid = threadIdx.x, l = tid & 63;
    w = __builtin_amdgcn_readfirstlane(tid >> 6);
    const int wp = w >> 2, wr = w & 3;
#pragma unroll
    for (int i = 0; i < 2; ++i) {
      const int j = w * 2 + i;
      const int row = 16 * j + (l >> 2), slot = l & 3;
      pOff[i] = (row * ldp + (slot ^ ((row >> 2) & 3)) * 8) * 2;
      const int half = j >> 3, jj = j & 7;
      const int krow = 4 * jj + (l >> 4), s16 = l & 15;
      const int c64 = (s16 >> 2) ^ (krow & 3);
      rOff[i] = (krow * ldr + half * 128 + c64 * 32 + (s16 & 3) * 8) * 2;
    }
    const int r32 = l & 31, hi = l >> 5, blk = (l >> 4) & 1, q = (l & 15) >> 2, p = l & 3;
    const int prow = 128 * wp + r32, s = (prow >> 2) & 3;
#pragma unroll
    for (int kk = 0; kk < 2; ++kk) pAddr[kk] = prow * 64 + (((2 * kk + hi) ^ s) << 4);
#pragma unroll
    for (int j = 0; j < 2; ++j) {
      const int c64 = 2 * (wr & 1) + j;
      rAddr[j] = 16384 + (wr >> 1) * 8192 + (8 * hi + q) * 256 + ((c64 ^ q) * 64) + blk * 32 + swapmid(p) * 8;
    }
  }
  __device__ __forceinline__ void stage(char LAS* buf, const bf16_t* P, const bf16_t* R) {
    const unsigned b = (unsigned)(uintptr_t)buf;
#pragma unroll
    for (int i = 0; i < 2; ++i) glds16(P, pOff[i], b + (w * 2 + i) * 1024);
#pragma unroll
    for (int i = 0; i < 2; ++i) glds16(R, rOff[i], b + 16384 + (w * 2 + i) * 1024);
  }
  __device__ __forceinline__ void compute(const char LAS* buf, f32x16 (&acc)[4][2]) {
#pragma unroll
    for (int kk = 0; kk < 2; ++kk) {
      bf16x8 pf[4], rf[2];
#pragma unroll
      for (int j = 0; j < 2; ++j) rf[j] = cat8(tr_read(buf + rAddr[j] + kk * 4096), tr_read(buf + rAddr[j] + kk * 4096 + 1024));
#pragma unroll
      for (int i = 0; i < 4; ++i) pf[i] = *(const LAS bf16x8*)(buf + pAddr[kk] + i * 2048);
#pragma unroll
      for (int i = 0; i < 4; ++i)
#pragma unroll
        for (int j = 0; j < 2; ++j) acc[i][j] = __builtin_amdgcn_mfma_f32_32x32x16_bf16(rf[j], pf[i], acc[i][j], 0, 0, 0);
    }
  }
  __device__ __forceinline__ void load_frags(const char LAS* buf, bf16x8 (&pf)[2][4], bf16x8 (&rf)[2][2]) {
#pragma unroll
    for (int kk = 0; kk < 2; ++kk) {
#pragma unroll
      for (int j = 0; j < 2; ++j) rf[kk][j] = cat8(tr_read(buf + rAddr[j] + kk * 4096), tr_read(buf + rAddr[j] + kk * 4096 + 1024));
#pragma unroll
      for (int i = 0; i < 4; ++i) pf[kk][i] = *(const LAS bf16x8*)(buf + pAddr[kk] + i * 2048);
    }
  }
  __device__ __forceinline__ void mma(const bf16x8 (&pf)[2][4], const bf16x8 (&rf)[2][2], f32x16 (&acc)[4][2]) {
#pragma unroll
    for (int kk = 0; kk < 2; ++kk)
#pragma unroll
      for (int i = 0; i < 4; ++i)
#pragma unroll
        for (int j = 0; j < 2; ++j) acc[i][j] = __builtin_amdgcn_mfma_f32_32x32x16_bf16(rf[kk][j], pf[kk][i], acc[i][j], 0, 0, 0);
  }
};
#define WAIT_LGKM0() asm volatile("s_waitcnt lgkmcnt(0)" ::: "memory")
#define SCHED0() __builtin_amdgcn_sched_barrier(0)
template <int N> __device__ __forceinline__ void wait_vm() { asm volatile("s_waitcnt vmcnt(%0)" ::"n"(N) : "memory"); }

struct Frag { bf16x8 pf[4], rf[2]; };
struct Desc { const char* p; unsigned stride; int v0, v1; };
template <int IMM> __device__ __forceinline__ void setm0(unsigned base) {
  asm volatile("s_add_u32 m0, %0, %1" ::"s"(base), "n"(IMM) : "memory", "scc");
}
template <int OFF> __device__ __forceinline__ void gl(const void* sbase, int voff) {
  asm volatile("global_load_lds_dwordx4 %0, %1 offset:%2" ::"v"(voff), "s"(sbase), "n"(OFF) : "memory");
}
template <int V> using IC = std::integral_constant<int, V>;
__device__ __forceinline__ void touch(unsigned& keep, const void* a) {
  asm volatile("global_load_dword %0, %1, off" : "+v"(keep) : "v"(a) : "memory");
}
template <int K, bool PRE, class F>
__device__ __forceinline__ void proj_tile(char LAS* lds, const bf16_t* A, const bf16_t* W, bf16_t* O, int tb, int hp, F pre) {
  const int l = threadIdx.x & 63, w = __builtin_amdgcn_readfirstlane(threadIdx.x >> 6);
  const unsigned sW = (unsigned)(uintptr_t)lds + w * 2048;
  const int r32 = l & 31, hi = l >> 5;
  constexpr int ngt = K / 128;
  Core256 g;
  g.init(K, HDN);
  const char* Ap = (const char*)(A + (size_t)tb * 256 * K);
  const char* Wp = (const char*)(W + hp * 256);
  const int wp = w >> 2, wr = w & 3;
  constexpr unsigned RSTR = 32u * HDN * 2;
  const int pv0 = g.pOff[0], pv1 = g.pOff[1] - 1024, rv0 = g.rOff[0], rv1 = g.rOff[1] - 1024;
  f32x16 acc[4][2];
  Frag F0, F1;
  auto half = [&](auto first, auto dst, const Frag& cur, Frag& nxt, const char LAS* nb, int nkk, const char* sp, int v0, int v1) {
    auto mm = [&](int i, int j) {
      acc[i][j] = __builtin_amdgcn_mfma_f32_32x32x16_bf16(cur.rf[j], cur.pf[i], first ? (f32x16){} : acc[i][j], 0, 0, 0);
      SCHED0();
    };
    auto rdR = [&](int j) {
      nxt.rf[j] = cat8(tr_read(nb + g.rAddr[j] + nkk * 4096), tr_read(nb + g.rAddr[j] + nkk * 4096 + 1024));
      SCHED0();
    };
    auto rdP = [&](int i) {
      nxt.pf[i] = *(const LAS bf16x8*)(nb + g.pAddr[nkk] + i * 2048);
      SCHED0();
    };
    mm(0, 0); rdR(0);
    mm(0, 1); rdP(0); rdR(1);
    mm(1, 0); rdP(1);
    mm(1, 1); rdP(2);
    mm(2, 0); rdP(3); if (dst >= 0) setm0<(dst >= 0 ? (int)dst : 0)>(sW); SCHED0();
    mm(2, 1); if (dst >= 0) gl<0>(sp, v0); SCHED0();
    mm(3, 0); if (dst >= 0) gl<1024>(sp, v1); SCHED0();
    mm(3, 1);
  };
  auto group = [&](auto VAR, int gi) {
    const char* Rc = Wp + (size_t)(4 * gi + 3) * RSTR;
    const char* Pn = Ap + (gi + 1) * 256;
    auto stp = [&](auto J) {
      constexpr int j = J, var = VAR;
      constexpr int dA = (var == 2 && j > 0) ? -1 : ((j + 3) & 3) * STAGE256 + 16384;
      constexpr int dB = var == 2 ? -1 : j * STAGE256;
      const char LAS* cb = lds + j * STAGE256;
      const char LAS* nb = lds + ((j + 1) & 3) * STAGE256;
      half(std::integral_constant<bool, var == 0 && j == 0>{}, IC<dA>{}, F0, F1, cb, 1, Rc + j * RSTR, rv0, rv1);
      if (var == 2) { if (j == 0) wait_vm<8>(); else if (j == 1) wait_vm<4>(); else if (j == 2) wait_vm<0>(); }
      else if (!(var == 0 && PRE && j < 2)) wait_vm<8>();
      WAIT_LGKM0();
      SCHED0();
      BARRIER();
      SCHED0();
      half(std::false_type{}, IC<dB>{}, F1, F0, nb, 0, Pn + j * 64, pv0, pv1);
    };
    stp(IC<0>{}); stp(IC<1>{}); stp(IC<2>{}); stp(IC<3>{});
  };
  setm0<0>(sW); SCHED0(); asm volatile("s_nop 0"); gl<0>(Ap, pv0); gl<1024>(Ap, pv1);
  setm0<16384>(sW); SCHED0(); asm volatile("s_nop 0"); gl<0>(Wp, rv0); gl<1024>(Wp, rv1);
  setm0<STAGE256>(sW); SCHED0(); asm volatile("s_nop 0"); gl<0>(Ap + 64, pv0); gl<1024>(Ap + 64, pv1);
  setm0<STAGE256 + 16384>(sW); SCHED0(); asm volatile("s_nop 0"); gl<0>(Wp + RSTR, rv0); gl<1024>(Wp + RSTR, rv1);
  setm0<2 * STAGE256>(sW); SCHED0(); asm volatile("s_nop 0"); gl<0>(Ap + 128, pv0); gl<1024>(Ap + 128, pv1);
  setm0<2 * STAGE256 + 16384>(sW); SCHED0(); asm volatile("s_nop 0"); gl<0>(Wp + 2 * RSTR, rv0); gl<1024>(Wp + 2 * RSTR, rv1);
  setm0<3 * STAGE256>(sW); SCHED0(); asm volatile("s_nop 0"); gl<0>(Ap + 192, pv0); gl<1024>(Ap + 192, pv1);
  SCHED0();
  pre();
  SCHED0();
  if (!PRE) wait_vm<10>();
  BARRIER();
#pragma unroll
  for (int j = 0; j < 2; ++j) F0.rf[j] = cat8(tr_read(lds + g.rAddr[j]), tr_read(lds + g.rAddr[j] + 1024));
#pragma unroll
  for (int i = 0; i < 4; ++i) F0.pf[i] = *(const LAS bf16x8*)(lds + g.pAddr[0] + i * 2048);
  SCHED0();
  group(IC<0>{}, 0);
  for (int gi = 1; gi < ngt - 1; ++gi) group(IC<1>{}, gi);
  group(IC<2>{}, ngt - 1);
  const int head = hp * 2 + (wr >> 1);
  char LAS* slab = lds + 4 * STAGE256 + w * 4096;
  const int wa = r32 * 128, wsw = r32 & 7;
  const int rrow = l >> 3, ra = rrow * 128 + (((l & 7) ^ rrow) << 4);
  bf16_t* obase = O + ((size_t)head * S_LEN + tb * 256 + 128 * wp + rrow) * HD + 64 * (wr & 1) + (l & 7) * 8;
#pragma unroll
  for (int i = 0; i < 4; ++i) {
#pragma unroll
    for (int j = 0; j < 2; ++j) {
      const f32x16 a = acc[i][j];
      u32x4 v0 = {pack2(a[0], a[1]), pack2(a[2], a[3]), pack2(a[4], a[5]), pack2(a[6], a[7])};
      u32x4 v1 = {pack2(a[8], a[9]), pack2(a[10], a[11]), pack2(a[12], a[13]), pack2(a[14], a[15])};
      *(u32x4 LAS*)(slab + wa + (((4 * j + hi) ^ wsw) << 4)) = v0;
      *(u32x4 LAS*)(slab + wa + (((4 * j + 2 + hi) ^ wsw) << 4)) = v1;
    }
#pragma unroll
    for (int it = 0; it < 4; ++it) {
      const u32x4 v = *(const u32x4 LAS*)(slab + ra + it * 1024);
      st16(obase + (size_t)(32 * i + 8 * it) * HD, v);
    }
  }
}

__global__ __launch_bounds__(512, 2) void kv_kernel(const bf16_t* __restrict__ x, const bf16_t* __restrict__ ckv,
                                                    const bf16_t* __restrict__ wq, const bf16_t* __restrict__ wuk,
                                                    const bf16_t* __restrict__ wuv, const bf16_t* __restrict__ w1,
                                                    const bf16_t* __restrict__ b1, bf16_t* __restrict__ xn,
                                                    bf16_t* __restrict__ hbuf, bf16_t* __restrict__ Ko,
                                                    bf16_t* __restrict__ Vo) {
  __shared__ __attribute__((aligned(16))) char smem[4 * STAGE256 + 32768];
  char LAS* lds = (char LAS*)smem;
  const int xcd = blockIdx.x & 7, c = blockIdx.x >> 3;
  TS(0, 0);
  unsigned keep = 0;
#pragma unroll
  for (int j = 0; j < 2; ++j) {
    const int idx = (int)blockIdx.x * 1024 + j * 512 + (int)threadIdx.x;
    const char* a = idx < 32768 ? (const char*)ckv + (size_t)idx * 64
                  : idx < 98304 ? (const char*)wuk + (size_t)(idx - 32768) * 64
                  : idx < 163840 ? (const char*)wuv + (size_t)(idx - 98304) * 64 : (const char*)ckv;
    touch(keep, a);
  }
  auto pre = [&]() {
    const int lane = threadIdx.x & 63, row = blockIdx.x * 8 + (threadIdx.x >> 6);
    const unsigned* xr = reinterpret_cast<const unsigned*>(x + (size_t)row * DM);
    float v[10];
    unsigned u[5];
#pragma unroll
    for (int j = 0; j < 5; ++j) u[j] = xr[lane + 64 * j];
    SCHED0();
    WAIT_VM0();
    SCHED0();
#pragma unroll
    for (int j = 0; j < 5; ++j) {
      v[2 * j] = __uint_as_float(u[j] << 16);
      v[2 * j + 1] = __uint_as_float(u[j] & 0xffff0000u);
    }
    ln_row(v);
    unsigned* xo = reinterpret_cast<unsigned*>(xn + (size_t)row * DM);
#pragma unroll
    for (int j = 0; j < 5; ++j) xo[lane + 64 * j] = pack2(v[2 * j], v[2 * j + 1]);
    ln_row(v);
    unsigned* ho = reinterpret_cast<unsigned*>(hbuf + (size_t)row * DM);
#pragma unroll
    for (int j = 0; j < 5; ++j) ho[lane + 64 * j] = pack2(v[2 * j], v[2 * j + 1]);
#pragma unroll
    for (int j = 0; j < 2; ++j) {
      const int idx = (int)blockIdx.x * 1024 + j * 512 + (int)threadIdx.x;
      const char* a = idx < 81920 ? (const char*)wq + (size_t)idx * 64
                    : idx < 192512 ? (const char*)w1 + (size_t)(idx - 81920) * 64
                    : (const char*)b1 + ((idx - 192512) % 54) * 64;
      touch(keep, a);
    }
  };
  proj_tile<DC, true>(lds, ckv, (c & 2) ? wuv : wuk, (c & 2) ? Vo : Ko, c >> 2, xcd * 2 + (c & 1), pre);
  asm volatile("" ::"v"(keep));
  TS(0, 2);
}

__global__ __launch_bounds__(512, 2) void qmix_kernel(const bf16_t* __restrict__ xn, const bf16_t* __restrict__ hbuf,
                                                      const bf16_t* __restrict__ wq, const bf16_t* __restrict__ w1,
                                                      const bf16_t* __restrict__ b1, const bf16_t* __restrict__ wo,
                                                      const bf16_t* __restrict__ w2, bf16_t* __restrict__ Qo,
                                                      bf16_t* __restrict__ Uo) {
  __shared__ __attribute__((aligned(16))) char smem[4 * STAGE256 + 32768];
  char LAS* lds = (char LAS*)smem;
  const int xcd = blockIdx.x & 7, c = blockIdx.x >> 3;
  const int l = threadIdx.x & 63, w = __builtin_amdgcn_readfirstlane(threadIdx.x >> 6);
  const unsigned sW = (unsigned)(uintptr_t)lds + w * 2048;
  const int r32 = l & 31, hi = l >> 5;
  TS(3, 0);
  const int mbase = (xcd * 90) >> 3, mcnt = (((xcd + 1) * 90) >> 3) - mbase;
  if (c < 16) {
    proj_tile<DM, false>(lds, xn, wq, Qo, c >> 1, xcd * 2 + (c & 1), []() {});
  } else if (c - 16 >= mcnt) {
    const int wi = xcd * 16 - mbase + (c - 16 - mcnt);
    unsigned keep = 0;
    for (int idx = wi * 512 + (int)threadIdx.x; idx < 192512; idx += 38 * 512)
      touch(keep, idx < 81920 ? (const char*)wo + (size_t)idx * 64 : (const char*)w2 + (size_t)(idx - 81920) * 64);
    WAIT_VM0();
    asm volatile("" ::"v"(keep));
  } else {
    const int mtile = mbase + (c - 16);
    const int d0 = (mtile % 5) * 128, f0 = (mtile / 5) * 96;
    const int ws = w & 3, kh = w >> 2;
    const int jk = kh == 0 ? 0 : 2;
    u32x4 bias[3];
    bias[0] = *(const u32x4*)(b1 + f0 + 8 * hi + 32 * jk);
    bias[1] = *(const u32x4*)(b1 + f0 + 8 * hi + 32 * jk + 16);
    bias[2] = *(const u32x4*)(b1 + f0 + 8 * hi + 32 + 16 * kh);
    SCHED0();
    Desc MP, MR;
    {
      int po[2], ro[2];
#pragma unroll
      for (int i = 0; i < 2; ++i) {
        const int j = w * 2 + i;
        const int krow = 4 * j + (l >> 4), s16 = l & 15;
        po[i] = (krow * DM + ((s16 >> 2) ^ (krow & 3)) * 32 + (s16 & 3) * 8) * 2 - 1024 * i;
        const int jr = j < 12 ? j : j - 4;
        const int cb = jr >> 2, kr = (jr & 3) * 16 + (l >> 2);
        ro[i] = (kr * FF + cb * 32 + (l & 3) * 8) * 2 - 1024 * i;
      }
      MP = Desc{(const char*)(hbuf + d0), 64u * DM * 2, po[0], po[1]};
      MR = Desc{(const char*)(w1 + f0), 64u * FF * 2, ro[0], ro[1]};
    }
    const int blk = (l >> 4) & 1, q = (l & 15) >> 2, p = l & 3;
    const int pB = (32 * kh + 8 * hi + q) * 256 + ((ws ^ q) * 64) + blk * 32 + p * 8;
    const int rA = 16384 + (32 * kh + 8 * hi + q) * 64 + blk * 32 + swapmid(p) * 8;
    f32x16 acc[3] = {};
    bf16x8 pa[2], ra[2][3], pb[2], rb[2][3];
    auto mstep = [&](auto VAR, auto J, const bf16x8 (&pc)[2], const bf16x8 (&rc)[2][3], bf16x8 (&pn)[2], bf16x8 (&rn)[2][3],
                     const char* sp, const char* sr) {
      constexpr int j = J, var = VAR;
      if (var == 1) wait_vm<8>();
      else { if (j == 0) wait_vm<8>(); else if (j == 1) wait_vm<4>(); else if (j == 2) wait_vm<0>(); }
      WAIT_LGKM0();
      SCHED0();
      BARRIER();
      SCHED0();
      const char LAS* nb = lds + ((j + 1) & 3) * STAGE256;
      auto mm = [&](int k2, int jj) {
        acc[jj] = __builtin_amdgcn_mfma_f32_32x32x16_bf16(rc[k2][jj], pc[k2], acc[jj], 0, 0, 0);
        SCHED0();
      };
      auto rdP = [&](int k2) { pn[k2] = cat8(tr_read(nb + pB + k2 * 4096), tr_read(nb + pB + k2 * 4096 + 1024)); SCHED0(); };
      auto rdR = [&](int k2, int jj) {
        rn[k2][jj] = cat8(tr_read(nb + rA + jj * 4096 + k2 * 1024), tr_read(nb + rA + jj * 4096 + k2 * 1024 + 256));
        SCHED0();
      };
      mm(0, 0); rdP(0); rdR(0, 0); rdR(0, 1);
      mm(0, 1); rdR(0, 2); rdP(1); rdR(1, 0);
      mm(0, 2); rdR(1, 1); rdR(1, 2); if (var != 2) setm0<j * STAGE256>(sW); SCHED0();
      mm(1, 0); if (var != 2) { gl<0>(sp, MP.v0); gl<1024>(sp, MP.v1); setm0<j * STAGE256 + 16384>(sW); } SCHED0();
      mm(1, 1); if (var != 2) gl<0>(sr, MR.v0); SCHED0();
      mm(1, 2); if (var != 2) gl<1024>(sr, MR.v1); SCHED0();
    };
    auto mgroup = [&](auto VAR, int m) {
      const char* sp = MP.p + (size_t)(m + 4) * MP.stride;
      const char* sr = MR.p + (size_t)(m + 4) * MR.stride;
      mstep(VAR, IC<0>{}, pa, ra, pb, rb, sp, sr);
      mstep(VAR, IC<1>{}, pb, rb, pa, ra, sp + MP.stride, sr + MR.stride);
      mstep(VAR, IC<2>{}, pa, ra, pb, rb, sp + 2 * MP.stride, sr + 2 * MR.stride);
      mstep(VAR, IC<3>{}, pb, rb, pa, ra, sp + 3 * MP.stride, sr + 3 * MR.stride);
    };
    auto mstage = [&](auto J) {
      constexpr int j = J;
      setm0<j * STAGE256>(sW); SCHED0(); asm volatile("s_nop 0");
      gl<0>(MP.p + j * MP.stride, MP.v0); gl<1024>(MP.p + j * MP.stride, MP.v1);
      setm0<j * STAGE256 + 16384>(sW); SCHED0(); asm volatile("s_nop 0");
      gl<0>(MR.p + j * MR.stride, MR.v0); gl<1024>(MR.p + j * MR.stride, MR.v1);
    };
    mstage(IC<0>{}); mstage(IC<1>{}); mstage(IC<2>{}); mstage(IC<3>{});
    wait_vm<12>();
    BARRIER();
#pragma unroll
    for (int k2 = 0; k2 < 2; ++k2) {
      pa[k2] = cat8(tr_read(lds + pB + k2 * 4096), tr_read(lds + pB + k2 * 4096 + 1024));
#pragma unroll
      for (int j = 0; j < 3; ++j) ra[k2][j] = cat8(tr_read(lds + rA + j * 4096 + k2 * 1024), tr_read(lds + rA + j * 4096 + k2 * 1024 + 256));
    }
    SCHED0();
    for (int m = 0; m < 28; m += 4) mgroup(IC<1>{}, m);
    mgroup(IC<2>{}, 28);
    WAIT_LGKM0();
    TS(3, 1);
    BARRIER();
    float LAS* dst = (float LAS*)(lds + w * 8192);
    const f32x16 sa = kh == 0 ? acc[2] : acc[0], ka = kh == 0 ? acc[0] : acc[2];
    const int so = kh == 0 ? 8 : 0;
#pragma unroll
    for (int g = 0; g < 4; ++g) *(f32x4 LAS*)(dst + (g * 64 + l) * 4) = (f32x4){sa[4 * g], sa[4 * g + 1], sa[4 * g + 2], sa[4 * g + 3]};
    float s1[8], k1[8];
#pragma unroll
    for (int e = 0; e < 8; ++e) { s1[e] = kh == 0 ? acc[1][8 + e] : acc[1][e]; k1[e] = kh == 0 ? acc[1][e] : acc[1][8 + e]; }
#pragma unroll
    for (int g = 0; g < 2; ++g) *(f32x4 LAS*)(dst + ((4 + g) * 64 + l) * 4) = (f32x4){s1[4 * g], s1[4 * g + 1], s1[4 * g + 2], s1[4 * g + 3]};
    (void)so;
    WAIT_LGKM0();
    BARRIER();
    const float LAS* src = (const float LAS*)(lds + (w ^ 4) * 8192);
    bf16_t* urow = Uo + (size_t)(d0 + 32 * ws + r32) * FF + f0 + 8 * hi;
    auto fin8 = [&](const float (&v)[8], int fo, const u32x4 bv) {
      float r[8];
#pragma unroll
      for (int e = 0; e < 4; ++e) {
        r[2 * e] = gelu_tanh(rbf(rbf(v[2 * e]) + __uint_as_float(bv[e] << 16)));
        r[2 * e + 1] = gelu_tanh(rbf(rbf(v[2 * e + 1]) + __uint_as_float(bv[e] & 0xffff0000u)));
      }
      st16(urow + fo, (u32x4){pack2(r[0], r[1]), pack2(r[2], r[3]), pack2(r[4], r[5]), pack2(r[6], r[7])});
    };
#pragma unroll
    for (int hh = 0; hh < 2; ++hh) {
      float v[8];
#pragma unroll
      for (int g = 0; g < 2; ++g) {
        const f32x4 o = *(const f32x4 LAS*)(src + ((2 * hh + g) * 64 + l) * 4);
#pragma unroll
        for (int e = 0; e < 4; ++e) v[4 * g + e] = ka[8 * hh + 4 * g + e] + o[e];
      }
      fin8(v, 32 * jk + 16 * hh, bias[hh]);
    }
    {
      float v[8];
#pragma unroll
      for (int g = 0; g < 2; ++g) {
        const f32x4 o = *(const f32x4 LAS*)(src + ((4 + g) * 64 + l) * 4);
#pragma unroll
        for (int e = 0; e < 4; ++e) v[4 * g + e] = k1[4 * g + e] + o[e];
      }
      fin8(v, 32 + 16 * kh, bias[2]);
    }
  }
  TS(3, 2);
}

constexpr int AT_HALF = 16384;
__global__ __launch_bounds__(512, 2) void attn_kernel(const bf16_t* __restrict__ Q, const bf16_t* __restrict__ K,
                                                      const bf16_t* __restrict__ V, bf16_t* __restrict__ ctx,
                                                      const bf16_t* __restrict__ wo, const bf16_t* __restrict__ w2) {
  __shared__ __attribute__((aligned(16))) char smem[8 * AT_HALF + 32768];
  char LAS* lds = (char LAS*)smem;
  const int tid = threadIdx.x, l = tid & 63, w = __builtin_amdgcn_readfirstlane(tid >> 6);
  const int rg = w & 3, g = w >> 2, r32 = l & 31, hi = l >> 5;
  const int xcd = blockIdx.x & 7, jj = blockIdx.x >> 3;
  const int head = xcd * 4 + (jj >> 3), pair = jj & 7;
  const int LB = 15 - pair, nshort = 2 * pair + 2;
  const bf16_t* Kh = K + (size_t)head * S_LEN * HD;
  const bf16_t* Vh = V + (size_t)head * S_LEN * HD;
  const bf16_t* Qh = Q + (size_t)head * S_LEN * HD;
  char LAS* gl = lds + g * 4 * AT_HALF;
  int kOff[4], vOff[4];
#pragma unroll
  for (int i = 0; i < 4; ++i) {
    const int j = rg * 4 + i, row = 4 * j + (l >> 4), s16 = l & 15;
    kOff[i] = (row * HD + ((s16 ^ (row & 15)) * 8)) * 2 - 1024 * i;
    vOff[i] = (row * HD + ((((s16 >> 2) ^ (row & 3)) * 4 + (s16 & 3)) * 8)) * 2 - 1024 * i;
  }
  const unsigned glb = (unsigned)(uintptr_t)gl;
  auto htile = [&](int i) { return (g == 0 || i < nshort) ? i : i + 15 - 2 * pair; };
  auto stageK = [&](int i) {
    const bf16_t* kp = Kh + (size_t)htile(i > 16 ? 16 : i) * 64 * HD;
    const unsigned bb = glb + (i & 1) * AT_HALF + rg * 4096;
    glds16x4(kp, kOff[0], kOff[1], kOff[2], kOff[3], bb);
  };
  auto stageV = [&](int i) {
    const bf16_t* vp = Vh + (size_t)htile(i) * 64 * HD;
    const unsigned bb = glb + (2 + (i & 1)) * AT_HALF + rg * 4096;
    glds16x4(vp, vOff[0], vOff[1], vOff[2], vOff[3], bb);
  };
  const int ksw = r32 & 15;
  const int blk = (l >> 4) & 1, q4 = (l & 15) >> 2, p4 = l & 3;
  int vAddr[4];
#pragma unroll
  for (int db = 0; db < 4; ++db)
    vAddr[db] = 2 * AT_HALF + (4 * hi + q4) * 256 + ((db ^ q4) * 64) + blk * 32 + swapmid(p4) * 8;
  constexpr float CS = 0.08838834764831845f * 1.4426950408889634f;

  TS(1, 0);
  stageK(0);
  stageV(0);
  stageK(1);
  int qb = g == 0 ? LB : pair;
  bf16x8 qf[8];
#pragma unroll
  for (int d0 = 0; d0 < 8; ++d0) qf[d0] = *(const bf16x8*)(Qh + (size_t)(qb * 128 + 32 * rg + r32) * HD + d0 * 16 + hi * 8);
  char LAS* qsave = lds + 8 * AT_HALF + rg * 8192 + l * 16;
  if (g == 0) {
#pragma unroll
    for (int d0 = 0; d0 < 8; ++d0) *(bf16x8 LAS*)(qsave + d0 * 1024) = qf[d0];
  }
  float m_run = -1e30f, l_run = 0.f, alpha = 1.f;
  f32x16 o[4] = {};
  f32x16 s0, s1;
  bf16x8 pf[4];
  u32x2 pfd = {0u, 0u};
  constexpr int AT_VM = 4;
#define AT_W 6
  bf16x8 fr[32];
  const int kAddr0 = r32 * 256 + ((hi ^ ksw) << 4);
  auto rdK = [&](int k, int slot) {
    fr[k] = *(const LAS bf16x8*)(gl + slot * AT_HALF + (kAddr0 ^ ((k >> 1) << 5)) + (k & 1) * 8192);
  };
  auto rdV = [&](int k, int slot) {
    const char LAS* a = gl + slot * AT_HALF + vAddr[(k - 16) >> 2] + (k & 3) * 4096;
    fr[k] = cat8(tr_read(a), tr_read(a + 2048));
  };
  auto mm = [&](int k) {
    if (k < 16) {
      if (k & 1) s1 = __builtin_amdgcn_mfma_f32_32x32x16_bf16(fr[k], qf[k >> 1], k < 2 ? (f32x16){} : s1, 0, 0, 0);
      else s0 = __builtin_amdgcn_mfma_f32_32x32x16_bf16(fr[k], qf[k >> 1], k < 2 ? (f32x16){} : s0, 0, 0, 0);
    } else {
      o[(k - 16) >> 2] = __builtin_amdgcn_mfma_f32_32x32x16_bf16(fr[k], pf[k & 3], o[(k - 16) >> 2], 0, 0, 0);
    }
  };
  auto preK = [&](int slot) {
#pragma unroll
    for (int k = 0; k < AT_W; ++k) rdK(k, slot);
  };
  auto preV = [&](int slot) {
#pragma unroll
    for (int k = 16; k < 16 + AT_W; ++k) rdV(k, slot);
  };
  auto xhalf_max = [&](float v) {
    auto rr = __builtin_amdgcn_permlane32_swap(__float_as_uint(v), __float_as_uint(v), false, false);
    return fmaxf(__uint_as_float(rr[0]), __uint_as_float(rr[1]));
  };
  auto xhalf_sum = [&](float v) {
    auto rr = __builtin_amdgcn_permlane32_swap(__float_as_uint(v), __float_as_uint(v), false, false);
    return __uint_as_float(rr[0]) + __uint_as_float(rr[1]);
  };
  auto softmax = [&](int i) {
    if (g == 1 && i == nshort) {
      const float inv = 1.0f / xhalf_sum(l_run);
      bf16_t* orow = ctx + (size_t)(pair * 128 + 32 * rg + r32) * CTX_LD + head * HD + 8 * hi;
#pragma unroll
      for (int db = 0; db < 4; ++db) {
        const f32x16 a = o[db];
        u32x4 v0 = {pack2(a[0] * inv, a[1] * inv), pack2(a[2] * inv, a[3] * inv), pack2(a[4] * inv, a[5] * inv), pack2(a[6] * inv, a[7] * inv)};
        u32x4 v1 = {pack2(a[8] * inv, a[9] * inv), pack2(a[10] * inv, a[11] * inv), pack2(a[12] * inv, a[13] * inv), pack2(a[14] * inv, a[15] * inv)};
        st16(orow + 32 * db, v0);
        st16(orow + 32 * db + 16, v1);
        o[db] = (f32x16){};
      }
      m_run = -1e30f;
      l_run = 0.f;
      qb = LB;
    }
    if (g == 1 && i == nshort - 1) {
#pragma unroll
      for (int d0 = 0; d0 < 8; ++d0) qf[d0] = *(const bf16x8 LAS*)(qsave + d0 * 1024);
    }
    SCHED0();
    if (i < 16) stageK(i + 2);
    SCHED0(); PH(i, 5); SCHED0();
    const int dh = htile(i) - 2 * qb;
    if (dh >= 0) {
      const int qi = 32 * rg + r32 - 64 * dh;
#pragma unroll
      for (int r = 0; r < 16; ++r) {
        const int key = (r & 3) + 8 * (r >> 2) + 4 * hi;
        if (key > qi) s0[r] = -__builtin_inff();
        if (key + 32 > qi) s1[r] = -__builtin_inff();
      }
    }
    float pm = s0[0];
#pragma unroll
    for (int r = 1; r < 16; ++r) pm = fmaxf(pm, s0[r]);
#pragma unroll
    for (int r = 0; r < 16; ++r) pm = fmaxf(pm, s1[r]);
    pm = xhalf_max(pm);
    const float mn = fmaxf(m_run, pm);
    alpha = __builtin_amdgcn_exp2f((m_run - mn) * CS);
    m_run = mn;
    const float mc = -mn * CS;
    SCHED0(); PH(i, 6); SCHED0();
    float ps = 0.f;
#pragma unroll
    for (int r = 0; r < 16; ++r) { s0[r] = __builtin_amdgcn_exp2f(fmaf(s0[r], CS, mc)); ps += s0[r]; }
#pragma unroll
    for (int r = 0; r < 16; ++r) { s1[r] = __builtin_amdgcn_exp2f(fmaf(s1[r], CS, mc)); ps += s1[r]; }
    l_run = l_run * alpha + ps;
    SCHED0(); PH(i, 7); SCHED0();
    u32x4 a0 = {pack2(s0[0], s0[1]), pack2(s0[2], s0[3]), pack2(s0[4], s0[5]), pack2(s0[6], s0[7])};
    u32x4 a1 = {pack2(s0[8], s0[9]), pack2(s0[10], s0[11]), pack2(s0[12], s0[13]), pack2(s0[14], s0[15])};
    u32x4 a2 = {pack2(s1[0], s1[1]), pack2(s1[2], s1[3]), pack2(s1[4], s1[5]), pack2(s1[6], s1[7])};
    u32x4 a3 = {pack2(s1[8], s1[9]), pack2(s1[10], s1[11]), pack2(s1[12], s1[13]), pack2(s1[14], s1[15])};
    pf[0] = *(bf16x8*)&a0; pf[1] = *(bf16x8*)&a1; pf[2] = *(bf16x8*)&a2; pf[3] = *(bf16x8*)&a3;
    asm volatile("" : "+v"(pf[0]), "+v"(pf[1]), "+v"(pf[2]), "+v"(pf[3]), "+v"(l_run), "+v"(alpha));
  };
#define AT_DK0 16
  WAIT_VM0();
  WAIT_LGKM0();
  BARRIER();
  TS(1, 3);
  if (g == 1) BARRIER();
  SCHED0();
  preK(0);
#pragma unroll
  for (int k = 0; k < 16; ++k) {
    if (k + AT_W < 16) rdK(k + AT_W, 0);
    mm(k);
    SCHED0();
  }
  BARRIER();
  for (int i = 0; i < 16; ++i) {
    SCHED0();
    PH(i, 0);
    softmax(i);
    SCHED0();
    const int ks = (i + 1) & 1, vs = i & 1;
    preK(ks);
    SCHED0();
    wait_vm<AT_VM>();
    PH(i, 1);
    BARRIER();
    SCHED0();
    PH(i, 2);
    __builtin_amdgcn_s_setprio(1);
#pragma unroll
    for (int k = 0; k < 32; ++k) {
      if (k + AT_W < 16) rdK(k + AT_W, ks);
      else if (k + AT_W < 32) rdV(k + AT_W, vs);
      mm(k);
      if (k < 16) {
#pragma unroll
        for (int e = 0; e < 4; ++e) {
          float t = o[k >> 2][4 * (k & 3) + e];
          asm volatile("v_mul_f32 %0, %1, %0" : "+v"(t) : "v"(alpha));
          o[k >> 2][4 * (k & 3) + e] = t;
        }
      }
      if (k == AT_DK0) stageV(i + 1);
      SCHED0();
    }
    __builtin_amdgcn_s_setprio(0);
    PH(i, 3);
    wait_vm<AT_VM>();
    PH(i, 4);
    BARRIER();
  }
  SCHED0();
  softmax(16);
#pragma unroll
  for (int db = 0; db < 4; ++db)
#pragma unroll
    for (int r = 0; r < 16; ++r) o[db][r] *= alpha;
  SCHED0();
  WAIT_VM0();
  asm volatile("" ::"v"(pfd));
  BARRIER();
  SCHED0();
  preV(0);
#pragma unroll
  for (int k = 16; k < 32; ++k) {
    if (k + AT_W < 32) rdV(k + AT_W, 0);
    mm(k);
    SCHED0();
  }
  BARRIER();
  if (g == 0) BARRIER();
  l_run = xhalf_sum(l_run);
  TS(1, 1);
  {
    const int kh = g;
    float LAS* ml = (float LAS*)(lds + 4 * AT_HALF);
    float LAS* dst = (float LAS*)(lds + w * 8192);
#pragma unroll
    for (int dd = 0; dd < 2; ++dd) {
      f32x16 a;
#pragma unroll
      for (int r = 0; r < 16; ++r) a[r] = kh == 0 ? o[2 + dd][r] : o[dd][r];
#pragma unroll
      for (int gg = 0; gg < 4; ++gg)
        *(f32x4 LAS*)(dst + ((dd * 4 + gg) * 64 + l) * 4) = (f32x4){a[4 * gg], a[4 * gg + 1], a[4 * gg + 2], a[4 * gg + 3]};
    }
    ml[w * 128 + l] = m_run;
    ml[w * 128 + 64 + l] = l_run;
    asm volatile("s_waitcnt lgkmcnt(0)" ::: "memory");
    BARRIER();
    const int wo = w ^ 4;
    const float m2 = ml[wo * 128 + l], l2 = ml[wo * 128 + 64 + l];
    const float mt = fmaxf(m_run, m2);
    const float a1 = __builtin_amdgcn_exp2f((m_run - mt) * CS), a2 = __builtin_amdgcn_exp2f((m2 - mt) * CS);
    const float inv = 1.0f / (l_run * a1 + l2 * a2);
    const float f1 = a1 * inv, f2 = a2 * inv;
    const float LAS* src = (const float LAS*)(lds + wo * 8192);
    bf16_t* orow = ctx + (size_t)(LB * 128 + 32 * rg + r32) * CTX_LD + head * HD + 8 * hi;
#pragma unroll
    for (int dd = 0; dd < 2; ++dd) {
      f32x16 a;
#pragma unroll
      for (int r = 0; r < 16; ++r) a[r] = kh == 0 ? o[dd][r] : o[2 + dd][r];
      float r[16];
#pragma unroll
      for (int gg = 0; gg < 4; ++gg) {
        const f32x4 b = *(const f32x4 LAS*)(src + ((dd * 4 + gg) * 64 + l) * 4);
#pragma unroll
        for (int e = 0; e < 4; ++e) r[4 * gg + e] = a[4 * gg + e] * f1 + b[e] * f2;
      }
      u32x4 v0 = {pack2(r[0], r[1]), pack2(r[2], r[3]), pack2(r[4], r[5]), pack2(r[6], r[7])};
      u32x4 v1 = {pack2(r[8], r[9]), pack2(r[10], r[11]), pack2(r[12], r[13]), pack2(r[14], r[15])};
      const int dcol = 32 * (2 * kh + dd);
      st16(orow + dcol, v0);
      st16(orow + dcol + 16, v1);
    }
  }
  TS(1, 2);
}

constexpr int G2_STAGE = 36864;
constexpr int G2_NST = 4;
__global__ __launch_bounds__(512, 2) void gemm2_kernel(const bf16_t* __restrict__ ctx, const bf16_t* __restrict__ wo,
                                                       const bf16_t* __restrict__ U, const bf16_t* __restrict__ w2,
                                                       float* __restrict__ part) {
  __shared__ __attribute__((aligned(16))) char smem[G2_NST * G2_STAGE];
  char LAS* lds = (char LAS*)smem;
  const int b = blockIdx.x, xcd = b & 7, c = b >> 3;
  const int ks = xcd >> 1;
  const int s0 = (xcd & 1) * 1024 + (c >> 2) * 128, m0 = (c & 3) * 160;
  const int l = threadIdx.x & 63, w = __builtin_amdgcn_readfirstlane(threadIdx.x >> 6);
  const int ws = w & 3, kh = w >> 2, r32 = l & 31, hi = l >> 5;
  const int blk = (l >> 4) & 1, q = (l & 15) >> 2, p = l & 3;
  TS(2, 0);
  int pOffA[2], pOffB[2], rOffA[3], rOffB[3];
#pragma unroll
  for (int i = 0; i < 2; ++i) {
    const int j = w * 2 + i;
    const int row = 8 * j + (l >> 3), slot = l & 7;
    pOffA[i] = (row * CTX_LD + (slot ^ ((row >> 1) & 7)) * 8) * 2;
    const int krow = 4 * j + (l >> 4), s16 = l & 15;
    pOffB[i] = (krow * S_LEN + ((s16 >> 2) ^ (krow & 3)) * 32 + (s16 & 3) * 8) * 2;
  }
#pragma unroll
  for (int i = 0; i < 3; ++i) {
    const int j = w + 8 * i;
    const int cb = j >> 2, kr = (j & 3) * 16 + (l >> 2);
    rOffA[i] = (kr * DM + cb * 32 + (l & 3) * 8) * 2;
    const int row = 8 * j + (l >> 3), slot = l & 7;
    rOffB[i] = (row * FF + (slot ^ ((row >> 1) & 7)) * 8) * 2;
  }
  const int f0 = ks * 7, nB = ks == 3 ? 6 : 7, total = 16 + nB;
  const bf16_t* PA = ctx + (size_t)s0 * CTX_LD + ks * 1024;
  const bf16_t* RA = wo + (size_t)ks * 1024 * DM + m0;
  const bf16_t* PB = w2 + (size_t)f0 * 64 * S_LEN + s0;
  const bf16_t* RB = U + (size_t)m0 * FF + f0 * 64;
  int pslot = 0, issued = 0;
  auto issue = [&]() {
    const unsigned bb = (unsigned)(uintptr_t)(lds + pslot * G2_STAGE);
    if (issued < 16) {
      const bf16_t* P = PA + issued * 64;
      const bf16_t* R = RA + (size_t)issued * 64 * DM;
      glds16(P, pOffA[0], bb + (w * 2) * 1024);
      glds16(P, pOffA[1], bb + (w * 2 + 1) * 1024);
      glds16(R, rOffA[0], bb + 16384 + w * 1024);
      glds16(R, rOffA[1], bb + 16384 + (w + 8) * 1024);
      if (w < 4) glds16(R, rOffA[2], bb + 16384 + (w + 16) * 1024);
    } else {
      const bf16_t* P = PB + (size_t)(issued - 16) * 64 * S_LEN;
      const bf16_t* R = RB + (issued - 16) * 64;
      glds16(P, pOffB[0], bb + (w * 2) * 1024);
      glds16(P, pOffB[1], bb + (w * 2 + 1) * 1024);
      glds16(R, rOffB[0], bb + 16384 + w * 1024);
      glds16(R, rOffB[1], bb + 16384 + (w + 8) * 1024);
      if (w < 4) glds16(R, rOffB[2], bb + 16384 + (w + 16) * 1024);
    }
    if (++pslot == G2_NST) pslot = 0;
    ++issued;
  };
  const int prow = 32 * ws + r32, psw = (prow >> 1) & 7;
  const int pA0 = prow * 128 + (((4 * kh + hi) ^ psw) << 4), pA1 = prow * 128 + (((4 * kh + 2 + hi) ^ psw) << 4);
  const int rA = 16384 + (32 * kh + 8 * hi + q) * 64 + blk * 32 + swapmid(p) * 8;
  const int pB = (32 * kh + 8 * hi + q) * 256 + ((ws ^ q) * 64) + blk * 32 + p * 8;
  const int rrow = rperm(r32), rsw = (rrow >> 1) & 7;
  const int rB0 = 16384 + rrow * 128 + (((4 * kh + hi) ^ rsw) << 4), rB1 = 16384 + rrow * 128 + (((4 * kh + 2 + hi) ^ rsw) << 4);
#pragma unroll
  for (int i = 0; i < G2_NST - 1; ++i) issue();
  f32x16 acc[5] = {};
  int buf = 0;
  for (int step = 0; step < 16; ++step) {
    if (w < 4) wait_vm<5 * (G2_NST - 2)>();
    else wait_vm<4 * (G2_NST - 2)>();
    BARRIER();
    issue();
    const char LAS* bp = lds + buf * G2_STAGE;
    bf16x8 pf[2], rf[2][5];
    pf[0] = *(const LAS bf16x8*)(bp + pA0);
#pragma unroll
    for (int j = 0; j < 5; ++j) rf[0][j] = cat8(tr_read(bp + rA + j * 4096), tr_read(bp + rA + j * 4096 + 256));
    pf[1] = *(const LAS bf16x8*)(bp + pA1);
#pragma unroll
    for (int j = 0; j < 5; ++j) rf[1][j] = cat8(tr_read(bp + rA + j * 4096 + 1024), tr_read(bp + rA + j * 4096 + 1024 + 256));
    __builtin_amdgcn_sched_barrier(0);
#pragma unroll
    for (int k2 = 0; k2 < 2; ++k2)
#pragma unroll
      for (int j = 0; j < 5; ++j) acc[j] = __builtin_amdgcn_mfma_f32_32x32x16_bf16(rf[k2][j], pf[k2], acc[j], 0, 0, 0);
    if (++buf == G2_NST) buf = 0;
  }
  for (int step = 16; step < total; ++step) {
    if (step + G2_NST - 1 > total) WAIT_VM0();
    else if (w < 4) wait_vm<5 * (G2_NST - 2)>();
    else wait_vm<4 * (G2_NST - 2)>();
    BARRIER();
    if (issued < total) issue();
    const char LAS* bp = lds + buf * G2_STAGE;
    bf16x8 pf[2], rf[2][5];
    pf[0] = cat8(tr_read(bp + pB), tr_read(bp + pB + 1024));
#pragma unroll
    for (int j = 0; j < 5; ++j) rf[0][j] = *(const LAS bf16x8*)(bp + rB0 + j * 4096);
    pf[1] = cat8(tr_read(bp + pB + 4096), tr_read(bp + pB + 4096 + 1024));
#pragma unroll
    for (int j = 0; j < 5; ++j) rf[1][j] = *(const LAS bf16x8*)(bp + rB1 + j * 4096);
    __builtin_amdgcn_sched_barrier(0);
#pragma unroll
    for (int k2 = 0; k2 < 2; ++k2)
#pragma unroll
      for (int j = 0; j < 5; ++j) acc[j] = __builtin_amdgcn_mfma_f32_32x32x16_bf16(rf[k2][j], pf[k2], acc[j], 0, 0, 0);
    if (++buf == G2_NST) buf = 0;
  }
  TS(2, 1);
  BARRIER();
  {
    float LAS* dst = (float LAS*)(lds + w * 12288);
    if (kh == 0) {
#pragma unroll
      for (int j = 3; j < 5; ++j)
#pragma unroll
        for (int g = 0; g < 4; ++g)
          *(f32x4 LAS*)(dst + (((j - 3) * 4 + g) * 64 + l) * 4) = (f32x4){acc[j][4 * g], acc[j][4 * g + 1], acc[j][4 * g + 2], acc[j][4 * g + 3]};
    } else {
#pragma unroll
      for (int j = 0; j < 3; ++j)
#pragma unroll
        for (int g = 0; g < 4; ++g)
          *(f32x4 LAS*)(dst + ((j * 4 + g) * 64 + l) * 4) = (f32x4){acc[j][4 * g], acc[j][4 * g + 1], acc[j][4 * g + 2], acc[j][4 * g + 3]};
    }
    asm volatile("s_waitcnt lgkmcnt(0)" ::: "memory");
    BARRIER();
    const float LAS* src = (const float LAS*)(lds + (w ^ 4) * 12288);
    float* orow = part + ((size_t)ks * S_LEN + s0 + 32 * ws + r32) * DM + m0 + 8 * hi;
    if (kh == 0) {
#pragma unroll
      for (int j = 0; j < 3; ++j)
#pragma unroll
        for (int g = 0; g < 4; ++g) {
          const f32x4 o = *(const f32x4 LAS*)(src + ((j * 4 + g) * 64 + l) * 4);
          st16f(orow + 32 * j + (g >> 1) * 16 + (g & 1) * 4, acc[j][4 * g] + o[0], acc[j][4 * g + 1] + o[1], acc[j][4 * g + 2] + o[2], acc[j][4 * g + 3] + o[3]);
        }
    } else {
#pragma unroll
      for (int j = 3; j < 5; ++j)
#pragma unroll
        for (int g = 0; g < 4; ++g) {
          const f32x4 o = *(const f32x4 LAS*)(src + (((j - 3) * 4 + g) * 64 + l) * 4);
          st16f(orow + 32 * j + (g >> 1) * 16 + (g & 1) * 4, acc[j][4 * g] + o[0], acc[j][4 * g + 1] + o[1], acc[j][4 * g + 2] + o[2], acc[j][4 * g + 3] + o[3]);
        }
    }
  }
  TS(2, 2);
}

__global__ __launch_bounds__(256) void reduce_kernel(const float* __restrict__ part, const bf16_t* __restrict__ x,
                                                     const bf16_t* __restrict__ b2, bf16_t* __restrict__ out) {
  const int i = blockIdx.x * 256 + threadIdx.x;
  const int e = i * 4;
  const int s = e / DM;
  f32x4 a = *(const f32x4*)(part + e);
#pragma unroll
  for (int sp = 1; sp < 4; ++sp) a += *(const f32x4*)(part + (size_t)sp * S_LEN * DM + e);
  const u32x2 xv = *(const u32x2*)(x + e);
  const float bb = bf2f(b2[s]);
  float r0 = __uint_as_float(xv[0] << 16) + a[0] + bb;
  float r1 = __uint_as_float(xv[0] & 0xffff0000u) + a[1] + bb;
  float r2 = __uint_as_float(xv[1] << 16) + a[2] + bb;
  float r3 = __uint_as_float(xv[1] & 0xffff0000u) + a[3] + bb;
  u32x2 o = {pack2(r0, r1), pack2(r2, r3)};
  *(u32x2*)(out + e) = o;
}

extern "C" void kernel_launch(void* const* d_in, const int* in_sizes, int n_in, void* d_out, int out_size, void* d_ws,
                              size_t ws_size, hipStream_t stream) {
  const bf16_t* x = (const bf16_t*)d_in[0];
  const bf16_t* ckv = (const bf16_t*)d_in[1];
  const bf16_t* wq = (const bf16_t*)d_in[2];
  const bf16_t* wuk = (const bf16_t*)d_in[3];
  const bf16_t* wuv = (const bf16_t*)d_in[4];
  const bf16_t* wo = (const bf16_t*)d_in[5];
  const bf16_t* w1 = (const bf16_t*)d_in[7];
  const bf16_t* b1 = (const bf16_t*)d_in[8];
  const bf16_t* w2 = (const bf16_t*)d_in[9];
  const bf16_t* b2 = (const bf16_t*)d_in[10];
  char* ws = (char*)d_ws;
  bf16_t* xn = (bf16_t*)(ws + WS_XN);
  bf16_t* h = (bf16_t*)(ws + WS_H);
  bf16_t* Q = (bf16_t*)(ws + WS_Q);
  bf16_t* K = (bf16_t*)(ws + WS_K);
  bf16_t* V = (bf16_t*)(ws + WS_V);
  bf16_t* U = (bf16_t*)(ws + WS_U);
  bf16_t* ctx = (bf16_t*)(ws + WS_CTX);
  float* part = (float*)(ws + WS_PART);
  kv_kernel<<<256, 512, 0, stream>>>(x, ckv, wq, wuk, wuv, w1, b1, xn, h, K, V);
  qmix_kernel<<<256, 512, 0, stream>>>(xn, h, wq, w1, b1, wo, w2, Q, U);
  attn_kernel<<<256, 512, 0, stream>>>(Q, K, V, ctx, wo, w2);
  gemm2_kernel<<<256, 512, 0, stream>>>(ctx, wo, U, w2, part);
  reduce_kernel<<<S_LEN * DM / 4 / 256, 256, 0, stream>>>(part, x, b2, (bf16_t*)d_out);
}
```

```cpp
#include <hip/hip_runtime.h>
#include <hip/hip_bf16.h>
#include <stdint.h>
#include <type_traits>

constexpr int S_LEN = 2048;
constexpr int DM = 640;
constexpr int NH = 32;
constexpr int HD = 128;
constexpr int DC = 512;
constexpr int FF = 1728;
constexpr int HDN = NH * HD;
constexpr int CTX_LD = HDN + 64;

typedef unsigned short bf16_t;
typedef short bf16x8 __attribute__((ext_vector_type(8)));
typedef short s16x4 __attribute__((ext_vector_type(4)));
typedef float f32x16 __attribute__((ext_vector_type(16)));
typedef float f32x4 __attribute__((ext_vector_type(4)));
typedef unsigned u32x4 __attribute__((ext_vector_type(4)));
typedef unsigned u32x2 __attribute__((ext_vector_type(2)));
typedef __bf16 bf16x2_t __attribute__((ext_vector_type(2)));
#define LAS __attribute__((address_space(3)))

__device__ __forceinline__ float bf2f(bf16_t v) { return __uint_as_float(((unsigned)v) << 16); }
__device__ __forceinline__ unsigned pack2(float a, float b) {
  bf16x2_t v = {(__bf16)a, (__bf16)b};
  return *reinterpret_cast<unsigned*>(&v);
}
__device__ __forceinline__ float rbf(float a) {
  __bf16 v = (__bf16)a;
  return __uint_as_float(((unsigned)*reinterpret_cast<unsigned short*>(&v)) << 16);
}

constexpr size_t WS_XN = 0;
constexpr size_t WS_H = WS_XN + (size_t)S_LEN * DM * 2;
constexpr size_t WS_Q = WS_H + (size_t)S_LEN * DM * 2;
constexpr size_t WS_K = WS_Q + (size_t)S_LEN * HDN * 2;
constexpr size_t WS_V = WS_K + (size_t)S_LEN * HDN * 2;
constexpr size_t WS_U = WS_V + (size_t)S_LEN * HDN * 2;
constexpr size_t WS_CTX = WS_U + (size_t)DM * FF * 2;
constexpr size_t WS_PART = WS_CTX + (size_t)S_LEN * CTX_LD * 2;
constexpr size_t WS_UP = WS_PART + (size_t)6 * S_LEN * DM * 4;

__device__ __forceinline__ float wave_sum(float v) {
#pragma unroll
  for (int o = 32; o > 0; o >>= 1) v += __shfl_xor(v, o);
  return v;
}
__device__ __forceinline__ void ln_row(float (&v)[10]) {
  float s = 0.f;
#pragma unroll
  for (int i = 0; i < 10; ++i) s += v[i];
  s = wave_sum(s);
  const float mean = s * (1.0f / DM);
  float q = 0.f;
#pragma unroll
  for (int i = 0; i < 10; ++i) { float d = v[i] - mean; q += d * d; }
  q = wave_sum(q);
  const float mb = rbf(mean);
  const float vb = rbf(q * (1.0f / DM));
  const float den = rbf(sqrtf(rbf(vb + 1e-5f)));
#pragma unroll
  for (int i = 0; i < 10; ++i) v[i] = rbf(rbf(v[i] - mb) / den);
}
__device__ __forceinline__ void touch64k(const void* base) {
  const char* p = (const char*)base + threadIdx.x * 64;
  unsigned a, b, c, d;
  asm volatile("global_load_dword %0, %4, off\n\tglobal_load_dword %1, %5, off\n\tglobal_load_dword %2, %6, off\n\t"
               "global_load_dword %3, %7, off\n\ts_waitcnt vmcnt(0)"
               : "=&v"(a), "=&v"(b), "=&v"(c), "=&v"(d)
               : "v"(p), "v"(p + 16384), "v"(p + 32768), "v"(p + 49152) : "memory");
}
#define PF_WO 0
constexpr int PF_BLOCKS = 32 + 80 + 64 + 64 + 108 + (PF_WO ? 80 + 108 : 0);
__global__ __launch_bounds__(256) void ln_kernel(const bf16_t* __restrict__ x, bf16_t* __restrict__ xn,
                                                 bf16_t* __restrict__ h, const bf16_t* __restrict__ ckv,
                                                 const bf16_t* __restrict__ wq, const bf16_t* __restrict__ wuk,
                                                 const bf16_t* __restrict__ wuv, const bf16_t* __restrict__ w1,
                                                 const bf16_t* __restrict__ wo, const bf16_t* __restrict__ w2) {
  if (blockIdx.x >= S_LEN / 4) {
    int b = blockIdx.x - S_LEN / 4;
    const bf16_t* src;
    if (b < 32) src = ckv;
    else if ((b -= 32) < 64) src = wuk;
    else if ((b -= 64) < 64) src = wuv;
    else if ((b -= 64) < 80) src = wq;
    else if ((b -= 80) < 108) src = w1;
    else if ((b -= 108) < 80) src = wo;
    else { b -= 80; src = w2; }
    touch64k((const char*)src + (size_t)b * 65536);
    return;
  }
  const int lane = threadIdx.x & 63, w = threadIdx.x >> 6;
  const int row = blockIdx.x * 4 + w;
  const unsigned* xr = reinterpret_cast<const unsigned*>(x + (size_t)row * DM);
  float v[10];
#pragma unroll
  for (int j = 0; j < 5; ++j) {
    unsigned u = xr[lane + 64 * j];
    v[2 * j] = __uint_as_float(u << 16);
    v[2 * j + 1] = __uint_as_float(u & 0xffff0000u);
  }
  ln_row(v);
  unsigned* xo = reinterpret_cast<unsigned*>(xn + (size_t)row * DM);
#pragma unroll
  for (int j = 0; j < 5; ++j) xo[lane + 64 * j] = pack2(v[2 * j], v[2 * j + 1]);
  ln_row(v);
  unsigned* ho = reinterpret_cast<unsigned*>(h + (size_t)row * DM);
#pragma unroll
  for (int j = 0; j < 5; ++j) ho[lane + 64 * j] = pack2(v[2 * j], v[2 * j + 1]);
}

constexpr int STAGE_BYTES = 49152;
constexpr int RBASE = 32768;

__device__ __forceinline__ int swapmid(int p) { return ((p & 1) << 1) | ((p >> 1) & 1); }
__device__ __forceinline__ int rperm(int r) {
  int g = r >> 2;
  int pg = (g & 4) | swapmid(g & 3);
  return pg * 4 + (r & 3);
}
__device__ __forceinline__ s16x4 tr_read(const char LAS* p) {
  return __builtin_amdgcn_ds_read_tr16_b64_v4i16((LAS s16x4*)p);
}
__device__ __forceinline__ bf16x8 cat8(s16x4 a, s16x4 b) {
  return (bf16x8){a[0], a[1], a[2], a[3], b[0], b[1], b[2], b[3]};
}

__device__ __forceinline__ void glds16(const void* sbase, int voff, unsigned lds_dst) {
  unsigned keep;
  asm volatile("s_mov_b32 %0, m0\n\ts_mov_b32 m0, %3\n\ts_nop 0\n\tglobal_load_lds_dwordx4 %1, %2\n\ts_mov_b32 m0, %0"
               : "=&s"(keep) : "v"(voff), "s"(sbase), "s"(lds_dst) : "memory");
}

__device__ __forceinline__ void glds16x4(const void* sbase, int v0, int v1, int v2, int v3, unsigned lds_dst) {
  unsigned keep;
  asm volatile("s_mov_b32 %0, m0\n\ts_mov_b32 m0, %6\n\ts_nop 0\n\tglobal_load_lds_dwordx4 %1, %5\n\t"
               "global_load_lds_dwordx4 %2, %5 offset:1024\n\tglobal_load_lds_dwordx4 %3, %5 offset:2048\n\t"
               "global_load_lds_dwordx4 %4, %5 offset:3072\n\ts_mov_b32 m0, %0"
               : "=&s"(keep) : "v"(v0), "v"(v1), "v"(v2), "v"(v3), "s"(sbase), "s"(lds_dst) : "memory");
}

#define ST_MODE 0
__device__ __forceinline__ void st16(void* p, u32x4 v) {
  *(u32x4*)p = v;
}
__device__ __forceinline__ void st16f(float* p, float a, float b, float c, float d) {
  st16(p, (u32x4){__float_as_uint(a), __float_as_uint(b), __float_as_uint(c), __float_as_uint(d)});
}

template <bool P_TR, bool R_TR, bool ROWS_P>
struct GemmCore {
  int pOff[4], rOff[2];
  int pAddr[4], rAddr[4];
  int w, l;
  __device__ __forceinline__ void init(int ldp, int ldr, int pcol_limit  ) {
    const int tid = threadIdx.x;
    l = tid & 63;
    w = __builtin_amdgcn_readfirstlane(tid >> 6);
    const int wp = w >> 1, wr = w & 1;
#pragma unroll
    for (int i = 0; i < 4; ++i) {
      const int j = w * 4 + i;
      if (!P_TR) {
        const int row = 8 * j + (l >> 3), slot = l & 7;
        const int chunk = slot ^ ((row >> 1) & 7);
        pOff[i] = row * ldp + chunk * 8;
      } else {
        const int half = j >> 4, jj = j & 15;
        const int krow = 4 * jj + (l >> 4), s16 = l & 15;
        const int c64 = (s16 >> 2) ^ (krow & 3);
        int col = half * 128 + c64 * 32 + (s16 & 3) * 8;
        if (col >= pcol_limit) col = pcol_limit - 8;
        pOff[i] = krow * ldp + col;
      }
    }
#pragma unroll
    for (int i = 0; i < 2; ++i) {
      const int j = w * 2 + i;
      if (!R_TR) {
        const int row = 8 * j + (l >> 3), slot = l & 7;
        const int chunk = slot ^ ((row >> 1) & 7);
        rOff[i] = row * ldr + chunk * 8;
      } else {
        const int krow = 4 * j + (l >> 4), s16 = l & 15;
        const int c64 = (s16 >> 2) ^ (krow & 3);
        const int col = c64 * 32 + (s16 & 3) * 8;
        rOff[i] = krow * ldr + col;
      }
    }
    const int r32 = l & 31, hi = l >> 5, blk = (l >> 4) & 1, q = (l & 15) >> 2, p = l & 3;
    if (!P_TR) {
      const int prow = 64 * wp + (ROWS_P ? rperm(r32) : r32);
      const int s = (prow >> 1) & 7;
#pragma unroll
      for (int kk = 0; kk < 4; ++kk) pAddr[kk] = prow * 128 + (((2 * kk + hi) ^ s) << 4);
    } else {
      const int half = wp >> 1, pp = ROWS_P ? swapmid(p) : p;
#pragma unroll
      for (int i = 0; i < 2; ++i) {
        const int c64 = 2 * (wp & 1) + i;
        pAddr[i] = half * 16384 + (8 * hi + q) * 256 + ((c64 ^ q) * 64) + blk * 32 + pp * 8;
      }
      pAddr[2] = pAddr[3] = 0;
    }
    if (!R_TR) {
      const int rrow = 64 * wr + (!ROWS_P ? rperm(r32) : r32);
      const int s = (rrow >> 1) & 7;
#pragma unroll
      for (int kk = 0; kk < 4; ++kk) rAddr[kk] = RBASE + rrow * 128 + (((2 * kk + hi) ^ s) << 4);
    } else {
      const int pp = !ROWS_P ? swapmid(p) : p;
#pragma unroll
      for (int i = 0; i < 2; ++i) {
        const int c64 = 2 * wr + i;
        rAddr[i] = RBASE + (8 * hi + q) * 256 + ((c64 ^ q) * 64) + blk * 32 + pp * 8;
      }
      rAddr[2] = rAddr[3] = 0;
    }
  }
  __device__ __forceinline__ void stage(char LAS* buf, const bf16_t* P, const bf16_t* R) {
    const unsigned b = (unsigned)(uintptr_t)buf;
#pragma unroll
    for (int i = 0; i < 4; ++i) glds16(P, pOff[i] * 2, b + (w * 4 + i) * 1024);
#pragma unroll
    for (int i = 0; i < 2; ++i) glds16(R, rOff[i] * 2, b + RBASE + (w * 2 + i) * 1024);
  }
  __device__ __forceinline__ void compute(const char LAS* buf, f32x16 (&acc)[2][2]) {
#pragma unroll
    for (int kk = 0; kk < 4; ++kk) {
      bf16x8 pf[2], rf[2];
#pragma unroll
      for (int i = 0; i < 2; ++i) {
        if (!P_TR) pf[i] = *(const LAS bf16x8*)(buf + pAddr[kk] + i * 4096);
        else pf[i] = cat8(tr_read(buf + pAddr[i] + kk * 4096), tr_read(buf + pAddr[i] + kk * 4096 + 1024));
        if (!R_TR) rf[i] = *(const LAS bf16x8*)(buf + rAddr[kk] + i * 4096);
        else rf[i] = cat8(tr_read(buf + rAddr[i] + kk * 4096), tr_read(buf + rAddr[i] + kk * 4096 + 1024));
      }
#pragma unroll
      for (int i = 0; i < 2; ++i)
#pragma unroll
        for (int j = 0; j < 2; ++j) {
          if (ROWS_P) acc[i][j] = __builtin_amdgcn_mfma_f32_32x32x16_bf16(pf[i], rf[j], acc[i][j], 0, 0, 0);
          else acc[i][j] = __builtin_amdgcn_mfma_f32_32x32x16_bf16(rf[j], pf[i], acc[i][j], 0, 0, 0);
        }
    }
  }
};

#define WAIT_VM0() asm volatile("s_waitcnt vmcnt(0)" ::: "memory")
#define WAIT_VM6() asm volatile("s_waitcnt vmcnt(6)" ::: "memory")
#define WAIT_VM16() asm volatile("s_waitcnt vmcnt(16)" ::: "memory")
#define BARRIER() __builtin_amdgcn_s_barrier()
#define TS(k, i)
#define PH(i, k)

__device__ __forceinline__ float gelu_tanh(float x) {
  const float y = 0.7978845608028654f * (x + 0.044715f * x * x * x);
  const float e = __expf(2.0f * y);
  const float t = 1.0f - 2.0f / (e + 1.0f);
  return 0.5f * x * (1.0f + t);
}

constexpr int STAGE256 = 32768;
#define NST256 4
struct Core256 {
  int pOff[2], rOff[2];
  int pAddr[2], rAddr[2];
  int w;
  __device__ __forceinline__ void init(int ldp, int ldr) {
    const int tid = threadIdx.x, l = tid & 63;
    w = __builtin_amdgcn_readfirstlane(tid >> 6);
    const int wp = w >> 2, wr = w & 3;
#pragma unroll
    for (int i = 0; i < 2; ++i) {
      const int j = w * 2 + i;
      const int row = 16 * j + (l >> 2), slot = l & 3;
      pOff[i] = (row * ldp + (slot ^ ((row >> 2) & 3)) * 8) * 2;
      const int half = j >> 3, jj = j & 7;
      const int krow = 4 * jj + (l >> 4), s16 = l & 15;
      const int c64 = (s16 >> 2) ^ (krow & 3);
      rOff[i] = (krow * ldr + half * 128 + c64 * 32 + (s16 & 3) * 8) * 2;
    }
    const int r32 = l & 31, hi = l >> 5, blk = (l >> 4) & 1, q = (l & 15) >> 2, p = l & 3;
    const int prow = 128 * wp + r32, s = (prow >> 2) & 3;
#pragma unroll
    for (int kk = 0; kk < 2; ++kk) pAddr[kk] = prow * 64 + (((2 * kk + hi) ^ s) << 4);
#pragma unroll
    for (int j = 0; j < 2; ++j) {
      const int c64 = 2 * (wr & 1) + j;
      rAddr[j] = 16384 + (wr >> 1) * 8192 + (8 * hi + q) * 256 + ((c64 ^ q) * 64) + blk * 32 + swapmid(p) * 8;
    }
  }
  __device__ __forceinline__ void stage(char LAS* buf, const bf16_t* P, const bf16_t* R) {
    const unsigned b = (unsigned)(uintptr_t)buf;
#pragma unroll
    for (int i = 0; i < 2; ++i) glds16(P, pOff[i], b + (w * 2 + i) * 1024);
#pragma unroll
    for (int i = 0; i < 2; ++i) glds16(R, rOff[i], b + 16384 + (w * 2 + i) * 1024);
  }
  __device__ __forceinline__ void compute(const char LAS* buf, f32x16 (&acc)[4][2]) {
#pragma unroll
    for (int kk = 0; kk < 2; ++kk) {
      bf16x8 pf[4], rf[2];
#pragma unroll
      for (int j = 0; j < 2; ++j) rf[j] = cat8(tr_read(buf + rAddr[j] + kk * 4096), tr_read(buf + rAddr[j] + kk * 4096 + 1024));
#pragma unroll
      for (int i = 0; i < 4; ++i) pf[i] = *(const LAS bf16x8*)(buf + pAddr[kk] + i * 2048);
#pragma unroll
      for (int i = 0; i < 4; ++i)
#pragma unroll
        for (int j = 0; j < 2; ++j) acc[i][j] = __builtin_amdgcn_mfma_f32_32x32x16_bf16(rf[j], pf[i], acc[i][j], 0, 0, 0);
    }
  }
  __device__ __forceinline__ void load_frags(const char LAS* buf, bf16x8 (&pf)[2][4], bf16x8 (&rf)[2][2]) {
#pragma unroll
    for (int kk = 0; kk < 2; ++kk) {
#pragma unroll
      for (int j = 0; j < 2; ++j) rf[kk][j] = cat8(tr_read(buf + rAddr[j] + kk * 4096), tr_read(buf + rAddr[j] + kk * 4096 + 1024));
#pragma unroll
      for (int i = 0; i < 4; ++i) pf[kk][i] = *(const LAS bf16x8*)(buf + pAddr[kk] + i * 2048);
    }
  }
  __device__ __forceinline__ void mma(const bf16x8 (&pf)[2][4], const bf16x8 (&rf)[2][2], f32x16 (&acc)[4][2]) {
#pragma unroll
    for (int kk = 0; kk < 2; ++kk)
#pragma unroll
      for (int i = 0; i < 4; ++i)
#pragma unroll
        for (int j = 0; j < 2; ++j) acc[i][j] = __builtin_amdgcn_mfma_f32_32x32x16_bf16(rf[kk][j], pf[kk][i], acc[i][j], 0, 0, 0);
  }
};
#define WAIT_LGKM0() asm volatile("s_waitcnt lgkmcnt(0)" ::: "memory")
#define SCHED0() __builtin_amdgcn_sched_barrier(0)
template <int N> __device__ __forceinline__ void wait_vm() { asm volatile("s_waitcnt vmcnt(%0)" ::"n"(N) : "memory"); }

struct Frag { bf16x8 pf[4], rf[2]; };
struct Desc { const char* p; unsigned stride; int v0, v1; };
template <int IMM> __device__ __forceinline__ void setm0(unsigned base) {
  asm volatile("s_add_u32 m0, %0, %1" ::"s"(base), "n"(IMM) : "memory", "scc");
}
template <int OFF> __device__ __forceinline__ void gl(const void* sbase, int voff) {
  asm volatile("global_load_lds_dwordx4 %0, %1 offset:%2" ::"v"(voff), "s"(sbase), "n"(OFF) : "memory");
}
template <int V> using IC = std::integral_constant<int, V>;
__global__ __launch_bounds__(512, 2) void gemm1_kernel(const bf16_t* __restrict__ xn, const bf16_t* __restrict__ hbuf,
                                                       const bf16_t* __restrict__ ckv, const bf16_t* __restrict__ wq,
                                                       const bf16_t* __restrict__ wuk, const bf16_t* __restrict__ wuv,
                                                       const bf16_t* __restrict__ w1, const bf16_t* __restrict__ b1,
                                                       bf16_t* __restrict__ Qo, bf16_t* __restrict__ Ko,
                                                       bf16_t* __restrict__ Vo, bf16_t* __restrict__ Uo) {
  __shared__ __attribute__((aligned(16))) char smem[4 * STAGE256 + 32768];
  char LAS* lds = (char LAS*)smem;
  const int xcd = blockIdx.x & 7, c = blockIdx.x >> 3;
  const int l = threadIdx.x & 63, w = __builtin_amdgcn_readfirstlane(threadIdx.x >> 6);
  const unsigned sW = (unsigned)(uintptr_t)lds + w * 2048;
  const int r32 = l & 31, hi = l >> 5;
  TS(0, 0);
  const bool isQ = c >= 16;
  const int mbase = (xcd * 90) >> 3, mcnt = (((xcd + 1) * 90) >> 3) - mbase;
  const bool hasMix = isQ && c - 16 < mcnt;
  const int mtile = mbase + (c - 16);
  const int d0 = (mtile % 5) * 128, f0 = (mtile / 5) * 96;
  Desc MP, MR;
  {
    int po[2], ro[2];
#pragma unroll
    for (int i = 0; i < 2; ++i) {
      const int j = w * 2 + i;
      const int krow = 4 * j + (l >> 4), s16 = l & 15;
      po[i] = (krow * DM + ((s16 >> 2) ^ (krow & 3)) * 32 + (s16 & 3) * 8) * 2 - 1024 * i;
      const int jr = j < 12 ? j : j - 4;
      const int cb = jr >> 2, kr = (jr & 3) * 16 + (l >> 2);
      ro[i] = (kr * FF + cb * 32 + (l & 3) * 8) * 2 - 1024 * i;
    }
    MP = Desc{(const char*)(hbuf + d0), 64u * DM * 2, po[0], po[1]};
    MR = Desc{(const char*)(w1 + f0), 64u * FF * 2, ro[0], ro[1]};
  }
  {
    int ntile, tb0, hp;
    const bf16_t* A; const bf16_t* W; bf16_t* O; int K;
    if (!isQ) {
      ntile = 2; tb0 = c >> 2; hp = xcd * 2 + (c & 1);
      A = ckv; K = DC;
      W = (c & 2) ? wuv : wuk; O = (c & 2) ? Vo : Ko;
    } else {
      ntile = 1; tb0 = (c - 16) >> 1; hp = xcd * 2 + (c & 1);
      A = xn; K = DM; W = wq; O = Qo;
    }
    const int ngt = K / 128;
    Core256 g;
    g.init(K, HDN);
    const char* Ap = (const char*)(A + (size_t)tb0 * 256 * K);
    const char* Wp = (const char*)(W + hp * 256);
    const int wp = w >> 2, wr = w & 3;
    constexpr unsigned RSTR = 32u * HDN * 2;
    f32x16 acc[4][2];
    Frag F0, F1;
    auto half = [&](auto first, auto dst, const Frag& cur, Frag& nxt, const char LAS* nb, int nkk, const char* sp, int v0, int v1) {
      auto mm = [&](int i, int j) {
        acc[i][j] = __builtin_amdgcn_mfma_f32_32x32x16_bf16(cur.rf[j], cur.pf[i], first ? (f32x16){} : acc[i][j], 0, 0, 0);
        SCHED0();
      };
      auto rdR = [&](int j) {
        nxt.rf[j] = cat8(tr_read(nb + g.rAddr[j] + nkk * 4096), tr_read(nb + g.rAddr[j] + nkk * 4096 + 1024));
        SCHED0();
      };
      auto rdP = [&](int i) {
        nxt.pf[i] = *(const LAS bf16x8*)(nb + g.pAddr[nkk] + i * 2048);
        SCHED0();
      };
      mm(0, 0); rdR(0);
      mm(0, 1); rdP(0); rdR(1);
      mm(1, 0); rdP(1);
      mm(1, 1); rdP(2);
      mm(2, 0); rdP(3); if (dst >= 0) setm0<(dst >= 0 ? (int)dst : 0)>(sW); SCHED0();
      mm(2, 1); if (dst >= 0) gl<0>(sp, v0); SCHED0();
      mm(3, 0); if (dst >= 0) gl<1024>(sp, v1); SCHED0();
      mm(3, 1);
    };
    auto group = [&](auto VAR, bool second, const Desc& Rc, const Desc& Pn, const Desc& Rn) {
      auto stp = [&](auto J) {
        constexpr int j = J, var = VAR;
        constexpr int dA = (var == 2 && j > 0) ? -1 : ((j + 3) & 3) * STAGE256 + 16384;
        constexpr int dB = var == 2 ? -1 : j * STAGE256;
        const char LAS* cb = lds + j * STAGE256;
        const char LAS* nb = lds + ((j + 1) & 3) * STAGE256;
        if (j == 0) half(std::integral_constant<bool, var == 0>{}, IC<dA>{}, F0, F1, cb, 1, Rc.p, Rc.v0, Rc.v1);
        else half(std::false_type{}, IC<dA>{}, F0, F1, cb, 1, Rn.p + (j - 1) * Rn.stride, Rn.v0, Rn.v1);
        if (var == 2) { if (j == 0) wait_vm<8>(); else if (j == 1) wait_vm<4>(); else if (j == 2) wait_vm<0>(); }
        else if (var == 0 && j < 2) { if (second) wait_vm<24>(); else wait_vm<8>(); }
        else wait_vm<8>();
        WAIT_LGKM0();
        SCHED0();
        BARRIER();
        SCHED0();
        half(std::false_type{}, IC<dB>{}, F1, F0, nb, 0, Pn.p + j * Pn.stride, Pn.v0, Pn.v1);
      };
      stp(IC<0>{}); stp(IC<1>{}); stp(IC<2>{}); stp(IC<3>{});
    };
    const int pv0 = g.pOff[0], pv1 = g.pOff[1] - 1024, rv0 = g.rOff[0], rv1 = g.rOff[1] - 1024;
    setm0<0>(sW); SCHED0(); asm volatile("s_nop 0"); gl<0>(Ap, pv0); gl<1024>(Ap, pv1);
    setm0<16384>(sW); SCHED0(); asm volatile("s_nop 0"); gl<0>(Wp, rv0); gl<1024>(Wp, rv1);
    setm0<STAGE256>(sW); SCHED0(); asm volatile("s_nop 0"); gl<0>(Ap + 64, pv0); gl<1024>(Ap + 64, pv1);
    setm0<STAGE256 + 16384>(sW); SCHED0(); asm volatile("s_nop 0"); gl<0>(Wp + RSTR, rv0); gl<1024>(Wp + RSTR, rv1);
    setm0<2 * STAGE256>(sW); SCHED0(); asm volatile("s_nop 0"); gl<0>(Ap + 128, pv0); gl<1024>(Ap + 128, pv1);
    setm0<2 * STAGE256 + 16384>(sW); SCHED0(); asm volatile("s_nop 0"); gl<0>(Wp + 2 * RSTR, rv0); gl<1024>(Wp + 2 * RSTR, rv1);
    setm0<3 * STAGE256>(sW); SCHED0(); asm volatile("s_nop 0"); gl<0>(Ap + 192, pv0); gl<1024>(Ap + 192, pv1);
    wait_vm<10>();
    BARRIER();
#pragma unroll
    for (int j = 0; j < 2; ++j) F0.rf[j] = cat8(tr_read(lds + g.rAddr[j]), tr_read(lds + g.rAddr[j] + 1024));
#pragma unroll
    for (int i = 0; i < 4; ++i) F0.pf[i] = *(const LAS bf16x8*)(lds + g.pAddr[0] + i * 2048);
    SCHED0();
    for (int t = 0; t < ntile; ++t) {
      const int tb = tb0 + 4 * t;
      auto run = [&](auto VAR, int gi) {
        const Desc Rc{Wp + (size_t)(4 * gi + 3) * RSTR, RSTR, rv0, rv1};
        const bool tl = gi + 1 == ngt;
        const bool sl = tl && t + 1 == ntile;
        const int gn = tl ? 0 : gi + 1;
        Desc Pn{Ap + (size_t)(tl && !sl ? t + 1 : t) * 1024 * K * 2 + gn * 256, 64u, pv0, pv1};
        Desc Rn{Wp + (size_t)(4 * gn) * RSTR, RSTR, rv0, rv1};
        if (sl && hasMix) { Pn = MP; Rn = MR; }
        SCHED0();
        group(VAR, t > 0, Rc, Pn, Rn);
      };
      run(IC<0>{}, 0);
      for (int gi = 1; gi < ngt; ++gi) run(IC<1>{}, gi);
      if (hasMix) {
        setm0<3 * STAGE256 + 16384>(sW); SCHED0(); asm volatile("s_nop 0");
        gl<0>(MR.p + 3 * MR.stride, MR.v0); gl<1024>(MR.p + 3 * MR.stride, MR.v1);
      }
      const int head = hp * 2 + (wr >> 1);
      char LAS* slab = lds + 4 * STAGE256 + w * 4096;
      const int wa = r32 * 128, wsw = r32 & 7;
      const int rrow = l >> 3, ra = rrow * 128 + (((l & 7) ^ rrow) << 4);
      bf16_t* obase = O + ((size_t)head * S_LEN + tb * 256 + 128 * wp + rrow) * HD + 64 * (wr & 1) + (l & 7) * 8;
#pragma unroll
      for (int i = 0; i < 4; ++i) {
#pragma unroll
        for (int j = 0; j < 2; ++j) {
          const f32x16 a = acc[i][j];
          u32x4 v0 = {pack2(a[0], a[1]), pack2(a[2], a[3]), pack2(a[4], a[5]), pack2(a[6], a[7])};
          u32x4 v1 = {pack2(a[8], a[9]), pack2(a[10], a[11]), pack2(a[12], a[13]), pack2(a[14], a[15])};
          *(u32x4 LAS*)(slab + wa + (((4 * j + hi) ^ wsw) << 4)) = v0;
          *(u32x4 LAS*)(slab + wa + (((4 * j + 2 + hi) ^ wsw) << 4)) = v1;
        }
#pragma unroll
        for (int it = 0; it < 4; ++it) {
          const u32x4 v = *(const u32x4 LAS*)(slab + ra + it * 1024);
          st16(obase + (size_t)(32 * i + 8 * it) * HD, v);
        }
      }
      SCHED0();
    }
  }
  TS(0, 1);
  if (isQ && !hasMix) {
    const int nW = 16 - mcnt, wi = c - 16 - mcnt;
    const int fb = ((mbase / 5) * 192) & ~127;
    unsigned acc0 = 0;
    for (int idx = wi * 512 + (int)threadIdx.x; idx < S_LEN * 16; idx += nW * 512) {
      const int row = idx >> 4, sl = idx & 15;
      const char* a = sl < 10 ? (const char*)hbuf + row * 1280 + sl * 128
                              : (const char*)w1 + (size_t)row * 3456 + min(fb + (sl - 10) * 128, 3456 - 128);
      asm volatile("global_load_dword %0, %1, off" : "+v"(acc0) : "v"(a) : "memory");
    }
    WAIT_VM0();
    asm volatile("" ::"v"(acc0));
  }
  if (!hasMix) wait_vm<(ST_MODE == 9 ? 0 : 16)>();
  if (hasMix) {
    const int ws = w & 3, kh = w >> 2;
    const int blk = (l >> 4) & 1, q = (l & 15) >> 2, p = l & 3;
    const int pB = (32 * kh + 8 * hi + q) * 256 + ((ws ^ q) * 64) + blk * 32 + p * 8;
    const int rA = 16384 + (32 * kh + 8 * hi + q) * 64 + blk * 32 + swapmid(p) * 8;
    f32x16 acc[3] = {};
    bf16x8 pa[2], ra[2][3], pb[2], rb[2][3];
    auto mstep = [&](auto VAR, auto J, const bf16x8 (&pc)[2], const bf16x8 (&rc)[2][3], bf16x8 (&pn)[2], bf16x8 (&rn)[2][3],
                     const char* sp, const char* sr) {
      constexpr int j = J, var = VAR;
      if (var == 0) { if (j < 3) wait_vm<24>(); else wait_vm<8>(); }
      else if (var == 1) wait_vm<8>();
      else { if (j == 0) wait_vm<8>(); else if (j == 1) wait_vm<4>(); else if (j == 2) wait_vm<0>(); }
      WAIT_LGKM0();
      SCHED0();
      BARRIER();
      SCHED0();
      const char LAS* nb = lds + ((j + 1) & 3) * STAGE256;
      auto mm = [&](int k2, int jj) {
        acc[jj] = __builtin_amdgcn_mfma_f32_32x32x16_bf16(rc[k2][jj], pc[k2], acc[jj], 0, 0, 0);
        SCHED0();
      };
      auto rdP = [&](int k2) { pn[k2] = cat8(tr_read(nb + pB + k2 * 4096), tr_read(nb + pB + k2 * 4096 + 1024)); SCHED0(); };
      auto rdR = [&](int k2, int jj) {
        rn[k2][jj] = cat8(tr_read(nb + rA + jj * 4096 + k2 * 1024), tr_read(nb + rA + jj * 4096 + k2 * 1024 + 256));
        SCHED0();
      };
      mm(0, 0); rdP(0); rdR(0, 0); rdR(0, 1);
      mm(0, 1); rdR(0, 2); rdP(1); rdR(1, 0);
      mm(0, 2); rdR(1, 1); rdR(1, 2); if (var != 2) setm0<j * STAGE256>(sW); SCHED0();
      mm(1, 0); if (var != 2) { gl<0>(sp, MP.v0); gl<1024>(sp, MP.v1); setm0<j * STAGE256 + 16384>(sW); } SCHED0();
      mm(1, 1); if (var != 2) gl<0>(sr, MR.v0); SCHED0();
      mm(1, 2); if (var != 2) gl<1024>(sr, MR.v1); SCHED0();
    };
    auto mgroup = [&](auto VAR, int m) {
      const char* sp = MP.p + (size_t)(m + 4) * MP.stride;
      const char* sr = MR.p + (size_t)(m + 4) * MR.stride;
      mstep(VAR, IC<0>{}, pa, ra, pb, rb, sp, sr);
      mstep(VAR, IC<1>{}, pb, rb, pa, ra, sp + MP.stride, sr + MR.stride);
      mstep(VAR, IC<2>{}, pa, ra, pb, rb, sp + 2 * MP.stride, sr + 2 * MR.stride);
      mstep(VAR, IC<3>{}, pb, rb, pa, ra, sp + 3 * MP.stride, sr + 3 * MR.stride);
    };
    wait_vm<28>();
    BARRIER();
#pragma unroll
    for (int k2 = 0; k2 < 2; ++k2) {
      pa[k2] = cat8(tr_read(lds + pB + k2 * 4096), tr_read(lds + pB + k2 * 4096 + 1024));
#pragma unroll
      for (int j = 0; j < 3; ++j) ra[k2][j] = cat8(tr_read(lds + rA + j * 4096 + k2 * 1024), tr_read(lds + rA + j * 4096 + k2 * 1024 + 256));
    }
    SCHED0();
    mgroup(IC<0>{}, 0);
    for (int m = 4; m < 28; m += 4) mgroup(IC<1>{}, m);
    mgroup(IC<2>{}, 28);
    WAIT_LGKM0();
    TS(0, 3);
    BARRIER();
    float LAS* dst = (float LAS*)(lds + w * 8192);
    const f32x16 sa = kh == 0 ? acc[2] : acc[0], ka = kh == 0 ? acc[0] : acc[2];
    const int so = kh == 0 ? 8 : 0;
#pragma unroll
    for (int g = 0; g < 4; ++g) *(f32x4 LAS*)(dst + (g * 64 + l) * 4) = (f32x4){sa[4 * g], sa[4 * g + 1], sa[4 * g + 2], sa[4 * g + 3]};
    float s1[8], k1[8];
#pragma unroll
    for (int e = 0; e < 8; ++e) { s1[e] = kh == 0 ? acc[1][8 + e] : acc[1][e]; k1[e] = kh == 0 ? acc[1][e] : acc[1][8 + e]; }
#pragma unroll
    for (int g = 0; g < 2; ++g) *(f32x4 LAS*)(dst + ((4 + g) * 64 + l) * 4) = (f32x4){s1[4 * g], s1[4 * g + 1], s1[4 * g + 2], s1[4 * g + 3]};
    (void)so;
    WAIT_LGKM0();
    BARRIER();
    const float LAS* src = (const float LAS*)(lds + (w ^ 4) * 8192);
    bf16_t* urow = Uo + (size_t)(d0 + 32 * ws + r32) * FF + f0 + 8 * hi;
    auto fin8 = [&](const float (&v)[8], int fo) {
      const u32x4 bv = *(const u32x4*)(b1 + f0 + 8 * hi + fo);
      float r[8];
#pragma unroll
      for (int e = 0; e < 4; ++e) {
        r[2 * e] = gelu_tanh(rbf(rbf(v[2 * e]) + __uint_as_float(bv[e] << 16)));
        r[2 * e + 1] = gelu_tanh(rbf(rbf(v[2 * e + 1]) + __uint_as_float(bv[e] & 0xffff0000u)));
      }
      st16(urow + fo, (u32x4){pack2(r[0], r[1]), pack2(r[2], r[3]), pack2(r[4], r[5]), pack2(r[6], r[7])});
    };
    const int jk = kh == 0 ? 0 : 2;
#pragma unroll
    for (int hh = 0; hh < 2; ++hh) {
      float v[8];
#pragma unroll
      for (int g = 0; g < 2; ++g) {
        const f32x4 o = *(const f32x4 LAS*)(src + ((2 * hh + g) * 64 + l) * 4);
#pragma unroll
        for (int e = 0; e < 4; ++e) v[4 * g + e] = ka[8 * hh + 4 * g + e] + o[e];
      }
      fin8(v, 32 * jk + 16 * hh);
    }
    {
      float v[8];
#pragma unroll
      for (int g = 0; g < 2; ++g) {
        const f32x4 o = *(const f32x4 LAS*)(src + ((4 + g) * 64 + l) * 4);
#pragma unroll
        for (int e = 0; e < 4; ++e) v[4 * g + e] = k1[4 * g + e] + o[e];
      }
      fin8(v, 32 + 16 * kh);
    }
  }
  TS(0, 2);
}

constexpr int AT_HALF = 16384;
__global__ __launch_bounds__(512, 2) void attn_kernel(const bf16_t* __restrict__ Q, const bf16_t* __restrict__ K,
                                                      const bf16_t* __restrict__ V, bf16_t* __restrict__ ctx,
                                                      const bf16_t* __restrict__ wo, const bf16_t* __restrict__ w2) {
  __shared__ __attribute__((aligned(16))) char smem[8 * AT_HALF + 32768];
  char LAS* lds = (char LAS*)smem;
  const int tid = threadIdx.x, l = tid & 63, w = __builtin_amdgcn_readfirstlane(tid >> 6);
  const int rg = w & 3, g = w >> 2, r32 = l & 31, hi = l >> 5;
  const int xcd = blockIdx.x & 7, jj = blockIdx.x >> 3;
  const int head = xcd * 4 + (jj >> 3), pair = jj & 7;
  const int LB = 15 - pair, nshort = 2 * pair + 2;
  const bf16_t* Kh = K + (size_t)head * S_LEN * HD;
  const bf16_t* Vh = V + (size_t)head * S_LEN * HD;
  const bf16_t* Qh = Q + (size_t)head * S_LEN * HD;
  char LAS* gl = lds + g * 4 * AT_HALF;
  int kOff[4], vOff[4];
#pragma unroll
  for (int i = 0; i < 4; ++i) {
    const int j = rg * 4 + i, row = 4 * j + (l >> 4), s16 = l & 15;
    kOff[i] = (row * HD + ((s16 ^ (row & 15)) * 8)) * 2 - 1024 * i;
    vOff[i] = (row * HD + ((((s16 >> 2) ^ (row & 3)) * 4 + (s16 & 3)) * 8)) * 2 - 1024 * i;
  }
  const unsigned glb = (unsigned)(uintptr_t)gl;
  auto htile = [&](int i) { return (g == 0 || i < nshort) ? i : i + 15 - 2 * pair; };
  auto stageK = [&](int i) {
    const bf16_t* kp = Kh + (size_t)htile(i > 16 ? 16 : i) * 64 * HD;
    const unsigned bb = glb + (i & 1) * AT_HALF + rg * 4096;
    glds16x4(kp, kOff[0], kOff[1], kOff[2], kOff[3], bb);
  };
  auto stageV = [&](int i) {
    const bf16_t* vp = Vh + (size_t)htile(i) * 64 * HD;
    const unsigned bb = glb + (2 + (i & 1)) * AT_HALF + rg * 4096;
    glds16x4(vp, vOff[0], vOff[1], vOff[2], vOff[3], bb);
  };
  const int ksw = r32 & 15;
  const int blk = (l >> 4) & 1, q4 = (l & 15) >> 2, p4 = l & 3;
  int vAddr[4];
#pragma unroll
  for (int db = 0; db < 4; ++db)
    vAddr[db] = 2 * AT_HALF + (4 * hi + q4) * 256 + ((db ^ q4) * 64) + blk * 32 + swapmid(p4) * 8;
  constexpr float CS = 0.08838834764831845f * 1.4426950408889634f;

  TS(1, 0);
  stageK(0);
  stageV(0);
  stageK(1);
  int qb = g == 0 ? LB : pair;
  bf16x8 qf[8];
#pragma unroll
  for (int d0 = 0; d0 < 8; ++d0) qf[d0] = *(const bf16x8*)(Qh + (size_t)(qb * 128 + 32 * rg + r32) * HD + d0 * 16 + hi * 8);
  char LAS* qsave = lds + 8 * AT_HALF + rg * 8192 + l * 16;
  if (g == 0) {
#pragma unroll
    for (int d0 = 0; d0 < 8; ++d0) *(bf16x8 LAS*)(qsave + d0 * 1024) = qf[d0];
  }
  float m_run = -1e30f, l_run = 0.f, alpha = 1.f;
  f32x16 o[4] = {};
  f32x16 s0, s1;
  bf16x8 pf[4];
  u32x2 pfd = {0u, 0u};
  constexpr int AT_VM = 5;
#define AT_W 6
  bf16x8 fr[32];
  const int kAddr0 = r32 * 256 + ((hi ^ ksw) << 4);
  auto rdK = [&](int k, int slot) {
    fr[k] = *(const LAS bf16x8*)(gl + slot * AT_HALF + (kAddr0 ^ ((k >> 1) << 5)) + (k & 1) * 8192);
  };
  auto rdV = [&](int k, int slot) {
    const char LAS* a = gl + slot * AT_HALF + vAddr[(k - 16) >> 2] + (k & 3) * 4096;
    fr[k] = cat8(tr_read(a), tr_read(a + 2048));
  };
  auto mm = [&](int k) {
    if (k < 16) {
      if (k & 1) s1 = __builtin_amdgcn_mfma_f32_32x32x16_bf16(fr[k], qf[k >> 1], k < 2 ? (f32x16){} : s1, 0, 0, 0);
      else s0 = __builtin_amdgcn_mfma_f32_32x32x16_bf16(fr[k], qf[k >> 1], k < 2 ? (f32x16){} : s0, 0, 0, 0);
    } else {
      o[(k - 16) >> 2] = __builtin_amdgcn_mfma_f32_32x32x16_bf16(fr[k], pf[k & 3], o[(k - 16) >> 2], 0, 0, 0);
    }
  };
  auto preK = [&](int slot) {
#pragma unroll
    for (int k = 0; k < AT_W; ++k) rdK(k, slot);
  };
  auto preV = [&](int slot) {
#pragma unroll
    for (int k = 16; k < 16 + AT_W; ++k) rdV(k, slot);
  };
  auto xhalf_max = [&](float v) {
    auto rr = __builtin_amdgcn_permlane32_swap(__float_as_uint(v), __float_as_uint(v), false, false);
    return fmaxf(__uint_as_float(rr[0]), __uint_as_float(rr[1]));
  };
  auto xhalf_sum = [&](float v) {
    auto rr = __builtin_amdgcn_permlane32_swap(__float_as_uint(v), __float_as_uint(v), false, false);
    return __uint_as_float(rr[0]) + __uint_as_float(rr[1]);
  };
  auto softmax = [&](int i) {
    if (g == 1 && i == nshort) {
      const float inv = 1.0f / xhalf_sum(l_run);
      bf16_t* orow = ctx + (size_t)(pair * 128 + 32 * rg + r32) * CTX_LD + head * HD + 8 * hi;
#pragma unroll
      for (int db = 0; db < 4; ++db) {
        const f32x16 a = o[db];
        u32x4 v0 = {pack2(a[0] * inv, a[1] * inv), pack2(a[2] * inv, a[3] * inv), pack2(a[4] * inv, a[5] * inv), pack2(a[6] * inv, a[7] * inv)};
        u32x4 v1 = {pack2(a[8] * inv, a[9] * inv), pack2(a[10] * inv, a[11] * inv), pack2(a[12] * inv, a[13] * inv), pack2(a[14] * inv, a[15] * inv)};
        st16(orow + 32 * db, v0);
        st16(orow + 32 * db + 16, v1);
        o[db] = (f32x16){};
      }
      m_run = -1e30f;
      l_run = 0.f;
      qb = LB;
    }
    if (g == 1 && i == nshort - 1) {
#pragma unroll
      for (int d0 = 0; d0 < 8; ++d0) qf[d0] = *(const bf16x8 LAS*)(qsave + d0 * 1024);
    }
    SCHED0();
    if (i < 16) stageK(i + 2);
    if (i < 16) {
      int idx = (i * 256 + (int)blockIdx.x) * 8 + w;
      if (idx >= 24064) idx -= 24064;
      const char* pb = idx < 10240 ? (const char*)wo + (size_t)idx * 512 : (const char*)w2 + (size_t)(idx - 10240) * 512;
      asm volatile("global_load_dwordx2 %0, %1, %2" : "+v"(pfd) : "v"(l * 8), "s"(pb) : "memory");
    }
    SCHED0(); PH(i, 5); SCHED0();
    const int dh = htile(i) - 2 * qb;
    if (dh >= 0) {
      const int qi = 32 * rg + r32 - 64 * dh;
#pragma unroll
      for (int r = 0; r < 16; ++r) {
        const int key = (r & 3) + 8 * (r >> 2) + 4 * hi;
        if (key > qi) s0[r] = -__builtin_inff();
        if (key + 32 > qi) s1[r] = -__builtin_inff();
      }
    }
    float pm = s0[0];
#pragma unroll
    for (int r = 1; r < 16; ++r) pm = fmaxf(pm, s0[r]);
#pragma unroll
    for (int r = 0; r < 16; ++r) pm = fmaxf(pm, s1[r]);
    pm = xhalf_max(pm);
    const float mn = fmaxf(m_run, pm);
    alpha = __builtin_amdgcn_exp2f((m_run - mn) * CS);
    m_run = mn;
    const float mc = -mn * CS;
    SCHED0(); PH(i, 6); SCHED0();
    float ps = 0.f;
#pragma unroll
    for (int r = 0; r < 16; ++r) { s0[r] = __builtin_amdgcn_exp2f(fmaf(s0[r], CS, mc)); ps += s0[r]; }
#pragma unroll
    for (int r = 0; r < 16; ++r) { s1[r] = __builtin_amdgcn_exp2f(fmaf(s1[r], CS, mc)); ps += s1[r]; }
    l_run = l_run * alpha + ps;
    SCHED0(); PH(i, 7); SCHED0();
    u32x4 a0 = {pack2(s0[0], s0[1]), pack2(s0[2], s0[3]), pack2(s0[4], s0[5]), pack2(s0[6], s0[7])};
    u32x4 a1 = {pack2(s0[8], s0[9]), pack2(s0[10], s0[11]), pack2(s0[12], s0[13]), pack2(s0[14], s0[15])};
    u32x4 a2 = {pack2(s1[0], s1[1]), pack2(s1[2], s1[3]), pack2(s1[4], s1[5]), pack2(s1[6], s1[7])};
    u32x4 a3 = {pack2(s1[8], s1[9]), pack2(s1[10], s1[11]), pack2(s1[12], s1[13]), pack2(s1[14], s1[15])};
    pf[0] = *(bf16x8*)&a0; pf[1] = *(bf16x8*)&a1; pf[2] = *(bf16x8*)&a2; pf[3] = *(bf16x8*)&a3;
    asm volatile("" : "+v"(pf[0]), "+v"(pf[1]), "+v"(pf[2]), "+v"(pf[3]), "+v"(l_run), "+v"(alpha));
  };
#define AT_DK0 16
  WAIT_VM0();
  WAIT_LGKM0();
  BARRIER();
  TS(1, 3);
  if (g == 1) BARRIER();
  SCHED0();
  preK(0);
#pragma unroll
  for (int k = 0; k < 16; ++k) {
    if (k + AT_W < 16) rdK(k + AT_W, 0);
    mm(k);
    SCHED0();
  }
  BARRIER();
  for (int i = 0; i < 16; ++i) {
    SCHED0();
    PH(i, 0);
    softmax(i);
    SCHED0();
    const int ks = (i + 1) & 1, vs = i & 1;
    preK(ks);
    SCHED0();
    wait_vm<AT_VM>();
    PH(i, 1);
    BARRIER();
    SCHED0();
    PH(i, 2);
    __builtin_amdgcn_s_setprio(1);
#pragma unroll
    for (int k = 0; k < 32; ++k) {
      if (k + AT_W < 16) rdK(k + AT_W, ks);
      else if (k + AT_W < 32) rdV(k + AT_W, vs);
      mm(k);
      if (k < 16) {
#pragma unroll
        for (int e = 0; e < 4; ++e) {
          float t = o[k >> 2][4 * (k & 3) + e];
          asm volatile("v_mul_f32 %0, %1, %0" : "+v"(t) : "v"(alpha));
          o[k >> 2][4 * (k & 3) + e] = t;
        }
      }
      if (k == AT_DK0) stageV(i + 1);
      SCHED0();
    }
    __builtin_amdgcn_s_setprio(0);
    PH(i, 3);
    wait_vm<AT_VM>();
    PH(i, 4);
    BARRIER();
  }
  SCHED0();
  softmax(16);
#pragma unroll
  for (int db = 0; db < 4; ++db)
#pragma unroll
    for (int r = 0; r < 16; ++r) o[db][r] *= alpha;
  SCHED0();
  WAIT_VM0();
  asm volatile("" ::"v"(pfd));
  BARRIER();
  SCHED0();
  preV(0);
#pragma unroll
  for (int k = 16; k < 32; ++k) {
    if (k + AT_W < 32) rdV(k + AT_W, 0);
    mm(k);
    SCHED0();
  }
  BARRIER();
  if (g == 0) BARRIER();
  l_run = xhalf_sum(l_run);
  TS(1, 1);
  {
    const int kh = g;
    float LAS* ml = (float LAS*)(lds + 4 * AT_HALF);
    float LAS* dst = (float LAS*)(lds + w * 8192);
#pragma unroll
    for (int dd = 0; dd < 2; ++dd) {
      f32x16 a;
#pragma unroll
      for (int r = 0; r < 16; ++r) a[r] = kh == 0 ? o[2 + dd][r] : o[dd][r];
#pragma unroll
      for (int gg = 0; gg < 4; ++gg)
        *(f32x4 LAS*)(dst + ((dd * 4 + gg) * 64 + l) * 4) = (f32x4){a[4 * gg], a[4 * gg + 1], a[4 * gg + 2], a[4 * gg + 3]};
    }
    ml[w * 128 + l] = m_run;
    ml[w * 128 + 64 + l] = l_run;
    asm volatile("s_waitcnt lgkmcnt(0)" ::: "memory");
    BARRIER();
    const int wo = w ^ 4;
    const float m2 = ml[wo * 128 + l], l2 = ml[wo * 128 + 64 + l];
    const float mt = fmaxf(m_run, m2);
    const float a1 = __builtin_amdgcn_exp2f((m_run - mt) * CS), a2 = __builtin_amdgcn_exp2f((m2 - mt) * CS);
    const float inv = 1.0f / (l_run * a1 + l2 * a2);
    const float f1 = a1 * inv, f2 = a2 * inv;
    const float LAS* src = (const float LAS*)(lds + wo * 8192);
    bf16_t* orow = ctx + (size_t)(LB * 128 + 32 * rg + r32) * CTX_LD + head * HD + 8 * hi;
#pragma unroll
    for (int dd = 0; dd < 2; ++dd) {
      f32x16 a;
#pragma unroll
      for (int r = 0; r < 16; ++r) a[r] = kh == 0 ? o[dd][r] : o[2 + dd][r];
      float r[16];
#pragma unroll
      for (int gg = 0; gg < 4; ++gg) {
        const f32x4 b = *(const f32x4 LAS*)(src + ((dd * 4 + gg) * 64 + l) * 4);
#pragma unroll
        for (int e = 0; e < 4; ++e) r[4 * gg + e] = a[4 * gg + e] * f1 + b[e] * f2;
      }
      u32x4 v0 = {pack2(r[0], r[1]), pack2(r[2], r[3]), pack2(r[4], r[5]), pack2(r[6], r[7])};
      u32x4 v1 = {pack2(r[8], r[9]), pack2(r[10], r[11]), pack2(r[12], r[13]), pack2(r[14], r[15])};
      const int dcol = 32 * (2 * kh + dd);
      st16(orow + dcol, v0);
      st16(orow + dcol + 16, v1);
    }
  }
  TS(1, 2);
}

constexpr int G2_STAGE = 36864;
constexpr int G2_NST = 4;
__global__ __launch_bounds__(512, 2) void gemm2_kernel(const bf16_t* __restrict__ ctx, const bf16_t* __restrict__ wo,
                                                       const bf16_t* __restrict__ U, const bf16_t* __restrict__ w2,
                                                       float* __restrict__ part) {
  __shared__ __attribute__((aligned(16))) char smem[G2_NST * G2_STAGE];
  char LAS* lds = (char LAS*)smem;
  const int b = blockIdx.x, xcd = b & 7, c = b >> 3;
  const int ks = xcd >> 1;
  const int s0 = (xcd & 1) * 1024 + (c >> 2) * 128, m0 = (c & 3) * 160;
  const int l = threadIdx.x & 63, w = __builtin_amdgcn_readfirstlane(threadIdx.x >> 6);
  const int ws = w & 3, kh = w >> 2, r32 = l & 31, hi = l >> 5;
  const int blk = (l >> 4) & 1, q = (l & 15) >> 2, p = l & 3;
  TS(2, 0);
  int pOffA[2], pOffB[2], rOffA[3], rOffB[3];
#pragma unroll
  for (int i = 0; i < 2; ++i) {
    const int j = w * 2 + i;
    const int row = 8 * j + (l >> 3), slot = l & 7;
    pOffA[i] = (row * CTX_LD + (slot ^ ((row >> 1) & 7)) * 8) * 2;
    const int krow = 4 * j + (l >> 4), s16 = l & 15;
    pOffB[i] = (krow * S_LEN + ((s16 >> 2) ^ (krow & 3)) * 32 + (s16 & 3) * 8) * 2;
  }
#pragma unroll
  for (int i = 0; i < 3; ++i) {
    const int j = w + 8 * i;
    const int cb = j >> 2, kr = (j & 3) * 16 + (l >> 2);
    rOffA[i] = (kr * DM + cb * 32 + (l & 3) * 8) * 2;
    const int row = 8 * j + (l >> 3), slot = l & 7;
    rOffB[i] = (row * FF + (slot ^ ((row >> 1) & 7)) * 8) * 2;
  }
  const int f0 = ks * 7, nB = ks == 3 ? 6 : 7, total = 16 + nB;
  const bf16_t* PA = ctx + (size_t)s0 * CTX_LD + ks * 1024;
  const bf16_t* RA = wo + (size_t)ks * 1024 * DM + m0;
  const bf16_t* PB = w2 + (size_t)f0 * 64 * S_LEN + s0;
  const bf16_t* RB = U + (size_t)m0 * FF + f0 * 64;
  int pslot = 0, issued = 0;
  auto issue = [&]() {
    const unsigned bb = (unsigned)(uintptr_t)(lds + pslot * G2_STAGE);
    if (issued < 16) {
      const bf16_t* P = PA + issued * 64;
      const bf16_t* R = RA + (size_t)issued * 64 * DM;
      glds16(P, pOffA[0], bb + (w * 2) * 1024);
      glds16(P, pOffA[1], bb + (w * 2 + 1) * 1024);
      glds16(R, rOffA[0], bb + 16384 + w * 1024);
      glds16(R, rOffA[1], bb + 16384 + (w + 8) * 1024);
      if (w < 4) glds16(R, rOffA[2], bb + 16384 + (w + 16) * 1024);
    } else {
      const bf16_t* P = PB + (size_t)(issued - 16) * 64 * S_LEN;
      const bf16_t* R = RB + (issued - 16) * 64;
      glds16(P, pOffB[0], bb + (w * 2) * 1024);
      glds16(P, pOffB[1], bb + (w * 2 + 1) * 1024);
      glds16(R, rOffB[0], bb + 16384 + w * 1024);
      glds16(R, rOffB[1], bb + 16384 + (w + 8) * 1024);
      if (w < 4) glds16(R, rOffB[2], bb + 16384 + (w + 16) * 1024);
    }
    if (++pslot == G2_NST) pslot = 0;
    ++issued;
  };
  const int prow = 32 * ws + r32, psw = (prow >> 1) & 7;
  const int pA0 = prow * 128 + (((4 * kh + hi) ^ psw) << 4), pA1 = prow * 128 + (((4 * kh + 2 + hi) ^ psw) << 4);
  const int rA = 16384 + (32 * kh + 8 * hi + q) * 64 + blk * 32 + swapmid(p) * 8;
  const int pB = (32 * kh + 8 * hi + q) * 256 + ((ws ^ q) * 64) + blk * 32 + p * 8;
  const int rrow = rperm(r32), rsw = (rrow >> 1) & 7;
  const int rB0 = 16384 + rrow * 128 + (((4 * kh + hi) ^ rsw) << 4), rB1 = 16384 + rrow * 128 + (((4 * kh + 2 + hi) ^ rsw) << 4);
#pragma unroll
  for (int i = 0; i < G2_NST - 1; ++i) issue();
  f32x16 acc[5] = {};
  int buf = 0;
  for (int step = 0; step < 16; ++step) {
    if (w < 4) wait_vm<5 * (G2_NST - 2)>();
    else wait_vm<4 * (G2_NST - 2)>();
    BARRIER();
    issue();
    const char LAS* bp = lds + buf * G2_STAGE;
    bf16x8 pf[2], rf[2][5];
    pf[0] = *(const LAS bf16x8*)(bp + pA0);
#pragma unroll
    for (int j = 0; j < 5; ++j) rf[0][j] = cat8(tr_read(bp + rA + j * 4096), tr_read(bp + rA + j * 4096 + 256));
    pf[1] = *(const LAS bf16x8*)(bp + pA1);
#pragma unroll
    for (int j = 0; j < 5; ++j) rf[1][j] = cat8(tr_read(bp + rA + j * 4096 + 1024), tr_read(bp + rA + j * 4096 + 1024 + 256));
    __builtin_amdgcn_sched_barrier(0);
#pragma unroll
    for (int k2 = 0; k2 < 2; ++k2)
#pragma unroll
      for (int j = 0; j < 5; ++j) acc[j] = __builtin_amdgcn_mfma_f32_32x32x16_bf16(rf[k2][j], pf[k2], acc[j], 0, 0, 0);
    if (++buf == G2_NST) buf = 0;
  }
  for (int step = 16; step < total; ++step) {
    if (step + G2_NST - 1 > total) WAIT_VM0();
    else if (w < 4) wait_vm<5 * (G2_NST - 2)>();
    else wait_vm<4 * (G2_NST - 2)>();
    BARRIER();
    if (issued < total) issue();
    const char LAS* bp = lds + buf * G2_STAGE;
    bf16x8 pf[2], rf[2][5];
    pf[0] = cat8(tr_read(bp + pB), tr_read(bp + pB + 1024));
#pragma unroll
    for (int j = 0; j < 5; ++j) rf[0][j] = *(const LAS bf16x8*)(bp + rB0 + j * 4096);
    pf[1] = cat8(tr_read(bp + pB + 4096), tr_read(bp + pB + 4096 + 1024));
#pragma unroll
    for (int j = 0; j < 5; ++j) rf[1][j] = *(const LAS bf16x8*)(bp + rB1 + j * 4096);
    __builtin_amdgcn_sched_barrier(0);
#pragma unroll
    for (int k2 = 0; k2 < 2; ++k2)
#pragma unroll
      for (int j = 0; j < 5; ++j) acc[j] = __builtin_amdgcn_mfma_f32_32x32x16_bf16(rf[k2][j], pf[k2], acc[j], 0, 0, 0);
    if (++buf == G2_NST) buf = 0;
  }
  TS(2, 1);
  BARRIER();
  {
    float LAS* dst = (float LAS*)(lds + w * 12288);
    if (kh == 0) {
#pragma unroll
      for (int j = 3; j < 5; ++j)
#pragma unroll
        for (int g = 0; g < 4; ++g)
          *(f32x4 LAS*)(dst + (((j - 3) * 4 + g) * 64 + l) * 4) = (f32x4){acc[j][4 * g], acc[j][4 * g + 1], acc[j][4 * g + 2], acc[j][4 * g + 3]};
    } else {
#pragma unroll
      for (int j = 0; j < 3; ++j)
#pragma unroll
        for (int g = 0; g < 4; ++g)
          *(f32x4 LAS*)(dst + ((j * 4 + g) * 64 + l) * 4) = (f32x4){acc[j][4 * g], acc[j][4 * g + 1], acc[j][4 * g + 2], acc[j][4 * g + 3]};
    }
    asm volatile("s_waitcnt lgkmcnt(0)" ::: "memory");
    BARRIER();
    const float LAS* src = (const float LAS*)(lds + (w ^ 4) * 12288);
    float* orow = part + ((size_t)ks * S_LEN + s0 + 32 * ws + r32) * DM + m0 + 8 * hi;
    if (kh == 0) {
#pragma unroll
      for (int j = 0; j < 3; ++j)
#pragma unroll
        for (int g = 0; g < 4; ++g) {
          const f32x4 o = *(const f32x4 LAS*)(src + ((j * 4 + g) * 64 + l) * 4);
          st16f(orow + 32 * j + (g >> 1) * 16 + (g & 1) * 4, acc[j][4 * g] + o[0], acc[j][4 * g + 1] + o[1], acc[j][4 * g + 2] + o[2], acc[j][4 * g + 3] + o[3]);
        }
    } else {
#pragma unroll
      for (int j = 3; j < 5; ++j)
#pragma unroll
        for (int g = 0; g < 4; ++g) {
          const f32x4 o = *(const f32x4 LAS*)(src + (((j - 3) * 4 + g) * 64 + l) * 4);
          st16f(orow + 32 * j + (g >> 1) * 16 + (g & 1) * 4, acc[j][4 * g] + o[0], acc[j][4 * g + 1] + o[1], acc[j][4 * g + 2] + o[2], acc[j][4 * g + 3] + o[3]);
        }
    }
  }
  TS(2, 2);
}

__global__ __launch_bounds__(256) void reduce_kernel(const float* __restrict__ part, const bf16_t* __restrict__ x,
                                                     const bf16_t* __restrict__ b2, bf16_t* __restrict__ out) {
  const int i = blockIdx.x * 256 + threadIdx.x;
  const int e = i * 4;
  const int s = e / DM;
  f32x4 a = *(const f32x4*)(part + e);
#pragma unroll
  for (int sp = 1; sp < 4; ++sp) a += *(const f32x4*)(part + (size_t)sp * S_LEN * DM + e);
  const u32x2 xv = *(const u32x2*)(x + e);
  const float bb = bf2f(b2[s]);
  float r0 = __uint_as_float(xv[0] << 16) + a[0] + bb;
  float r1 = __uint_as_float(xv[0] & 0xffff0000u) + a[1] + bb;
  float r2 = __uint_as_float(xv[1] << 16) + a[2] + bb;
  float r3 = __uint_as_float(xv[1] & 0xffff0000u) + a[3] + bb;
  u32x2 o = {pack2(r0, r1), pack2(r2, r3)};
  *(u32x2*)(out + e) = o;
}

extern "C" void kernel_launch(void* const* d_in, const int* in_sizes, int n_in, void* d_out, int out_size, void* d_ws,
                              size_t ws_size, hipStream_t stream) {
  const bf16_t* x = (const bf16_t*)d_in[0];
  const bf16_t* ckv = (const bf16_t*)d_in[1];
  const bf16_t* wq = (const bf16_t*)d_in[2];
  const bf16_t* wuk = (const bf16_t*)d_in[3];
  const bf16_t* wuv = (const bf16_t*)d_in[4];
  const bf16_t* wo = (const bf16_t*)d_in[5];
  const bf16_t* w1 = (const bf16_t*)d_in[7];
  const bf16_t* b1 = (const bf16_t*)d_in[8];
  const bf16_t* w2 = (const bf16_t*)d_in[9];
  const bf16_t* b2 = (const bf16_t*)d_in[10];
  char* ws = (char*)d_ws;
  bf16_t* xn = (bf16_t*)(ws + WS_XN);
  bf16_t* h = (bf16_t*)(ws + WS_H);
  bf16_t* Q = (bf16_t*)(ws + WS_Q);
  bf16_t* K = (bf16_t*)(ws + WS_K);
  bf16_t* V = (bf16_t*)(ws + WS_V);
  bf16_t* U = (bf16_t*)(ws + WS_U);
  bf16_t* ctx = (bf16_t*)(ws + WS_CTX);
  float* part = (float*)(ws + WS_PART);
  ln_kernel<<<S_LEN / 4 + PF_BLOCKS, 256, 0, stream>>>(x, xn, h, ckv, wq, wuk, wuv, w1, wo, w2);
  gemm1_kernel<<<256, 512, 0, stream>>>(xn, h, ckv, wq, wuk, wuv, w1, b1, Q, K, V, U);
  attn_kernel<<<256, 512, 0, stream>>>(Q, K, V, ctx, wo, w2);
  gemm2_kernel<<<256, 512, 0, stream>>>(ctx, wo, U, w2, part);
  reduce_kernel<<<S_LEN * DM / 4 / 256, 256, 0, stream>>>(part, x, b2, (bf16_t*)d_out);
}
```

```cpp
#include <hip/hip_runtime.h>
#include <hip/hip_bf16.h>
#include <stdint.h>
#include <type_traits>

constexpr int S_LEN = 2048;
constexpr int DM = 640;
constexpr int NH = 32;
constexpr int HD = 128;
constexpr int DC = 512;
constexpr int FF = 1728;
constexpr int HDN = NH * HD;
constexpr int CTX_LD = HDN + 64;

typedef unsigned short bf16_t;
typedef short bf16x8 __attribute__((ext_vector_type(8)));
typedef short s16x4 __attribute__((ext_vector_type(4)));
typedef float f32x16 __attribute__((ext_vector_type(16)));
typedef float f32x4 __attribute__((ext_vector_type(4)));
typedef unsigned u32x4 __attribute__((ext_vector_type(4)));
typedef unsigned u32x2 __attribute__((ext_vector_type(2)));
typedef __bf16 bf16x2_t __attribute__((ext_vector_type(2)));
#define LAS __attribute__((address_space(3)))

__device__ __forceinline__ float bf2f(bf16_t v) { return __uint_as_float(((unsigned)v) << 16); }
__device__ __forceinline__ unsigned pack2(float a, float b) {
  bf16x2_t v = {(__bf16)a, (__bf16)b};
  return *reinterpret_cast<unsigned*>(&v);
}
__device__ __forceinline__ float rbf(float a) {
  __bf16 v = (__bf16)a;
  return __uint_as_float(((unsigned)*reinterpret_cast<unsigned short*>(&v)) << 16);
}

template <bool X16> __device__ __forceinline__ f32x16 mf(bf16x8 a, bf16x8 b, f32x16 c, int sel) {
  if (X16) {
    f32x4 q0 = {c[8 * sel], c[8 * sel + 1], c[8 * sel + 2], c[8 * sel + 3]};
    f32x4 q1 = {c[8 * sel + 4], c[8 * sel + 5], c[8 * sel + 6], c[8 * sel + 7]};
    q0 = __builtin_amdgcn_mfma_f32_16x16x32_bf16(a, b, q0, 0, 0, 0);
    q1 = __builtin_amdgcn_mfma_f32_16x16x32_bf16(b, a, q1, 0, 0, 0);
#pragma unroll
    for (int e = 0; e < 4; ++e) { c[8 * sel + e] = q0[e]; c[8 * sel + 4 + e] = q1[e]; }
    return c;
  }
  return __builtin_amdgcn_mfma_f32_32x32x16_bf16(a, b, c, 0, 0, 0);
}
#define X16_G1 0
#define X16_AT 0
#define X16_G2 0
constexpr size_t WS_XN = 0;
constexpr size_t WS_H = WS_XN + (size_t)S_LEN * DM * 2;
constexpr size_t WS_Q = WS_H + (size_t)S_LEN * DM * 2;
constexpr size_t WS_K = WS_Q + (size_t)S_LEN * HDN * 2;
constexpr size_t WS_V = WS_K + (size_t)S_LEN * HDN * 2;
constexpr size_t WS_U = WS_V + (size_t)S_LEN * HDN * 2;
constexpr size_t WS_CTX = WS_U + (size_t)DM * FF * 2;
constexpr size_t WS_PART = WS_CTX + (size_t)S_LEN * CTX_LD * 2;
constexpr size_t WS_UP = WS_PART + (size_t)6 * S_LEN * DM * 4;

__device__ __forceinline__ float wave_sum(float v) {
#pragma unroll
  for (int o = 32; o > 0; o >>= 1) v += __shfl_xor(v, o);
  return v;
}
__device__ __forceinline__ void ln_row(float (&v)[10]) {
  float s = 0.f;
#pragma unroll
  for (int i = 0; i < 10; ++i) s += v[i];
  s = wave_sum(s);
  const float mean = s * (1.0f / DM);
  float q = 0.f;
#pragma unroll
  for (int i = 0; i < 10; ++i) { float d = v[i] - mean; q += d * d; }
  q = wave_sum(q);
  const float mb = rbf(mean);
  const float vb = rbf(q * (1.0f / DM));
  const float den = rbf(sqrtf(rbf(vb + 1e-5f)));
#pragma unroll
  for (int i = 0; i < 10; ++i) v[i] = rbf(rbf(v[i] - mb) / den);
}
__device__ __forceinline__ const char* warm_addr(int kb, int i, const bf16_t* ckv, const bf16_t* wq, const bf16_t* wuk,
                                                 const bf16_t* wuv) {
  if (i >= 24576) return (const char*)ckv + (size_t)((i - 24576) >> 1) * 1024 + kb * 256 + (i & 1) * 128;
  const bf16_t* src = i < 8192 ? wq : i < 16384 ? wuk : wuv;
  return (const char*)src + (size_t)kb * 1048576 + (size_t)(i & 8191) * 128;
}
#define PF_KB 1
constexpr int PF_BLOCKS = 28 * PF_KB + 4;
__global__ __launch_bounds__(256) void ln_kernel(const bf16_t* __restrict__ x, bf16_t* __restrict__ xn,
                                                 bf16_t* __restrict__ h, const bf16_t* __restrict__ ckv,
                                                 const bf16_t* __restrict__ wq, const bf16_t* __restrict__ wuk,
                                                 const bf16_t* __restrict__ wuv) {
  if (blockIdx.x < PF_BLOCKS) {
    if (blockIdx.x < 28 * PF_KB) {
      unsigned keep = 0;
#pragma unroll
      for (int j = 0; j < 4; ++j) {
        const char* a = warm_addr(blockIdx.x / 28, (blockIdx.x % 28) * 1024 + j * 256 + threadIdx.x, ckv, wq, wuk, wuv);
        asm volatile("global_load_dword %0, %1, off" : "+v"(keep) : "v"(a) : "memory");
      }
      asm volatile("s_waitcnt vmcnt(0)" : "+v"(keep)::"memory");
    }
    return;
  }
  const int lane = threadIdx.x & 63, w = threadIdx.x >> 6;
  const int row = (blockIdx.x - PF_BLOCKS) * 4 + w;
  const unsigned* xr = reinterpret_cast<const unsigned*>(x + (size_t)row * DM);
  float v[10];
#pragma unroll
  for (int j = 0; j < 5; ++j) {
    unsigned u = xr[lane + 64 * j];
    v[2 * j] = __uint_as_float(u << 16);
    v[2 * j + 1] = __uint_as_float(u & 0xffff0000u);
  }
  ln_row(v);
  unsigned* xo = reinterpret_cast<unsigned*>(xn + (size_t)row * DM);
#pragma unroll
  for (int j = 0; j < 5; ++j) xo[lane + 64 * j] = pack2(v[2 * j], v[2 * j + 1]);
  ln_row(v);
  unsigned* ho = reinterpret_cast<unsigned*>(h + (size_t)row * DM);
#pragma unroll
  for (int j = 0; j < 5; ++j) ho[lane + 64 * j] = pack2(v[2 * j], v[2 * j + 1]);
}

constexpr int STAGE_BYTES = 49152;
constexpr int RBASE = 32768;

__device__ __forceinline__ int swapmid(int p) { return ((p & 1) << 1) | ((p >> 1) & 1); }
__device__ __forceinline__ int rperm(int r) {
  int g = r >> 2;
  int pg = (g & 4) | swapmid(g & 3);
  return pg * 4 + (r & 3);
}
__device__ __forceinline__ s16x4 tr_read(const char LAS* p) {
  return __builtin_amdgcn_ds_read_tr16_b64_v4i16((LAS s16x4*)p);
}
__device__ __forceinline__ bf16x8 cat8(s16x4 a, s16x4 b) {
  return (bf16x8){a[0], a[1], a[2], a[3], b[0], b[1], b[2], b[3]};
}

__device__ __forceinline__ void glds16(const void* sbase, int voff, unsigned lds_dst) {
  unsigned keep;
  asm volatile("s_mov_b32 %0, m0\n\ts_mov_b32 m0, %3\n\ts_nop 0\n\tglobal_load_lds_dwordx4 %1, %2\n\ts_mov_b32 m0, %0"
               : "=&s"(keep) : "v"(voff), "s"(sbase), "s"(lds_dst) : "memory");
}

__device__ __forceinline__ void glds16x4(const void* sbase, int v0, int v1, int v2, int v3, unsigned lds_dst) {
  unsigned keep;
  asm volatile("s_mov_b32 %0, m0\n\ts_mov_b32 m0, %6\n\ts_nop 0\n\tglobal_load_lds_dwordx4 %1, %5\n\t"
               "global_load_lds_dwordx4 %2, %5 offset:1024\n\tglobal_load_lds_dwordx4 %3, %5 offset:2048\n\t"
               "global_load_lds_dwordx4 %4, %5 offset:3072\n\ts_mov_b32 m0, %0"
               : "=&s"(keep) : "v"(v0), "v"(v1), "v"(v2), "v"(v3), "s"(sbase), "s"(lds_dst) : "memory");
}

#define ST_MODE 0
__device__ __forceinline__ void st16(void* p, u32x4 v) {
  *(u32x4*)p = v;
}
__device__ __forceinline__ void st16f(float* p, float a, float b, float c, float d) {
  st16(p, (u32x4){__float_as_uint(a), __float_as_uint(b), __float_as_uint(c), __float_as_uint(d)});
}

template <bool P_TR, bool R_TR, bool ROWS_P>
struct GemmCore {
  int pOff[4], rOff[2];
  int pAddr[4], rAddr[4];
  int w, l;
  __device__ __forceinline__ void init(int ldp, int ldr, int pcol_limit  ) {
    const int tid = threadIdx.x;
    l = tid & 63;
    w = __builtin_amdgcn_readfirstlane(tid >> 6);
    const int wp = w >> 1, wr = w & 1;
#pragma unroll
    for (int i = 0; i < 4; ++i) {
      const int j = w * 4 + i;
      if (!P_TR) {
        const int row = 8 * j + (l >> 3), slot = l & 7;
        const int chunk = slot ^ ((row >> 1) & 7);
        pOff[i] = row * ldp + chunk * 8;
      } else {
        const int half = j >> 4, jj = j & 15;
        const int krow = 4 * jj + (l >> 4), s16 = l & 15;
        const int c64 = (s16 >> 2) ^ (krow & 3);
        int col = half * 128 + c64 * 32 + (s16 & 3) * 8;
        if (col >= pcol_limit) col = pcol_limit - 8;
        pOff[i] = krow * ldp + col;
      }
    }
#pragma unroll
    for (int i = 0; i < 2; ++i) {
      const int j = w * 2 + i;
      if (!R_TR) {
        const int row = 8 * j + (l >> 3), slot = l & 7;
        const int chunk = slot ^ ((row >> 1) & 7);
        rOff[i] = row * ldr + chunk * 8;
      } else {
        const int krow = 4 * j + (l >> 4), s16 = l & 15;
        const int c64 = (s16 >> 2) ^ (krow & 3);
        const int col = c64 * 32 + (s16 & 3) * 8;
        rOff[i] = krow * ldr + col;
      }
    }
    const int r32 = l & 31, hi = l >> 5, blk = (l >> 4) & 1, q = (l & 15) >> 2, p = l & 3;
    if (!P_TR) {
      const int prow = 64 * wp + (ROWS_P ? rperm(r32) : r32);
      const int s = (prow >> 1) & 7;
#pragma unroll
      for (int kk = 0; kk < 4; ++kk) pAddr[kk] = prow * 128 + (((2 * kk + hi) ^ s) << 4);
    } else {
      const int half = wp >> 1, pp = ROWS_P ? swapmid(p) : p;
#pragma unroll
      for (int i = 0; i < 2; ++i) {
        const int c64 = 2 * (wp & 1) + i;
        pAddr[i] = half * 16384 + (8 * hi + q) * 256 + ((c64 ^ q) * 64) + blk * 32 + pp * 8;
      }
      pAddr[2] = pAddr[3] = 0;
    }
    if (!R_TR) {
      const int rrow = 64 * wr + (!ROWS_P ? rperm(r32) : r32);
      const int s = (rrow >> 1) & 7;
#pragma unroll
      for (int kk = 0; kk < 4; ++kk) rAddr[kk] = RBASE + rrow * 128 + (((2 * kk + hi) ^ s) << 4);
    } else {
      const int pp = !ROWS_P ? swapmid(p) : p;
#pragma unroll
      for (int i = 0; i < 2; ++i) {
        const int c64 = 2 * wr + i;
        rAddr[i] = RBASE + (8 * hi + q) * 256 + ((c64 ^ q) * 64) + blk * 32 + pp * 8;
      }
      rAddr[2] = rAddr[3] = 0;
    }
  }
  __device__ __forceinline__ void stage(char LAS* buf, const bf16_t* P, const bf16_t* R) {
    const unsigned b = (unsigned)(uintptr_t)buf;
#pragma unroll
    for (int i = 0; i < 4; ++i) glds16(P, pOff[i] * 2, b + (w * 4 + i) * 1024);
#pragma unroll
    for (int i = 0; i < 2; ++i) glds16(R, rOff[i] * 2, b + RBASE + (w * 2 + i) * 1024);
  }
  __device__ __forceinline__ void compute(const char LAS* buf, f32x16 (&acc)[2][2]) {
#pragma unroll
    for (int kk = 0; kk < 4; ++kk) {
      bf16x8 pf[2], rf[2];
#pragma unroll
      for (int i = 0; i < 2; ++i) {
        if (!P_TR) pf[i] = *(const LAS bf16x8*)(buf + pAddr[kk] + i * 4096);
        else pf[i] = cat8(tr_read(buf + pAddr[i] + kk * 4096), tr_read(buf + pAddr[i] + kk * 4096 + 1024));
        if (!R_TR) rf[i] = *(const LAS bf16x8*)(buf + rAddr[kk] + i * 4096);
        else rf[i] = cat8(tr_read(buf + rAddr[i] + kk * 4096), tr_read(buf + rAddr[i] + kk * 4096 + 1024));
      }
#pragma unroll
      for (int i = 0; i < 2; ++i)
#pragma unroll
        for (int j = 0; j < 2; ++j) {
          if (ROWS_P) acc[i][j] = __builtin_amdgcn_mfma_f32_32x32x16_bf16(pf[i], rf[j], acc[i][j], 0, 0, 0);
          else acc[i][j] = __builtin_amdgcn_mfma_f32_32x32x16_bf16(rf[j], pf[i], acc[i][j], 0, 0, 0);
        }
    }
  }
};

#define WAIT_VM0() asm volatile("s_waitcnt vmcnt(0)" ::: "memory")
#define WAIT_VM6() asm volatile("s_waitcnt vmcnt(6)" ::: "memory")
#define WAIT_VM16() asm volatile("s_waitcnt vmcnt(16)" ::: "memory")
#define BARRIER() __builtin_amdgcn_s_barrier()
#define TS(k, i)
#define PH(i, k)

__device__ __forceinline__ float gelu_tanh(float x) {
  const float y = 0.7978845608028654f * (x + 0.044715f * x * x * x);
  const float e = __expf(2.0f * y);
  const float t = 1.0f - 2.0f / (e + 1.0f);
  return 0.5f * x * (1.0f + t);
}

constexpr int STAGE256 = 32768;
#define NST256 4
struct Core256 {
  int pOff[2], rOff[2];
  int pAddr[2], rAddr[2];
  int w;
  __device__ __forceinline__ void init(int ldp, int ldr) {
    const int tid = threadIdx.x, l = tid & 63;
    w = __builtin_amdgcn_readfirstlane(tid >> 6);
    const int wp = w >> 2, wr = w & 3;
#pragma unroll
    for (int i = 0; i < 2; ++i) {
      const int j = w * 2 + i;
      const int row = 16 * j + (l >> 2), slot = l & 3;
      pOff[i] = (row * ldp + (slot ^ ((row >> 2) & 3)) * 8) * 2;
      const int half = j >> 3, jj = j & 7;
      const int krow = 4 * jj + (l >> 4), s16 = l & 15;
      const int c64 = (s16 >> 2) ^ (krow & 3);
      rOff[i] = (krow * ldr + half * 128 + c64 * 32 + (s16 & 3) * 8) * 2;
    }
    const int r32 = l & 31, hi = l >> 5, blk = (l >> 4) & 1, q = (l & 15) >> 2, p = l & 3;
    const int prow = 128 * wp + r32, s = (prow >> 2) & 3;
#pragma unroll
    for (int kk = 0; kk < 2; ++kk) pAddr[kk] = prow * 64 + (((2 * kk + hi) ^ s) << 4);
#pragma unroll
    for (int j = 0; j < 2; ++j) {
      const int c64 = 2 * (wr & 1) + j;
      rAddr[j] = 16384 + (wr >> 1) * 8192 + (8 * hi + q) * 256 + ((c64 ^ q) * 64) + blk * 32 + swapmid(p) * 8;
    }
  }
  __device__ __forceinline__ void stage(char LAS* buf, const bf16_t* P, const bf16_t* R) {
    const unsigned b = (unsigned)(uintptr_t)buf;
#pragma unroll
    for (int i = 0; i < 2; ++i) glds16(P, pOff[i], b + (w * 2 + i) * 1024);
#pragma unroll
    for (int i = 0; i < 2; ++i) glds16(R, rOff[i], b + 16384 + (w * 2 + i) * 1024);
  }
  __device__ __forceinline__ void compute(const char LAS* buf, f32x16 (&acc)[4][2]) {
#pragma unroll
    for (int kk = 0; kk < 2; ++kk) {
      bf16x8 pf[4], rf[2];
#pragma unroll
      for (int j = 0; j < 2; ++j) rf[j] = cat8(tr_read(buf + rAddr[j] + kk * 4096), tr_read(buf + rAddr[j] + kk * 4096 + 1024));
#pragma unroll
      for (int i = 0; i < 4; ++i) pf[i] = *(const LAS bf16x8*)(buf + pAddr[kk] + i * 2048);
#pragma unroll
      for (int i = 0; i < 4; ++i)
#pragma unroll
        for (int j = 0; j < 2; ++j) acc[i][j] = __builtin_amdgcn_mfma_f32_32x32x16_bf16(rf[j], pf[i], acc[i][j], 0, 0, 0);
    }
  }
  __device__ __forceinline__ void load_frags(const char LAS* buf, bf16x8 (&pf)[2][4], bf16x8 (&rf)[2][2]) {
#pragma unroll
    for (int kk = 0; kk < 2; ++kk) {
#pragma unroll
      for (int j = 0; j < 2; ++j) rf[kk][j] = cat8(tr_read(buf + rAddr[j] + kk * 4096), tr_read(buf + rAddr[j] + kk * 4096 + 1024));
#pragma unroll
      for (int i = 0; i < 4; ++i) pf[kk][i] = *(const LAS bf16x8*)(buf + pAddr[kk] + i * 2048);
    }
  }
  __device__ __forceinline__ void mma(const bf16x8 (&pf)[2][4], const bf16x8 (&rf)[2][2], f32x16 (&acc)[4][2]) {
#pragma unroll
    for (int kk = 0; kk < 2; ++kk)
#pragma unroll
      for (int i = 0; i < 4; ++i)
#pragma unroll
        for (int j = 0; j < 2; ++j) acc[i][j] = __builtin_amdgcn_mfma_f32_32x32x16_bf16(rf[kk][j], pf[kk][i], acc[i][j], 0, 0, 0);
  }
};
#define WAIT_LGKM0() asm volatile("s_waitcnt lgkmcnt(0)" ::: "memory")
#define SCHED0() __builtin_amdgcn_sched_barrier(0)
template <int N> __device__ __forceinline__ void wait_vm() { asm volatile("s_waitcnt vmcnt(%0)" ::"n"(N) : "memory"); }

struct Frag { bf16x8 pf[4], rf[2]; };
struct Desc { const char* p; unsigned stride; int v0, v1; };
template <int IMM> __device__ __forceinline__ void setm0(unsigned base) {
  asm volatile("s_add_u32 m0, %0, %1" ::"s"(base), "n"(IMM) : "memory", "scc");
}
template <int OFF> __device__ __forceinline__ void gl(const void* sbase, int voff) {
  asm volatile("global_load_lds_dwordx4 %0, %1 offset:%2" ::"v"(voff), "s"(sbase), "n"(OFF) : "memory");
}
template <int V> using IC = std::integral_constant<int, V>;
__global__ __launch_bounds__(512, 2) void gemm1_kernel(const bf16_t* __restrict__ xn, const bf16_t* __restrict__ hbuf,
                                                       const bf16_t* __restrict__ ckv, const bf16_t* __restrict__ wq,
                                                       const bf16_t* __restrict__ wuk, const bf16_t* __restrict__ wuv,
                                                       const bf16_t* __restrict__ w1, const bf16_t* __restrict__ b1,
                                                       const bf16_t* __restrict__ wo, const bf16_t* __restrict__ w2,
                                                       bf16_t* __restrict__ Qo, bf16_t* __restrict__ Ko,
                                                       bf16_t* __restrict__ Vo, bf16_t* __restrict__ Uo) {
  __shared__ __attribute__((aligned(16))) char smem[4 * STAGE256 + 32768];
  char LAS* lds = (char LAS*)smem;
  const int xcd = blockIdx.x & 7, c = blockIdx.x >> 3;
  const int l = threadIdx.x & 63, w = __builtin_amdgcn_readfirstlane(threadIdx.x >> 6);
  const unsigned sW = (unsigned)(uintptr_t)lds + w * 2048;
  const int r32 = l & 31, hi = l >> 5;
  TS(0, 0);
  const bool isQ = c >= 16;
  const int mbase = (xcd * 90) >> 3, mcnt = (((xcd + 1) * 90) >> 3) - mbase;
  const bool hasMix = isQ && c - 16 < mcnt;
  const int spare = xcd * 16 - mbase + (c - 16 - mcnt);
  if (isQ && !hasMix) {
    unsigned keep = 0;
#pragma unroll
    for (int j = 0; j < 5; ++j) {
      const int idx = (j * 38 + spare) * 512 + (int)threadIdx.x + PF_KB * 28672;
      const int kb = idx / 28672, i = idx % 28672;
      const char* a = warm_addr(kb < 4 ? kb : 4, kb < 4 ? i : (i & 8191), ckv, wq, wuk, wuv);
      asm volatile("global_load_dword %0, %1, off" : "+v"(keep) : "v"(a) : "memory");
    }
#pragma unroll
    for (int j = 0; j < 3; ++j) {
      const int idx = (j * 38 + spare) * 512 + (int)threadIdx.x;
      const char* a = idx < 55296 ? (const char*)w1 + (size_t)idx * 128 : (const char*)b1 + (idx % 27) * 128;
      asm volatile("global_load_dword %0, %1, off" : "+v"(keep) : "v"(a) : "memory");
    }
    asm volatile("s_waitcnt vmcnt(0)" : "+v"(keep)::"memory");
  }
  const int mtile = mbase + (c - 16);
  const int d0 = (mtile % 5) * 128, f0 = (mtile / 5) * 96;
  Desc MP, MR;
  {
    int po[2], ro[2];
#pragma unroll
    for (int i = 0; i < 2; ++i) {
      const int j = w * 2 + i;
      const int krow = 4 * j + (l >> 4), s16 = l & 15;
      po[i] = (krow * DM + ((s16 >> 2) ^ (krow & 3)) * 32 + (s16 & 3) * 8) * 2 - 1024 * i;
      const int jr = j < 12 ? j : j - 4;
      const int cb = jr >> 2, kr = (jr & 3) * 16 + (l >> 2);
      ro[i] = (kr * FF + cb * 32 + (l & 3) * 8) * 2 - 1024 * i;
    }
    MP = Desc{(const char*)(hbuf + d0), 64u * DM * 2, po[0], po[1]};
    MR = Desc{(const char*)(w1 + f0), 64u * FF * 2, ro[0], ro[1]};
  }
  {
    int ntile, tb0, hp;
    const bf16_t* A; const bf16_t* W; bf16_t* O; int K;
    if (!isQ) {
      ntile = 2; tb0 = c >> 2; hp = xcd * 2 + (c & 1);
      A = ckv; K = DC;
      W = (c & 2) ? wuv : wuk; O = (c & 2) ? Vo : Ko;
    } else {
      ntile = 1; tb0 = (c - 16) >> 1; hp = xcd * 2 + (c & 1);
      A = xn; K = DM; W = wq; O = Qo;
    }
    const int ngt = K / 128;
    Core256 g;
    g.init(K, HDN);
    const char* Ap = (const char*)(A + (size_t)tb0 * 256 * K);
    const char* Wp = (const char*)(W + hp * 256);
    const int wp = w >> 2, wr = w & 3;
    constexpr unsigned RSTR = 32u * HDN * 2;
    f32x16 acc[4][2];
    Frag F0, F1;
    auto half = [&](auto first, auto dst, const Frag& cur, Frag& nxt, const char LAS* nb, int nkk, const char* sp, int v0, int v1) {
      auto mm = [&](int i, int j) {
        acc[i][j] = mf<X16_G1>(cur.rf[j], cur.pf[i], first ? (f32x16){} : acc[i][j], nkk ^ 1);
        SCHED0();
      };
      auto rdR = [&](int j) {
        nxt.rf[j] = cat8(tr_read(nb + g.rAddr[j] + nkk * 4096), tr_read(nb + g.rAddr[j] + nkk * 4096 + 1024));
        SCHED0();
      };
      auto rdP = [&](int i) {
        nxt.pf[i] = *(const LAS bf16x8*)(nb + g.pAddr[nkk] + i * 2048);
        SCHED0();
      };
      mm(0, 0); rdR(0);
      mm(0, 1); rdP(0); rdR(1);
      mm(1, 0); rdP(1);
      mm(1, 1); rdP(2);
      mm(2, 0); rdP(3); if (dst >= 0) setm0<(dst >= 0 ? (int)dst : 0)>(sW); SCHED0();
      mm(2, 1); if (dst >= 0) gl<0>(sp, v0); SCHED0();
      mm(3, 0); if (dst >= 0) gl<1024>(sp, v1); SCHED0();
      mm(3, 1);
    };
    auto group = [&](auto VAR, bool second, const Desc& Rc, const Desc& Pn, const Desc& Rn) {
      auto stp = [&](auto J) {
        constexpr int j = J, var = VAR;
        constexpr int dA = (var == 2 && j > 0) ? -1 : ((j + 3) & 3) * STAGE256 + 16384;
        constexpr int dB = var == 2 ? -1 : j * STAGE256;
        const char LAS* cb = lds + j * STAGE256;
        const char LAS* nb = lds + ((j + 1) & 3) * STAGE256;
        if (j == 0) half(std::integral_constant<bool, var == 0>{}, IC<dA>{}, F0, F1, cb, 1, Rc.p, Rc.v0, Rc.v1);
        else half(std::false_type{}, IC<dA>{}, F0, F1, cb, 1, Rn.p + (j - 1) * Rn.stride, Rn.v0, Rn.v1);
        if (var == 2) { if (j == 0) wait_vm<8>(); else if (j == 1) wait_vm<4>(); else if (j == 2) wait_vm<0>(); }
        else if (var == 0 && j < 2) { if (second) wait_vm<24>(); else wait_vm<8>(); }
        else wait_vm<8>();
        WAIT_LGKM0();
        SCHED0();
        BARRIER();
        SCHED0();
        half(std::false_type{}, IC<dB>{}, F1, F0, nb, 0, Pn.p + j * Pn.stride, Pn.v0, Pn.v1);
      };
      stp(IC<0>{}); stp(IC<1>{}); stp(IC<2>{}); stp(IC<3>{});
    };
    const int pv0 = g.pOff[0], pv1 = g.pOff[1] - 1024, rv0 = g.rOff[0], rv1 = g.rOff[1] - 1024;
    setm0<0>(sW); SCHED0(); asm volatile("s_nop 0"); gl<0>(Ap, pv0); gl<1024>(Ap, pv1);
    setm0<16384>(sW); SCHED0(); asm volatile("s_nop 0"); gl<0>(Wp, rv0); gl<1024>(Wp, rv1);
    setm0<STAGE256>(sW); SCHED0(); asm volatile("s_nop 0"); gl<0>(Ap + 64, pv0); gl<1024>(Ap + 64, pv1);
    setm0<STAGE256 + 16384>(sW); SCHED0(); asm volatile("s_nop 0"); gl<0>(Wp + RSTR, rv0); gl<1024>(Wp + RSTR, rv1);
    setm0<2 * STAGE256>(sW); SCHED0(); asm volatile("s_nop 0"); gl<0>(Ap + 128, pv0); gl<1024>(Ap + 128, pv1);
    setm0<2 * STAGE256 + 16384>(sW); SCHED0(); asm volatile("s_nop 0"); gl<0>(Wp + 2 * RSTR, rv0); gl<1024>(Wp + 2 * RSTR, rv1);
    setm0<3 * STAGE256>(sW); SCHED0(); asm volatile("s_nop 0"); gl<0>(Ap + 192, pv0); gl<1024>(Ap + 192, pv1);
    wait_vm<10>();
    BARRIER();
#pragma unroll
    for (int j = 0; j < 2; ++j) F0.rf[j] = cat8(tr_read(lds + g.rAddr[j]), tr_read(lds + g.rAddr[j] + 1024));
#pragma unroll
    for (int i = 0; i < 4; ++i) F0.pf[i] = *(const LAS bf16x8*)(lds + g.pAddr[0] + i * 2048);
    SCHED0();
    for (int t = 0; t < ntile; ++t) {
      const int tb = tb0 + 4 * t;
      auto run = [&](auto VAR, int gi) {
        const Desc Rc{Wp + (size_t)(4 * gi + 3) * RSTR, RSTR, rv0, rv1};
        const bool tl = gi + 1 == ngt;
        const bool sl = tl && t + 1 == ntile;
        const int gn = tl ? 0 : gi + 1;
        Desc Pn{Ap + (size_t)(tl && !sl ? t + 1 : t) * 1024 * K * 2 + gn * 256, 64u, pv0, pv1};
        Desc Rn{Wp + (size_t)(4 * gn) * RSTR, RSTR, rv0, rv1};
        if (sl && hasMix) { Pn = MP; Rn = MR; }
        SCHED0();
        group(VAR, t > 0, Rc, Pn, Rn);
      };
      run(IC<0>{}, 0);
      for (int gi = 1; gi < ngt; ++gi) run(IC<1>{}, gi);
      if (hasMix) {
        setm0<3 * STAGE256 + 16384>(sW); SCHED0(); asm volatile("s_nop 0");
        gl<0>(MR.p + 3 * MR.stride, MR.v0); gl<1024>(MR.p + 3 * MR.stride, MR.v1);
      }
      const int head = hp * 2 + (wr >> 1);
      char LAS* slab = lds + 4 * STAGE256 + w * 4096;
      const int wa = r32 * 128, wsw = r32 & 7;
      const int rrow = l >> 3, ra = rrow * 128 + (((l & 7) ^ rrow) << 4);
      bf16_t* obase = O + ((size_t)head * S_LEN + tb * 256 + 128 * wp + rrow) * HD + 64 * (wr & 1) + (l & 7) * 8;
#pragma unroll
      for (int i = 0; i < 4; ++i) {
#pragma unroll
        for (int j = 0; j < 2; ++j) {
          const f32x16 a = acc[i][j];
          u32x4 v0 = {pack2(a[0], a[1]), pack2(a[2], a[3]), pack2(a[4], a[5]), pack2(a[6], a[7])};
          u32x4 v1 = {pack2(a[8], a[9]), pack2(a[10], a[11]), pack2(a[12], a[13]), pack2(a[14], a[15])};
          *(u32x4 LAS*)(slab + wa + (((4 * j + hi) ^ wsw) << 4)) = v0;
          *(u32x4 LAS*)(slab + wa + (((4 * j + 2 + hi) ^ wsw) << 4)) = v1;
        }
#pragma unroll
        for (int it = 0; it < 4; ++it) {
          const u32x4 v = *(const u32x4 LAS*)(slab + ra + it * 1024);
          st16(obase + (size_t)(32 * i + 8 * it) * HD, v);
        }
      }
      SCHED0();
    }
  }
  TS(0, 1);
  if (!hasMix) wait_vm<(ST_MODE == 9 ? 0 : 16)>();
  if (hasMix) {
    const int ws = w & 3, kh = w >> 2;
    const int blk = (l >> 4) & 1, q = (l & 15) >> 2, p = l & 3;
    const int pB = (32 * kh + 8 * hi + q) * 256 + ((ws ^ q) * 64) + blk * 32 + p * 8;
    const int rA = 16384 + (32 * kh + 8 * hi + q) * 64 + blk * 32 + swapmid(p) * 8;
    f32x16 acc[3] = {};
    bf16x8 pa[2], ra[2][3], pb[2], rb[2][3];
    auto mstep = [&](auto VAR, auto J, const bf16x8 (&pc)[2], const bf16x8 (&rc)[2][3], bf16x8 (&pn)[2], bf16x8 (&rn)[2][3],
                     const char* sp, const char* sr) {
      constexpr int j = J, var = VAR;
      if (var == 0) { if (j < 3) wait_vm<24>(); else wait_vm<8>(); }
      else if (var == 1) wait_vm<8>();
      else { if (j == 0) wait_vm<8>(); else if (j == 1) wait_vm<4>(); else if (j == 2) wait_vm<0>(); }
      WAIT_LGKM0();
      SCHED0();
      BARRIER();
      SCHED0();
      const char LAS* nb = lds + ((j + 1) & 3) * STAGE256;
      auto mm = [&](int k2, int jj) {
        acc[jj] = mf<X16_G1>(rc[k2][jj], pc[k2], acc[jj], k2);
        SCHED0();
      };
      auto rdP = [&](int k2) { pn[k2] = cat8(tr_read(nb + pB + k2 * 4096), tr_read(nb + pB + k2 * 4096 + 1024)); SCHED0(); };
      auto rdR = [&](int k2, int jj) {
        rn[k2][jj] = cat8(tr_read(nb + rA + jj * 4096 + k2 * 1024), tr_read(nb + rA + jj * 4096 + k2 * 1024 + 256));
        SCHED0();
      };
      mm(0, 0); rdP(0); rdR(0, 0); rdR(0, 1);
      mm(0, 1); rdR(0, 2); rdP(1); rdR(1, 0);
      mm(0, 2); rdR(1, 1); rdR(1, 2); if (var != 2) setm0<j * STAGE256>(sW); SCHED0();
      mm(1, 0); if (var != 2) { gl<0>(sp, MP.v0); gl<1024>(sp, MP.v1); setm0<j * STAGE256 + 16384>(sW); } SCHED0();
      mm(1, 1); if (var != 2) gl<0>(sr, MR.v0); SCHED0();
      mm(1, 2); if (var != 2) gl<1024>(sr, MR.v1); SCHED0();
    };
    auto mgroup = [&](auto VAR, int m) {
      const char* sp = MP.p + (size_t)(m + 4) * MP.stride;
      const char* sr = MR.p + (size_t)(m + 4) * MR.stride;
      mstep(VAR, IC<0>{}, pa, ra, pb, rb, sp, sr);
      mstep(VAR, IC<1>{}, pb, rb, pa, ra, sp + MP.stride, sr + MR.stride);
      mstep(VAR, IC<2>{}, pa, ra, pb, rb, sp + 2 * MP.stride, sr + 2 * MR.stride);
      mstep(VAR, IC<3>{}, pb, rb, pa, ra, sp + 3 * MP.stride, sr + 3 * MR.stride);
    };
    wait_vm<28>();
    BARRIER();
#pragma unroll
    for (int k2 = 0; k2 < 2; ++k2) {
      pa[k2] = cat8(tr_read(lds + pB + k2 * 4096), tr_read(lds + pB + k2 * 4096 + 1024));
#pragma unroll
      for (int j = 0; j < 3; ++j) ra[k2][j] = cat8(tr_read(lds + rA + j * 4096 + k2 * 1024), tr_read(lds + rA + j * 4096 + k2 * 1024 + 256));
    }
    SCHED0();
    mgroup(IC<0>{}, 0);
    for (int m = 4; m < 28; m += 4) mgroup(IC<1>{}, m);
    mgroup(IC<2>{}, 28);
    WAIT_LGKM0();
    TS(0, 3);
    BARRIER();
    float LAS* dst = (float LAS*)(lds + w * 8192);
    const f32x16 sa = kh == 0 ? acc[2] : acc[0], ka = kh == 0 ? acc[0] : acc[2];
    const int so = kh == 0 ? 8 : 0;
#pragma unroll
    for (int g = 0; g < 4; ++g) *(f32x4 LAS*)(dst + (g * 64 + l) * 4) = (f32x4){sa[4 * g], sa[4 * g + 1], sa[4 * g + 2], sa[4 * g + 3]};
    float s1[8], k1[8];
#pragma unroll
    for (int e = 0; e < 8; ++e) { s1[e] = kh == 0 ? acc[1][8 + e] : acc[1][e]; k1[e] = kh == 0 ? acc[1][e] : acc[1][8 + e]; }
#pragma unroll
    for (int g = 0; g < 2; ++g) *(f32x4 LAS*)(dst + ((4 + g) * 64 + l) * 4) = (f32x4){s1[4 * g], s1[4 * g + 1], s1[4 * g + 2], s1[4 * g + 3]};
    (void)so;
    WAIT_LGKM0();
    BARRIER();
    const float LAS* src = (const float LAS*)(lds + (w ^ 4) * 8192);
    bf16_t* urow = Uo + (size_t)(d0 + 32 * ws + r32) * FF + f0 + 8 * hi;
    auto fin8 = [&](const float (&v)[8], int fo) {
      const u32x4 bv = *(const u32x4*)(b1 + f0 + 8 * hi + fo);
      float r[8];
#pragma unroll
      for (int e = 0; e < 4; ++e) {
        r[2 * e] = gelu_tanh(rbf(rbf(v[2 * e]) + __uint_as_float(bv[e] << 16)));
        r[2 * e + 1] = gelu_tanh(rbf(rbf(v[2 * e + 1]) + __uint_as_float(bv[e] & 0xffff0000u)));
      }
      st16(urow + fo, (u32x4){pack2(r[0], r[1]), pack2(r[2], r[3]), pack2(r[4], r[5]), pack2(r[6], r[7])});
    };
    const int jk = kh == 0 ? 0 : 2;
#pragma unroll
    for (int hh = 0; hh < 2; ++hh) {
      float v[8];
#pragma unroll
      for (int g = 0; g < 2; ++g) {
        const f32x4 o = *(const f32x4 LAS*)(src + ((2 * hh + g) * 64 + l) * 4);
#pragma unroll
        for (int e = 0; e < 4; ++e) v[4 * g + e] = ka[8 * hh + 4 * g + e] + o[e];
      }
      fin8(v, 32 * jk + 16 * hh);
    }
    {
      float v[8];
#pragma unroll
      for (int g = 0; g < 2; ++g) {
        const f32x4 o = *(const f32x4 LAS*)(src + ((4 + g) * 64 + l) * 4);
#pragma unroll
        for (int e = 0; e < 4; ++e) v[4 * g + e] = k1[4 * g + e] + o[e];
      }
      fin8(v, 32 + 16 * kh);
    }
  }
  TS(0, 2);
}

constexpr int AT_HALF = 16384;
__global__ __launch_bounds__(512, 2) void attn_kernel(const bf16_t* __restrict__ Q, const bf16_t* __restrict__ K,
                                                      const bf16_t* __restrict__ V, bf16_t* __restrict__ ctx,
                                                      const bf16_t* __restrict__ wo, const bf16_t* __restrict__ w2) {
  __shared__ __attribute__((aligned(16))) char smem[8 * AT_HALF + 32768];
  char LAS* lds = (char LAS*)smem;
  const int tid = threadIdx.x, l = tid & 63, w = __builtin_amdgcn_readfirstlane(tid >> 6);
  const int rg = w & 3, g = w >> 2, r32 = l & 31, hi = l >> 5;
  const int xcd = blockIdx.x & 7, jj = blockIdx.x >> 3;
  const int head = xcd * 4 + (jj >> 3), pair = jj & 7;
  const int LB = 15 - pair, nshort = 2 * pair + 2;
  const bf16_t* Kh = K + (size_t)head * S_LEN * HD;
  const bf16_t* Vh = V + (size_t)head * S_LEN * HD;
  const bf16_t* Qh = Q + (size_t)head * S_LEN * HD;
  char LAS* gl = lds + g * 4 * AT_HALF;
  int kOff[4], vOff[4];
#pragma unroll
  for (int i = 0; i < 4; ++i) {
    const int j = rg * 4 + i, row = 4 * j + (l >> 4), s16 = l & 15;
    kOff[i] = (row * HD + ((s16 ^ (row & 15)) * 8)) * 2 - 1024 * i;
    vOff[i] = (row * HD + ((((s16 >> 2) ^ (row & 3)) * 4 + (s16 & 3)) * 8)) * 2 - 1024 * i;
  }
  const unsigned glb = (unsigned)(uintptr_t)gl;
  auto htile = [&](int i) { return (g == 0 || i < nshort) ? i : i + 15 - 2 * pair; };
  auto stageK = [&](int i) {
    const bf16_t* kp = Kh + (size_t)htile(i > 16 ? 16 : i) * 64 * HD;
    const unsigned bb = glb + (i & 1) * AT_HALF + rg * 4096;
    glds16x4(kp, kOff[0], kOff[1], kOff[2], kOff[3], bb);
  };
  auto stageV = [&](int i) {
    const bf16_t* vp = Vh + (size_t)htile(i) * 64 * HD;
    const unsigned bb = glb + (2 + (i & 1)) * AT_HALF + rg * 4096;
    glds16x4(vp, vOff[0], vOff[1], vOff[2], vOff[3], bb);
  };
  const int ksw = r32 & 15;
  const int blk = (l >> 4) & 1, q4 = (l & 15) >> 2, p4 = l & 3;
  int vAddr[4];
#pragma unroll
  for (int db = 0; db < 4; ++db)
    vAddr[db] = 2 * AT_HALF + (4 * hi + q4) * 256 + ((db ^ q4) * 64) + blk * 32 + swapmid(p4) * 8;
  constexpr float CS = 0.08838834764831845f * 1.4426950408889634f;

  TS(1, 0);
  stageK(0);
  stageV(0);
  stageK(1);
  int qb = g == 0 ? LB : pair;
  bf16x8 qf[8];
#pragma unroll
  for (int d0 = 0; d0 < 8; ++d0) qf[d0] = *(const bf16x8*)(Qh + (size_t)(qb * 128 + 32 * rg + r32) * HD + d0 * 16 + hi * 8);
  char LAS* qsave = lds + 8 * AT_HALF + rg * 8192 + l * 16;
  if (g == 0) {
#pragma unroll
    for (int d0 = 0; d0 < 8; ++d0) *(bf16x8 LAS*)(qsave + d0 * 1024) = qf[d0];
  }
  float m_run = -1e30f, l_run = 0.f, alpha = 1.f;
  f32x16 o[4] = {};
  f32x16 s0, s1;
  bf16x8 pf[4];
  u32x2 pfd = {0u, 0u};
  constexpr int AT_VM = 5;
#define AT_W 6
  bf16x8 fr[32];
  const int kAddr0 = r32 * 256 + ((hi ^ ksw) << 4);
  auto rdK = [&](int k, int slot) {
    fr[k] = *(const LAS bf16x8*)(gl + slot * AT_HALF + (kAddr0 ^ ((k >> 1) << 5)) + (k & 1) * 8192);
  };
  auto rdV = [&](int k, int slot) {
    const char LAS* a = gl + slot * AT_HALF + vAddr[(k - 16) >> 2] + (k & 3) * 4096;
    fr[k] = cat8(tr_read(a), tr_read(a + 2048));
  };
  auto mm = [&](int k) {
    if (k < 16) {
      if (k & 1) s1 = mf<X16_AT>(fr[k], qf[k >> 1], k < 2 ? (f32x16){} : s1, (k >> 1) & 1);
      else s0 = mf<X16_AT>(fr[k], qf[k >> 1], k < 2 ? (f32x16){} : s0, (k >> 1) & 1);
    } else {
      o[(k - 16) >> 2] = mf<X16_AT>(fr[k], pf[k & 3], o[(k - 16) >> 2], k & 1);
    }
  };
  auto preK = [&](int slot) {
#pragma unroll
    for (int k = 0; k < AT_W; ++k) rdK(k, slot);
  };
  auto preV = [&](int slot) {
#pragma unroll
    for (int k = 16; k < 16 + AT_W; ++k) rdV(k, slot);
  };
  auto xhalf_max = [&](float v) {
    auto rr = __builtin_amdgcn_permlane32_swap(__float_as_uint(v), __float_as_uint(v), false, false);
    return fmaxf(__uint_as_float(rr[0]), __uint_as_float(rr[1]));
  };
  auto xhalf_sum = [&](float v) {
    auto rr = __builtin_amdgcn_permlane32_swap(__float_as_uint(v), __float_as_uint(v), false, false);
    return __uint_as_float(rr[0]) + __uint_as_float(rr[1]);
  };
  auto softmax = [&](int i) {
    if (g == 1 && i == nshort) {
      const float inv = 1.0f / xhalf_sum(l_run);
      bf16_t* orow = ctx + (size_t)(pair * 128 + 32 * rg + r32) * CTX_LD + head * HD + 8 * hi;
#pragma unroll
      for (int db = 0; db < 4; ++db) {
        const f32x16 a = o[db];
        u32x4 v0 = {pack2(a[0] * inv, a[1] * inv), pack2(a[2] * inv, a[3] * inv), pack2(a[4] * inv, a[5] * inv), pack2(a[6] * inv, a[7] * inv)};
        u32x4 v1 = {pack2(a[8] * inv, a[9] * inv), pack2(a[10] * inv, a[11] * inv), pack2(a[12] * inv, a[13] * inv), pack2(a[14] * inv, a[15] * inv)};
        st16(orow + 32 * db, v0);
        st16(orow + 32 * db + 16, v1);
        o[db] = (f32x16){};
      }
      m_run = -1e30f;
      l_run = 0.f;
      qb = LB;
    }
    if (g == 1 && i == nshort - 1) {
#pragma unroll
      for (int d0 = 0; d0 < 8; ++d0) qf[d0] = *(const bf16x8 LAS*)(qsave + d0 * 1024);
    }
    SCHED0();
    if (i < 16) stageK(i + 2);
    if (i < 16) {
      int idx = (i * 256 + (int)blockIdx.x) * 8 + w;
      if (idx >= 24064) idx -= 24064;
      const char* pb = idx < 10240 ? (const char*)wo + (size_t)idx * 512 : (const char*)w2 + (size_t)(idx - 10240) * 512;
      asm volatile("global_load_dwordx2 %0, %1, %2" : "+v"(pfd) : "v"(l * 8), "s"(pb) : "memory");
    }
    SCHED0(); PH(i, 5); SCHED0();
    const int dh = htile(i) - 2 * qb;
    if (dh >= 0) {
      const int qi = 32 * rg + r32 - 64 * dh;
#pragma unroll
      for (int r = 0; r < 16; ++r) {
        const int key = (r & 3) + 8 * (r >> 2) + 4 * hi;
        if (key > qi) s0[r] = -__builtin_inff();
        if (key + 32 > qi) s1[r] = -__builtin_inff();
      }
    }
    float pm = s0[0];
#pragma unroll
    for (int r = 1; r < 16; ++r) pm = fmaxf(pm, s0[r]);
#pragma unroll
    for (int r = 0; r < 16; ++r) pm = fmaxf(pm, s1[r]);
    pm = xhalf_max(pm);
    const float mn = fmaxf(m_run, pm);
    alpha = __builtin_amdgcn_exp2f((m_run - mn) * CS);
    m_run = mn;
    const float mc = -mn * CS;
    SCHED0(); PH(i, 6); SCHED0();
    float ps = 0.f;
#pragma unroll
    for (int r = 0; r < 16; ++r) { s0[r] = __builtin_amdgcn_exp2f(fmaf(s0[r], CS, mc)); ps += s0[r]; }
#pragma unroll
    for (int r = 0; r < 16; ++r) { s1[r] = __builtin_amdgcn_exp2f(fmaf(s1[r], CS, mc)); ps += s1[r]; }
    l_run = l_run * alpha + ps;
    SCHED0(); PH(i, 7); SCHED0();
    u32x4 a0 = {pack2(s0[0], s0[1]), pack2(s0[2], s0[3]), pack2(s0[4], s0[5]), pack2(s0[6], s0[7])};
    u32x4 a1 = {pack2(s0[8], s0[9]), pack2(s0[10], s0[11]), pack2(s0[12], s0[13]), pack2(s0[14], s0[15])};
    u32x4 a2 = {pack2(s1[0], s1[1]), pack2(s1[2], s1[3]), pack2(s1[4], s1[5]), pack2(s1[6], s1[7])};
    u32x4 a3 = {pack2(s1[8], s1[9]), pack2(s1[10], s1[11]), pack2(s1[12], s1[13]), pack2(s1[14], s1[15])};
    pf[0] = *(bf16x8*)&a0; pf[1] = *(bf16x8*)&a1; pf[2] = *(bf16x8*)&a2; pf[3] = *(bf16x8*)&a3;
    asm volatile("" : "+v"(pf[0]), "+v"(pf[1]), "+v"(pf[2]), "+v"(pf[3]), "+v"(l_run), "+v"(alpha));
  };
#define AT_DK0 16
  WAIT_VM0();
  WAIT_LGKM0();
  BARRIER();
  TS(1, 3);
  if (g == 1) BARRIER();
  SCHED0();
  preK(0);
#pragma unroll
  for (int k = 0; k < 16; ++k) {
    if (k + AT_W < 16) rdK(k + AT_W, 0);
    mm(k);
    SCHED0();
  }
  BARRIER();
  for (int i = 0; i < 16; ++i) {
    SCHED0();
    PH(i, 0);
    softmax(i);
    SCHED0();
    const int ks = (i + 1) & 1, vs = i & 1;
    preK(ks);
    SCHED0();
    wait_vm<AT_VM>();
    PH(i, 1);
    BARRIER();
    SCHED0();
    PH(i, 2);
    __builtin_amdgcn_s_setprio(1);
#pragma unroll
    for (int k = 0; k < 32; ++k) {
      if (k + AT_W < 16) rdK(k + AT_W, ks);
      else if (k + AT_W < 32) rdV(k + AT_W, vs);
      mm(k);
      if (k < 16) {
#pragma unroll
        for (int e = 0; e < 4; ++e) {
          float t = o[k >> 2][4 * (k & 3) + e];
          asm volatile("v_mul_f32 %0, %1, %0" : "+v"(t) : "v"(alpha));
          o[k >> 2][4 * (k & 3) + e] = t;
        }
      }
      if (k == AT_DK0) stageV(i + 1);
      SCHED0();
    }
    __builtin_amdgcn_s_setprio(0);
    PH(i, 3);
    wait_vm<AT_VM>();
    PH(i, 4);
    BARRIER();
  }
  SCHED0();
  softmax(16);
#pragma unroll
  for (int db = 0; db < 4; ++db)
#pragma unroll
    for (int r = 0; r < 16; ++r) o[db][r] *= alpha;
  SCHED0();
  WAIT_VM0();
  asm volatile("" ::"v"(pfd));
  BARRIER();
  SCHED0();
  preV(0);
#pragma unroll
  for (int k = 16; k < 32; ++k) {
    if (k + AT_W < 32) rdV(k + AT_W, 0);
    mm(k);
    SCHED0();
  }
  BARRIER();
  if (g == 0) BARRIER();
  l_run = xhalf_sum(l_run);
  TS(1, 1);
  {
    const int kh = g;
    float LAS* ml = (float LAS*)(lds + 4 * AT_HALF);
    float LAS* dst = (float LAS*)(lds + w * 8192);
#pragma unroll
    for (int dd = 0; dd < 2; ++dd) {
      f32x16 a;
#pragma unroll
      for (int r = 0; r < 16; ++r) a[r] = kh == 0 ? o[2 + dd][r] : o[dd][r];
#pragma unroll
      for (int gg = 0; gg < 4; ++gg)
        *(f32x4 LAS*)(dst + ((dd * 4 + gg) * 64 + l) * 4) = (f32x4){a[4 * gg], a[4 * gg + 1], a[4 * gg + 2], a[4 * gg + 3]};
    }
    ml[w * 128 + l] = m_run;
    ml[w * 128 + 64 + l] = l_run;
    asm volatile("s_waitcnt lgkmcnt(0)" ::: "memory");
    BARRIER();
    const int wo = w ^ 4;
    const float m2 = ml[wo * 128 + l], l2 = ml[wo * 128 + 64 + l];
    const float mt = fmaxf(m_run, m2);
    const float a1 = __builtin_amdgcn_exp2f((m_run - mt) * CS), a2 = __builtin_amdgcn_exp2f((m2 - mt) * CS);
    const float inv = 1.0f / (l_run * a1 + l2 * a2);
    const float f1 = a1 * inv, f2 = a2 * inv;
    const float LAS* src = (const float LAS*)(lds + wo * 8192);
    bf16_t* orow = ctx + (size_t)(LB * 128 + 32 * rg + r32) * CTX_LD + head * HD + 8 * hi;
#pragma unroll
    for (int dd = 0; dd < 2; ++dd) {
      f32x16 a;
#pragma unroll
      for (int r = 0; r < 16; ++r) a[r] = kh == 0 ? o[dd][r] : o[2 + dd][r];
      float r[16];
#pragma unroll
      for (int gg = 0; gg < 4; ++gg) {
        const f32x4 b = *(const f32x4 LAS*)(src + ((dd * 4 + gg) * 64 + l) * 4);
#pragma unroll
        for (int e = 0; e < 4; ++e) r[4 * gg + e] = a[4 * gg + e] * f1 + b[e] * f2;
      }
      u32x4 v0 = {pack2(r[0], r[1]), pack2(r[2], r[3]), pack2(r[4], r[5]), pack2(r[6], r[7])};
      u32x4 v1 = {pack2(r[8], r[9]), pack2(r[10], r[11]), pack2(r[12], r[13]), pack2(r[14], r[15])};
      const int dcol = 32 * (2 * kh + dd);
      st16(orow + dcol, v0);
      st16(orow + dcol + 16, v1);
    }
  }
  TS(1, 2);
}

constexpr int G2_STAGE = 36864;
constexpr int G2_NST = 4;
__global__ __launch_bounds__(512, 2) void gemm2_kernel(const bf16_t* __restrict__ ctx, const bf16_t* __restrict__ wo,
                                                       const bf16_t* __restrict__ U, const bf16_t* __restrict__ w2,
                                                       float* __restrict__ part) {
  __shared__ __attribute__((aligned(16))) char smem[G2_NST * G2_STAGE];
  char LAS* lds = (char LAS*)smem;
  const int b = blockIdx.x, xcd = b & 7, c = b >> 3;
  const int ks = xcd >> 1;
  const int s0 = (xcd & 1) * 1024 + (c >> 2) * 128, m0 = (c & 3) * 160;
  const int l = threadIdx.x & 63, w = __builtin_amdgcn_readfirstlane(threadIdx.x >> 6);
  const int ws = w & 3, kh = w >> 2, r32 = l & 31, hi = l >> 5;
  const int blk = (l >> 4) & 1, q = (l & 15) >> 2, p = l & 3;
  TS(2, 0);
  int pOffA[2], pOffB[2], rOffA[3], rOffB[3];
#pragma unroll
  for (int i = 0; i < 2; ++i) {
    const int j = w * 2 + i;
    const int row = 8 * j + (l >> 3), slot = l & 7;
    pOffA[i] = (row * CTX_LD + (slot ^ ((row >> 1) & 7)) * 8) * 2;
    const int krow = 4 * j + (l >> 4), s16 = l & 15;
    pOffB[i] = (krow * S_LEN + ((s16 >> 2) ^ (krow & 3)) * 32 + (s16 & 3) * 8) * 2;
  }
#pragma unroll
  for (int i = 0; i < 3; ++i) {
    const int j = w + 8 * i;
    const int cb = j >> 2, kr = (j & 3) * 16 + (l >> 2);
    rOffA[i] = (kr * DM + cb * 32 + (l & 3) * 8) * 2;
    const int row = 8 * j + (l >> 3), slot = l & 7;
    rOffB[i] = (row * FF + (slot ^ ((row >> 1) & 7)) * 8) * 2;
  }
  const int f0 = ks * 7, nB = ks == 3 ? 6 : 7, total = 16 + nB;
  const bf16_t* PA = ctx + (size_t)s0 * CTX_LD + ks * 1024;
  const bf16_t* RA = wo + (size_t)ks * 1024 * DM + m0;
  const bf16_t* PB = w2 + (size_t)f0 * 64 * S_LEN + s0;
  const bf16_t* RB = U + (size_t)m0 * FF + f0 * 64;
  int pslot = 0, issued = 0;
  auto issue = [&]() {
    const unsigned bb = (unsigned)(uintptr_t)(lds + pslot * G2_STAGE);
    if (issued < 16) {
      const bf16_t* P = PA + issued * 64;
      const bf16_t* R = RA + (size_t)issued * 64 * DM;
      glds16(P, pOffA[0], bb + (w * 2) * 1024);
      glds16(P, pOffA[1], bb + (w * 2 + 1) * 1024);
      glds16(R, rOffA[0], bb + 16384 + w * 1024);
      glds16(R, rOffA[1], bb + 16384 + (w + 8) * 1024);
      if (w < 4) glds16(R, rOffA[2], bb + 16384 + (w + 16) * 1024);
    } else {
      const bf16_t* P = PB + (size_t)(issued - 16) * 64 * S_LEN;
      const bf16_t* R = RB + (issued - 16) * 64;
      glds16(P, pOffB[0], bb + (w * 2) * 1024);
      glds16(P, pOffB[1], bb + (w * 2 + 1) * 1024);
      glds16(R, rOffB[0], bb + 16384 + w * 1024);
      glds16(R, rOffB[1], bb + 16384 + (w + 8) * 1024);
      if (w < 4) glds16(R, rOffB[2], bb + 16384 + (w + 16) * 1024);
    }
    if (++pslot == G2_NST) pslot = 0;
    ++issued;
  };
  const int prow = 32 * ws + r32, psw = (prow >> 1) & 7;
  const int pA0 = prow * 128 + (((4 * kh + hi) ^ psw) << 4), pA1 = prow * 128 + (((4 * kh + 2 + hi) ^ psw) << 4);
  const int rA = 16384 + (32 * kh + 8 * hi + q) * 64 + blk * 32 + swapmid(p) * 8;
  const int pB = (32 * kh + 8 * hi + q) * 256 + ((ws ^ q) * 64) + blk * 32 + p * 8;
  const int rrow = rperm(r32), rsw = (rrow >> 1) & 7;
  const int rB0 = 16384 + rrow * 128 + (((4 * kh + hi) ^ rsw) << 4), rB1 = 16384 + rrow * 128 + (((4 * kh + 2 + hi) ^ rsw) << 4);
#pragma unroll
  for (int i = 0; i < G2_NST - 1; ++i) issue();
  f32x16 acc[5] = {};
  int buf = 0;
  for (int step = 0; step < 16; ++step) {
    if (w < 4) wait_vm<5 * (G2_NST - 2)>();
    else wait_vm<4 * (G2_NST - 2)>();
    BARRIER();
    issue();
    const char LAS* bp = lds + buf * G2_STAGE;
    bf16x8 pf[2], rf[2][5];
    pf[0] = *(const LAS bf16x8*)(bp + pA0);
#pragma unroll
    for (int j = 0; j < 5; ++j) rf[0][j] = cat8(tr_read(bp + rA + j * 4096), tr_read(bp + rA + j * 4096 + 256));
    pf[1] = *(const LAS bf16x8*)(bp + pA1);
#pragma unroll
    for (int j = 0; j < 5; ++j) rf[1][j] = cat8(tr_read(bp + rA + j * 4096 + 1024), tr_read(bp + rA + j * 4096 + 1024 + 256));
    __builtin_amdgcn_sched_barrier(0);
#pragma unroll
    for (int k2 = 0; k2 < 2; ++k2)
#pragma unroll
      for (int j = 0; j < 5; ++j) acc[j] = mf<X16_G2>(rf[k2][j], pf[k2], acc[j], k2);
    if (++buf == G2_NST) buf = 0;
  }
  for (int step = 16; step < total; ++step) {
    if (step + G2_NST - 1 > total) WAIT_VM0();
    else if (w < 4) wait_vm<5 * (G2_NST - 2)>();
    else wait_vm<4 * (G2_NST - 2)>();
    BARRIER();
    if (issued < total) issue();
    const char LAS* bp = lds + buf * G2_STAGE;
    bf16x8 pf[2], rf[2][5];
    pf[0] = cat8(tr_read(bp + pB), tr_read(bp + pB + 1024));
#pragma unroll
    for (int j = 0; j < 5; ++j) rf[0][j] = *(const LAS bf16x8*)(bp + rB0 + j * 4096);
    pf[1] = cat8(tr_read(bp + pB + 4096), tr_read(bp + pB + 4096 + 1024));
#pragma unroll
    for (int j = 0; j < 5; ++j) rf[1][j] = *(const LAS bf16x8*)(bp + rB1 + j * 4096);
    __builtin_amdgcn_sched_barrier(0);
#pragma unroll
    for (int k2 = 0; k2 < 2; ++k2)
#pragma unroll
      for (int j = 0; j < 5; ++j) acc[j] = mf<X16_G2>(rf[k2][j], pf[k2], acc[j], k2);
    if (++buf == G2_NST) buf = 0;
  }
  TS(2, 1);
  BARRIER();
  {
    float LAS* dst = (float LAS*)(lds + w * 12288);
    if (kh == 0) {
#pragma unroll
      for (int j = 3; j < 5; ++j)
#pragma unroll
        for (int g = 0; g < 4; ++g)
          *(f32x4 LAS*)(dst + (((j - 3) * 4 + g) * 64 + l) * 4) = (f32x4){acc[j][4 * g], acc[j][4 * g + 1], acc[j][4 * g + 2], acc[j][4 * g + 3]};
    } else {
#pragma unroll
      for (int j = 0; j < 3; ++j)
#pragma unroll
        for (int g = 0; g < 4; ++g)
          *(f32x4 LAS*)(dst + ((j * 4 + g) * 64 + l) * 4) = (f32x4){acc[j][4 * g], acc[j][4 * g + 1], acc[j][4 * g + 2], acc[j][4 * g + 3]};
    }
    asm volatile("s_waitcnt lgkmcnt(0)" ::: "memory");
    BARRIER();
    const float LAS* src = (const float LAS*)(lds + (w ^ 4) * 12288);
    float* orow = part + ((size_t)ks * S_LEN + s0 + 32 * ws + r32) * DM + m0 + 8 * hi;
    if (kh == 0) {
#pragma unroll
      for (int j = 0; j < 3; ++j)
#pragma unroll
        for (int g = 0; g < 4; ++g) {
          const f32x4 o = *(const f32x4 LAS*)(src + ((j * 4 + g) * 64 + l) * 4);
          st16f(orow + 32 * j + (g >> 1) * 16 + (g & 1) * 4, acc[j][4 * g] + o[0], acc[j][4 * g + 1] + o[1], acc[j][4 * g + 2] + o[2], acc[j][4 * g + 3] + o[3]);
        }
    } else {
#pragma unroll
      for (int j = 3; j < 5; ++j)
#pragma unroll
        for (int g = 0; g < 4; ++g) {
          const f32x4 o = *(const f32x4 LAS*)(src + (((j - 3) * 4 + g) * 64 + l) * 4);
          st16f(orow + 32 * j + (g >> 1) * 16 + (g & 1) * 4, acc[j][4 * g] + o[0], acc[j][4 * g + 1] + o[1], acc[j][4 * g + 2] + o[2], acc[j][4 * g + 3] + o[3]);
        }
    }
  }
  TS(2, 2);
}

__global__ __launch_bounds__(256) void reduce_kernel(const float* __restrict__ part, const bf16_t* __restrict__ x,
                                                     const bf16_t* __restrict__ b2, bf16_t* __restrict__ out) {
  TS(4, 0);
  const int i = blockIdx.x * 256 + threadIdx.x;
  const int e = i * 4;
  const int s = e / DM;
  f32x4 a = *(const f32x4*)(part + e);
#pragma unroll
  for (int sp = 1; sp < 4; ++sp) a += *(const f32x4*)(part + (size_t)sp * S_LEN * DM + e);
  const u32x2 xv = *(const u32x2*)(x + e);
  const float bb = bf2f(b2[s]);
  float r0 = __uint_as_float(xv[0] << 16) + a[0] + bb;
  float r1 = __uint_as_float(xv[0] & 0xffff0000u) + a[1] + bb;
  float r2 = __uint_as_float(xv[1] << 16) + a[2] + bb;
  float r3 = __uint_as_float(xv[1] & 0xffff0000u) + a[3] + bb;
  u32x2 o = {pack2(r0, r1), pack2(r2, r3)};
  *(u32x2*)(out + e) = o;
  TS(4, 2);
}

extern "C" void kernel_launch(void* const* d_in, const int* in_sizes, int n_in, void* d_out, int out_size, void* d_ws,
                              size_t ws_size, hipStream_t stream) {
  const bf16_t* x = (const bf16_t*)d_in[0];
  const bf16_t* ckv = (const bf16_t*)d_in[1];
  const bf16_t* wq = (const bf16_t*)d_in[2];
  const bf16_t* wuk = (const bf16_t*)d_in[3];
  const bf16_t* wuv = (const bf16_t*)d_in[4];
  const bf16_t* wo = (const bf16_t*)d_in[5];
  const bf16_t* w1 = (const bf16_t*)d_in[7];
  const bf16_t* b1 = (const bf16_t*)d_in[8];
  const bf16_t* w2 = (const bf16_t*)d_in[9];
  const bf16_t* b2 = (const bf16_t*)d_in[10];
  char* ws = (char*)d_ws;
  bf16_t* xn = (bf16_t*)(ws + WS_XN);
  bf16_t* h = (bf16_t*)(ws + WS_H);
  bf16_t* Q = (bf16_t*)(ws + WS_Q);
  bf16_t* K = (bf16_t*)(ws + WS_K);
  bf16_t* V = (bf16_t*)(ws + WS_V);
  bf16_t* U = (bf16_t*)(ws + WS_U);
  bf16_t* ctx = (bf16_t*)(ws + WS_CTX);
  float* part = (float*)(ws + WS_PART);
  ln_kernel<<<S_LEN / 4 + PF_BLOCKS, 256, 0, stream>>>(x, xn, h, ckv, wq, wuk, wuv);
  gemm1_kernel<<<256, 512, 0, stream>>>(xn, h, ckv, wq, wuk, wuv, w1, b1, wo, w2, Q, K, V, U);
  attn_kernel<<<256, 512, 0, stream>>>(Q, K, V, ctx, wo, w2);
  gemm2_kernel<<<256, 512, 0, stream>>>(ctx, wo, U, w2, part);
  reduce_kernel<<<S_LEN * DM / 4 / 256, 256, 0, stream>>>(part, x, b2, (bf16_t*)d_out);
}
```

```cpp
#include <hip/hip_runtime.h>
#include <hip/hip_bf16.h>
#include <stdint.h>
#include <type_traits>

constexpr int S_LEN = 2048;
constexpr int DM = 640;
constexpr int NH = 32;
constexpr int HD = 128;
constexpr int DC = 512;
constexpr int FF = 1728;
constexpr int HDN = NH * HD;
constexpr int CTX_LD = HDN + 64;

typedef unsigned short bf16_t;
typedef short bf16x8 __attribute__((ext_vector_type(8)));
typedef short s16x4 __attribute__((ext_vector_type(4)));
typedef float f32x16 __attribute__((ext_vector_type(16)));
typedef float f32x4 __attribute__((ext_vector_type(4)));
typedef unsigned u32x4 __attribute__((ext_vector_type(4)));
typedef unsigned u32x2 __attribute__((ext_vector_type(2)));
typedef __bf16 bf16x2_t __attribute__((ext_vector_type(2)));
#define LAS __attribute__((address_space(3)))

__device__ __forceinline__ float bf2f(bf16_t v) { return __uint_as_float(((unsigned)v) << 16); }
__device__ __forceinline__ unsigned pack2(float a, float b) {
  bf16x2_t v = {(__bf16)a, (__bf16)b};
  return *reinterpret_cast<unsigned*>(&v);
}
__device__ __forceinline__ float rbf(float a) {
  __bf16 v = (__bf16)a;
  return __uint_as_float(((unsigned)*reinterpret_cast<unsigned short*>(&v)) << 16);
}

template <bool X16> __device__ __forceinline__ f32x16 mf(bf16x8 a, bf16x8 b, f32x16 c, int sel) {
  if (X16) {
    f32x4 q0 = {c[8 * sel], c[8 * sel + 1], c[8 * sel + 2], c[8 * sel + 3]};
    f32x4 q1 = {c[8 * sel + 4], c[8 * sel + 5], c[8 * sel + 6], c[8 * sel + 7]};
    q0 = __builtin_amdgcn_mfma_f32_16x16x32_bf16(a, b, q0, 0, 0, 0);
    q1 = __builtin_amdgcn_mfma_f32_16x16x32_bf16(b, a, q1, 0, 0, 0);
#pragma unroll
    for (int e = 0; e < 4; ++e) { c[8 * sel + e] = q0[e]; c[8 * sel + 4 + e] = q1[e]; }
    return c;
  }
  return __builtin_amdgcn_mfma_f32_32x32x16_bf16(a, b, c, 0, 0, 0);
}
#define X16_G1 0
#define X16_AT 0
#define X16_G2 0
constexpr size_t WS_XN = 0;
constexpr size_t WS_H = WS_XN + (size_t)S_LEN * DM * 2;
constexpr size_t WS_Q = WS_H + (size_t)S_LEN * DM * 2;
constexpr size_t WS_K = WS_Q + (size_t)S_LEN * HDN * 2;
constexpr size_t WS_V = WS_K + (size_t)S_LEN * HDN * 2;
constexpr size_t WS_U = WS_V + (size_t)S_LEN * HDN * 2;
constexpr size_t WS_CTX = WS_U + (size_t)DM * FF * 2;
constexpr size_t WS_PART = WS_CTX + (size_t)S_LEN * CTX_LD * 2;
constexpr size_t WS_UP = WS_PART + (size_t)4 * S_LEN * DM * 4;
constexpr size_t WS_END = WS_UP + (size_t)S_LEN * DC * 2;

__device__ __forceinline__ float wave_sum(float v) {
#pragma unroll
  for (int o = 32; o > 0; o >>= 1) v += __shfl_xor(v, o);
  return v;
}
__device__ __forceinline__ void ln_row(float (&v)[10]) {
  float s = 0.f;
#pragma unroll
  for (int i = 0; i < 10; ++i) s += v[i];
  s = wave_sum(s);
  const float mean = s * (1.0f / DM);
  float q = 0.f;
#pragma unroll
  for (int i = 0; i < 10; ++i) { float d = v[i] - mean; q += d * d; }
  q = wave_sum(q);
  const float mb = rbf(mean);
  const float vb = rbf(q * (1.0f / DM));
  const float den = rbf(sqrtf(rbf(vb + 1e-5f)));
#pragma unroll
  for (int i = 0; i < 10; ++i) v[i] = rbf(rbf(v[i] - mb) / den);
}
__device__ __forceinline__ const char* warm_addr(int kb, int i, const bf16_t* wq, const bf16_t* wuk, const bf16_t* wuv) {
  const bf16_t* src = i < 8192 ? wq : i < 16384 ? wuk : wuv;
  return (const char*)src + (size_t)kb * 1048576 + (size_t)(i & 8191) * 128;
}
#define PF_KB 1
constexpr int PF_BLOCKS = 24 * PF_KB + 128;
__global__ __launch_bounds__(256) void ln_kernel(const bf16_t* __restrict__ x, bf16_t* __restrict__ xn,
                                                 bf16_t* __restrict__ h, const bf16_t* __restrict__ ckv,
                                                 bf16_t* __restrict__ ct, const bf16_t* __restrict__ wq,
                                                 const bf16_t* __restrict__ wuk, const bf16_t* __restrict__ wuv) {
  if (blockIdx.x < PF_BLOCKS) {
    if (blockIdx.x < 24 * PF_KB) {
      unsigned keep = 0;
#pragma unroll
      for (int j = 0; j < 4; ++j) {
        const char* a = warm_addr(blockIdx.x / 24, (blockIdx.x % 24) * 1024 + j * 256 + threadIdx.x, wq, wuk, wuv);
        asm volatile("global_load_dword %0, %1, off" : "+v"(keep) : "v"(a) : "memory");
      }
      asm volatile("s_waitcnt vmcnt(0)" : "+v"(keep)::"memory");
    } else {
      const int r0 = (blockIdx.x - 24 * PF_KB) * 16 + (threadIdx.x >> 6), c8 = threadIdx.x & 63;
      u32x4 v[4];
#pragma unroll
      for (int j = 0; j < 4; ++j) v[j] = *(const u32x4*)(ckv + (size_t)(r0 + 4 * j) * DC + c8 * 8);
#pragma unroll
      for (int j = 0; j < 4; ++j) *(u32x4*)(ct + ((size_t)(c8 >> 2) * S_LEN + r0 + 4 * j) * 32 + (c8 & 3) * 8) = v[j];
    }
    return;
  }
  const int lane = threadIdx.x & 63, w = threadIdx.x >> 6;
  const int row = (blockIdx.x - PF_BLOCKS) * 4 + w;
  const unsigned* xr = reinterpret_cast<const unsigned*>(x + (size_t)row * DM);
  float v[10];
#pragma unroll
  for (int j = 0; j < 5; ++j) {
    unsigned u = xr[lane + 64 * j];
    v[2 * j] = __uint_as_float(u << 16);
    v[2 * j + 1] = __uint_as_float(u & 0xffff0000u);
  }
  ln_row(v);
  unsigned* xo = reinterpret_cast<unsigned*>(xn + ((size_t)(lane >> 4) * S_LEN + row) * 32) + (lane & 15);
#pragma unroll
  for (int j = 0; j < 5; ++j) xo[j * 4 * S_LEN * 16] = pack2(v[2 * j], v[2 * j + 1]);
  ln_row(v);
  unsigned* ho = reinterpret_cast<unsigned*>(h + (size_t)row * DM);
#pragma unroll
  for (int j = 0; j < 5; ++j) ho[lane + 64 * j] = pack2(v[2 * j], v[2 * j + 1]);
}

constexpr int STAGE_BYTES = 49152;
constexpr int RBASE = 32768;

__device__ __forceinline__ int swapmid(int p) { return ((p & 1) << 1) | ((p >> 1) & 1); }
__device__ __forceinline__ int rperm(int r) {
  int g = r >> 2;
  int pg = (g & 4) | swapmid(g & 3);
  return pg * 4 + (r & 3);
}
__device__ __forceinline__ s16x4 tr_read(const char LAS* p) {
  return __builtin_amdgcn_ds_read_tr16_b64_v4i16((LAS s16x4*)p);
}
__device__ __forceinline__ bf16x8 cat8(s16x4 a, s16x4 b) {
  return (bf16x8){a[0], a[1], a[2], a[3], b[0], b[1], b[2], b[3]};
}

__device__ __forceinline__ void glds16(const void* sbase, int voff, unsigned lds_dst) {
  unsigned keep;
  asm volatile("s_mov_b32 %0, m0\n\ts_mov_b32 m0, %3\n\ts_nop 0\n\tglobal_load_lds_dwordx4 %1, %2\n\ts_mov_b32 m0, %0"
               : "=&s"(keep) : "v"(voff), "s"(sbase), "s"(lds_dst) : "memory");
}

__device__ __forceinline__ void glds16x4(const void* sbase, int v0, int v1, int v2, int v3, unsigned lds_dst) {
  unsigned keep;
  asm volatile("s_mov_b32 %0, m0\n\ts_mov_b32 m0, %6\n\ts_nop 0\n\tglobal_load_lds_dwordx4 %1, %5\n\t"
               "global_load_lds_dwordx4 %2, %5 offset:1024\n\tglobal_load_lds_dwordx4 %3, %5 offset:2048\n\t"
               "global_load_lds_dwordx4 %4, %5 offset:3072\n\ts_mov_b32 m0, %0"
               : "=&s"(keep) : "v"(v0), "v"(v1), "v"(v2), "v"(v3), "s"(sbase), "s"(lds_dst) : "memory");
}

#define ST_MODE 0
__device__ __forceinline__ void st16(void* p, u32x4 v) {
  *(u32x4*)p = v;
}
__device__ __forceinline__ void st16f(float* p, float a, float b, float c, float d) {
  st16(p, (u32x4){__float_as_uint(a), __float_as_uint(b), __float_as_uint(c), __float_as_uint(d)});
}

template <bool P_TR, bool R_TR, bool ROWS_P>
struct GemmCore {
  int pOff[4], rOff[2];
  int pAddr[4], rAddr[4];
  int w, l;
  __device__ __forceinline__ void init(int ldp, int ldr, int pcol_limit  ) {
    const int tid = threadIdx.x;
    l = tid & 63;
    w = __builtin_amdgcn_readfirstlane(tid >> 6);
    const int wp = w >> 1, wr = w & 1;
#pragma unroll
    for (int i = 0; i < 4; ++i) {
      const int j = w * 4 + i;
      if (!P_TR) {
        const int row = 8 * j + (l >> 3), slot = l & 7;
        const int chunk = slot ^ ((row >> 1) & 7);
        pOff[i] = row * ldp + chunk * 8;
      } else {
        const int half = j >> 4, jj = j & 15;
        const int krow = 4 * jj + (l >> 4), s16 = l & 15;
        const int c64 = (s16 >> 2) ^ (krow & 3);
        int col = half * 128 + c64 * 32 + (s16 & 3) * 8;
        if (col >= pcol_limit) col = pcol_limit - 8;
        pOff[i] = krow * ldp + col;
      }
    }
#pragma unroll
    for (int i = 0; i < 2; ++i) {
      const int j = w * 2 + i;
      if (!R_TR) {
        const int row = 8 * j + (l >> 3), slot = l & 7;
        const int chunk = slot ^ ((row >> 1) & 7);
        rOff[i] = row * ldr + chunk * 8;
      } else {
        const int krow = 4 * j + (l >> 4), s16 = l & 15;
        const int c64 = (s16 >> 2) ^ (krow & 3);
        const int col = c64 * 32 + (s16 & 3) * 8;
        rOff[i] = krow * ldr + col;
      }
    }
    const int r32 = l & 31, hi = l >> 5, blk = (l >> 4) & 1, q = (l & 15) >> 2, p = l & 3;
    if (!P_TR) {
      const int prow = 64 * wp + (ROWS_P ? rperm(r32) : r32);
      const int s = (prow >> 1) & 7;
#pragma unroll
      for (int kk = 0; kk < 4; ++kk) pAddr[kk] = prow * 128 + (((2 * kk + hi) ^ s) << 4);
    } else {
      const int half = wp >> 1, pp = ROWS_P ? swapmid(p) : p;
#pragma unroll
      for (int i = 0; i < 2; ++i) {
        const int c64 = 2 * (wp & 1) + i;
        pAddr[i] = half * 16384 + (8 * hi + q) * 256 + ((c64 ^ q) * 64) + blk * 32 + pp * 8;
      }
      pAddr[2] = pAddr[3] = 0;
    }
    if (!R_TR) {
      const int rrow = 64 * wr + (!ROWS_P ? rperm(r32) : r32);
      const int s = (rrow >> 1) & 7;
#pragma unroll
      for (int kk = 0; kk < 4; ++kk) rAddr[kk] = RBASE + rrow * 128 + (((2 * kk + hi) ^ s) << 4);
    } else {
      const int pp = !ROWS_P ? swapmid(p) : p;
#pragma unroll
      for (int i = 0; i < 2; ++i) {
        const int c64 = 2 * wr + i;
        rAddr[i] = RBASE + (8 * hi + q) * 256 + ((c64 ^ q) * 64) + blk * 32 + pp * 8;
      }
      rAddr[2] = rAddr[3] = 0;
    }
  }
  __device__ __forceinline__ void stage(char LAS* buf, const bf16_t* P, const bf16_t* R) {
    const unsigned b = (unsigned)(uintptr_t)buf;
#pragma unroll
    for (int i = 0; i < 4; ++i) glds16(P, pOff[i] * 2, b + (w * 4 + i) * 1024);
#pragma unroll
    for (int i = 0; i < 2; ++i) glds16(R, rOff[i] * 2, b + RBASE + (w * 2 + i) * 1024);
  }
  __device__ __forceinline__ void compute(const char LAS* buf, f32x16 (&acc)[2][2]) {
#pragma unroll
    for (int kk = 0; kk < 4; ++kk) {
      bf16x8 pf[2], rf[2];
#pragma unroll
      for (int i = 0; i < 2; ++i) {
        if (!P_TR) pf[i] = *(const LAS bf16x8*)(buf + pAddr[kk] + i * 4096);
        else pf[i] = cat8(tr_read(buf + pAddr[i] + kk * 4096), tr_read(buf + pAddr[i] + kk * 4096 + 1024));
        if (!R_TR) rf[i] = *(const LAS bf16x8*)(buf + rAddr[kk] + i * 4096);
        else rf[i] = cat8(tr_read(buf + rAddr[i] + kk * 4096), tr_read(buf + rAddr[i] + kk * 4096 + 1024));
      }
#pragma unroll
      for (int i = 0; i < 2; ++i)
#pragma unroll
        for (int j = 0; j < 2; ++j) {
          if (ROWS_P) acc[i][j] = __builtin_amdgcn_mfma_f32_32x32x16_bf16(pf[i], rf[j], acc[i][j], 0, 0, 0);
          else acc[i][j] = __builtin_amdgcn_mfma_f32_32x32x16_bf16(rf[j], pf[i], acc[i][j], 0, 0, 0);
        }
    }
  }
};

#define WAIT_VM0() asm volatile("s_waitcnt vmcnt(0)" ::: "memory")
#define WAIT_VM6() asm volatile("s_waitcnt vmcnt(6)" ::: "memory")
#define WAIT_VM16() asm volatile("s_waitcnt vmcnt(16)" ::: "memory")
#define BARRIER() __builtin_amdgcn_s_barrier()
#define TS(k, i)
#define PH(i, k)

__device__ __forceinline__ float gelu_tanh(float x) {
  const float y = 0.7978845608028654f * (x + 0.044715f * x * x * x);
  const float e = __expf(2.0f * y);
  const float t = 1.0f - 2.0f / (e + 1.0f);
  return 0.5f * x * (1.0f + t);
}

constexpr int STAGE256 = 32768;
#define NST256 4
struct Core256 {
  int pOff[2], rOff[2];
  int pAddr[2], rAddr[2];
  int w;
  __device__ __forceinline__ void init(int ldp, int ldr) {
    const int tid = threadIdx.x, l = tid & 63;
    w = __builtin_amdgcn_readfirstlane(tid >> 6);
    const int wp = w >> 2, wr = w & 3;
#pragma unroll
    for (int i = 0; i < 2; ++i) {
      const int j = w * 2 + i;
      const int row = 16 * j + (l >> 2), slot = l & 3;
      pOff[i] = (row * ldp + (slot ^ ((row >> 2) & 3)) * 8) * 2;
      const int half = j >> 3, jj = j & 7;
      const int krow = 4 * jj + (l >> 4), s16 = l & 15;
      const int c64 = (s16 >> 2) ^ (krow & 3);
      rOff[i] = (krow * ldr + half * 128 + c64 * 32 + (s16 & 3) * 8) * 2;
    }
    const int r32 = l & 31, hi = l >> 5, blk = (l >> 4) & 1, q = (l & 15) >> 2, p = l & 3;
    const int prow = 128 * wp + r32, s = (prow >> 2) & 3;
#pragma unroll
    for (int kk = 0; kk < 2; ++kk) pAddr[kk] = prow * 64 + (((2 * kk + hi) ^ s) << 4);
#pragma unroll
    for (int j = 0; j < 2; ++j) {
      const int c64 = 2 * (wr & 1) + j;
      rAddr[j] = 16384 + (wr >> 1) * 8192 + (8 * hi + q) * 256 + ((c64 ^ q) * 64) + blk * 32 + swapmid(p) * 8;
    }
  }
  __device__ __forceinline__ void stage(char LAS* buf, const bf16_t* P, const bf16_t* R) {
    const unsigned b = (unsigned)(uintptr_t)buf;
#pragma unroll
    for (int i = 0; i < 2; ++i) glds16(P, pOff[i], b + (w * 2 + i) * 1024);
#pragma unroll
    for (int i = 0; i < 2; ++i) glds16(R, rOff[i], b + 16384 + (w * 2 + i) * 1024);
  }
  __device__ __forceinline__ void compute(const char LAS* buf, f32x16 (&acc)[4][2]) {
#pragma unroll
    for (int kk = 0; kk < 2; ++kk) {
      bf16x8 pf[4], rf[2];
#pragma unroll
      for (int j = 0; j < 2; ++j) rf[j] = cat8(tr_read(buf + rAddr[j] + kk * 4096), tr_read(buf + rAddr[j] + kk * 4096 + 1024));
#pragma unroll
      for (int i = 0; i < 4; ++i) pf[i] = *(const LAS bf16x8*)(buf + pAddr[kk] + i * 2048);
#pragma unroll
      for (int i = 0; i < 4; ++i)
#pragma unroll
        for (int j = 0; j < 2; ++j) acc[i][j] = __builtin_amdgcn_mfma_f32_32x32x16_bf16(rf[j], pf[i], acc[i][j], 0, 0, 0);
    }
  }
  __device__ __forceinline__ void load_frags(const char LAS* buf, bf16x8 (&pf)[2][4], bf16x8 (&rf)[2][2]) {
#pragma unroll
    for (int kk = 0; kk < 2; ++kk) {
#pragma unroll
      for (int j = 0; j < 2; ++j) rf[kk][j] = cat8(tr_read(buf + rAddr[j] + kk * 4096), tr_read(buf + rAddr[j] + kk * 4096 + 1024));
#pragma unroll
      for (int i = 0; i < 4; ++i) pf[kk][i] = *(const LAS bf16x8*)(buf + pAddr[kk] + i * 2048);
    }
  }
  __device__ __forceinline__ void mma(const bf16x8 (&pf)[2][4], const bf16x8 (&rf)[2][2], f32x16 (&acc)[4][2]) {
#pragma unroll
    for (int kk = 0; kk < 2; ++kk)
#pragma unroll
      for (int i = 0; i < 4; ++i)
#pragma unroll
        for (int j = 0; j < 2; ++j) acc[i][j] = __builtin_amdgcn_mfma_f32_32x32x16_bf16(rf[kk][j], pf[kk][i], acc[i][j], 0, 0, 0);
  }
};
#define WAIT_LGKM0() asm volatile("s_waitcnt lgkmcnt(0)" ::: "memory")
#define SCHED0() __builtin_amdgcn_sched_barrier(0)
template <int N> __device__ __forceinline__ void wait_vm() { asm volatile("s_waitcnt vmcnt(%0)" ::"n"(N) : "memory"); }

struct Frag { bf16x8 pf[4], rf[2]; };
struct Desc { const char* p; unsigned stride; int v0, v1; };
template <int IMM> __device__ __forceinline__ void setm0(unsigned base) {
  asm volatile("s_add_u32 m0, %0, %1" ::"s"(base), "n"(IMM) : "memory", "scc");
}
template <int OFF> __device__ __forceinline__ void gl(const void* sbase, int voff) {
  asm volatile("global_load_lds_dwordx4 %0, %1 offset:%2" ::"v"(voff), "s"(sbase), "n"(OFF) : "memory");
}
template <int V> using IC = std::integral_constant<int, V>;
__global__ __launch_bounds__(512, 2) void gemm1_kernel(const bf16_t* __restrict__ xn, const bf16_t* __restrict__ hbuf,
                                                       const bf16_t* __restrict__ ckv, const bf16_t* __restrict__ wq,
                                                       const bf16_t* __restrict__ wuk, const bf16_t* __restrict__ wuv,
                                                       const bf16_t* __restrict__ w1, const bf16_t* __restrict__ b1,
                                                       const bf16_t* __restrict__ wo, const bf16_t* __restrict__ w2,
                                                       bf16_t* __restrict__ Qo, bf16_t* __restrict__ Ko,
                                                       bf16_t* __restrict__ Vo, bf16_t* __restrict__ Uo) {
  __shared__ __attribute__((aligned(16))) char smem[4 * STAGE256 + 32768];
  char LAS* lds = (char LAS*)smem;
  const int xcd = blockIdx.x & 7, c = blockIdx.x >> 3;
  const int l = threadIdx.x & 63, w = __builtin_amdgcn_readfirstlane(threadIdx.x >> 6);
  const unsigned sW = (unsigned)(uintptr_t)lds + w * 2048;
  const int r32 = l & 31, hi = l >> 5;
  TS(0, 0);
  const bool isQ = c >= 16;
  const int mbase = (xcd * 90) >> 3, mcnt = (((xcd + 1) * 90) >> 3) - mbase;
  const bool hasMix = isQ && c - 16 < mcnt;
  const int spare = xcd * 16 - mbase + (c - 16 - mcnt);
  if (isQ && !hasMix) {
    unsigned keep = 0;
#pragma unroll
    for (int j = 0; j < 5; ++j) {
      const int idx = (j * 38 + spare) * 512 + (int)threadIdx.x + PF_KB * 24576;
      const int kb = idx / 24576, i = idx % 24576;
      const char* a = warm_addr(kb < 4 ? kb : 4, kb < 4 ? i : (i & 8191), wq, wuk, wuv);
      asm volatile("global_load_dword %0, %1, off" : "+v"(keep) : "v"(a) : "memory");
    }
#pragma unroll
    for (int j = 0; j < 3; ++j) {
      const int idx = (j * 38 + spare) * 512 + (int)threadIdx.x;
      const char* a = idx < 55296 ? (const char*)w1 + (size_t)idx * 128 : (const char*)b1 + (idx % 27) * 128;
      asm volatile("global_load_dword %0, %1, off" : "+v"(keep) : "v"(a) : "memory");
    }
    asm volatile("s_waitcnt vmcnt(0)" : "+v"(keep)::"memory");
  }
  const int mtile = mbase + (c - 16);
  const int d0 = (mtile % 5) * 128, f0 = (mtile / 5) * 96;
  Desc MP, MR;
  {
    int po[2], ro[2];
#pragma unroll
    for (int i = 0; i < 2; ++i) {
      const int j = w * 2 + i;
      const int krow = 4 * j + (l >> 4), s16 = l & 15;
      po[i] = (krow * DM + ((s16 >> 2) ^ (krow & 3)) * 32 + (s16 & 3) * 8) * 2 - 1024 * i;
      const int cb = j >> 2, kr = (j & 3) * 16 + (l >> 2);
      ro[i] = j < 12 ? (kr * FF + cb * 32 + (l & 3) * 8) * 2 - 1024 * i : 0;
    }
    MP = Desc{(const char*)(hbuf + d0), 64u * DM * 2, po[0], po[1]};
    MR = Desc{(const char*)(w1 + f0), 64u * FF * 2, ro[0], ro[1]};
  }
  {
    int ntile, tb0, hp;
    const bf16_t* A; const bf16_t* W; bf16_t* O; int K;
    if (!isQ) {
      ntile = 2; tb0 = c >> 2; hp = xcd * 2 + (c & 1);
      A = ckv; K = DC;
      W = (c & 2) ? wuv : wuk; O = (c & 2) ? Vo : Ko;
    } else {
      ntile = 1; tb0 = (c - 16) >> 1; hp = xcd * 2 + (c & 1);
      A = xn; K = DM; W = wq; O = Qo;
    }
    const int ngt = K / 128;
    Core256 g;
    g.init(32, HDN);
    constexpr unsigned PSTR = S_LEN * 64u;
    const char* Ap = (const char*)(A + (size_t)tb0 * 256 * 32);
    const char* Wp = (const char*)(W + hp * 256);
    const int wp = w >> 2, wr = w & 3;
    constexpr unsigned RSTR = 32u * HDN * 2;
    f32x16 acc[4][2];
    Frag F0, F1;
    auto half = [&](auto first, auto dst, const Frag& cur, Frag& nxt, const char LAS* nb, int nkk, const char* sp, int v0, int v1) {
      auto mm = [&](int i, int j) {
        acc[i][j] = mf<X16_G1>(cur.rf[j], cur.pf[i], first ? (f32x16){} : acc[i][j], nkk ^ 1);
        SCHED0();
      };
      auto rdR = [&](int j) {
        nxt.rf[j] = cat8(tr_read(nb + g.rAddr[j] + nkk * 4096), tr_read(nb + g.rAddr[j] + nkk * 4096 + 1024));
        SCHED0();
      };
      auto rdP = [&](int i) {
        nxt.pf[i] = *(const LAS bf16x8*)(nb + g.pAddr[nkk] + i * 2048);
        SCHED0();
      };
      mm(0, 0); rdR(0);
      mm(0, 1); rdP(0); rdR(1);
      mm(1, 0); rdP(1);
      mm(1, 1); rdP(2);
      mm(2, 0); rdP(3); if (dst >= 0) setm0<(dst >= 0 ? (int)dst : 0)>(sW); SCHED0();
      mm(2, 1); if (dst >= 0) gl<0>(sp, v0); SCHED0();
      mm(3, 0); if (dst >= 0) gl<1024>(sp, v1); SCHED0();
      mm(3, 1);
    };
    auto group = [&](auto VAR, bool second, const Desc& Rc, const Desc& Pn, const Desc& Rn) {
      auto stp = [&](auto J) {
        constexpr int j = J, var = VAR;
        constexpr int dA = (var == 2 && j > 0) ? -1 : ((j + 3) & 3) * STAGE256 + 16384;
        constexpr int dB = var == 2 ? -1 : j * STAGE256;
        const char LAS* cb = lds + j * STAGE256;
        const char LAS* nb = lds + ((j + 1) & 3) * STAGE256;
        if (j == 0) half(std::integral_constant<bool, var == 0>{}, IC<dA>{}, F0, F1, cb, 1, Rc.p, Rc.v0, Rc.v1);
        else half(std::false_type{}, IC<dA>{}, F0, F1, cb, 1, Rn.p + (j - 1) * Rn.stride, Rn.v0, Rn.v1);
        if (var == 2) { if (j == 0) wait_vm<8>(); else if (j == 1) wait_vm<4>(); else if (j == 2) wait_vm<0>(); }
        else if (var == 0 && j < 2) { if (second) wait_vm<24>(); else wait_vm<8>(); }
        else wait_vm<8>();
        WAIT_LGKM0();
        SCHED0();
        BARRIER();
        SCHED0();
        half(std::false_type{}, IC<dB>{}, F1, F0, nb, 0, Pn.p + j * Pn.stride, Pn.v0, Pn.v1);
      };
      stp(IC<0>{}); stp(IC<1>{}); stp(IC<2>{}); stp(IC<3>{});
    };
    const int pv0 = g.pOff[0], pv1 = g.pOff[1] - 1024, rv0 = g.rOff[0], rv1 = g.rOff[1] - 1024;
    setm0<0>(sW); SCHED0(); asm volatile("s_nop 0"); gl<0>(Ap, pv0); gl<1024>(Ap, pv1);
    setm0<16384>(sW); SCHED0(); asm volatile("s_nop 0"); gl<0>(Wp, rv0); gl<1024>(Wp, rv1);
    setm0<STAGE256>(sW); SCHED0(); asm volatile("s_nop 0"); gl<0>(Ap + PSTR, pv0); gl<1024>(Ap + PSTR, pv1);
    setm0<STAGE256 + 16384>(sW); SCHED0(); asm volatile("s_nop 0"); gl<0>(Wp + RSTR, rv0); gl<1024>(Wp + RSTR, rv1);
    setm0<2 * STAGE256>(sW); SCHED0(); asm volatile("s_nop 0"); gl<0>(Ap + 2 * PSTR, pv0); gl<1024>(Ap + 2 * PSTR, pv1);
    setm0<2 * STAGE256 + 16384>(sW); SCHED0(); asm volatile("s_nop 0"); gl<0>(Wp + 2 * RSTR, rv0); gl<1024>(Wp + 2 * RSTR, rv1);
    setm0<3 * STAGE256>(sW); SCHED0(); asm volatile("s_nop 0"); gl<0>(Ap + 3 * PSTR, pv0); gl<1024>(Ap + 3 * PSTR, pv1);
    wait_vm<10>();
    BARRIER();
#pragma unroll
    for (int j = 0; j < 2; ++j) F0.rf[j] = cat8(tr_read(lds + g.rAddr[j]), tr_read(lds + g.rAddr[j] + 1024));
#pragma unroll
    for (int i = 0; i < 4; ++i) F0.pf[i] = *(const LAS bf16x8*)(lds + g.pAddr[0] + i * 2048);
    SCHED0();
    for (int t = 0; t < ntile; ++t) {
      const int tb = tb0 + 4 * t;
      auto run = [&](auto VAR, int gi) {
        const Desc Rc{Wp + (size_t)(4 * gi + 3) * RSTR, RSTR, rv0, rv1};
        const bool tl = gi + 1 == ngt;
        const bool sl = tl && t + 1 == ntile;
        const int gn = tl ? 0 : gi + 1;
        Desc Pn{Ap + (size_t)(tl && !sl ? t + 1 : t) * 1024 * 64 + (size_t)gn * 4 * PSTR, PSTR, pv0, pv1};
        Desc Rn{Wp + (size_t)(4 * gn) * RSTR, RSTR, rv0, rv1};
        if (sl && hasMix) { Pn = MP; Rn = MR; }
        SCHED0();
        group(VAR, t > 0, Rc, Pn, Rn);
      };
      run(IC<0>{}, 0);
      for (int gi = 1; gi < ngt; ++gi) run(IC<1>{}, gi);
      if (hasMix) {
        setm0<3 * STAGE256 + 16384>(sW); SCHED0(); asm volatile("s_nop 0");
        gl<0>(MR.p + 3 * MR.stride, MR.v0); gl<1024>(MR.p + 3 * MR.stride, MR.v1);
      }
      const int head = hp * 2 + (wr >> 1);
      char LAS* slab = lds + 4 * STAGE256 + w * 4096;
      const int wa = r32 * 128, wsw = r32 & 7;
      const int rrow = l >> 3, ra = rrow * 128 + (((l & 7) ^ rrow) << 4);
      bf16_t* obase = O + ((size_t)head * S_LEN + tb * 256 + 128 * wp + rrow) * HD + 64 * (wr & 1) + (l & 7) * 8;
#pragma unroll
      for (int i = 0; i < 4; ++i) {
#pragma unroll
        for (int j = 0; j < 2; ++j) {
          const f32x16 a = acc[i][j];
          u32x4 v0 = {pack2(a[0], a[1]), pack2(a[2], a[3]), pack2(a[4], a[5]), pack2(a[6], a[7])};
          u32x4 v1 = {pack2(a[8], a[9]), pack2(a[10], a[11]), pack2(a[12], a[13]), pack2(a[14], a[15])};
          *(u32x4 LAS*)(slab + wa + (((4 * j + hi) ^ wsw) << 4)) = v0;
          *(u32x4 LAS*)(slab + wa + (((4 * j + 2 + hi) ^ wsw) << 4)) = v1;
        }
#pragma unroll
        for (int it = 0; it < 4; ++it) {
          const u32x4 v = *(const u32x4 LAS*)(slab + ra + it * 1024);
          st16(obase + (size_t)(32 * i + 8 * it) * HD, v);
        }
      }
      SCHED0();
    }
  }
  TS(0, 1);
  if (!hasMix) wait_vm<(ST_MODE == 9 ? 0 : 16)>();
  if (hasMix) {
    const int ws = w & 3, kh = w >> 2;
    const int blk = (l >> 4) & 1, q = (l & 15) >> 2, p = l & 3;
    const int pB = (32 * kh + 8 * hi + q) * 256 + ((ws ^ q) * 64) + blk * 32 + p * 8;
    const int rA = 16384 + (32 * kh + 8 * hi + q) * 64 + blk * 32 + swapmid(p) * 8;
    f32x16 acc[3] = {};
    bf16x8 pa[2], ra[2][3], pb[2], rb[2][3];
    auto mstep = [&](auto VAR, auto J, const bf16x8 (&pc)[2], const bf16x8 (&rc)[2][3], bf16x8 (&pn)[2], bf16x8 (&rn)[2][3],
                     const char* sp, const char* sr) {
      constexpr int j = J, var = VAR;
      if (var == 0) { if (j < 3) wait_vm<27>(); else wait_vm<8>(); }
      else if (var == 1) wait_vm<8>();
      else { if (j == 0) wait_vm<8>(); else if (j == 1) wait_vm<4>(); else if (j == 2) wait_vm<0>(); }
      WAIT_LGKM0();
      SCHED0();
      BARRIER();
      SCHED0();
      const char LAS* nb = lds + ((j + 1) & 3) * STAGE256;
      auto mm = [&](int k2, int jj) {
        acc[jj] = mf<X16_G1>(rc[k2][jj], pc[k2], acc[jj], k2);
        SCHED0();
      };
      auto rdP = [&](int k2) { pn[k2] = cat8(tr_read(nb + pB + k2 * 4096), tr_read(nb + pB + k2 * 4096 + 1024)); SCHED0(); };
      auto rdR = [&](int k2, int jj) {
        rn[k2][jj] = cat8(tr_read(nb + rA + jj * 4096 + k2 * 1024), tr_read(nb + rA + jj * 4096 + k2 * 1024 + 256));
        SCHED0();
      };
      mm(0, 0); rdP(0); rdR(0, 0); rdR(0, 1);
      mm(0, 1); rdR(0, 2); rdP(1); rdR(1, 0);
      mm(0, 2); rdR(1, 1); rdR(1, 2); if (var != 2) setm0<j * STAGE256>(sW); SCHED0();
      mm(1, 0); if (var != 2) { gl<0>(sp, MP.v0); gl<1024>(sp, MP.v1); setm0<j * STAGE256 + 16384>(sW); } SCHED0();
      mm(1, 1); if (var != 2) gl<0>(sr, MR.v0); SCHED0();
      mm(1, 2); if (var != 2) gl<1024>(sr, MR.v1); SCHED0();
    };
    auto mgroup = [&](auto VAR, int m) {
      const char* sp = MP.p + (size_t)(m + 4) * MP.stride;
      const char* sr = MR.p + (size_t)(m + 4) * MR.stride;
      mstep(VAR, IC<0>{}, pa, ra, pb, rb, sp, sr);
      mstep(VAR, IC<1>{}, pb, rb, pa, ra, sp + MP.stride, sr + MR.stride);
      mstep(VAR, IC<2>{}, pa, ra, pb, rb, sp + 2 * MP.stride, sr + 2 * MR.stride);
      mstep(VAR, IC<3>{}, pb, rb, pa, ra, sp + 3 * MP.stride, sr + 3 * MR.stride);
    };
    wait_vm<28>();
    BARRIER();
    const int jk = kh == 0 ? 0 : 2;
    u32x4 bias[3];
    bias[0] = *(const u32x4*)(b1 + f0 + 8 * hi + 32 * jk);
    bias[1] = *(const u32x4*)(b1 + f0 + 8 * hi + 32 * jk + 16);
    bias[2] = *(const u32x4*)(b1 + f0 + 8 * hi + 32 + 16 * kh);
    SCHED0();
#pragma unroll
    for (int k2 = 0; k2 < 2; ++k2) {
      pa[k2] = cat8(tr_read(lds + pB + k2 * 4096), tr_read(lds + pB + k2 * 4096 + 1024));
#pragma unroll
      for (int j = 0; j < 3; ++j) ra[k2][j] = cat8(tr_read(lds + rA + j * 4096 + k2 * 1024), tr_read(lds + rA + j * 4096 + k2 * 1024 + 256));
    }
    SCHED0();
    mgroup(IC<0>{}, 0);
    for (int m = 4; m < 28; m += 4) mgroup(IC<1>{}, m);
    mgroup(IC<2>{}, 28);
    WAIT_LGKM0();
    TS(0, 3);
    BARRIER();
    float LAS* dst = (float LAS*)(lds + w * 8192);
    const f32x16 sa = kh == 0 ? acc[2] : acc[0], ka = kh == 0 ? acc[0] : acc[2];
    const int so = kh == 0 ? 8 : 0;
#pragma unroll
    for (int g = 0; g < 4; ++g) *(f32x4 LAS*)(dst + (g * 64 + l) * 4) = (f32x4){sa[4 * g], sa[4 * g + 1], sa[4 * g + 2], sa[4 * g + 3]};
    float s1[8], k1[8];
#pragma unroll
    for (int e = 0; e < 8; ++e) { s1[e] = kh == 0 ? acc[1][8 + e] : acc[1][e]; k1[e] = kh == 0 ? acc[1][e] : acc[1][8 + e]; }
#pragma unroll
    for (int g = 0; g < 2; ++g) *(f32x4 LAS*)(dst + ((4 + g) * 64 + l) * 4) = (f32x4){s1[4 * g], s1[4 * g + 1], s1[4 * g + 2], s1[4 * g + 3]};
    (void)so;
    WAIT_LGKM0();
    BARRIER();
    const float LAS* src = (const float LAS*)(lds + (w ^ 4) * 8192);
    bf16_t* urow = Uo + (size_t)(d0 + 32 * ws + r32) * FF + f0 + 8 * hi;
    auto fin8 = [&](const float (&v)[8], int fo, const u32x4 bv) {
      float r[8];
#pragma unroll
      for (int e = 0; e < 4; ++e) {
        r[2 * e] = gelu_tanh(rbf(rbf(v[2 * e]) + __uint_as_float(bv[e] << 16)));
        r[2 * e + 1] = gelu_tanh(rbf(rbf(v[2 * e + 1]) + __uint_as_float(bv[e] & 0xffff0000u)));
      }
      st16(urow + fo, (u32x4){pack2(r[0], r[1]), pack2(r[2], r[3]), pack2(r[4], r[5]), pack2(r[6], r[7])});
    };
#pragma unroll
    for (int hh = 0; hh < 2; ++hh) {
      float v[8];
#pragma unroll
      for (int g = 0; g < 2; ++g) {
        const f32x4 o = *(const f32x4 LAS*)(src + ((2 * hh + g) * 64 + l) * 4);
#pragma unroll
        for (int e = 0; e < 4; ++e) v[4 * g + e] = ka[8 * hh + 4 * g + e] + o[e];
      }
      fin8(v, 32 * jk + 16 * hh, bias[hh]);
    }
    {
      float v[8];
#pragma unroll
      for (int g = 0; g < 2; ++g) {
        const f32x4 o = *(const f32x4 LAS*)(src + ((4 + g) * 64 + l) * 4);
#pragma unroll
        for (int e = 0; e < 4; ++e) v[4 * g + e] = k1[4 * g + e] + o[e];
      }
      fin8(v, 32 + 16 * kh, bias[2]);
    }
  }
  TS(0, 2);
}

constexpr int AT_HALF = 16384;
__global__ __launch_bounds__(512, 2) void attn_kernel(const bf16_t* __restrict__ Q, const bf16_t* __restrict__ K,
                                                      const bf16_t* __restrict__ V, bf16_t* __restrict__ ctx,
                                                      const bf16_t* __restrict__ wo, const bf16_t* __restrict__ w2) {
  __shared__ __attribute__((aligned(16))) char smem[8 * AT_HALF + 32768];
  char LAS* lds = (char LAS*)smem;
  const int tid = threadIdx.x, l = tid & 63, w = __builtin_amdgcn_readfirstlane(tid >> 6);
  const int rg = w & 3, g = w >> 2, r32 = l & 31, hi = l >> 5;
  const int xcd = blockIdx.x & 7, jj = blockIdx.x >> 3;
  const int head = xcd * 4 + (jj >> 3), pair = jj & 7;
  const int LB = 15 - pair, nshort = 2 * pair + 2;
  const bf16_t* Kh = K + (size_t)head * S_LEN * HD;
  const bf16_t* Vh = V + (size_t)head * S_LEN * HD;
  const bf16_t* Qh = Q + (size_t)head * S_LEN * HD;
  char LAS* gl = lds + g * 4 * AT_HALF;
  int kOff[4], vOff[4];
#pragma unroll
  for (int i = 0; i < 4; ++i) {
    const int j = rg * 4 + i, row = 4 * j + (l >> 4), s16 = l & 15;
    kOff[i] = (row * HD + ((s16 ^ (row & 15)) * 8)) * 2 - 1024 * i;
    vOff[i] = (row * HD + ((((s16 >> 2) ^ (row & 3)) * 4 + (s16 & 3)) * 8)) * 2 - 1024 * i;
  }
  const unsigned glb = (unsigned)(uintptr_t)gl;
  auto htile = [&](int i) { return (g == 0 || i < nshort) ? i : i + 15 - 2 * pair; };
  auto stageK = [&](int i) {
    const bf16_t* kp = Kh + (size_t)htile(i > 16 ? 16 : i) * 64 * HD;
    const unsigned bb = glb + (i & 1) * AT_HALF + rg * 4096;
    glds16x4(kp, kOff[0], kOff[1], kOff[2], kOff[3], bb);
  };
  auto stageV = [&](int i) {
    const bf16_t* vp = Vh + (size_t)htile(i) * 64 * HD;
    const unsigned bb = glb + (2 + (i & 1)) * AT_HALF + rg * 4096;
    glds16x4(vp, vOff[0], vOff[1], vOff[2], vOff[3], bb);
  };
  const int ksw = r32 & 15;
  const int blk = (l >> 4) & 1, q4 = (l & 15) >> 2, p4 = l & 3;
  int vAddr[4];
#pragma unroll
  for (int db = 0; db < 4; ++db)
    vAddr[db] = 2 * AT_HALF + (4 * hi + q4) * 256 + ((db ^ q4) * 64) + blk * 32 + swapmid(p4) * 8;
  constexpr float CS = 0.08838834764831845f * 1.4426950408889634f;

  TS(1, 0);
  stageK(0);
  stageV(0);
  stageK(1);
  int qb = g == 0 ? LB : pair;
  bf16x8 qf[8];
#pragma unroll
  for (int d0 = 0; d0 < 8; ++d0) qf[d0] = *(const bf16x8*)(Qh + (size_t)(qb * 128 + 32 * rg + r32) * HD + d0 * 16 + hi * 8);
  char LAS* qsave = lds + 8 * AT_HALF + rg * 8192 + l * 16;
  if (g == 0) {
#pragma unroll
    for (int d0 = 0; d0 < 8; ++d0) *(bf16x8 LAS*)(qsave + d0 * 1024) = qf[d0];
  }
  float m_run = -1e30f, l_run = 0.f, alpha = 1.f;
  f32x16 o[4] = {};
  f32x16 s0, s1;
  bf16x8 pf[4];
  u32x2 pfd = {0u, 0u};
  constexpr int AT_VM = 5;
#define AT_W 6
  bf16x8 fr[32];
  const int kAddr0 = r32 * 256 + ((hi ^ ksw) << 4);
  auto rdK = [&](int k, int slot) {
    fr[k] = *(const LAS bf16x8*)(gl + slot * AT_HALF + (kAddr0 ^ ((k >> 1) << 5)) + (k & 1) * 8192);
  };
  auto rdV = [&](int k, int slot) {
    const char LAS* a = gl + slot * AT_HALF + vAddr[(k - 16) >> 2] + (k & 3) * 4096;
    fr[k] = cat8(tr_read(a), tr_read(a + 2048));
  };
  auto mm = [&](int k) {
    if (k < 16) {
      if (k & 1) s1 = mf<X16_AT>(fr[k], qf[k >> 1], k < 2 ? (f32x16){} : s1, (k >> 1) & 1);
      else s0 = mf<X16_AT>(fr[k], qf[k >> 1], k < 2 ? (f32x16){} : s0, (k >> 1) & 1);
    } else {
      o[(k - 16) >> 2] = mf<X16_AT>(fr[k], pf[k & 3], o[(k - 16) >> 2], k & 1);
    }
  };
  auto preK = [&](int slot) {
#pragma unroll
    for (int k = 0; k < AT_W; ++k) rdK(k, slot);
  };
  auto preV = [&](int slot) {
#pragma unroll
    for (int k = 16; k < 16 + AT_W; ++k) rdV(k, slot);
  };
  auto xhalf_max = [&](float v) {
    auto rr = __builtin_amdgcn_permlane32_swap(__float_as_uint(v), __float_as_uint(v), false, false);
    return fmaxf(__uint_as_float(rr[0]), __uint_as_float(rr[1]));
  };
  auto xhalf_sum = [&](float v) {
    auto rr = __builtin_amdgcn_permlane32_swap(__float_as_uint(v), __float_as_uint(v), false, false);
    return __uint_as_float(rr[0]) + __uint_as_float(rr[1]);
  };
  auto softmax = [&](int i) {
    if (g == 1 && i == nshort) {
      const float inv = 1.0f / xhalf_sum(l_run);
      bf16_t* orow = ctx + (size_t)(pair * 128 + 32 * rg + r32) * CTX_LD + head * HD + 8 * hi;
#pragma unroll
      for (int db = 0; db < 4; ++db) {
        const f32x16 a = o[db];
        u32x4 v0 = {pack2(a[0] * inv, a[1] * inv), pack2(a[2] * inv, a[3] * inv), pack2(a[4] * inv, a[5] * inv), pack2(a[6] * inv, a[7] * inv)};
        u32x4 v1 = {pack2(a[8] * inv, a[9] * inv), pack2(a[10] * inv, a[11] * inv), pack2(a[12] * inv, a[13] * inv), pack2(a[14] * inv, a[15] * inv)};
        st16(orow + 32 * db, v0);
        st16(orow + 32 * db + 16, v1);
        o[db] = (f32x16){};
      }
      m_run = -1e30f;
      l_run = 0.f;
      qb = LB;
    }
    if (g == 1 && i == nshort - 1) {
#pragma unroll
      for (int d0 = 0; d0 < 8; ++d0) qf[d0] = *(const bf16x8 LAS*)(qsave + d0 * 1024);
    }
    SCHED0();
    if (i < 16) stageK(i + 2);
    if (i < 16) {
      int idx = (i * 256 + (int)blockIdx.x) * 8 + w;
      if (idx >= 24064) idx -= 24064;
      const char* pb = idx < 10240 ? (const char*)wo + (size_t)idx * 512 : (const char*)w2 + (size_t)(idx - 10240) * 512;
      asm volatile("global_load_dwordx2 %0, %1, %2" : "+v"(pfd) : "v"(l * 8), "s"(pb) : "memory");
    }
    SCHED0(); PH(i, 5); SCHED0();
    const int dh = htile(i) - 2 * qb;
    if (dh >= 0) {
      const int qi = 32 * rg + r32 - 64 * dh;
#pragma unroll
      for (int r = 0; r < 16; ++r) {
        const int key = (r & 3) + 8 * (r >> 2) + 4 * hi;
        if (key > qi) s0[r] = -__builtin_inff();
        if (key + 32 > qi) s1[r] = -__builtin_inff();
      }
    }
    float pm = s0[0];
#pragma unroll
    for (int r = 1; r < 16; ++r) pm = fmaxf(pm, s0[r]);
#pragma unroll
    for (int r = 0; r < 16; ++r) pm = fmaxf(pm, s1[r]);
    pm = xhalf_max(pm);
    const float mn = fmaxf(m_run, pm);
    alpha = __builtin_amdgcn_exp2f((m_run - mn) * CS);
    m_run = mn;
    const float mc = -mn * CS;
    SCHED0(); PH(i, 6); SCHED0();
    float ps = 0.f;
#pragma unroll
    for (int r = 0; r < 16; ++r) { s0[r] = __builtin_amdgcn_exp2f(fmaf(s0[r], CS, mc)); ps += s0[r]; }
#pragma unroll
    for (int r = 0; r < 16; ++r) { s1[r] = __builtin_amdgcn_exp2f(fmaf(s1[r], CS, mc)); ps += s1[r]; }
    l_run = l_run * alpha + ps;
    SCHED0(); PH(i, 7); SCHED0();
    u32x4 a0 = {pack2(s0[0], s0[1]), pack2(s0[2], s0[3]), pack2(s0[4], s0[5]), pack2(s0[6], s0[7])};
    u32x4 a1 = {pack2(s0[8], s0[9]), pack2(s0[10], s0[11]), pack2(s0[12], s0[13]), pack2(s0[14], s0[15])};
    u32x4 a2 = {pack2(s1[0], s1[1]), pack2(s1[2], s1[3]), pack2(s1[4], s1[5]), pack2(s1[6], s1[7])};
    u32x4 a3 = {pack2(s1[8], s1[9]), pack2(s1[10], s1[11]), pack2(s1[12], s1[13]), pack2(s1[14], s1[15])};
    pf[0] = *(bf16x8*)&a0; pf[1] = *(bf16x8*)&a1; pf[2] = *(bf16x8*)&a2; pf[3] = *(bf16x8*)&a3;
    asm volatile("" : "+v"(pf[0]), "+v"(pf[1]), "+v"(pf[2]), "+v"(pf[3]), "+v"(l_run), "+v"(alpha));
  };
#define AT_DK0 16
  WAIT_VM0();
  WAIT_LGKM0();
  BARRIER();
  TS(1, 3);
  if (g == 1) BARRIER();
  SCHED0();
  preK(0);
#pragma unroll
  for (int k = 0; k < 16; ++k) {
    if (k + AT_W < 16) rdK(k + AT_W, 0);
    mm(k);
    SCHED0();
  }
  BARRIER();
  for (int i = 0; i < 16; ++i) {
    SCHED0();
    PH(i, 0);
    softmax(i);
    SCHED0();
    const int ks = (i + 1) & 1, vs = i & 1;
    preK(ks);
    SCHED0();
    wait_vm<AT_VM>();
    PH(i, 1);
    BARRIER();
    SCHED0();
    PH(i, 2);
    __builtin_amdgcn_s_setprio(1);
#pragma unroll
    for (int k = 0; k < 32; ++k) {
      if (k + AT_W < 16) rdK(k + AT_W, ks);
      else if (k + AT_W < 32) rdV(k + AT_W, vs);
      mm(k);
      if (k < 16) {
#pragma unroll
        for (int e = 0; e < 4; ++e) {
          float t = o[k >> 2][4 * (k & 3) + e];
          asm volatile("v_mul_f32 %0, %1, %0" : "+v"(t) : "v"(alpha));
          o[k >> 2][4 * (k & 3) + e] = t;
        }
      }
      if (k == AT_DK0) stageV(i + 1);
      SCHED0();
    }
    __builtin_amdgcn_s_setprio(0);
    PH(i, 3);
    wait_vm<AT_VM>();
    PH(i, 4);
    BARRIER();
  }
  SCHED0();
  softmax(16);
#pragma unroll
  for (int db = 0; db < 4; ++db)
#pragma unroll
    for (int r = 0; r < 16; ++r) o[db][r] *= alpha;
  SCHED0();
  WAIT_VM0();
  asm volatile("" ::"v"(pfd));
  BARRIER();
  SCHED0();
  preV(0);
#pragma unroll
  for (int k = 16; k < 32; ++k) {
    if (k + AT_W < 32) rdV(k + AT_W, 0);
    mm(k);
    SCHED0();
  }
  BARRIER();
  if (g == 0) BARRIER();
  l_run = xhalf_sum(l_run);
  TS(1, 1);
  {
    const int kh = g;
    float LAS* ml = (float LAS*)(lds + 4 * AT_HALF);
    float LAS* dst = (float LAS*)(lds + w * 8192);
#pragma unroll
    for (int dd = 0; dd < 2; ++dd) {
      f32x16 a;
#pragma unroll
      for (int r = 0; r < 16; ++r) a[r] = kh == 0 ? o[2 + dd][r] : o[dd][r];
#pragma unroll
      for (int gg = 0; gg < 4; ++gg)
        *(f32x4 LAS*)(dst + ((dd * 4 + gg) * 64 + l) * 4) = (f32x4){a[4 * gg], a[4 * gg + 1], a[4 * gg + 2], a[4 * gg + 3]};
    }
    ml[w * 128 + l] = m_run;
    ml[w * 128 + 64 + l] = l_run;
    asm volatile("s_waitcnt lgkmcnt(0)" ::: "memory");
    BARRIER();
    const int wo = w ^ 4;
    const float m2 = ml[wo * 128 + l], l2 = ml[wo * 128 + 64 + l];
    const float mt = fmaxf(m_run, m2);
    const float a1 = __builtin_amdgcn_exp2f((m_run - mt) * CS), a2 = __builtin_amdgcn_exp2f((m2 - mt) * CS);
    const float inv = 1.0f / (l_run * a1 + l2 * a2);
    const float f1 = a1 * inv, f2 = a2 * inv;
    const float LAS* src = (const float LAS*)(lds + wo * 8192);
    bf16_t* orow = ctx + (size_t)(LB * 128 + 32 * rg + r32) * CTX_LD + head * HD + 8 * hi;
#pragma unroll
    for (int dd = 0; dd < 2; ++dd) {
      f32x16 a;
#pragma unroll
      for (int r = 0; r < 16; ++r) a[r] = kh == 0 ? o[dd][r] : o[2 + dd][r];
      float r[16];
#pragma unroll
      for (int gg = 0; gg < 4; ++gg) {
        const f32x4 b = *(const f32x4 LAS*)(src + ((dd * 4 + gg) * 64 + l) * 4);
#pragma unroll
        for (int e = 0; e < 4; ++e) r[4 * gg + e] = a[4 * gg + e] * f1 + b[e] * f2;
      }
      u32x4 v0 = {pack2(r[0], r[1]), pack2(r[2], r[3]), pack2(r[4], r[5]), pack2(r[6], r[7])};
      u32x4 v1 = {pack2(r[8], r[9]), pack2(r[10], r[11]), pack2(r[12], r[13]), pack2(r[14], r[15])};
      const int dcol = 32 * (2 * kh + dd);
      st16(orow + dcol, v0);
      st16(orow + dcol + 16, v1);
    }
  }
  TS(1, 2);
}

constexpr int G2_STAGE = 36864;
constexpr int G2_NST = 4;
__global__ __launch_bounds__(512, 2) void gemm2_kernel(const bf16_t* __restrict__ ctx, const bf16_t* __restrict__ wo,
                                                       const bf16_t* __restrict__ U, const bf16_t* __restrict__ w2,
                                                       float* __restrict__ part) {
  __shared__ __attribute__((aligned(16))) char smem[G2_NST * G2_STAGE];
  char LAS* lds = (char LAS*)smem;
  const int b = blockIdx.x, xcd = b & 7, c = b >> 3;
  const int ks = xcd >> 1;
  const int s0 = (xcd & 1) * 1024 + (c >> 2) * 128, m0 = (c & 3) * 160;
  const int l = threadIdx.x & 63, w = __builtin_amdgcn_readfirstlane(threadIdx.x >> 6);
  const int ws = w & 3, kh = w >> 2, r32 = l & 31, hi = l >> 5;
  const int blk = (l >> 4) & 1, q = (l & 15) >> 2, p = l & 3;
  TS(2, 0);
  int pOffA[2], pOffB[2], rOffA[3], rOffB[3];
#pragma unroll
  for (int i = 0; i < 2; ++i) {
    const int j = w * 2 + i;
    const int row = 8 * j + (l >> 3), slot = l & 7;
    pOffA[i] = (row * CTX_LD + (slot ^ ((row >> 1) & 7)) * 8) * 2;
    const int krow = 4 * j + (l >> 4), s16 = l & 15;
    pOffB[i] = (krow * S_LEN + ((s16 >> 2) ^ (krow & 3)) * 32 + (s16 & 3) * 8) * 2;
  }
#pragma unroll
  for (int i = 0; i < 3; ++i) {
    const int j = w + 8 * i;
    const int cb = j >> 2, kr = (j & 3) * 16 + (l >> 2);
    rOffA[i] = (kr * DM + cb * 32 + (l & 3) * 8) * 2;
    const int row = 8 * j + (l >> 3), slot = l & 7;
    rOffB[i] = (row * FF + (slot ^ ((row >> 1) & 7)) * 8) * 2;
  }
  const int f0 = ks * 7, nB = ks == 3 ? 6 : 7, total = 16 + nB;
  const bf16_t* PA = ctx + (size_t)s0 * CTX_LD + ks * 1024;
  const bf16_t* RA = wo + (size_t)ks * 1024 * DM + m0;
  const bf16_t* PB = w2 + (size_t)f0 * 64 * S_LEN + s0;
  const bf16_t* RB = U + (size_t)m0 * FF + f0 * 64;
  int pslot = 0, issued = 0;
  auto issue = [&]() {
    const unsigned bb = (unsigned)(uintptr_t)(lds + pslot * G2_STAGE);
    if (issued < 16) {
      const bf16_t* P = PA + issued * 64;
      const bf16_t* R = RA + (size_t)issued * 64 * DM;
      glds16(P, pOffA[0], bb + (w * 2) * 1024);
      glds16(P, pOffA[1], bb + (w * 2 + 1) * 1024);
      glds16(R, rOffA[0], bb + 16384 + w * 1024);
      glds16(R, rOffA[1], bb + 16384 + (w + 8) * 1024);
      if (w < 4) glds16(R, rOffA[2], bb + 16384 + (w + 16) * 1024);
    } else {
      const bf16_t* P = PB + (size_t)(issued - 16) * 64 * S_LEN;
      const bf16_t* R = RB + (issued - 16) * 64;
      glds16(P, pOffB[0], bb + (w * 2) * 1024);
      glds16(P, pOffB[1], bb + (w * 2 + 1) * 1024);
      glds16(R, rOffB[0], bb + 16384 + w * 1024);
      glds16(R, rOffB[1], bb + 16384 + (w + 8) * 1024);
      if (w < 4) glds16(R, rOffB[2], bb + 16384 + (w + 16) * 1024);
    }
    if (++pslot == G2_NST) pslot = 0;
    ++issued;
  };
  const int prow = 32 * ws + r32, psw = (prow >> 1) & 7;
  const int pA0 = prow * 128 + (((4 * kh + hi) ^ psw) << 4), pA1 = prow * 128 + (((4 * kh + 2 + hi) ^ psw) << 4);
  const int rA = 16384 + (32 * kh + 8 * hi + q) * 64 + blk * 32 + swapmid(p) * 8;
  const int pB = (32 * kh + 8 * hi + q) * 256 + ((ws ^ q) * 64) + blk * 32 + p * 8;
  const int rrow = rperm(r32), rsw = (rrow >> 1) & 7;
  const int rB0 = 16384 + rrow * 128 + (((4 * kh + hi) ^ rsw) << 4), rB1 = 16384 + rrow * 128 + (((4 * kh + 2 + hi) ^ rsw) << 4);
#pragma unroll
  for (int i = 0; i < G2_NST - 1; ++i) issue();
  f32x16 acc[5] = {};
  int buf = 0;
  for (int step = 0; step < 16; ++step) {
    if (w < 4) wait_vm<5 * (G2_NST - 2)>();
    else wait_vm<4 * (G2_NST - 2)>();
    BARRIER();
    issue();
    const char LAS* bp = lds + buf * G2_STAGE;
    bf16x8 pf[2], rf[2][5];
    pf[0] = *(const LAS bf16x8*)(bp + pA0);
#pragma unroll
    for (int j = 0; j < 5; ++j) rf[0][j] = cat8(tr_read(bp + rA + j * 4096), tr_read(bp + rA + j * 4096 + 256));
    pf[1] = *(const LAS bf16x8*)(bp + pA1);
#pragma unroll
    for (int j = 0; j < 5; ++j) rf[1][j] = cat8(tr_read(bp + rA + j * 4096 + 1024), tr_read(bp + rA + j * 4096 + 1024 + 256));
    __builtin_amdgcn_sched_barrier(0);
#pragma unroll
    for (int k2 = 0; k2 < 2; ++k2)
#pragma unroll
      for (int j = 0; j < 5; ++j) acc[j] = mf<X16_G2>(rf[k2][j], pf[k2], acc[j], k2);
    if (++buf == G2_NST) buf = 0;
  }
  for (int step = 16; step < total; ++step) {
    if (step + G2_NST - 1 > total) WAIT_VM0();
    else if (w < 4) wait_vm<5 * (G2_NST - 2)>();
    else wait_vm<4 * (G2_NST - 2)>();
    BARRIER();
    if (issued < total) issue();
    const char LAS* bp = lds + buf * G2_STAGE;
    bf16x8 pf[2], rf[2][5];
    pf[0] = cat8(tr_read(bp + pB), tr_read(bp + pB + 1024));
#pragma unroll
    for (int j = 0; j < 5; ++j) rf[0][j] = *(const LAS bf16x8*)(bp + rB0 + j * 4096);
    pf[1] = cat8(tr_read(bp + pB + 4096), tr_read(bp + pB + 4096 + 1024));
#pragma unroll
    for (int j = 0; j < 5; ++j) rf[1][j] = *(const LAS bf16x8*)(bp + rB1 + j * 4096);
    __builtin_amdgcn_sched_barrier(0);
#pragma unroll
    for (int k2 = 0; k2 < 2; ++k2)
#pragma unroll
      for (int j = 0; j < 5; ++j) acc[j] = mf<X16_G2>(rf[k2][j], pf[k2], acc[j], k2);
    if (++buf == G2_NST) buf = 0;
  }
  TS(2, 1);
  BARRIER();
  {
    float LAS* dst = (float LAS*)(lds + w * 12288);
    if (kh == 0) {
#pragma unroll
      for (int j = 3; j < 5; ++j)
#pragma unroll
        for (int g = 0; g < 4; ++g)
          *(f32x4 LAS*)(dst + (((j - 3) * 4 + g) * 64 + l) * 4) = (f32x4){acc[j][4 * g], acc[j][4 * g + 1], acc[j][4 * g + 2], acc[j][4 * g + 3]};
    } else {
#pragma unroll
      for (int j = 0; j < 3; ++j)
#pragma unroll
        for (int g = 0; g < 4; ++g)
          *(f32x4 LAS*)(dst + ((j * 4 + g) * 64 + l) * 4) = (f32x4){acc[j][4 * g], acc[j][4 * g + 1], acc[j][4 * g + 2], acc[j][4 * g + 3]};
    }
    asm volatile("s_waitcnt lgkmcnt(0)" ::: "memory");
    BARRIER();
    const float LAS* src = (const float LAS*)(lds + (w ^ 4) * 12288);
    float* orow = part + ((size_t)ks * S_LEN + s0 + 32 * ws + r32) * DM + m0 + 8 * hi;
    if (kh == 0) {
#pragma unroll
      for (int j = 0; j < 3; ++j)
#pragma unroll
        for (int g = 0; g < 4; ++g) {
          const f32x4 o = *(const f32x4 LAS*)(src + ((j * 4 + g) * 64 + l) * 4);
          st16f(orow + 32 * j + (g >> 1) * 16 + (g & 1) * 4, acc[j][4 * g] + o[0], acc[j][4 * g + 1] + o[1], acc[j][4 * g + 2] + o[2], acc[j][4 * g + 3] + o[3]);
        }
    } else {
#pragma unroll
      for (int j = 3; j < 5; ++j)
#pragma unroll
        for (int g = 0; g < 4; ++g) {
          const f32x4 o = *(const f32x4 LAS*)(src + (((j - 3) * 4 + g) * 64 + l) * 4);
          st16f(orow + 32 * j + (g >> 1) * 16 + (g & 1) * 4, acc[j][4 * g] + o[0], acc[j][4 * g + 1] + o[1], acc[j][4 * g + 2] + o[2], acc[j][4 * g + 3] + o[3]);
        }
    }
  }
  TS(2, 2);
}

__global__ __launch_bounds__(256) void reduce_kernel(const float* __restrict__ part, const bf16_t* __restrict__ x,
                                                     const bf16_t* __restrict__ b2, bf16_t* __restrict__ out) {
  TS(4, 0);
  const int i = blockIdx.x * 256 + threadIdx.x;
  const int e = i * 4;
  const int s = e / DM;
  f32x4 a = *(const f32x4*)(part + e);
#pragma unroll
  for (int sp = 1; sp < 4; ++sp) a += *(const f32x4*)(part + (size_t)sp * S_LEN * DM + e);
  const u32x2 xv = *(const u32x2*)(x + e);
  const float bb = bf2f(b2[s]);
  float r0 = __uint_as_float(xv[0] << 16) + a[0] + bb;
  float r1 = __uint_as_float(xv[0] & 0xffff0000u) + a[1] + bb;
  float r2 = __uint_as_float(xv[1] << 16) + a[2] + bb;
  float r3 = __uint_as_float(xv[1] & 0xffff0000u) + a[3] + bb;
  u32x2 o = {pack2(r0, r1), pack2(r2, r3)};
  *(u32x2*)(out + e) = o;
  TS(4, 2);
}

extern "C" void kernel_launch(void* const* d_in, const int* in_sizes, int n_in, void* d_out, int out_size, void* d_ws,
                              size_t ws_size, hipStream_t stream) {
  const bf16_t* x = (const bf16_t*)d_in[0];
  const bf16_t* ckv = (const bf16_t*)d_in[1];
  const bf16_t* wq = (const bf16_t*)d_in[2];
  const bf16_t* wuk = (const bf16_t*)d_in[3];
  const bf16_t* wuv = (const bf16_t*)d_in[4];
  const bf16_t* wo = (const bf16_t*)d_in[5];
  const bf16_t* w1 = (const bf16_t*)d_in[7];
  const bf16_t* b1 = (const bf16_t*)d_in[8];
  const bf16_t* w2 = (const bf16_t*)d_in[9];
  const bf16_t* b2 = (const bf16_t*)d_in[10];
  char* ws = (char*)d_ws;
  bf16_t* xn = (bf16_t*)(ws + WS_XN);
  bf16_t* h = (bf16_t*)(ws + WS_H);
  bf16_t* Q = (bf16_t*)(ws + WS_Q);
  bf16_t* K = (bf16_t*)(ws + WS_K);
  bf16_t* V = (bf16_t*)(ws + WS_V);
  bf16_t* U = (bf16_t*)(ws + WS_U);
  bf16_t* ctx = (bf16_t*)(ws + WS_CTX);
  float* part = (float*)(ws + WS_PART);
  bf16_t* ct = (bf16_t*)(ws + WS_UP);
  ln_kernel<<<S_LEN / 4 + PF_BLOCKS, 256, 0, stream>>>(x, xn, h, ckv, ct, wq, wuk, wuv);
  gemm1_kernel<<<256, 512, 0, stream>>>(xn, h, ct, wq, wuk, wuv, w1, b1, wo, w2, Q, K, V, U);
  attn_kernel<<<256, 512, 0, stream>>>(Q, K, V, ctx, wo, w2);
  gemm2_kernel<<<256, 512, 0, stream>>>(ctx, wo, U, w2, part);
  reduce_kernel<<<S_LEN * DM / 4 / 256, 256, 0, stream>>>(part, x, b2, (bf16_t*)d_out);
}
```

```cpp
#include <hip/hip_runtime.h>
#include <hip/hip_bf16.h>
#include <stdint.h>
#include <type_traits>

constexpr int S_LEN = 2048;
constexpr int DM = 640;
constexpr int NH = 32;
constexpr int HD = 128;
constexpr int DC = 512;
constexpr int FF = 1728;
constexpr int HDN = NH * HD;
constexpr int CTX_LD = HDN + 64;

typedef unsigned short bf16_t;
typedef short bf16x8 __attribute__((ext_vector_type(8)));
typedef short s16x4 __attribute__((ext_vector_type(4)));
typedef float f32x16 __attribute__((ext_vector_type(16)));
typedef float f32x4 __attribute__((ext_vector_type(4)));
typedef float f32x2 __attribute__((ext_vector_type(2)));
typedef unsigned u32x4 __attribute__((ext_vector_type(4)));
typedef unsigned u32x2 __attribute__((ext_vector_type(2)));
typedef __bf16 bf16x2_t __attribute__((ext_vector_type(2)));
#define LAS __attribute__((address_space(3)))

__device__ __forceinline__ float bf2f(bf16_t v) { return __uint_as_float(((unsigned)v) << 16); }
__device__ __forceinline__ unsigned pack2(float a, float b) {
  bf16x2_t v = {(__bf16)a, (__bf16)b};
  return *reinterpret_cast<unsigned*>(&v);
}
__device__ __forceinline__ float rbf(float a) {
  __bf16 v = (__bf16)a;
  return __uint_as_float(((unsigned)*reinterpret_cast<unsigned short*>(&v)) << 16);
}

template <bool X16> __device__ __forceinline__ f32x16 mf(bf16x8 a, bf16x8 b, f32x16 c, int sel) {
  if (X16) {
    f32x4 q0 = {c[8 * sel], c[8 * sel + 1], c[8 * sel + 2], c[8 * sel + 3]};
    f32x4 q1 = {c[8 * sel + 4], c[8 * sel + 5], c[8 * sel + 6], c[8 * sel + 7]};
    q0 = __builtin_amdgcn_mfma_f32_16x16x32_bf16(a, b, q0, 0, 0, 0);
    q1 = __builtin_amdgcn_mfma_f32_16x16x32_bf16(b, a, q1, 0, 0, 0);
#pragma unroll
    for (int e = 0; e < 4; ++e) { c[8 * sel + e] = q0[e]; c[8 * sel + 4 + e] = q1[e]; }
    return c;
  }
  return __builtin_amdgcn_mfma_f32_32x32x16_bf16(a, b, c, 0, 0, 0);
}
#define X16_G1 0
#define X16_AT 0
#define X16_G2 0
constexpr size_t WS_XN = 0;
constexpr size_t WS_H = WS_XN + (size_t)S_LEN * DM * 2;
constexpr size_t WS_Q = WS_H + (size_t)S_LEN * DM * 2;
constexpr size_t WS_K = WS_Q + (size_t)S_LEN * HDN * 2;
constexpr size_t WS_V = WS_K + (size_t)S_LEN * HDN * 2;
constexpr size_t WS_U = WS_V + (size_t)S_LEN * HDN * 2;
constexpr size_t WS_CTX = WS_U + (size_t)DM * FF * 2;
constexpr size_t WS_PART = WS_CTX + (size_t)S_LEN * CTX_LD * 2;
constexpr size_t WS_UP = WS_PART + (size_t)4 * S_LEN * DM * 4;
constexpr size_t WS_CNT = WS_UP + (size_t)S_LEN * DC * 2;
constexpr size_t WS_END = WS_CNT + 64 * 128;
#define G2_FUSE 1

__device__ __forceinline__ float wave_sum(float v) {
#pragma unroll
  for (int o = 32; o > 0; o >>= 1) v += __shfl_xor(v, o);
  return v;
}
__device__ __forceinline__ void ln_row(float (&v)[10]) {
  float s = 0.f;
#pragma unroll
  for (int i = 0; i < 10; ++i) s += v[i];
  s = wave_sum(s);
  const float mean = s * (1.0f / DM);
  float q = 0.f;
#pragma unroll
  for (int i = 0; i < 10; ++i) { float d = v[i] - mean; q += d * d; }
  q = wave_sum(q);
  const float mb = rbf(mean);
  const float vb = rbf(q * (1.0f / DM));
  const float den = rbf(sqrtf(rbf(vb + 1e-5f)));
#pragma unroll
  for (int i = 0; i < 10; ++i) v[i] = rbf(rbf(v[i] - mb) / den);
}
__device__ __forceinline__ const char* warm_addr(int kb, int i, const bf16_t* wq, const bf16_t* wuk, const bf16_t* wuv) {
  const bf16_t* src = i < 8192 ? wq : i < 16384 ? wuk : wuv;
  return (const char*)src + (size_t)kb * 1048576 + (size_t)(i & 8191) * 128;
}
#define PF_KB 2
constexpr int PF_BLOCKS = 24 * PF_KB + 128;
__global__ __launch_bounds__(256) void ln_kernel(const bf16_t* __restrict__ x, bf16_t* __restrict__ xn,
                                                 bf16_t* __restrict__ h, const bf16_t* __restrict__ ckv,
                                                 bf16_t* __restrict__ ct, const bf16_t* __restrict__ wq,
                                                 const bf16_t* __restrict__ wuk, const bf16_t* __restrict__ wuv,
                                                 unsigned* __restrict__ cnt) {
  if (blockIdx.x == PF_BLOCKS && threadIdx.x < 64) cnt[threadIdx.x * 32] = 0u;
  if (blockIdx.x < PF_BLOCKS) {
    if (blockIdx.x < 24 * PF_KB) {
      unsigned keep = 0;
#pragma unroll
      for (int j = 0; j < 4; ++j) {
        const char* a = warm_addr(blockIdx.x / 24, (blockIdx.x % 24) * 1024 + j * 256 + threadIdx.x, wq, wuk, wuv);
        asm volatile("global_load_dword %0, %1, off" : "+v"(keep) : "v"(a) : "memory");
      }
      asm volatile("s_waitcnt vmcnt(0)" : "+v"(keep)::"memory");
    } else {
      const int r0 = (blockIdx.x - 24 * PF_KB) * 16 + (threadIdx.x >> 6), c8 = threadIdx.x & 63;
      u32x4 v[4];
#pragma unroll
      for (int j = 0; j < 4; ++j) v[j] = *(const u32x4*)(ckv + (size_t)(r0 + 4 * j) * DC + c8 * 8);
#pragma unroll
      for (int j = 0; j < 4; ++j) *(u32x4*)(ct + ((size_t)(c8 >> 2) * S_LEN + r0 + 4 * j) * 32 + (c8 & 3) * 8) = v[j];
    }
    return;
  }
  const int lane = threadIdx.x & 63, w = threadIdx.x >> 6;
  const int row = (blockIdx.x - PF_BLOCKS) * 4 + w;
  const unsigned* xr = reinterpret_cast<const unsigned*>(x + (size_t)row * DM);
  float v[10];
#pragma unroll
  for (int j = 0; j < 5; ++j) {
    unsigned u = xr[lane + 64 * j];
    v[2 * j] = __uint_as_float(u << 16);
    v[2 * j + 1] = __uint_as_float(u & 0xffff0000u);
  }
  ln_row(v);
  unsigned* xo = reinterpret_cast<unsigned*>(xn + ((size_t)(lane >> 4) * S_LEN + row) * 32) + (lane & 15);
#pragma unroll
  for (int j = 0; j < 5; ++j) xo[j * 4 * S_LEN * 16] = pack2(v[2 * j], v[2 * j + 1]);
  ln_row(v);
  unsigned* ho = reinterpret_cast<unsigned*>(h + (size_t)row * DM);
#pragma unroll
  for (int j = 0; j < 5; ++j) ho[lane + 64 * j] = pack2(v[2 * j], v[2 * j + 1]);
}

constexpr int STAGE_BYTES = 49152;
constexpr int RBASE = 32768;

__device__ __forceinline__ int swapmid(int p) { return ((p & 1) << 1) | ((p >> 1) & 1); }
__device__ __forceinline__ int rperm(int r) {
  int g = r >> 2;
  int pg = (g & 4) | swapmid(g & 3);
  return pg * 4 + (r & 3);
}
__device__ __forceinline__ s16x4 tr_read(const char LAS* p) {
  return __builtin_amdgcn_ds_read_tr16_b64_v4i16((LAS s16x4*)p);
}
__device__ __forceinline__ bf16x8 cat8(s16x4 a, s16x4 b) {
  return (bf16x8){a[0], a[1], a[2], a[3], b[0], b[1], b[2], b[3]};
}

__device__ __forceinline__ void glds16(const void* sbase, int voff, unsigned lds_dst) {
  unsigned keep;
  asm volatile("s_mov_b32 %0, m0\n\ts_mov_b32 m0, %3\n\ts_nop 0\n\tglobal_load_lds_dwordx4 %1, %2\n\ts_mov_b32 m0, %0"
               : "=&s"(keep) : "v"(voff), "s"(sbase), "s"(lds_dst) : "memory");
}

__device__ __forceinline__ void glds16x4(const void* sbase, int v0, int v1, int v2, int v3, unsigned lds_dst) {
  unsigned keep;
  asm volatile("s_mov_b32 %0, m0\n\ts_mov_b32 m0, %6\n\ts_nop 0\n\tglobal_load_lds_dwordx4 %1, %5\n\t"
               "global_load_lds_dwordx4 %2, %5 offset:1024\n\tglobal_load_lds_dwordx4 %3, %5 offset:2048\n\t"
               "global_load_lds_dwordx4 %4, %5 offset:3072\n\ts_mov_b32 m0, %0"
               : "=&s"(keep) : "v"(v0), "v"(v1), "v"(v2), "v"(v3), "s"(sbase), "s"(lds_dst) : "memory");
}

template <int J> __device__ __forceinline__ void glds16p(const void* sbase, int v, unsigned lds_dst) {
  asm volatile("s_mov_b32 m0, %2\n\ts_nop 0\n\tglobal_load_lds_dwordx4 %0, %1 offset:%3"
               ::"v"(v), "s"(sbase), "s"(lds_dst), "n"(J * 1024) : "memory");
}

#define ST_MODE 0
__device__ __forceinline__ void st16(void* p, u32x4 v) {
  *(u32x4*)p = v;
}
__device__ __forceinline__ void st16f(float* p, float a, float b, float c, float d) {
  st16(p, (u32x4){__float_as_uint(a), __float_as_uint(b), __float_as_uint(c), __float_as_uint(d)});
}

template <bool P_TR, bool R_TR, bool ROWS_P>
struct GemmCore {
  int pOff[4], rOff[2];
  int pAddr[4], rAddr[4];
  int w, l;
  __device__ __forceinline__ void init(int ldp, int ldr, int pcol_limit  ) {
    const int tid = threadIdx.x;
    l = tid & 63;
    w = __builtin_amdgcn_readfirstlane(tid >> 6);
    const int wp = w >> 1, wr = w & 1;
#pragma unroll
    for (int i = 0; i < 4; ++i) {
      const int j = w * 4 + i;
      if (!P_TR) {
        const int row = 8 * j + (l >> 3), slot = l & 7;
        const int chunk = slot ^ ((row >> 1) & 7);
        pOff[i] = row * ldp + chunk * 8;
      } else {
        const int half = j >> 4, jj = j & 15;
        const int krow = 4 * jj + (l >> 4), s16 = l & 15;
        const int c64 = (s16 >> 2) ^ (krow & 3);
        int col = half * 128 + c64 * 32 + (s16 & 3) * 8;
        if (col >= pcol_limit) col = pcol_limit - 8;
        pOff[i] = krow * ldp + col;
      }
    }
#pragma unroll
    for (int i = 0; i < 2; ++i) {
      const int j = w * 2 + i;
      if (!R_TR) {
        const int row = 8 * j + (l >> 3), slot = l & 7;
        const int chunk = slot ^ ((row >> 1) & 7);
        rOff[i] = row * ldr + chunk * 8;
      } else {
        const int krow = 4 * j + (l >> 4), s16 = l & 15;
        const int c64 = (s16 >> 2) ^ (krow & 3);
        const int col = c64 * 32 + (s16 & 3) * 8;
        rOff[i] = krow * ldr + col;
      }
    }
    const int r32 = l & 31, hi = l >> 5, blk = (l >> 4) & 1, q = (l & 15) >> 2, p = l & 3;
    if (!P_TR) {
      const int prow = 64 * wp + (ROWS_P ? rperm(r32) : r32);
      const int s = (prow >> 1) & 7;
#pragma unroll
      for (int kk = 0; kk < 4; ++kk) pAddr[kk] = prow * 128 + (((2 * kk + hi) ^ s) << 4);
    } else {
      const int half = wp >> 1, pp = ROWS_P ? swapmid(p) : p;
#pragma unroll
      for (int i = 0; i < 2; ++i) {
        const int c64 = 2 * (wp & 1) + i;
        pAddr[i] = half * 16384 + (8 * hi + q) * 256 + ((c64 ^ q) * 64) + blk * 32 + pp * 8;
      }
      pAddr[2] = pAddr[3] = 0;
    }
    if (!R_TR) {
      const int rrow = 64 * wr + (!ROWS_P ? rperm(r32) : r32);
      const int s = (rrow >> 1) & 7;
#pragma unroll
      for (int kk = 0; kk < 4; ++kk) rAddr[kk] = RBASE + rrow * 128 + (((2 * kk + hi) ^ s) << 4);
    } else {
      const int pp = !ROWS_P ? swapmid(p) : p;
#pragma unroll
      for (int i = 0; i < 2; ++i) {
        const int c64 = 2 * wr + i;
        rAddr[i] = RBASE + (8 * hi + q) * 256 + ((c64 ^ q) * 64) + blk * 32 + pp * 8;
      }
      rAddr[2] = rAddr[3] = 0;
    }
  }
  __device__ __forceinline__ void stage(char LAS* buf, const bf16_t* P, const bf16_t* R) {
    const unsigned b = (unsigned)(uintptr_t)buf;
#pragma unroll
    for (int i = 0; i < 4; ++i) glds16(P, pOff[i] * 2, b + (w * 4 + i) * 1024);
#pragma unroll
    for (int i = 0; i < 2; ++i) glds16(R, rOff[i] * 2, b + RBASE + (w * 2 + i) * 1024);
  }
  __device__ __forceinline__ void compute(const char LAS* buf, f32x16 (&acc)[2][2]) {
#pragma unroll
    for (int kk = 0; kk < 4; ++kk) {
      bf16x8 pf[2], rf[2];
#pragma unroll
      for (int i = 0; i < 2; ++i) {
        if (!P_TR) pf[i] = *(const LAS bf16x8*)(buf + pAddr[kk] + i * 4096);
        else pf[i] = cat8(tr_read(buf + pAddr[i] + kk * 4096), tr_read(buf + pAddr[i] + kk * 4096 + 1024));
        if (!R_TR) rf[i] = *(const LAS bf16x8*)(buf + rAddr[kk] + i * 4096);
        else rf[i] = cat8(tr_read(buf + rAddr[i] + kk * 4096), tr_read(buf + rAddr[i] + kk * 4096 + 1024));
      }
#pragma unroll
      for (int i = 0; i < 2; ++i)
#pragma unroll
        for (int j = 0; j < 2; ++j) {
          if (ROWS_P) acc[i][j] = __builtin_amdgcn_mfma_f32_32x32x16_bf16(pf[i], rf[j], acc[i][j], 0, 0, 0);
          else acc[i][j] = __builtin_amdgcn_mfma_f32_32x32x16_bf16(rf[j], pf[i], acc[i][j], 0, 0, 0);
        }
    }
  }
};

#define WAIT_VM0() asm volatile("s_waitcnt vmcnt(0)" ::: "memory")
#define WAIT_VM6() asm volatile("s_waitcnt vmcnt(6)" ::: "memory")
#define WAIT_VM16() asm volatile("s_waitcnt vmcnt(16)" ::: "memory")
#define BARRIER() __builtin_amdgcn_s_barrier()
#define TS(k, i)
#define PH(i, k)

__device__ __forceinline__ float gelu_tanh(float x) {
  const float y = 0.7978845608028654f * (x + 0.044715f * x * x * x);
  const float e = __expf(2.0f * y);
  const float t = 1.0f - 2.0f / (e + 1.0f);
  return 0.5f * x * (1.0f + t);
}

constexpr int STAGE256 = 32768;
#define NST256 4
struct Core256 {
  int pOff[2], rOff[2];
  int pAddr[2], rAddr[2];
  int w;
  __device__ __forceinline__ void init(int ldp, int ldr) {
    const int tid = threadIdx.x, l = tid & 63;
    w = __builtin_amdgcn_readfirstlane(tid >> 6);
    const int wp = w >> 2, wr = w & 3;
#pragma unroll
    for (int i = 0; i < 2; ++i) {
      const int j = w * 2 + i;
      const int row = 16 * j + (l >> 2), slot = l & 3;
      pOff[i] = (row * ldp + (slot ^ ((row >> 2) & 3)) * 8) * 2;
      const int half = j >> 3, jj = j & 7;
      const int krow = 4 * jj + (l >> 4), s16 = l & 15;
      const int c64 = (s16 >> 2) ^ (krow & 3);
      rOff[i] = (krow * ldr + half * 128 + c64 * 32 + (s16 & 3) * 8) * 2;
    }
    const int r32 = l & 31, hi = l >> 5, blk = (l >> 4) & 1, q = (l & 15) >> 2, p = l & 3;
    const int prow = 128 * wp + r32, s = (prow >> 2) & 3;
#pragma unroll
    for (int kk = 0; kk < 2; ++kk) pAddr[kk] = prow * 64 + (((2 * kk + hi) ^ s) << 4);
#pragma unroll
    for (int j = 0; j < 2; ++j) {
      const int c64 = 2 * (wr & 1) + j;
      rAddr[j] = 16384 + (wr >> 1) * 8192 + (8 * hi + q) * 256 + ((c64 ^ q) * 64) + blk * 32 + swapmid(p) * 8;
    }
  }
  __device__ __forceinline__ void stage(char LAS* buf, const bf16_t* P, const bf16_t* R) {
    const unsigned b = (unsigned)(uintptr_t)buf;
#pragma unroll
    for (int i = 0; i < 2; ++i) glds16(P, pOff[i], b + (w * 2 + i) * 1024);
#pragma unroll
    for (int i = 0; i < 2; ++i) glds16(R, rOff[i], b + 16384 + (w * 2 + i) * 1024);
  }
  __device__ __forceinline__ void compute(const char LAS* buf, f32x16 (&acc)[4][2]) {
#pragma unroll
    for (int kk = 0; kk < 2; ++kk) {
      bf16x8 pf[4], rf[2];
#pragma unroll
      for (int j = 0; j < 2; ++j) rf[j] = cat8(tr_read(buf + rAddr[j] + kk * 4096), tr_read(buf + rAddr[j] + kk * 4096 + 1024));
#pragma unroll
      for (int i = 0; i < 4; ++i) pf[i] = *(const LAS bf16x8*)(buf + pAddr[kk] + i * 2048);
#pragma unroll
      for (int i = 0; i < 4; ++i)
#pragma unroll
        for (int j = 0; j < 2; ++j) acc[i][j] = __builtin_amdgcn_mfma_f32_32x32x16_bf16(rf[j], pf[i], acc[i][j], 0, 0, 0);
    }
  }
  __device__ __forceinline__ void load_frags(const char LAS* buf, bf16x8 (&pf)[2][4], bf16x8 (&rf)[2][2]) {
#pragma unroll
    for (int kk = 0; kk < 2; ++kk) {
#pragma unroll
      for (int j = 0; j < 2; ++j) rf[kk][j] = cat8(tr_read(buf + rAddr[j] + kk * 4096), tr_read(buf + rAddr[j] + kk * 4096 + 1024));
#pragma unroll
      for (int i = 0; i < 4; ++i) pf[kk][i] = *(const LAS bf16x8*)(buf + pAddr[kk] + i * 2048);
    }
  }
  __device__ __forceinline__ void mma(const bf16x8 (&pf)[2][4], const bf16x8 (&rf)[2][2], f32x16 (&acc)[4][2]) {
#pragma unroll
    for (int kk = 0; kk < 2; ++kk)
#pragma unroll
      for (int i = 0; i < 4; ++i)
#pragma unroll
        for (int j = 0; j < 2; ++j) acc[i][j] = __builtin_amdgcn_mfma_f32_32x32x16_bf16(rf[kk][j], pf[kk][i], acc[i][j], 0, 0, 0);
  }
};
#define WAIT_LGKM0() asm volatile("s_waitcnt lgkmcnt(0)" ::: "memory")
#define SCHED0() __builtin_amdgcn_sched_barrier(0)
template <int N> __device__ __forceinline__ void wait_vm() { asm volatile("s_waitcnt vmcnt(%0)" ::"n"(N) : "memory"); }

struct Frag { bf16x8 pf[4], rf[2]; };
struct Desc { const char* p; unsigned stride; int v0, v1; };
template <int IMM> __device__ __forceinline__ void setm0(unsigned base) {
  asm volatile("s_add_u32 m0, %0, %1" ::"s"(base), "n"(IMM) : "memory", "scc");
}
template <int OFF> __device__ __forceinline__ void gl(const void* sbase, int voff) {
  asm volatile("global_load_lds_dwordx4 %0, %1 offset:%2" ::"v"(voff), "s"(sbase), "n"(OFF) : "memory");
}
template <int V> using IC = std::integral_constant<int, V>;
__global__ __launch_bounds__(512, 2) void gemm1_kernel(const bf16_t* __restrict__ xn, const bf16_t* __restrict__ hbuf,
                                                       const bf16_t* __restrict__ ckv, const bf16_t* __restrict__ wq,
                                                       const bf16_t* __restrict__ wuk, const bf16_t* __restrict__ wuv,
                                                       const bf16_t* __restrict__ w1, const bf16_t* __restrict__ b1,
                                                       const bf16_t* __restrict__ wo, const bf16_t* __restrict__ w2,
                                                       bf16_t* __restrict__ Qo, bf16_t* __restrict__ Ko,
                                                       bf16_t* __restrict__ Vo, bf16_t* __restrict__ Uo) {
  __shared__ __attribute__((aligned(16))) char smem[4 * STAGE256 + 32768];
  char LAS* lds = (char LAS*)smem;
  const int xcd = blockIdx.x & 7, c = blockIdx.x >> 3;
  const int l = threadIdx.x & 63, w = __builtin_amdgcn_readfirstlane(threadIdx.x >> 6);
  const unsigned sW = (unsigned)(uintptr_t)lds + w * 2048;
  const int r32 = l & 31, hi = l >> 5;
  TS(0, 0);
  const bool isQ = c >= 16;
  const int mbase = (xcd * 90) >> 3, mcnt = (((xcd + 1) * 90) >> 3) - mbase;
  const bool hasMix = isQ && c - 16 < mcnt;
  const int spare = xcd * 16 - mbase + (c - 16 - mcnt);
  if (isQ && !hasMix) {
    unsigned keep = 0;
#pragma unroll
    for (int j = 0; j < 5; ++j) {
      const int idx = (j * 38 + spare) * 512 + (int)threadIdx.x + PF_KB * 24576;
      const int kb = idx / 24576, i = idx % 24576;
      const char* a = warm_addr(kb < 4 ? kb : 4, kb < 4 ? i : (i & 8191), wq, wuk, wuv);
      asm volatile("global_load_dword %0, %1, off" : "+v"(keep) : "v"(a) : "memory");
    }
#pragma unroll
    for (int j = 0; j < 3; ++j) {
      const int idx = (j * 38 + spare) * 512 + (int)threadIdx.x;
      const char* a = idx < 55296 ? (const char*)w1 + (size_t)idx * 128 : (const char*)b1 + (idx % 27) * 128;
      asm volatile("global_load_dword %0, %1, off" : "+v"(keep) : "v"(a) : "memory");
    }
    asm volatile("s_waitcnt vmcnt(0)" : "+v"(keep)::"memory");
  }
  const int mtile = mbase + (c - 16);
  const int d0 = (mtile % 5) * 128, f0 = (mtile / 5) * 96;
  Desc MP, MR;
  {
    int po[2], ro[2];
#pragma unroll
    for (int i = 0; i < 2; ++i) {
      const int j = w * 2 + i;
      const int krow = 4 * j + (l >> 4), s16 = l & 15;
      po[i] = (krow * DM + ((s16 >> 2) ^ (krow & 3)) * 32 + (s16 & 3) * 8) * 2 - 1024 * i;
      const int cb = j >> 2, kr = (j & 3) * 16 + (l >> 2);
      ro[i] = j < 12 ? (kr * FF + cb * 32 + (l & 3) * 8) * 2 - 1024 * i : 0;
    }
    MP = Desc{(const char*)(hbuf + d0), 64u * DM * 2, po[0], po[1]};
    MR = Desc{(const char*)(w1 + f0), 64u * FF * 2, ro[0], ro[1]};
  }
  {
    int ntile, tb0, hp;
    const bf16_t* A; const bf16_t* W; bf16_t* O; int K;
    if (!isQ) {
      ntile = 2; tb0 = c >> 2; hp = xcd * 2 + (c & 1);
      A = ckv; K = DC;
      W = (c & 2) ? wuv : wuk; O = (c & 2) ? Vo : Ko;
    } else {
      ntile = 1; tb0 = (c - 16) >> 1; hp = xcd * 2 + (c & 1);
      A = xn; K = DM; W = wq; O = Qo;
    }
    const int ngt = K / 128;
    Core256 g;
    g.init(32, HDN);
    constexpr unsigned PSTR = S_LEN * 64u;
    const char* Ap = (const char*)(A + (size_t)tb0 * 256 * 32);
    const char* Wp = (const char*)(W + hp * 256);
    const int wp = w >> 2, wr = w & 3;
    constexpr unsigned RSTR = 32u * HDN * 2;
    f32x16 acc[4][2];
    Frag F0, F1;
    auto half = [&](auto first, auto dst, const Frag& cur, Frag& nxt, const char LAS* nb, int nkk, const char* sp, int v0, int v1) {
      auto mm = [&](int i, int j) {
        acc[i][j] = mf<X16_G1>(cur.rf[j], cur.pf[i], first ? (f32x16){} : acc[i][j], nkk ^ 1);
        SCHED0();
      };
      auto rdR = [&](int j) {
        nxt.rf[j] = cat8(tr_read(nb + g.rAddr[j] + nkk * 4096), tr_read(nb + g.rAddr[j] + nkk * 4096 + 1024));
        SCHED0();
      };
      auto rdP = [&](int i) {
        nxt.pf[i] = *(const LAS bf16x8*)(nb + g.pAddr[nkk] + i * 2048);
        SCHED0();
      };
      mm(0, 0); rdR(0);
      mm(0, 1); rdP(0); rdR(1);
      mm(1, 0); rdP(1);
      mm(1, 1); rdP(2);
      mm(2, 0); rdP(3); if (dst >= 0) setm0<(dst >= 0 ? (int)dst : 0)>(sW); SCHED0();
      mm(2, 1); if (dst >= 0) gl<0>(sp, v0); SCHED0();
      mm(3, 0); if (dst >= 0) gl<1024>(sp, v1); SCHED0();
      mm(3, 1);
    };
    auto group = [&](auto VAR, bool second, const Desc& Rc, const Desc& Pn, const Desc& Rn) {
      auto stp = [&](auto J) {
        constexpr int j = J, var = VAR;
        constexpr int dA = (var == 2 && j > 0) ? -1 : ((j + 3) & 3) * STAGE256 + 16384;
        constexpr int dB = var == 2 ? -1 : j * STAGE256;
        const char LAS* cb = lds + j * STAGE256;
        const char LAS* nb = lds + ((j + 1) & 3) * STAGE256;
        if (j == 0) half(std::integral_constant<bool, var == 0>{}, IC<dA>{}, F0, F1, cb, 1, Rc.p, Rc.v0, Rc.v1);
        else half(std::false_type{}, IC<dA>{}, F0, F1, cb, 1, Rn.p + (j - 1) * Rn.stride, Rn.v0, Rn.v1);
        if (var == 2) { if (j == 0) wait_vm<8>(); else if (j == 1) wait_vm<4>(); else if (j == 2) wait_vm<0>(); }
        else if (var == 0 && j < 2) { if (second) wait_vm<24>(); else wait_vm<8>(); }
        else wait_vm<8>();
        WAIT_LGKM0();
        SCHED0();
        BARRIER();
        SCHED0();
        half(std::false_type{}, IC<dB>{}, F1, F0, nb, 0, Pn.p + j * Pn.stride, Pn.v0, Pn.v1);
      };
      stp(IC<0>{}); stp(IC<1>{}); stp(IC<2>{}); stp(IC<3>{});
    };
    const int pv0 = g.pOff[0], pv1 = g.pOff[1] - 1024, rv0 = g.rOff[0], rv1 = g.rOff[1] - 1024;
    setm0<0>(sW); SCHED0(); asm volatile("s_nop 0"); gl<0>(Ap, pv0); gl<1024>(Ap, pv1);
    setm0<16384>(sW); SCHED0(); asm volatile("s_nop 0"); gl<0>(Wp, rv0); gl<1024>(Wp, rv1);
    setm0<STAGE256>(sW); SCHED0(); asm volatile("s_nop 0"); gl<0>(Ap + PSTR, pv0); gl<1024>(Ap + PSTR, pv1);
    setm0<STAGE256 + 16384>(sW); SCHED0(); asm volatile("s_nop 0"); gl<0>(Wp + RSTR, rv0); gl<1024>(Wp + RSTR, rv1);
    setm0<2 * STAGE256>(sW); SCHED0(); asm volatile("s_nop 0"); gl<0>(Ap + 2 * PSTR, pv0); gl<1024>(Ap + 2 * PSTR, pv1);
    setm0<2 * STAGE256 + 16384>(sW); SCHED0(); asm volatile("s_nop 0"); gl<0>(Wp + 2 * RSTR, rv0); gl<1024>(Wp + 2 * RSTR, rv1);
    setm0<3 * STAGE256>(sW); SCHED0(); asm volatile("s_nop 0"); gl<0>(Ap + 3 * PSTR, pv0); gl<1024>(Ap + 3 * PSTR, pv1);
    wait_vm<10>();
    BARRIER();
#pragma unroll
    for (int j = 0; j < 2; ++j) F0.rf[j] = cat8(tr_read(lds + g.rAddr[j]), tr_read(lds + g.rAddr[j] + 1024));
#pragma unroll
    for (int i = 0; i < 4; ++i) F0.pf[i] = *(const LAS bf16x8*)(lds + g.pAddr[0] + i * 2048);
    SCHED0();
    for (int t = 0; t < ntile; ++t) {
      const int tb = tb0 + 4 * t;
      auto run = [&](auto VAR, int gi) {
        const Desc Rc{Wp + (size_t)(4 * gi + 3) * RSTR, RSTR, rv0, rv1};
        const bool tl = gi + 1 == ngt;
        const bool sl = tl && t + 1 == ntile;
        const int gn = tl ? 0 : gi + 1;
        Desc Pn{Ap + (size_t)(tl && !sl ? t + 1 : t) * 1024 * 64 + (size_t)gn * 4 * PSTR, PSTR, pv0, pv1};
        Desc Rn{Wp + (size_t)(4 * gn) * RSTR, RSTR, rv0, rv1};
        if (sl && hasMix) { Pn = MP; Rn = MR; }
        SCHED0();
        group(VAR, t > 0, Rc, Pn, Rn);
      };
      run(IC<0>{}, 0);
      for (int gi = 1; gi < ngt; ++gi) run(IC<1>{}, gi);
      if (hasMix) {
        setm0<3 * STAGE256 + 16384>(sW); SCHED0(); asm volatile("s_nop 0");
        gl<0>(MR.p + 3 * MR.stride, MR.v0); gl<1024>(MR.p + 3 * MR.stride, MR.v1);
      }
      const int head = hp * 2 + (wr >> 1);
      char LAS* slab = lds + 4 * STAGE256 + w * 4096;
      const int wa = r32 * 128, wsw = r32 & 7;
      const int rrow = l >> 3, ra = rrow * 128 + (((l & 7) ^ rrow) << 4);
      bf16_t* obase = O + ((size_t)head * S_LEN + tb * 256 + 128 * wp + rrow) * HD + 64 * (wr & 1) + (l & 7) * 8;
#pragma unroll
      for (int i = 0; i < 4; ++i) {
#pragma unroll
        for (int j = 0; j < 2; ++j) {
          const f32x16 a = acc[i][j];
          u32x4 v0 = {pack2(a[0], a[1]), pack2(a[2], a[3]), pack2(a[4], a[5]), pack2(a[6], a[7])};
          u32x4 v1 = {pack2(a[8], a[9]), pack2(a[10], a[11]), pack2(a[12], a[13]), pack2(a[14], a[15])};
          *(u32x4 LAS*)(slab + wa + (((4 * j + hi) ^ wsw) << 4)) = v0;
          *(u32x4 LAS*)(slab + wa + (((4 * j + 2 + hi) ^ wsw) << 4)) = v1;
        }
#pragma unroll
        for (int it = 0; it < 4; ++it) {
          const u32x4 v = *(const u32x4 LAS*)(slab + ra + it * 1024);
          st16(obase + (size_t)(32 * i + 8 * it) * HD, v);
        }
      }
      SCHED0();
    }
  }
  TS(0, 1);
  if (!hasMix) wait_vm<(ST_MODE == 9 ? 0 : 16)>();
  if (hasMix) {
    const int ws = w & 3, kh = w >> 2;
    const int blk = (l >> 4) & 1, q = (l & 15) >> 2, p = l & 3;
    const int pB = (32 * kh + 8 * hi + q) * 256 + ((ws ^ q) * 64) + blk * 32 + p * 8;
    const int rA = 16384 + (32 * kh + 8 * hi + q) * 64 + blk * 32 + swapmid(p) * 8;
    f32x16 acc[3] = {};
    bf16x8 pa[2], ra[2][3], pb[2], rb[2][3];
    auto mstep = [&](auto VAR, auto J, const bf16x8 (&pc)[2], const bf16x8 (&rc)[2][3], bf16x8 (&pn)[2], bf16x8 (&rn)[2][3],
                     const char* sp, const char* sr) {
      constexpr int j = J, var = VAR;
      if (var == 0) { if (j < 3) wait_vm<27>(); else wait_vm<8>(); }
      else if (var == 1) wait_vm<8>();
      else { if (j == 0) wait_vm<8>(); else if (j == 1) wait_vm<4>(); else if (j == 2) wait_vm<0>(); }
      WAIT_LGKM0();
      SCHED0();
      BARRIER();
      SCHED0();
      const char LAS* nb = lds + ((j + 1) & 3) * STAGE256;
      auto mm = [&](int k2, int jj) {
        acc[jj] = mf<X16_G1>(rc[k2][jj], pc[k2], acc[jj], k2);
        SCHED0();
      };
      auto rdP = [&](int k2) { pn[k2] = cat8(tr_read(nb + pB + k2 * 4096), tr_read(nb + pB + k2 * 4096 + 1024)); SCHED0(); };
      auto rdR = [&](int k2, int jj) {
        rn[k2][jj] = cat8(tr_read(nb + rA + jj * 4096 + k2 * 1024), tr_read(nb + rA + jj * 4096 + k2 * 1024 + 256));
        SCHED0();
      };
      mm(0, 0); rdP(0); rdR(0, 0); rdR(0, 1);
      mm(0, 1); rdR(0, 2); rdP(1); rdR(1, 0);
      mm(0, 2); rdR(1, 1); rdR(1, 2); if (var != 2) setm0<j * STAGE256>(sW); SCHED0();
      mm(1, 0); if (var != 2) { gl<0>(sp, MP.v0); gl<1024>(sp, MP.v1); setm0<j * STAGE256 + 16384>(sW); } SCHED0();
      mm(1, 1); if (var != 2) gl<0>(sr, MR.v0); SCHED0();
      mm(1, 2); if (var != 2) gl<1024>(sr, MR.v1); SCHED0();
    };
    auto mgroup = [&](auto VAR, int m) {
      const char* sp = MP.p + (size_t)(m + 4) * MP.stride;
      const char* sr = MR.p + (size_t)(m + 4) * MR.stride;
      mstep(VAR, IC<0>{}, pa, ra, pb, rb, sp, sr);
      mstep(VAR, IC<1>{}, pb, rb, pa, ra, sp + MP.stride, sr + MR.stride);
      mstep(VAR, IC<2>{}, pa, ra, pb, rb, sp + 2 * MP.stride, sr + 2 * MR.stride);
      mstep(VAR, IC<3>{}, pb, rb, pa, ra, sp + 3 * MP.stride, sr + 3 * MR.stride);
    };
    wait_vm<28>();
    BARRIER();
    const int jk = kh == 0 ? 0 : 2;
    u32x4 bias[3];
    bias[0] = *(const u32x4*)(b1 + f0 + 8 * hi + 32 * jk);
    bias[1] = *(const u32x4*)(b1 + f0 + 8 * hi + 32 * jk + 16);
    bias[2] = *(const u32x4*)(b1 + f0 + 8 * hi + 32 + 16 * kh);
    SCHED0();
#pragma unroll
    for (int k2 = 0; k2 < 2; ++k2) {
      pa[k2] = cat8(tr_read(lds + pB + k2 * 4096), tr_read(lds + pB + k2 * 4096 + 1024));
#pragma unroll
      for (int j = 0; j < 3; ++j) ra[k2][j] = cat8(tr_read(lds + rA + j * 4096 + k2 * 1024), tr_read(lds + rA + j * 4096 + k2 * 1024 + 256));
    }
    SCHED0();
    mgroup(IC<0>{}, 0);
    for (int m = 4; m < 28; m += 4) mgroup(IC<1>{}, m);
    mgroup(IC<2>{}, 28);
    WAIT_LGKM0();
    TS(0, 3);
    BARRIER();
    float LAS* dst = (float LAS*)(lds + w * 8192);
    const f32x16 sa = kh == 0 ? acc[2] : acc[0], ka = kh == 0 ? acc[0] : acc[2];
    const int so = kh == 0 ? 8 : 0;
#pragma unroll
    for (int g = 0; g < 4; ++g) *(f32x4 LAS*)(dst + (g * 64 + l) * 4) = (f32x4){sa[4 * g], sa[4 * g + 1], sa[4 * g + 2], sa[4 * g + 3]};
    float s1[8], k1[8];
#pragma unroll
    for (int e = 0; e < 8; ++e) { s1[e] = kh == 0 ? acc[1][8 + e] : acc[1][e]; k1[e] = kh == 0 ? acc[1][e] : acc[1][8 + e]; }
#pragma unroll
    for (int g = 0; g < 2; ++g) *(f32x4 LAS*)(dst + ((4 + g) * 64 + l) * 4) = (f32x4){s1[4 * g], s1[4 * g + 1], s1[4 * g + 2], s1[4 * g + 3]};
    (void)so;
    WAIT_LGKM0();
    BARRIER();
    const float LAS* src = (const float LAS*)(lds + (w ^ 4) * 8192);
    bf16_t* urow = Uo + (size_t)(d0 + 32 * ws + r32) * FF + f0 + 8 * hi;
    auto fin8 = [&](const float (&v)[8], int fo, const u32x4 bv) {
      float r[8];
#pragma unroll
      for (int e = 0; e < 4; ++e) {
        r[2 * e] = gelu_tanh(rbf(rbf(v[2 * e]) + __uint_as_float(bv[e] << 16)));
        r[2 * e + 1] = gelu_tanh(rbf(rbf(v[2 * e + 1]) + __uint_as_float(bv[e] & 0xffff0000u)));
      }
      st16(urow + fo, (u32x4){pack2(r[0], r[1]), pack2(r[2], r[3]), pack2(r[4], r[5]), pack2(r[6], r[7])});
    };
#pragma unroll
    for (int hh = 0; hh < 2; ++hh) {
      float v[8];
#pragma unroll
      for (int g = 0; g < 2; ++g) {
        const f32x4 o = *(const f32x4 LAS*)(src + ((2 * hh + g) * 64 + l) * 4);
#pragma unroll
        for (int e = 0; e < 4; ++e) v[4 * g + e] = ka[8 * hh + 4 * g + e] + o[e];
      }
      fin8(v, 32 * jk + 16 * hh, bias[hh]);
    }
    {
      float v[8];
#pragma unroll
      for (int g = 0; g < 2; ++g) {
        const f32x4 o = *(const f32x4 LAS*)(src + ((4 + g) * 64 + l) * 4);
#pragma unroll
        for (int e = 0; e < 4; ++e) v[4 * g + e] = k1[4 * g + e] + o[e];
      }
      fin8(v, 32 + 16 * kh, bias[2]);
    }
  }
  TS(0, 2);
}

constexpr int AT_HALF = 16384;
__global__ __launch_bounds__(512, 2) void attn_kernel(const bf16_t* __restrict__ Q, const bf16_t* __restrict__ K,
                                                      const bf16_t* __restrict__ V, bf16_t* __restrict__ ctx,
                                                      const bf16_t* __restrict__ wo, const bf16_t* __restrict__ w2) {
  __shared__ __attribute__((aligned(16))) char smem[8 * AT_HALF + 32768];
  char LAS* lds = (char LAS*)smem;
  const int tid = threadIdx.x, l = tid & 63, w = __builtin_amdgcn_readfirstlane(tid >> 6);
  const int rg = w & 3, g = w >> 2, r32 = l & 31, hi = l >> 5;
  const int xcd = blockIdx.x & 7, jj = blockIdx.x >> 3;
  const int head = xcd * 4 + (jj >> 3), pair = jj & 7;
  const int LB = 15 - pair, nshort = 2 * pair + 2;
  const bf16_t* Kh = K + (size_t)head * S_LEN * HD;
  const bf16_t* Vh = V + (size_t)head * S_LEN * HD;
  const bf16_t* Qh = Q + (size_t)head * S_LEN * HD;
  char LAS* gl = lds + g * 4 * AT_HALF;
  int kOff[4], vOff[4];
#pragma unroll
  for (int i = 0; i < 4; ++i) {
    const int j = rg * 4 + i, row = 4 * j + (l >> 4), s16 = l & 15;
    kOff[i] = (row * HD + ((s16 ^ (row & 15)) * 8)) * 2 - 1024 * i;
    vOff[i] = (row * HD + ((((s16 >> 2) ^ (row & 3)) * 4 + (s16 & 3)) * 8)) * 2 - 1024 * i;
  }
  const unsigned glb = (unsigned)(uintptr_t)gl;
  auto htile = [&](int i) { return (g == 0 || i < nshort) ? i : i + 15 - 2 * pair; };
  auto stageK = [&](int i) {
    const bf16_t* kp = Kh + (size_t)htile(i > 16 ? 16 : i) * 64 * HD;
    const unsigned bb = glb + (i & 1) * AT_HALF + rg * 4096;
    glds16x4(kp, kOff[0], kOff[1], kOff[2], kOff[3], bb);
  };
  auto stageV = [&](int i) {
    const bf16_t* vp = Vh + (size_t)htile(i) * 64 * HD;
    const unsigned bb = glb + (2 + (i & 1)) * AT_HALF + rg * 4096;
    glds16x4(vp, vOff[0], vOff[1], vOff[2], vOff[3], bb);
  };
  auto stageKp = [&](int i, auto J) {
    const bf16_t* kp = Kh + (size_t)htile(i > 16 ? 16 : i) * 64 * HD;
    glds16p<J>(kp, kOff[J], glb + (i & 1) * AT_HALF + rg * 4096);
  };
  auto stageVp = [&](int i, auto J) {
    const bf16_t* vp = Vh + (size_t)htile(i) * 64 * HD;
    glds16p<J>(vp, vOff[J], glb + (2 + (i & 1)) * AT_HALF + rg * 4096);
  };
  const int ksw = r32 & 15;
  const int blk = (l >> 4) & 1, q4 = (l & 15) >> 2, p4 = l & 3;
  int vAddr[4];
#pragma unroll
  for (int db = 0; db < 4; ++db)
    vAddr[db] = 2 * AT_HALF + (4 * hi + q4) * 256 + ((db ^ q4) * 64) + blk * 32 + swapmid(p4) * 8;
  constexpr float CS = 0.08838834764831845f * 1.4426950408889634f;

  TS(1, 0);
  stageK(0);
  stageV(0);
  stageK(1);
  int qb = g == 0 ? LB : pair;
  bf16x8 qf[8];
#pragma unroll
  for (int d0 = 0; d0 < 8; ++d0) qf[d0] = *(const bf16x8*)(Qh + (size_t)(qb * 128 + 32 * rg + r32) * HD + d0 * 16 + hi * 8);
  char LAS* qsave = lds + 8 * AT_HALF + rg * 8192 + l * 16;
  if (g == 0) {
#pragma unroll
    for (int d0 = 0; d0 < 8; ++d0) *(bf16x8 LAS*)(qsave + d0 * 1024) = qf[d0];
  }
  float m_run = -1e30f, l_run = 0.f, alpha = 1.f;
  f32x16 o[4] = {};
  f32x16 s0, s1;
  bf16x8 pf[4];
  u32x2 pfd = {0u, 0u};
  constexpr int AT_VM = 5;
#define AT_W 6
  bf16x8 fr[32];
  const int kAddr0 = r32 * 256 + ((hi ^ ksw) << 4);
  auto rdK = [&](int k, int slot) {
    fr[k] = *(const LAS bf16x8*)(gl + slot * AT_HALF + (kAddr0 ^ ((k >> 1) << 5)) + (k & 1) * 8192);
  };
  auto rdV = [&](int k, int slot) {
    const char LAS* a = gl + slot * AT_HALF + vAddr[(k - 16) >> 2] + (k & 3) * 4096;
    fr[k] = cat8(tr_read(a), tr_read(a + 2048));
  };
  auto mm = [&](int k) {
    if (k < 16) {
      if (k & 1) s1 = mf<X16_AT>(fr[k], qf[k >> 1], k < 2 ? (f32x16){} : s1, (k >> 1) & 1);
      else s0 = mf<X16_AT>(fr[k], qf[k >> 1], k < 2 ? (f32x16){} : s0, (k >> 1) & 1);
    } else {
      o[(k - 16) >> 2] = mf<X16_AT>(fr[k], pf[k & 3], o[(k - 16) >> 2], k & 1);
    }
  };
  auto preK = [&](int slot) {
#pragma unroll
    for (int k = 0; k < AT_W; ++k) rdK(k, slot);
  };
  auto preV = [&](int slot) {
#pragma unroll
    for (int k = 16; k < 16 + AT_W; ++k) rdV(k, slot);
  };
  auto xhalf_max = [&](float v) {
    auto rr = __builtin_amdgcn_permlane32_swap(__float_as_uint(v), __float_as_uint(v), false, false);
    return fmaxf(__uint_as_float(rr[0]), __uint_as_float(rr[1]));
  };
  auto xhalf_sum = [&](float v) {
    auto rr = __builtin_amdgcn_permlane32_swap(__float_as_uint(v), __float_as_uint(v), false, false);
    return __uint_as_float(rr[0]) + __uint_as_float(rr[1]);
  };
#define AT_PREP 1
  const char* nx_k = nullptr; const char* nx_t = nullptr; unsigned nx_kd = 0; int nx_dh = 0, nx_sw = 0;
#define PREP(i_)                                                                                                        \
  do {                                                                                                                  \
    const int ii = (i_);                                                                                                \
    const char* a_k = (const char*)(Kh + (size_t)htile(ii + 2 > 16 ? 16 : ii + 2) * 64 * HD);                           \
    const unsigned a_kd = glb + (ii & 1) * AT_HALF + rg * 4096;                                                         \
    int idx = (ii * 256 + (int)blockIdx.x) * 8 + w;                                                                     \
    if (idx >= 24064) idx -= 24064;                                                                                     \
    const char* a_t = idx < 10240 ? (const char*)wo + (size_t)idx * 512 : (const char*)w2 + (size_t)(idx - 10240) * 512; \
    const int a_dh = htile(ii) - 2 * ((g == 0 || ii >= nshort) ? LB : pair);                                            \
    const int a_sw = g == 1 ? ii - nshort : 1;                    \
    asm volatile("" ::"s"(a_k), "s"(a_t), "s"(a_kd), "s"(a_dh), "s"(a_sw));                                             \
    nx_k = a_k; nx_t = a_t; nx_kd = a_kd; nx_dh = a_dh; nx_sw = a_sw;                                                   \
  } while (0)
  auto softmax = [&](int i) {
    if (AT_PREP ? nx_sw == 0 : (g == 1 && i == nshort)) {
      const float inv = 1.0f / xhalf_sum(l_run);
      bf16_t* orow = ctx + (size_t)(pair * 128 + 32 * rg + r32) * CTX_LD + head * HD + 8 * hi;
#pragma unroll
      for (int db = 0; db < 4; ++db) {
        const f32x16 a = o[db];
        u32x4 v0 = {pack2(a[0] * inv, a[1] * inv), pack2(a[2] * inv, a[3] * inv), pack2(a[4] * inv, a[5] * inv), pack2(a[6] * inv, a[7] * inv)};
        u32x4 v1 = {pack2(a[8] * inv, a[9] * inv), pack2(a[10] * inv, a[11] * inv), pack2(a[12] * inv, a[13] * inv), pack2(a[14] * inv, a[15] * inv)};
        st16(orow + 32 * db, v0);
        st16(orow + 32 * db + 16, v1);
        o[db] = (f32x16){};
      }
      m_run = -1e30f;
      l_run = 0.f;
      qb = LB;
    }
    if (AT_PREP ? nx_sw == -1 : (g == 1 && i == nshort - 1)) {
#pragma unroll
      for (int d0 = 0; d0 < 8; ++d0) qf[d0] = *(const bf16x8 LAS*)(qsave + d0 * 1024);
    }
    SCHED0();
#define AT_SPREAD 0
#define AT_PK 0
    auto touch = [&]() {
      if (i < 16) {
        int idx = (i * 256 + (int)blockIdx.x) * 8 + w;
        if (idx >= 24064) idx -= 24064;
        const char* pb = idx < 10240 ? (const char*)wo + (size_t)idx * 512 : (const char*)w2 + (size_t)(idx - 10240) * 512;
        if (AT_PREP) pb = nx_t;
        asm volatile("global_load_dwordx2 %0, %1, %2" : "+v"(pfd) : "v"(l * 8), "s"(pb) : "memory");
      }
    };
    if (AT_SPREAD) {
      if (i < 16) stageKp(i + 2, IC<0>{});
    } else {
      if (AT_PREP) { if (i < 16) glds16x4(nx_k, kOff[0], kOff[1], kOff[2], kOff[3], nx_kd); }
      else if (i < 16) stageK(i + 2);
      touch();
    }
    SCHED0(); PH(i, 5); SCHED0();
    const int dh = AT_PREP ? nx_dh : htile(i) - 2 * qb;
    if (dh >= 0) {
      const int qi = 32 * rg + r32 - 64 * dh;
#pragma unroll
      for (int r = 0; r < 16; ++r) {
        const int key = (r & 3) + 8 * (r >> 2) + 4 * hi;
        if (key > qi) s0[r] = -__builtin_inff();
        if (key + 32 > qi) s1[r] = -__builtin_inff();
      }
    }
    float pm = s0[0];
#pragma unroll
    for (int r = 1; r < 16; ++r) pm = fmaxf(pm, s0[r]);
#pragma unroll
    for (int r = 0; r < 16; ++r) pm = fmaxf(pm, s1[r]);
    pm = xhalf_max(pm);
    const float mn = fmaxf(m_run, pm);
    alpha = __builtin_amdgcn_exp2f((m_run - mn) * CS);
    m_run = mn;
    const float mc = -mn * CS;
    SCHED0(); PH(i, 6); SCHED0();
    float ps = 0.f;
    f32x2 ps2 = {0.f, 0.f};
    const f32x2 cs2 = {CS, CS}, mc2 = {mc, mc};
    auto exps = [&](f32x16& s, int r0) {
      if (AT_PK) {
#pragma unroll
        for (int r = r0; r < r0 + 8; r += 2) {
          f32x2 t = {s[r], s[r + 1]};
          t = __builtin_elementwise_fma(t, cs2, mc2);
          t[0] = __builtin_amdgcn_exp2f(t[0]);
          t[1] = __builtin_amdgcn_exp2f(t[1]);
          ps2 += t;
          s[r] = t[0];
          s[r + 1] = t[1];
        }
      } else {
#pragma unroll
        for (int r = r0; r < r0 + 8; ++r) { s[r] = __builtin_amdgcn_exp2f(fmaf(s[r], CS, mc)); ps += s[r]; }
      }
    };
    if (AT_SPREAD) { if (i < 16) stageKp(i + 2, IC<1>{}); SCHED0(); }
    exps(s0, 0);
    if (AT_SPREAD) { SCHED0(); if (i < 16) stageKp(i + 2, IC<2>{}); SCHED0(); }
    exps(s0, 8);
    if (AT_SPREAD) { SCHED0(); if (i < 16) stageKp(i + 2, IC<3>{}); SCHED0(); }
    exps(s1, 0);
    if (AT_SPREAD) { SCHED0(); touch(); SCHED0(); }
    exps(s1, 8);
    if (AT_PK) ps = ps2[0] + ps2[1];
    l_run = l_run * alpha + ps;
    SCHED0(); PH(i, 7); SCHED0();
    u32x4 a0 = {pack2(s0[0], s0[1]), pack2(s0[2], s0[3]), pack2(s0[4], s0[5]), pack2(s0[6], s0[7])};
    u32x4 a1 = {pack2(s0[8], s0[9]), pack2(s0[10], s0[11]), pack2(s0[12], s0[13]), pack2(s0[14], s0[15])};
    u32x4 a2 = {pack2(s1[0], s1[1]), pack2(s1[2], s1[3]), pack2(s1[4], s1[5]), pack2(s1[6], s1[7])};
    u32x4 a3 = {pack2(s1[8], s1[9]), pack2(s1[10], s1[11]), pack2(s1[12], s1[13]), pack2(s1[14], s1[15])};
    pf[0] = *(bf16x8*)&a0; pf[1] = *(bf16x8*)&a1; pf[2] = *(bf16x8*)&a2; pf[3] = *(bf16x8*)&a3;
    asm volatile("" : "+v"(pf[0]), "+v"(pf[1]), "+v"(pf[2]), "+v"(pf[3]), "+v"(l_run), "+v"(alpha));
  };
#define AT_DK0 16
#define AT_DKS 3
  WAIT_VM0();
  WAIT_LGKM0();
  BARRIER();
  TS(1, 3);
  if (g == 1) BARRIER();
  SCHED0();
  preK(0);
#pragma unroll
  for (int k = 0; k < 16; ++k) {
    if (k + AT_W < 16) rdK(k + AT_W, 0);
    mm(k);
    SCHED0();
  }
  if (AT_PREP) PREP(0);
  SCHED0();
  BARRIER();
  for (int i = 0; i < 16; ++i) {
    SCHED0();
    PH(i, 0);
    softmax(i);
    SCHED0();
    const int ks = (i + 1) & 1, vs = i & 1;
    const f32x2 alpha2 = {alpha, alpha};
    preK(ks);
    SCHED0();
    wait_vm<AT_VM>();
    PH(i, 1);
    BARRIER();
    SCHED0();
    PH(i, 2);
    __builtin_amdgcn_s_setprio(1);
#pragma unroll
    for (int k = 0; k < 32; ++k) {
      if (k + AT_W < 16) rdK(k + AT_W, ks);
      else if (k + AT_W < 32) rdV(k + AT_W, vs);
      mm(k);
      if (k < 16) {
        if (AT_PK) {
#pragma unroll
          for (int e = 0; e < 4; e += 2) {
            f32x2 t = {o[k >> 2][4 * (k & 3) + e], o[k >> 2][4 * (k & 3) + e + 1]};
            asm volatile("v_pk_mul_f32 %0, %1, %0" : "+v"(t) : "v"(alpha2));
            o[k >> 2][4 * (k & 3) + e] = t[0];
            o[k >> 2][4 * (k & 3) + e + 1] = t[1];
          }
        } else {
#pragma unroll
          for (int e = 0; e < 4; ++e) {
            float t = o[k >> 2][4 * (k & 3) + e];
            asm volatile("v_mul_f32 %0, %1, %0" : "+v"(t) : "v"(alpha));
            o[k >> 2][4 * (k & 3) + e] = t;
          }
        }
      }
      if (AT_SPREAD) {
        if (k == AT_DK0) stageVp(i + 1, IC<0>{});
        if (k == AT_DK0 + AT_DKS) stageVp(i + 1, IC<1>{});
        if (k == AT_DK0 + 2 * AT_DKS) stageVp(i + 1, IC<2>{});
        if (k == AT_DK0 + 3 * AT_DKS) stageVp(i + 1, IC<3>{});
      } else {
        if (k == AT_DK0) stageV(i + 1);
      }
      SCHED0();
    }
    __builtin_amdgcn_s_setprio(0);
    if (AT_PREP) PREP(i + 1);
    SCHED0();
    PH(i, 3);
    wait_vm<AT_VM>();
    PH(i, 4);
    BARRIER();
  }
  SCHED0();
  softmax(16);
#pragma unroll
  for (int db = 0; db < 4; ++db)
#pragma unroll
    for (int r = 0; r < 16; ++r) o[db][r] *= alpha;
  SCHED0();
  WAIT_VM0();
  asm volatile("" ::"v"(pfd));
  BARRIER();
  SCHED0();
  preV(0);
#pragma unroll
  for (int k = 16; k < 32; ++k) {
    if (k + AT_W < 32) rdV(k + AT_W, 0);
    mm(k);
    SCHED0();
  }
  BARRIER();
  if (g == 0) BARRIER();
  l_run = xhalf_sum(l_run);
  TS(1, 1);
  {
    const int kh = g;
    float LAS* ml = (float LAS*)(lds + 4 * AT_HALF);
    float LAS* dst = (float LAS*)(lds + w * 8192);
#pragma unroll
    for (int dd = 0; dd < 2; ++dd) {
      f32x16 a;
#pragma unroll
      for (int r = 0; r < 16; ++r) a[r] = kh == 0 ? o[2 + dd][r] : o[dd][r];
#pragma unroll
      for (int gg = 0; gg < 4; ++gg)
        *(f32x4 LAS*)(dst + ((dd * 4 + gg) * 64 + l) * 4) = (f32x4){a[4 * gg], a[4 * gg + 1], a[4 * gg + 2], a[4 * gg + 3]};
    }
    ml[w * 128 + l] = m_run;
    ml[w * 128 + 64 + l] = l_run;
    asm volatile("s_waitcnt lgkmcnt(0)" ::: "memory");
    BARRIER();
    const int wo = w ^ 4;
    const float m2 = ml[wo * 128 + l], l2 = ml[wo * 128 + 64 + l];
    const float mt = fmaxf(m_run, m2);
    const float a1 = __builtin_amdgcn_exp2f((m_run - mt) * CS), a2 = __builtin_amdgcn_exp2f((m2 - mt) * CS);
    const float inv = 1.0f / (l_run * a1 + l2 * a2);
    const float f1 = a1 * inv, f2 = a2 * inv;
    const float LAS* src = (const float LAS*)(lds + wo * 8192);
    bf16_t* orow = ctx + (size_t)(LB * 128 + 32 * rg + r32) * CTX_LD + head * HD + 8 * hi;
#pragma unroll
    for (int dd = 0; dd < 2; ++dd) {
      f32x16 a;
#pragma unroll
      for (int r = 0; r < 16; ++r) a[r] = kh == 0 ? o[dd][r] : o[2 + dd][r];
      float r[16];
#pragma unroll
      for (int gg = 0; gg < 4; ++gg) {
        const f32x4 b = *(const f32x4 LAS*)(src + ((dd * 4 + gg) * 64 + l) * 4);
#pragma unroll
        for (int e = 0; e < 4; ++e) r[4 * gg + e] = a[4 * gg + e] * f1 + b[e] * f2;
      }
      u32x4 v0 = {pack2(r[0], r[1]), pack2(r[2], r[3]), pack2(r[4], r[5]), pack2(r[6], r[7])};
      u32x4 v1 = {pack2(r[8], r[9]), pack2(r[10], r[11]), pack2(r[12], r[13]), pack2(r[14], r[15])};
      const int dcol = 32 * (2 * kh + dd);
      st16(orow + dcol, v0);
      st16(orow + dcol + 16, v1);
    }
  }
  TS(1, 2);
}

constexpr int G2_STAGE = 36864;
constexpr int G2_NST = 4;
__global__ __launch_bounds__(512, 2) void gemm2_kernel(const bf16_t* __restrict__ ctx, const bf16_t* __restrict__ wo,
                                                       const bf16_t* __restrict__ U, const bf16_t* __restrict__ w2,
                                                       float* __restrict__ part, unsigned* __restrict__ cnt,
                                                       const bf16_t* __restrict__ x, const bf16_t* __restrict__ b2,
                                                       bf16_t* __restrict__ out) {
  __shared__ __attribute__((aligned(16))) char smem[G2_NST * G2_STAGE];
  char LAS* lds = (char LAS*)smem;
  const int b = blockIdx.x, xcd = b & 7, c = b >> 3;
  const int ks = xcd >> 1;
  const int s0 = (xcd & 1) * 1024 + (c >> 2) * 128, m0 = (c & 3) * 160;
  const int l = threadIdx.x & 63, w = __builtin_amdgcn_readfirstlane(threadIdx.x >> 6);
  const int ws = w & 3, kh = w >> 2, r32 = l & 31, hi = l >> 5;
  const int blk = (l >> 4) & 1, q = (l & 15) >> 2, p = l & 3;
  TS(2, 0);
  int pOffA[2], pOffB[2], rOffA[3], rOffB[3];
#pragma unroll
  for (int i = 0; i < 2; ++i) {
    const int j = w * 2 + i;
    const int row = 8 * j + (l >> 3), slot = l & 7;
    pOffA[i] = (row * CTX_LD + (slot ^ ((row >> 1) & 7)) * 8) * 2;
    const int krow = 4 * j + (l >> 4), s16 = l & 15;
    pOffB[i] = (krow * S_LEN + ((s16 >> 2) ^ (krow & 3)) * 32 + (s16 & 3) * 8) * 2;
  }
#pragma unroll
  for (int i = 0; i < 3; ++i) {
    const int j = w + 8 * i;
    const int cb = j >> 2, kr = (j & 3) * 16 + (l >> 2);
    rOffA[i] = (kr * DM + cb * 32 + (l & 3) * 8) * 2;
    const int row = 8 * j + (l >> 3), slot = l & 7;
    rOffB[i] = (row * FF + (slot ^ ((row >> 1) & 7)) * 8) * 2;
  }
  const int f0 = ks * 7, nB = ks == 3 ? 6 : 7, total = 16 + nB;
  const bf16_t* PA = ctx + (size_t)s0 * CTX_LD + ks * 1024;
  const bf16_t* RA = wo + (size_t)ks * 1024 * DM + m0;
  const bf16_t* PB = w2 + (size_t)f0 * 64 * S_LEN + s0;
  const bf16_t* RB = U + (size_t)m0 * FF + f0 * 64;
  int pslot = 0, issued = 0;
  auto issue = [&]() {
    const unsigned bb = (unsigned)(uintptr_t)(lds + pslot * G2_STAGE);
    if (issued < 16) {
      const bf16_t* P = PA + issued * 64;
      const bf16_t* R = RA + (size_t)issued * 64 * DM;
      glds16(P, pOffA[0], bb + (w * 2) * 1024);
      glds16(P, pOffA[1], bb + (w * 2 + 1) * 1024);
      glds16(R, rOffA[0], bb + 16384 + w * 1024);
      glds16(R, rOffA[1], bb + 16384 + (w + 8) * 1024);
      if (w < 4) glds16(R, rOffA[2], bb + 16384 + (w + 16) * 1024);
    } else {
      const bf16_t* P = PB + (size_t)(issued - 16) * 64 * S_LEN;
      const bf16_t* R = RB + (issued - 16) * 64;
      glds16(P, pOffB[0], bb + (w * 2) * 1024);
      glds16(P, pOffB[1], bb + (w * 2 + 1) * 1024);
      glds16(R, rOffB[0], bb + 16384 + w * 1024);
      glds16(R, rOffB[1], bb + 16384 + (w + 8) * 1024);
      if (w < 4) glds16(R, rOffB[2], bb + 16384 + (w + 16) * 1024);
    }
    if (++pslot == G2_NST) pslot = 0;
    ++issued;
  };
  const int prow = 32 * ws + r32, psw = (prow >> 1) & 7;
  const int pA0 = prow * 128 + (((4 * kh + hi) ^ psw) << 4), pA1 = prow * 128 + (((4 * kh + 2 + hi) ^ psw) << 4);
  const int rA = 16384 + (32 * kh + 8 * hi + q) * 64 + blk * 32 + swapmid(p) * 8;
  const int pB = (32 * kh + 8 * hi + q) * 256 + ((ws ^ q) * 64) + blk * 32 + p * 8;
  const int rrow = rperm(r32), rsw = (rrow >> 1) & 7;
  const int rB0 = 16384 + rrow * 128 + (((4 * kh + hi) ^ rsw) << 4), rB1 = 16384 + rrow * 128 + (((4 * kh + 2 + hi) ^ rsw) << 4);
#pragma unroll
  for (int i = 0; i < G2_NST - 1; ++i) issue();
  f32x16 acc[5] = {};
  int buf = 0;
  for (int step = 0; step < 16; ++step) {
    if (w < 4) wait_vm<5 * (G2_NST - 2)>();
    else wait_vm<4 * (G2_NST - 2)>();
    BARRIER();
    issue();
    const char LAS* bp = lds + buf * G2_STAGE;
    bf16x8 pf[2], rf[2][5];
    pf[0] = *(const LAS bf16x8*)(bp + pA0);
#pragma unroll
    for (int j = 0; j < 5; ++j) rf[0][j] = cat8(tr_read(bp + rA + j * 4096), tr_read(bp + rA + j * 4096 + 256));
    pf[1] = *(const LAS bf16x8*)(bp + pA1);
#pragma unroll
    for (int j = 0; j < 5; ++j) rf[1][j] = cat8(tr_read(bp + rA + j * 4096 + 1024), tr_read(bp + rA + j * 4096 + 1024 + 256));
    __builtin_amdgcn_sched_barrier(0);
#pragma unroll
    for (int k2 = 0; k2 < 2; ++k2)
#pragma unroll
      for (int j = 0; j < 5; ++j) acc[j] = mf<X16_G2>(rf[k2][j], pf[k2], acc[j], k2);
    if (++buf == G2_NST) buf = 0;
  }
  for (int step = 16; step < total; ++step) {
    if (step + G2_NST - 1 > total) WAIT_VM0();
    else if (w < 4) wait_vm<5 * (G2_NST - 2)>();
    else wait_vm<4 * (G2_NST - 2)>();
    BARRIER();
    if (issued < total) issue();
    const char LAS* bp = lds + buf * G2_STAGE;
    bf16x8 pf[2], rf[2][5];
    pf[0] = cat8(tr_read(bp + pB), tr_read(bp + pB + 1024));
#pragma unroll
    for (int j = 0; j < 5; ++j) rf[0][j] = *(const LAS bf16x8*)(bp + rB0 + j * 4096);
    pf[1] = cat8(tr_read(bp + pB + 4096), tr_read(bp + pB + 4096 + 1024));
#pragma unroll
    for (int j = 0; j < 5; ++j) rf[1][j] = *(const LAS bf16x8*)(bp + rB1 + j * 4096);
    __builtin_amdgcn_sched_barrier(0);
#pragma unroll
    for (int k2 = 0; k2 < 2; ++k2)
#pragma unroll
      for (int j = 0; j < 5; ++j) acc[j] = mf<X16_G2>(rf[k2][j], pf[k2], acc[j], k2);
    if (++buf == G2_NST) buf = 0;
  }
  TS(2, 1);
  BARRIER();
  {
    float LAS* dst = (float LAS*)(lds + w * 12288);
    if (kh == 0) {
#pragma unroll
      for (int j = 3; j < 5; ++j)
#pragma unroll
        for (int g = 0; g < 4; ++g)
          *(f32x4 LAS*)(dst + (((j - 3) * 4 + g) * 64 + l) * 4) = (f32x4){acc[j][4 * g], acc[j][4 * g + 1], acc[j][4 * g + 2], acc[j][4 * g + 3]};
    } else {
#pragma unroll
      for (int j = 0; j < 3; ++j)
#pragma unroll
        for (int g = 0; g < 4; ++g)
          *(f32x4 LAS*)(dst + ((j * 4 + g) * 64 + l) * 4) = (f32x4){acc[j][4 * g], acc[j][4 * g + 1], acc[j][4 * g + 2], acc[j][4 * g + 3]};
    }
    asm volatile("s_waitcnt lgkmcnt(0)" ::: "memory");
    BARRIER();
    const float LAS* src = (const float LAS*)(lds + (w ^ 4) * 12288);
    const int tile = (xcd & 1) * 32 + c;
    const __amdgpu_buffer_rsrc_t rs = __builtin_amdgcn_make_buffer_rsrc((void*)part, (short)0, 64 * 4 * 81920, 0x00020000);
    unsigned* ctr = cnt + tile * 32;
    auto fin = [&](auto NJ_, auto J0_) {
      constexpr int NJ = NJ_, J0 = J0_, SL = NJ * 4096;
      f32x4 r[NJ][4];
#pragma unroll
      for (int j = 0; j < NJ; ++j)
#pragma unroll
        for (int g = 0; g < 4; ++g) {
          const f32x4 o = *(const f32x4 LAS*)(src + ((j * 4 + g) * 64 + l) * 4);
          r[j][g] = (f32x4){acc[J0 + j][4 * g] + o[0], acc[J0 + j][4 * g + 1] + o[1], acc[J0 + j][4 * g + 2] + o[2], acc[J0 + j][4 * g + 3] + o[3]};
        }
      const int sb = (tile * 4 + ws) * 81920 + (J0 ? 49152 : 0);
      const size_t orow = (size_t)(s0 + 32 * ws + r32) * DM + m0 + 32 * J0 + 8 * hi;
      u32x4 xv[NJ][2];
      float bb = 0.f;
      if (ws != ks) {
#pragma unroll
        for (int j = 0; j < NJ; ++j)
#pragma unroll
          for (int g = 0; g < 4; ++g)
            __builtin_amdgcn_raw_buffer_store_b128(__builtin_bit_cast(u32x4, r[j][g]), rs, l * 16 + (j * 4 + g) * 1024, sb + ks * SL, 16);
        asm volatile("s_waitcnt vmcnt(0)" ::: "memory");
      } else {
#pragma unroll
        for (int j = 0; j < NJ; ++j)
#pragma unroll
          for (int gp = 0; gp < 2; ++gp) xv[j][gp] = *(const u32x4*)(x + orow + 32 * j + 16 * gp);
        bb = bf2f(b2[s0 + 32 * ws + r32]);
      }
      BARRIER();
      if (threadIdx.x == 0) __hip_atomic_fetch_add(ctr, 1u, __ATOMIC_RELAXED, __HIP_MEMORY_SCOPE_AGENT);
      if (ws == ks) {
        for (int spin = 0; spin < (1 << 22); ++spin) {
          const unsigned v = __builtin_amdgcn_readfirstlane(__hip_atomic_load(ctr, __ATOMIC_RELAXED, __HIP_MEMORY_SCOPE_AGENT));
          if (v >= 4u) break;
          __builtin_amdgcn_s_sleep(1);
        }
        __builtin_amdgcn_fence(__ATOMIC_ACQUIRE, "wavefront");
        asm volatile("" ::: "memory");
        TS(2, 3);
        u32x4 pv[3][NJ][4];
#pragma unroll
        for (int t = 1; t < 4; ++t)
#pragma unroll
          for (int j = 0; j < NJ; ++j)
#pragma unroll
            for (int g = 0; g < 4; ++g)
              pv[t - 1][j][g] = __builtin_amdgcn_raw_buffer_load_b128(rs, l * 16 + (j * 4 + g) * 1024, sb + (ks ^ t) * SL, 16);
#pragma unroll
        for (int j = 0; j < NJ; ++j)
#pragma unroll
          for (int gp = 0; gp < 2; ++gp) {
            float o[8];
#pragma unroll
            for (int e = 0; e < 8; ++e) {
              const int g = 2 * gp + (e >> 2), q4 = e & 3;
              const float a = (r[j][g][q4] + __uint_as_float(pv[0][j][g][q4])) + (__uint_as_float(pv[1][j][g][q4]) + __uint_as_float(pv[2][j][g][q4]));
              const unsigned xw = xv[j][gp][e >> 1];
              o[e] = __uint_as_float((e & 1) ? (xw & 0xffff0000u) : (xw << 16)) + a + bb;
            }
            *(u32x4*)(out + orow + 32 * j + 16 * gp) = (u32x4){pack2(o[0], o[1]), pack2(o[2], o[3]), pack2(o[4], o[5]), pack2(o[6], o[7])};
          }
      }
    };
    if (kh == 0) fin(IC<3>{}, IC<0>{});
    else fin(IC<2>{}, IC<3>{});
  }
  TS(2, 2);
}

__global__ __launch_bounds__(256) void reduce_kernel(const float* __restrict__ part, const bf16_t* __restrict__ x,
                                                     const bf16_t* __restrict__ b2, bf16_t* __restrict__ out) {
  TS(4, 0);
  const int i = blockIdx.x * 256 + threadIdx.x;
  const int e = i * 4;
  const int s = e / DM;
  f32x4 a = *(const f32x4*)(part + e);
#pragma unroll
  for (int sp = 1; sp < 4; ++sp) a += *(const f32x4*)(part + (size_t)sp * S_LEN * DM + e);
  const u32x2 xv = *(const u32x2*)(x + e);
  const float bb = bf2f(b2[s]);
  float r0 = __uint_as_float(xv[0] << 16) + a[0] + bb;
  float r1 = __uint_as_float(xv[0] & 0xffff0000u) + a[1] + bb;
  float r2 = __uint_as_float(xv[1] << 16) + a[2] + bb;
  float r3 = __uint_as_float(xv[1] & 0xffff0000u) + a[3] + bb;
  u32x2 o = {pack2(r0, r1), pack2(r2, r3)};
  *(u32x2*)(out + e) = o;
  TS(4, 2);
}

extern "C" void kernel_launch(void* const* d_in, const int* in_sizes, int n_in, void* d_out, int out_size, void* d_ws,
                              size_t ws_size, hipStream_t stream) {
  const bf16_t* x = (const bf16_t*)d_in[0];
  const bf16_t* ckv = (const bf16_t*)d_in[1];
  const bf16_t* wq = (const bf16_t*)d_in[2];
  const bf16_t* wuk = (const bf16_t*)d_in[3];
  const bf16_t* wuv = (const bf16_t*)d_in[4];
  const bf16_t* wo = (const bf16_t*)d_in[5];
  const bf16_t* w1 = (const bf16_t*)d_in[7];
  const bf16_t* b1 = (const bf16_t*)d_in[8];
  const bf16_t* w2 = (const bf16_t*)d_in[9];
  const bf16_t* b2 = (const bf16_t*)d_in[10];
  char* ws = (char*)d_ws;
  bf16_t* xn = (bf16_t*)(ws + WS_XN);
  bf16_t* h = (bf16_t*)(ws + WS_H);
  bf16_t* Q = (bf16_t*)(ws + WS_Q);
  bf16_t* K = (bf16_t*)(ws + WS_K);
  bf16_t* V = (bf16_t*)(ws + WS_V);
  bf16_t* U = (bf16_t*)(ws + WS_U);
  bf16_t* ctx = (bf16_t*)(ws + WS_CTX);
  float* part = (float*)(ws + WS_PART);
  bf16_t* ct = (bf16_t*)(ws + WS_UP);
  unsigned* cnt = (unsigned*)(ws + WS_CNT);
  ln_kernel<<<S_LEN / 4 + PF_BLOCKS, 256, 0, stream>>>(x, xn, h, ckv, ct, wq, wuk, wuv, cnt);
  gemm1_kernel<<<256, 512, 0, stream>>>(xn, h, ct, wq, wuk, wuv, w1, b1, wo, w2, Q, K, V, U);
  attn_kernel<<<256, 512, 0, stream>>>(Q, K, V, ctx, wo, w2);
  gemm2_kernel<<<256, 512, 0, stream>>>(ctx, wo, U, w2, part, cnt, x, b2, (bf16_t*)d_out);
}
```

```cpp
#include <hip/hip_runtime.h>
#include <hip/hip_bf16.h>
#include <stdint.h>
#include <type_traits>

constexpr int S_LEN = 2048;
constexpr int DM = 640;
constexpr int NH = 32;
constexpr int HD = 128;
constexpr int DC = 512;
constexpr int FF = 1728;
constexpr int HDN = NH * HD;
constexpr int CTX_LD = HDN + 64;

typedef unsigned short bf16_t;
typedef short bf16x8 __attribute__((ext_vector_type(8)));
typedef short s16x4 __attribute__((ext_vector_type(4)));
typedef float f32x16 __attribute__((ext_vector_type(16)));
typedef float f32x4 __attribute__((ext_vector_type(4)));
typedef float f32x2 __attribute__((ext_vector_type(2)));
typedef unsigned u32x4 __attribute__((ext_vector_type(4)));
typedef unsigned u32x2 __attribute__((ext_vector_type(2)));
typedef __bf16 bf16x2_t __attribute__((ext_vector_type(2)));
#define LAS __attribute__((address_space(3)))

__device__ __forceinline__ float bf2f(bf16_t v) { return __uint_as_float(((unsigned)v) << 16); }
__device__ __forceinline__ unsigned pack2(float a, float b) {
  bf16x2_t v = {(__bf16)a, (__bf16)b};
  return *reinterpret_cast<unsigned*>(&v);
}
__device__ __forceinline__ unsigned pack2a(float a, float b) {
  unsigned r;
  asm("v_cvt_pk_bf16_f32 %0, %1, %2" : "=v"(r) : "v"(a), "v"(b));
  return r;
}
__device__ __forceinline__ float rbf(float a) {
  __bf16 v = (__bf16)a;
  return __uint_as_float(((unsigned)*reinterpret_cast<unsigned short*>(&v)) << 16);
}

template <bool X16> __device__ __forceinline__ f32x16 mf(bf16x8 a, bf16x8 b, f32x16 c, int sel) {
  if (X16) {
    f32x4 q0 = {c[8 * sel], c[8 * sel + 1], c[8 * sel + 2], c[8 * sel + 3]};
    f32x4 q1 = {c[8 * sel + 4], c[8 * sel + 5], c[8 * sel + 6], c[8 * sel + 7]};
    q0 = __builtin_amdgcn_mfma_f32_16x16x32_bf16(a, b, q0, 0, 0, 0);
    q1 = __builtin_amdgcn_mfma_f32_16x16x32_bf16(b, a, q1, 0, 0, 0);
#pragma unroll
    for (int e = 0; e < 4; ++e) { c[8 * sel + e] = q0[e]; c[8 * sel + 4 + e] = q1[e]; }
    return c;
  }
  return __builtin_amdgcn_mfma_f32_32x32x16_bf16(a, b, c, 0, 0, 0);
}
#define X16_G1 0
#define X16_AT 0
#define X16_G2 0
constexpr size_t WS_XN = 0;
constexpr size_t WS_H = WS_XN + (size_t)S_LEN * DM * 2;
constexpr size_t WS_Q = WS_H + (size_t)S_LEN * DM * 2;
constexpr size_t WS_K = WS_Q + (size_t)S_LEN * HDN * 2;
constexpr size_t WS_V = WS_K + (size_t)S_LEN * HDN * 2;
constexpr size_t WS_U = WS_V + (size_t)S_LEN * HDN * 2;
constexpr size_t WS_CTX = WS_U + (size_t)DM * FF * 2;
constexpr size_t WS_PART = WS_CTX + (size_t)S_LEN * CTX_LD * 2;
constexpr size_t WS_UP = WS_PART + (size_t)4 * S_LEN * DM * 4;
constexpr size_t WS_CNT = WS_UP + (size_t)S_LEN * DC * 2;
constexpr size_t WS_END = WS_CNT + 192 * 128;
#define G2_FUSE 1
#define MX5 1
#define MX_PF 0
#define MX_NOFILL 1
#define MX_ORD 1
#define G2_PP 1
#define QF 1
#define XF 1
#define PLB 2
#define K1PF 0
#define K1SC 1
#define K1TS 0
#define LNPOS 9
#define LNPOS2 (LNPOS + 1)
#define POLL4 2
#define XWAVE 0
#define PSLEEP 14
#define XSLEEP 1
#define CNT1 1
#define SSIG 1
#define MIXPOS 0
#define W1PF (MIXPOS ? 5 : 4)
#define LINEST 7
#define FUSE3 2
#define CDIR (PLB == 2)
#define QLEAD 3

__device__ __forceinline__ float wave_sum(float v) {
#pragma unroll
  for (int o = 32; o > 0; o >>= 1) v += __shfl_xor(v, o);
  return v;
}
__device__ __forceinline__ void ln_row(float (&v)[10]) {
  float s = 0.f;
#pragma unroll
  for (int i = 0; i < 10; ++i) s += v[i];
  s = wave_sum(s);
  const float mean = s * (1.0f / DM);
  float q = 0.f;
#pragma unroll
  for (int i = 0; i < 10; ++i) { float d = v[i] - mean; q += d * d; }
  q = wave_sum(q);
  const float mb = rbf(mean);
  const float vb = rbf(q * (1.0f / DM));
  const float den = rbf(sqrtf(rbf(vb + 1e-5f)));
#pragma unroll
  for (int i = 0; i < 10; ++i) v[i] = rbf(rbf(v[i] - mb) / den);
}
__device__ __forceinline__ const char* warm_addr(int kb, int i, const bf16_t* wq, const bf16_t* wuk, const bf16_t* wuv) {
  const bf16_t* src = i < 8192 ? wq : i < 16384 ? wuk : wuv;
  return (const char*)src + (size_t)kb * 1048576 + (size_t)(i & 8191) * 128;
}
#define PF_KB 2
#define G1_BAL 0
__device__ __forceinline__ const char* warm_addr2(int i, const bf16_t* wq, const bf16_t* wuk, const bf16_t* wuv) {
  if (i >= 65536) return (const char*)wq + (size_t)(i - 65536) * 128;
  const bf16_t* src = (i & 8192) ? wuv : wuk;
  return (const char*)src + (size_t)(i >> 14) * 1048576 + (size_t)(i & 8191) * 128;
}
#define CODE_PF 7
#define TLB_SH 21
#define CODE_ADDR(sym, out)                                                                                                  \
  asm volatile("s_getpc_b64 s[90:91]\n\ts_add_u32 s90, s90, " sym "@rel32@lo+4\n\ts_addc_u32 s91, s91, " sym "@rel32@hi+12\n\t" \
               "s_mov_b64 %0, s[90:91]" : "=s"(out)::"s90", "s91", "scc")
template <int WHICH, int NT> __device__ __forceinline__ void code_warm() {
  unsigned long long a, b;
  if (WHICH == 0) { CODE_ADDR("gemm1_kernel", a); CODE_ADDR("reduce_kernel", b); }
  if (WHICH == 1) { CODE_ADDR("attn_kernel", a); CODE_ADDR("gemm2_kernel", b); }
  if (WHICH == 2) { CODE_ADDR("gemm2_kernel", a); CODE_ADDR("reduce_kernel", b); }
  if (b <= a || b - a > 131072) return;
  unsigned keep = 0;
  for (unsigned long long p = a + threadIdx.x * 128; p < b; p += NT * 128)
    asm volatile("global_load_dword %0, %1, off" : "+v"(keep) : "v"(p) : "memory");
  asm volatile("s_waitcnt vmcnt(0)" : "+v"(keep)::"memory");
}
constexpr int PF_WB = QF ? 16 : 24;
constexpr int PF_BLOCKS = PF_WB * PF_KB + 128;
extern "C" __global__ __launch_bounds__(256) void ln_kernel(const bf16_t* __restrict__ x, bf16_t* __restrict__ xn,
                                                 bf16_t* __restrict__ h, const bf16_t* __restrict__ ckv,
                                                 bf16_t* __restrict__ ct, const bf16_t* __restrict__ wq,
                                                 const bf16_t* __restrict__ wuk, const bf16_t* __restrict__ wuv,
                                                 unsigned* __restrict__ cnt
                                                 ) {
  if (blockIdx.x >= S_LEN / 4 + PF_BLOCKS) {
    code_warm<0, 256>();
    return;
  }
  if (blockIdx.x == PF_BLOCKS && threadIdx.x < 64) cnt[threadIdx.x * 32] = 0u;
  if (blockIdx.x < PF_BLOCKS) {
    if (blockIdx.x < PF_WB * PF_KB) {
      unsigned keep = 0;
#pragma unroll
      for (int j = 0; j < 4; ++j) {
        const char* a = warm_addr2(blockIdx.x * 1024 + j * 256 + threadIdx.x, wq, wuk, wuv);
        asm volatile("global_load_dword %0, %1, off" : "+v"(keep) : "v"(a) : "memory");
      }
      asm volatile("s_waitcnt vmcnt(0)" : "+v"(keep)::"memory");
    } else {
      const int r0 = (blockIdx.x - PF_WB * PF_KB) * 16 + (threadIdx.x >> 6), c8 = threadIdx.x & 63;
      u32x4 v[4];
#pragma unroll
      for (int j = 0; j < 4; ++j) v[j] = *(const u32x4*)(ckv + (size_t)(r0 + 4 * j) * DC + c8 * 8);
#pragma unroll
      for (int j = 0; j < 4; ++j) *(u32x4*)(ct + ((size_t)(c8 >> 2) * S_LEN + r0 + 4 * j) * 32 + (c8 & 3) * 8) = v[j];
    }
    return;
  }
  const int lane = threadIdx.x & 63, w = threadIdx.x >> 6;
  const int row = (blockIdx.x - PF_BLOCKS) * 4 + w;
  const unsigned* xr = reinterpret_cast<const unsigned*>(x + (size_t)row * DM);
  float v[10];
#pragma unroll
  for (int j = 0; j < 5; ++j) {
    unsigned u = xr[lane + 64 * j];
    v[2 * j] = __uint_as_float(u << 16);
    v[2 * j + 1] = __uint_as_float(u & 0xffff0000u);
  }
  ln_row(v);
  unsigned* xo = reinterpret_cast<unsigned*>(xn) + ((((size_t)(row >> 5) * 40 + (lane >> 3)) * 64 + ((lane >> 2) & 1) * 32 + (row & 31)) * 4 + (lane & 3));
#pragma unroll
  for (int j = 0; j < 5; ++j) xo[j * 8 * 256] = pack2(v[2 * j], v[2 * j + 1]);
  ln_row(v);
  unsigned* ho = reinterpret_cast<unsigned*>(h + (size_t)row * DM);
#pragma unroll
  for (int j = 0; j < 5; ++j) ho[lane + 64 * j] = pack2(v[2 * j], v[2 * j + 1]);
}

constexpr int STAGE_BYTES = 49152;
constexpr int RBASE = 32768;

__device__ __forceinline__ int swapmid(int p) { return ((p & 1) << 1) | ((p >> 1) & 1); }
__device__ __forceinline__ int rperm(int r) {
  int g = r >> 2;
  int pg = (g & 4) | swapmid(g & 3);
  return pg * 4 + (r & 3);
}
__device__ __forceinline__ s16x4 tr_read(const char LAS* p) {
  return __builtin_amdgcn_ds_read_tr16_b64_v4i16((LAS s16x4*)p);
}
__device__ __forceinline__ bf16x8 cat8(s16x4 a, s16x4 b) {
  return (bf16x8){a[0], a[1], a[2], a[3], b[0], b[1], b[2], b[3]};
}

__device__ __forceinline__ void glds16(const void* sbase, int voff, unsigned lds_dst) {
  unsigned keep;
  asm volatile("s_mov_b32 %0, m0\n\ts_mov_b32 m0, %3\n\ts_nop 0\n\tglobal_load_lds_dwordx4 %1, %2\n\ts_mov_b32 m0, %0"
               : "=&s"(keep) : "v"(voff), "s"(sbase), "s"(lds_dst) : "memory");
}

__device__ __forceinline__ void glds16s(const void* sbase, int voff, unsigned lds_dst) {
  unsigned keep;
  asm volatile("s_mov_b32 %0, m0\n\ts_mov_b32 m0, %3\n\ts_nop 0\n\tglobal_load_lds_dwordx4 %1, %2 sc1\n\ts_mov_b32 m0, %0"
               : "=&s"(keep) : "v"(voff), "s"(sbase), "s"(lds_dst) : "memory");
}

__device__ __forceinline__ void glds16x4(const void* sbase, int v0, int v1, int v2, int v3, unsigned lds_dst) {
  unsigned keep;
  asm volatile("s_mov_b32 %0, m0\n\ts_mov_b32 m0, %6\n\ts_nop 0\n\tglobal_load_lds_dwordx4 %1, %5\n\t"
               "global_load_lds_dwordx4 %2, %5 offset:1024\n\tglobal_load_lds_dwordx4 %3, %5 offset:2048\n\t"
               "global_load_lds_dwordx4 %4, %5 offset:3072\n\ts_mov_b32 m0, %0"
               : "=&s"(keep) : "v"(v0), "v"(v1), "v"(v2), "v"(v3), "s"(sbase), "s"(lds_dst) : "memory");
}

template <int J> __device__ __forceinline__ void glds16p(const void* sbase, int v, unsigned lds_dst) {
  asm volatile("s_mov_b32 m0, %2\n\ts_nop 0\n\tglobal_load_lds_dwordx4 %0, %1 offset:%3"
               ::"v"(v), "s"(sbase), "s"(lds_dst), "n"(J * 1024) : "memory");
}

#define ST_MODE 0
__device__ __forceinline__ void st16(void* p, u32x4 v) {
  *(u32x4*)p = v;
}
__device__ __forceinline__ void st16sc(void* p, u32x4 v) {
  if (K1SC == 1) asm volatile("global_store_dwordx4 %0, %1, off sc1\n\ts_nop 1" ::"v"(p), "v"(v) : "memory");
  if (K1SC == 2) asm volatile("global_store_dwordx4 %0, %1, off sc0 sc1\n\ts_nop 1" ::"v"(p), "v"(v) : "memory");
  if (K1SC == 3) asm volatile("global_store_dwordx4 %0, %1, off sc1 nt\n\ts_nop 1" ::"v"(p), "v"(v) : "memory");
  if (K1SC == 4) asm volatile("global_store_dwordx4 %0, %1, off sc0 sc1 nt\n\ts_nop 1" ::"v"(p), "v"(v) : "memory");
}
__device__ __forceinline__ void st16h(void* p, u32x4 v) {
  if (FUSE3) asm volatile("global_store_dwordx4 %0, %1, off sc1\n\ts_nop 1" ::"v"(p), "v"(v) : "memory");
  else st16(p, v);
}
__device__ __forceinline__ void st16f(float* p, float a, float b, float c, float d) {
  st16(p, (u32x4){__float_as_uint(a), __float_as_uint(b), __float_as_uint(c), __float_as_uint(d)});
}

template <bool P_TR, bool R_TR, bool ROWS_P>
struct GemmCore {
  int pOff[4], rOff[2];
  int pAddr[4], rAddr[4];
  int w, l;
  __device__ __forceinline__ void init(int ldp, int ldr, int pcol_limit  ) {
    const int tid = threadIdx.x;
    l = tid & 63;
    w = __builtin_amdgcn_readfirstlane(tid >> 6);
    const int wp = w >> 1, wr = w & 1;
#pragma unroll
    for (int i = 0; i < 4; ++i) {
      const int j = w * 4 + i;
      if (!P_TR) {
        const int row = 8 * j + (l >> 3), slot = l & 7;
        const int chunk = slot ^ ((row >> 1) & 7);
        pOff[i] = row * ldp + chunk * 8;
      } else {
        const int half = j >> 4, jj = j & 15;
        const int krow = 4 * jj + (l >> 4), s16 = l & 15;
        const int c64 = (s16 >> 2) ^ (krow & 3);
        int col = half * 128 + c64 * 32 + (s16 & 3) * 8;
        if (col >= pcol_limit) col = pcol_limit - 8;
        pOff[i] = krow * ldp + col;
      }
    }
#pragma unroll
    for (int i = 0; i < 2; ++i) {
      const int j = w * 2 + i;
      if (!R_TR) {
        const int row = 8 * j + (l >> 3), slot = l & 7;
        const int chunk = slot ^ ((row >> 1) & 7);
        rOff[i] = row * ldr + chunk * 8;
      } else {
        const int krow = 4 * j + (l >> 4), s16 = l & 15;
        const int c64 = (s16 >> 2) ^ (krow & 3);
        const int col = c64 * 32 + (s16 & 3) * 8;
        rOff[i] = krow * ldr + col;
      }
    }
    const int r32 = l & 31, hi = l >> 5, blk = (l >> 4) & 1, q = (l & 15) >> 2, p = l & 3;
    if (!P_TR) {
      const int prow = 64 * wp + (ROWS_P ? rperm(r32) : r32);
      const int s = (prow >> 1) & 7;
#pragma unroll
      for (int kk = 0; kk < 4; ++kk) pAddr[kk] = prow * 128 + (((2 * kk + hi) ^ s) << 4);
    } else {
      const int half = wp >> 1, pp = ROWS_P ? swapmid(p) : p;
#pragma unroll
      for (int i = 0; i < 2; ++i) {
        const int c64 = 2 * (wp & 1) + i;
        pAddr[i] = half * 16384 + (8 * hi + q) * 256 + ((c64 ^ q) * 64) + blk * 32 + pp * 8;
      }
      pAddr[2] = pAddr[3] = 0;
    }
    if (!R_TR) {
      const int rrow = 64 * wr + (!ROWS_P ? rperm(r32) : r32);
      const int s = (rrow >> 1) & 7;
#pragma unroll
      for (int kk = 0; kk < 4; ++kk) rAddr[kk] = RBASE + rrow * 128 + (((2 * kk + hi) ^ s) << 4);
    } else {
      const int pp = !ROWS_P ? swapmid(p) : p;
#pragma unroll
      for (int i = 0; i < 2; ++i) {
        const int c64 = 2 * wr + i;
        rAddr[i] = RBASE + (8 * hi + q) * 256 + ((c64 ^ q) * 64) + blk * 32 + pp * 8;
      }
      rAddr[2] = rAddr[3] = 0;
    }
  }
  __device__ __forceinline__ void stage(char LAS* buf, const bf16_t* P, const bf16_t* R) {
    const unsigned b = (unsigned)(uintptr_t)buf;
#pragma unroll
    for (int i = 0; i < 4; ++i) glds16(P, pOff[i] * 2, b + (w * 4 + i) * 1024);
#pragma unroll
    for (int i = 0; i < 2; ++i) glds16(R, rOff[i] * 2, b + RBASE + (w * 2 + i) * 1024);
  }
  __device__ __forceinline__ void compute(const char LAS* buf, f32x16 (&acc)[2][2]) {
#pragma unroll
    for (int kk = 0; kk < 4; ++kk) {
      bf16x8 pf[2], rf[2];
#pragma unroll
      for (int i = 0; i < 2; ++i) {
        if (!P_TR) pf[i] = *(const LAS bf16x8*)(buf + pAddr[kk] + i * 4096);
        else pf[i] = cat8(tr_read(buf + pAddr[i] + kk * 4096), tr_read(buf + pAddr[i] + kk * 4096 + 1024));
        if (!R_TR) rf[i] = *(const LAS bf16x8*)(buf + rAddr[kk] + i * 4096);
        else rf[i] = cat8(tr_read(buf + rAddr[i] + kk * 4096), tr_read(buf + rAddr[i] + kk * 4096 + 1024));
      }
#pragma unroll
      for (int i = 0; i < 2; ++i)
#pragma unroll
        for (int j = 0; j < 2; ++j) {
          if (ROWS_P) acc[i][j] = __builtin_amdgcn_mfma_f32_32x32x16_bf16(pf[i], rf[j], acc[i][j], 0, 0, 0);
          else acc[i][j] = __builtin_amdgcn_mfma_f32_32x32x16_bf16(rf[j], pf[i], acc[i][j], 0, 0, 0);
        }
    }
  }
};

#define WAIT_VM0() asm volatile("s_waitcnt vmcnt(0)" ::: "memory")
#define WAIT_VM6() asm volatile("s_waitcnt vmcnt(6)" ::: "memory")
#define WAIT_VM16() asm volatile("s_waitcnt vmcnt(16)" ::: "memory")
#define BARRIER() __builtin_amdgcn_s_barrier()
#define TS(k, i)
#define PH(i, k)

#define GELU_FAST 1
__device__ __forceinline__ float gelu_tanh(float x) {
  constexpr float k0 = -2.0f * 0.7978845608028654f * 1.4426950408889634f;
  const float z = x * fmaf(x * x, 0.044715f * k0, k0);
  return x * __builtin_amdgcn_rcpf(1.0f + __builtin_amdgcn_exp2f(z));
}

constexpr int STAGE256 = 32768;
#define NST256 4
struct Core256 {
  int pOff[2], rOff[2];
  int pAddr[2], rAddr[2];
  int w;
  __device__ __forceinline__ void init(int ldp, int ldr) {
    const int tid = threadIdx.x, l = tid & 63;
    w = __builtin_amdgcn_readfirstlane(tid >> 6);
    const int wp = w >> 2, wr = w & 3;
#pragma unroll
    for (int i = 0; i < 2; ++i) {
      const int j = w * 2 + i;
      const int row = 16 * j + (l >> 2), slot = l & 3;
      pOff[i] = (row * ldp + (slot ^ ((row >> 2) & 3)) * 8) * 2;
      const int half = j >> 3, jj = j & 7;
      const int krow = 4 * jj + (l >> 4), s16 = l & 15;
      const int c64 = (s16 >> 2) ^ (krow & 3);
      rOff[i] = (krow * ldr + half * 128 + c64 * 32 + (s16 & 3) * 8) * 2;
    }
    const int r32 = l & 31, hi = l >> 5, blk = (l >> 4) & 1, q = (l & 15) >> 2, p = l & 3;
    const int prow = 128 * wp + r32, s = (prow >> 2) & 3;
#pragma unroll
    for (int kk = 0; kk < 2; ++kk) pAddr[kk] = prow * 64 + (((2 * kk + hi) ^ s) << 4);
#pragma unroll
    for (int j = 0; j < 2; ++j) {
      const int c64 = 2 * (wr & 1) + j;
      rAddr[j] = 16384 + (wr >> 1) * 8192 + (8 * hi + q) * 256 + ((c64 ^ q) * 64) + blk * 32 + swapmid(p) * 8;
    }
  }
  __device__ __forceinline__ void stage(char LAS* buf, const bf16_t* P, const bf16_t* R) {
    const unsigned b = (unsigned)(uintptr_t)buf;
#pragma unroll
    for (int i = 0; i < 2; ++i) glds16(P, pOff[i], b + (w * 2 + i) * 1024);
#pragma unroll
    for (int i = 0; i < 2; ++i) glds16(R, rOff[i], b + 16384 + (w * 2 + i) * 1024);
  }
  __device__ __forceinline__ void compute(const char LAS* buf, f32x16 (&acc)[4][2]) {
#pragma unroll
    for (int kk = 0; kk < 2; ++kk) {
      bf16x8 pf[4], rf[2];
#pragma unroll
      for (int j = 0; j < 2; ++j) rf[j] = cat8(tr_read(buf + rAddr[j] + kk * 4096), tr_read(buf + rAddr[j] + kk * 4096 + 1024));
#pragma unroll
      for (int i = 0; i < 4; ++i) pf[i] = *(const LAS bf16x8*)(buf + pAddr[kk] + i * 2048);
#pragma unroll
      for (int i = 0; i < 4; ++i)
#pragma unroll
        for (int j = 0; j < 2; ++j) acc[i][j] = __builtin_amdgcn_mfma_f32_32x32x16_bf16(rf[j], pf[i], acc[i][j], 0, 0, 0);
    }
  }
  __device__ __forceinline__ void load_frags(const char LAS* buf, bf16x8 (&pf)[2][4], bf16x8 (&rf)[2][2]) {
#pragma unroll
    for (int kk = 0; kk < 2; ++kk) {
#pragma unroll
      for (int j = 0; j < 2; ++j) rf[kk][j] = cat8(tr_read(buf + rAddr[j] + kk * 4096), tr_read(buf + rAddr[j] + kk * 4096 + 1024));
#pragma unroll
      for (int i = 0; i < 4; ++i) pf[kk][i] = *(const LAS bf16x8*)(buf + pAddr[kk] + i * 2048);
    }
  }
  __device__ __forceinline__ void mma(const bf16x8 (&pf)[2][4], const bf16x8 (&rf)[2][2], f32x16 (&acc)[4][2]) {
#pragma unroll
    for (int kk = 0; kk < 2; ++kk)
#pragma unroll
      for (int i = 0; i < 4; ++i)
#pragma unroll
        for (int j = 0; j < 2; ++j) acc[i][j] = __builtin_amdgcn_mfma_f32_32x32x16_bf16(rf[kk][j], pf[kk][i], acc[i][j], 0, 0, 0);
  }
};
#define WAIT_LGKM0() asm volatile("s_waitcnt lgkmcnt(0)" ::: "memory")
#define SCHED0() __builtin_amdgcn_sched_barrier(0)
template <int N> __device__ __forceinline__ void wait_vm() { asm volatile("s_waitcnt vmcnt(%0)" ::"n"(N) : "memory"); }

struct Frag { bf16x8 pf[4], rf[2]; };
struct Desc { const char* p; unsigned stride; int v0, v1; };
template <int IMM> __device__ __forceinline__ void setm0(unsigned base) {
  asm volatile("s_add_u32 m0, %0, %1" ::"s"(base), "n"(IMM) : "memory", "scc");
}
template <int OFF> __device__ __forceinline__ void gl(const void* sbase, int voff) {
  asm volatile("global_load_lds_dwordx4 %0, %1 offset:%2" ::"v"(voff), "s"(sbase), "n"(OFF) : "memory");
}
template <int OFF> __device__ __forceinline__ void glm(const void* sbase, int voff, unsigned long long mask) {
  asm volatile("s_mov_b64 exec, %3\n\tglobal_load_lds_dwordx4 %0, %1 offset:%2\n\ts_mov_b64 exec, -1"
               ::"v"(voff), "s"(sbase), "n"(OFF), "s"(mask) : "memory");
}
template <int OFF> __device__ __forceinline__ void gld16(bf16x8& d, const void* sbase, int voff) {
  asm volatile("global_load_dwordx4 %0, %1, %2 offset:%3" : "=v"(d) : "v"(voff), "s"(sbase), "n"(OFF) : "memory");
}
template <int N> __device__ __forceinline__ void wait_vm4(bf16x8& a, bf16x8& b, bf16x8& c, bf16x8& d) {
  asm volatile("s_waitcnt vmcnt(%4)" : "+v"(a), "+v"(b), "+v"(c), "+v"(d) : "n"(N) : "memory");
}
__device__ __forceinline__ void pin4(f32x16& a, f32x16& b, f32x16& c, f32x16& d) {
  asm volatile("" : "+v"(a), "+v"(b), "+v"(c), "+v"(d));
}
__device__ __forceinline__ void pin3(f32x16& a, f32x16& b, f32x16& c) { asm volatile("" : "+v"(a), "+v"(b), "+v"(c)); }
__device__ __forceinline__ void atomic_inc_masked(unsigned* p, unsigned long long mask) {
  const int m = __builtin_amdgcn_readfirstlane((int)mask);
  asm volatile("s_mov_b32 exec_lo, %2\n\ts_mov_b32 exec_hi, 0\n\tglobal_atomic_add %0, %1, off\n\ts_mov_b64 exec, -1" ::"v"(p), "v"(1u), "s"(m) : "memory");
}
__device__ __forceinline__ void touch4(unsigned& keep, int voff, const void* sbase) {
  asm volatile("s_mov_b64 exec, 15\n\tglobal_load_dword %0, %1, %2\n\ts_mov_b64 exec, -1" : "+v"(keep) : "v"(voff), "s"(sbase) : "memory");
}
__device__ __forceinline__ void touch3(unsigned& keep, int voff, const void* sbase) {
  asm volatile("s_mov_b64 exec, 7\n\tglobal_load_dword %0, %1, %2\n\ts_mov_b64 exec, -1" : "+v"(keep) : "v"(voff), "s"(sbase) : "memory");
}
template <int V> using IC = std::integral_constant<int, V>;
struct Mix { Desc P, R; int d0, f0; unsigned long long rmask; };
__device__ __forceinline__ Mix mix_init(const bf16_t* hbuf, const bf16_t* w1, int mtile, int w, int l) {
  Mix M;
  M.d0 = (mtile % 5) * 128;
  M.f0 = (mtile / 5) * 96;
  int po[2], ro[2];
#pragma unroll
  for (int i = 0; i < 2; ++i) {
    const int j = w * 2 + i;
    const int krow = 4 * j + (l >> 4), s16 = l & 15;
    po[i] = (krow * DM + ((s16 >> 2) ^ (krow & 3)) * 32 + (s16 & 3) * 8) * 2 - 1024 * i;
    const int cb = j >> 2, kr = (j & 3) * 16 + (l >> 2);
    ro[i] = j < 12 ? (kr * FF + cb * 32 + (l & 3) * 8) * 2 - 1024 * i : 0;
  }
  M.P = Desc{(const char*)(hbuf + M.d0), 64u * DM * 2, po[0], po[1]};
  M.R = Desc{(const char*)(w1 + M.f0), 64u * FF * 2, ro[0], ro[1]};
  M.rmask = w >= 6 ? 1ull : ~0ull;
  return M;
}
template <int ST, int SLOT> __device__ __forceinline__ void mix_stage(const Mix& M, unsigned sW) {
  setm0<SLOT * STAGE256>(sW); SCHED0(); asm volatile("s_nop 0");
  gl<0>(M.P.p + ST * M.P.stride, M.P.v0); gl<1024>(M.P.p + ST * M.P.stride, M.P.v1);
  setm0<SLOT * STAGE256 + 16384>(sW); SCHED0(); asm volatile("s_nop 0");
  glm<0>(M.R.p + ST * M.R.stride, M.R.v0, M.rmask); glm<1024>(M.R.p + ST * M.R.stride, M.R.v1, M.rmask);
  SCHED0();
}
struct MixAcc { f32x16 acc[3]; u32x4 bias[3]; };
template <int NS, int S0 = 3> __device__ __forceinline__ void mix_main(char LAS* lds, const Mix& M, const bf16_t* __restrict__ b1,
                                                           MixAcc& A, int w, int l, unsigned* sig) {
  const unsigned sW = (unsigned)(uintptr_t)lds + w * 2048;
  const int hi = l >> 5;
  const int ws = w & 3, kh = w >> 2;
  const int blk = (l >> 4) & 1, q = (l & 15) >> 2, p = l & 3;
  const int pB = (32 * kh + 8 * hi + q) * 256 + ((ws ^ q) * 64) + blk * 32 + p * 8;
  const int rA = 16384 + (32 * kh + 8 * hi + q) * 64 + blk * 32 + swapmid(p) * 8;
  f32x16 (&acc)[3] = A.acc;
  acc[0] = (f32x16){}; acc[1] = (f32x16){}; acc[2] = (f32x16){};
  bf16x8 pa[2], ra[2][3], pb[2], rb[2][3];
  unsigned sc = S0 * STAGE256, sn = (S0 + 1) * STAGE256;
  const char* sp = M.P.p + (size_t)5 * M.P.stride;
  const char* sr = M.R.p + (size_t)5 * M.R.stride;
  auto mstep = [&](auto WN, auto ISS, const bf16x8 (&pc)[2], const bf16x8 (&rc)[2][3], bf16x8 (&pn)[2], bf16x8 (&rn)[2][3]) {
    constexpr int wn = WN, iss = ISS;
    if (wn >= 0) wait_vm<(wn >= 0 ? wn : 0)>();
    WAIT_LGKM0();
    SCHED0();
    BARRIER();
    SCHED0();
    const char LAS* nb = lds + sn;
    const unsigned sI = sW + sc;
    auto mm = [&](int k2, int jj) {
      acc[jj] = __builtin_amdgcn_mfma_f32_32x32x16_bf16(rc[k2][jj], pc[k2], acc[jj], 0, 0, 0);
      SCHED0();
    };
    auto rdP = [&](int k2) { if (wn >= 0) pn[k2] = cat8(tr_read(nb + pB + k2 * 4096), tr_read(nb + pB + k2 * 4096 + 1024)); SCHED0(); };
    auto rdR = [&](int k2, int jj) {
      if (wn >= 0) rn[k2][jj] = cat8(tr_read(nb + rA + jj * 4096 + k2 * 1024), tr_read(nb + rA + jj * 4096 + k2 * 1024 + 256));
      SCHED0();
    };
    if (iss) setm0<0>(sI); SCHED0();
    mm(0, 0); if (iss) gl<0>(sp, M.P.v0); SCHED0(); rdP(0); rdR(0, 0); rdR(0, 1);
    mm(0, 1); if (iss) { gl<1024>(sp, M.P.v1); setm0<16384>(sI); } SCHED0(); rdR(0, 2); rdP(1); rdR(1, 0);
    mm(0, 2); if (iss) glm<0>(sr, M.R.v0, M.rmask); SCHED0(); rdR(1, 1); rdR(1, 2);
    mm(1, 0); if (iss) glm<1024>(sr, M.R.v1, M.rmask); SCHED0();
    mm(1, 1);
    mm(1, 2);
    if (!iss) pin3(acc[0], acc[1], acc[2]);
    sc = sn;
    sn = sn == 4 * STAGE256 ? 0 : sn + STAGE256;
    sp += M.P.stride;
    sr += M.R.stride;
  };
  const int jk = kh == 0 ? 0 : 2;
  u32x4 (&bias)[3] = A.bias;
  bias[0] = *(const u32x4*)(b1 + M.f0 + 8 * hi + 32 * jk);
  bias[1] = *(const u32x4*)(b1 + M.f0 + 8 * hi + 32 * jk + 16);
  bias[2] = *(const u32x4*)(b1 + M.f0 + 8 * hi + 32 + 16 * kh);
  SCHED0();
  mix_stage<4, (S0 + 4) % 5>(M, sW);
  wait_vm<19 + NS>();
  BARRIER();
  SCHED0();
#pragma unroll
  for (int k2 = 0; k2 < 2; ++k2) {
    pa[k2] = cat8(tr_read(lds + sc + pB + k2 * 4096), tr_read(lds + sc + pB + k2 * 4096 + 1024));
#pragma unroll
    for (int j = 0; j < 3; ++j) ra[k2][j] = cat8(tr_read(lds + sc + rA + j * 4096 + k2 * 1024), tr_read(lds + sc + rA + j * 4096 + k2 * 1024 + 256));
  }
  SCHED0();
  mstep(IC<15 + NS>{}, IC<1>{}, pa, ra, pb, rb);
  mstep(IC<15>{}, IC<1>{}, pb, rb, pa, ra);
  if (FUSE3 == 1) atomic_inc_masked(sig, w == 0 ? 3ull : 0ull);
  mstep(IC<15>{}, IC<1>{}, pa, ra, pb, rb);
#pragma unroll 1
  for (int m = 3; m < 27; m += 2) {
    mstep(IC<12>{}, IC<1>{}, pb, rb, pa, ra);
    mstep(IC<12>{}, IC<1>{}, pa, ra, pb, rb);
  }
  mstep(IC<12>{}, IC<0>{}, pb, rb, pa, ra);
  mstep(IC<8>{}, IC<0>{}, pa, ra, pb, rb);
  mstep(IC<4>{}, IC<0>{}, pb, rb, pa, ra);
  mstep(IC<0>{}, IC<0>{}, pa, ra, pb, rb);
  mstep(IC<-1>{}, IC<0>{}, pb, rb, pa, ra);
  asm volatile("" : "+v"(bias[0]), "+v"(bias[1]), "+v"(bias[2]));
  WAIT_LGKM0();
  TS(1, 5);
  BARRIER();
}
__device__ __forceinline__ void mix_fin(char LAS* xa, const Mix& M, const MixAcc& A, bf16_t* Uo, int w, int l) {
  const int r32 = l & 31, hi = l >> 5;
  const int ws = w & 3, kh = w >> 2;
  const f32x16 (&acc)[3] = A.acc;
  const u32x4 (&bias)[3] = A.bias;
  float LAS* dst = (float LAS*)(xa + w * 6144);
  const float LAS* src = (const float LAS*)(xa + (w ^ 4) * 6144);
  bf16_t* urow = Uo + (size_t)(M.d0 + 32 * ws + r32) * FF + M.f0 + 8 * hi;
  auto fin8 = [&](const float (&v)[8], int fo, const u32x4 bv) {
    float r[8];
#pragma unroll
    for (int e = 0; e < 4; ++e) {
      const unsigned t = pack2a(v[2 * e], v[2 * e + 1]);
      const unsigned u = pack2(__uint_as_float(t << 16) + __uint_as_float(bv[e] << 16),
                               __uint_as_float(t & 0xffff0000u) + __uint_as_float(bv[e] & 0xffff0000u));
      r[2 * e] = gelu_tanh(__uint_as_float(u << 16));
      r[2 * e + 1] = gelu_tanh(__uint_as_float(u & 0xffff0000u));
    }
    const u32x4 pk = {pack2(r[0], r[1]), pack2(r[2], r[3]), pack2(r[4], r[5]), pack2(r[6], r[7])};
    if (LINEST & 4) *(u32x4 LAS*)((char LAS*)src + r32 * 96 + (fo - 48 * kh + 8 * hi) * 2) = pk;
    else st16h(urow + fo, pk);
  };
  auto fin = [&](auto KH_) {
    constexpr int KH = KH_, SA = KH == 0 ? 2 : 0, KA = 2 - SA, S1 = KH == 0 ? 8 : 0, K1 = 8 - S1;
#pragma unroll
    for (int g = 0; g < 4; ++g)
      *(f32x4 LAS*)(dst + (g * 64 + l) * 4) = (f32x4){acc[SA][4 * g], acc[SA][4 * g + 1], acc[SA][4 * g + 2], acc[SA][4 * g + 3]};
#pragma unroll
    for (int g = 0; g < 2; ++g)
      *(f32x4 LAS*)(dst + ((4 + g) * 64 + l) * 4) = (f32x4){acc[1][S1 + 4 * g], acc[1][S1 + 4 * g + 1], acc[1][S1 + 4 * g + 2], acc[1][S1 + 4 * g + 3]};
    WAIT_LGKM0();
    BARRIER();
    f32x4 o[6];
#pragma unroll
    for (int g = 0; g < 6; ++g) o[g] = *(const f32x4 LAS*)(src + (g * 64 + l) * 4);
    asm volatile("" ::: "memory");
#pragma unroll
    for (int hh = 0; hh < 2; ++hh) {
      float v[8];
#pragma unroll
      for (int e = 0; e < 8; ++e) v[e] = acc[KA][8 * hh + e] + o[2 * hh + (e >> 2)][e & 3];
      fin8(v, 32 * KA + 16 * hh, bias[hh]);
    }
    float v[8];
#pragma unroll
    for (int e = 0; e < 8; ++e) v[e] = acc[1][K1 + e] + o[4 + (e >> 2)][e & 3];
    fin8(v, 32 + 16 * KH, bias[2]);
  };
  if (kh == 0) fin(IC<0>{});
  else fin(IC<1>{});
  if (LINEST & 4) {
    bf16_t* ub = Uo + (size_t)(M.d0 + 32 * ws) * FF + M.f0 + 48 * kh;
    u32x4 t[3];
#pragma unroll
    for (int i = 0; i < 3; ++i) t[i] = *(const u32x4 LAS*)((const char LAS*)src + (i * 64 + l) * 16);
#pragma unroll
    for (int i = 0; i < 3; ++i) {
      const int n = i * 64 + l, row = n / 6;
      st16h(ub + row * FF + (n - 6 * row) * 8, t[i]);
    }
  }
}
template <int CTRL, int RM> __device__ __forceinline__ float dpp_add(float v) {
  return v + __int_as_float(__builtin_amdgcn_update_dpp(0, __float_as_int(v), CTRL, RM, 0xf, false));
}
__device__ __forceinline__ float wave_total(float v) {
  v = dpp_add<0x111, 0xf>(v); v = dpp_add<0x112, 0xf>(v); v = dpp_add<0x114, 0xf>(v); v = dpp_add<0x118, 0xf>(v);
  v = dpp_add<0x142, 0xa>(v); v = dpp_add<0x143, 0xc>(v);
  return __int_as_float(__builtin_amdgcn_readlane(__float_as_int(v), 63));
}
__device__ __forceinline__ f32x2 unpk(unsigned u) { return (f32x2){__uint_as_float(u << 16), __uint_as_float(u & 0xffff0000u)}; }
__device__ __forceinline__ void ln_row_pk(unsigned (&u)[5]) {
  f32x2 v[5];
#pragma unroll
  for (int i = 0; i < 5; ++i) v[i] = unpk(u[i]);
  const f32x2 s2 = ((v[0] + v[1]) + (v[2] + v[3])) + v[4];
  const float mean = wave_total(s2[0] + s2[1]) * (1.0f / DM);
  f32x2 q2 = {0.f, 0.f};
#pragma unroll
  for (int i = 0; i < 5; ++i) { const f32x2 d = v[i] - mean; q2 = __builtin_elementwise_fma(d, d, q2); }
  const float q = wave_total(q2[0] + q2[1]);
  const float mb = rbf(mean);
  const float vb = rbf(q * (1.0f / DM));
  const float den = rbf(sqrtf(rbf(vb + 1e-5f)));
#pragma unroll
  for (int i = 0; i < 5; ++i) { const f32x2 d = v[i] - mb; v[i] = unpk(pack2(d[0], d[1])); }
  if (den > 1e-3f && den < 1e30f) {
    float r = __builtin_amdgcn_rcpf(den);
    r = fmaf(fmaf(-den, r, 1.0f), r, r);
    const f32x2 r2 = {r, r}, nd2 = {-den, -den};
#pragma unroll
    for (int i = 0; i < 5; ++i) {
      f32x2 t = v[i] * r2;
      t = __builtin_elementwise_fma(__builtin_elementwise_fma(t, nd2, v[i]), r2, t);
      u[i] = pack2(t[0], t[1]);
    }
  } else {
#pragma unroll
    for (int i = 0; i < 5; ++i) u[i] = pack2(v[i][0] / den, v[i][1] / den);
  }
}
struct LnS { f32x2 v[5], q2; float a, mean, den, r; };
template <int K> __device__ __forceinline__ void ln_red(float& a) {
  if (K == 0) a = dpp_add<0x111, 0xf>(a);
  if (K == 1) a = dpp_add<0x112, 0xf>(a);
  if (K == 2) a = dpp_add<0x114, 0xf>(a);
  if (K == 3) a = dpp_add<0x118, 0xf>(a);
  if (K == 4) a = dpp_add<0x142, 0xa>(a);
  if (K == 5) a = dpp_add<0x143, 0xc>(a);
}
template <int K> __device__ __forceinline__ void ln_slice(LnS& s, const unsigned (&in)[5], unsigned (&out)[5]) {
  if (K == 0) { s.v[0] = unpk(in[0]); s.v[1] = unpk(in[1]); s.q2 = s.v[0] + s.v[1]; }
  if (K == 1) { s.v[2] = unpk(in[2]); s.v[3] = unpk(in[3]); s.q2 += s.v[2] + s.v[3]; }
  if (K == 2) { s.v[4] = unpk(in[4]); s.q2 += s.v[4]; s.a = s.q2[0] + s.q2[1]; }
  if (K >= 3 && K < 9) ln_red<K - 3>(s.a);
  if (K == 9) s.mean = __int_as_float(__builtin_amdgcn_readlane(__float_as_int(s.a), 63)) * (1.0f / DM);
  if (K == 10) { const f32x2 d0 = s.v[0] - s.mean, d1 = s.v[1] - s.mean; s.q2 = __builtin_elementwise_fma(d1, d1, d0 * d0); }
  if (K == 11) {
    const f32x2 d0 = s.v[2] - s.mean, d1 = s.v[3] - s.mean;
    s.q2 = __builtin_elementwise_fma(d1, d1, __builtin_elementwise_fma(d0, d0, s.q2));
  }
  if (K == 12) { const f32x2 d = s.v[4] - s.mean; s.q2 = __builtin_elementwise_fma(d, d, s.q2); s.a = s.q2[0] + s.q2[1]; s.mean = rbf(s.mean); }
  if (K >= 13 && K < 19) ln_red<K - 13>(s.a);
  if (K >= 13 && K < 18) { const f32x2 d = s.v[K - 13] - s.mean; s.v[K - 13] = unpk(pack2a(d[0], d[1])); }
  if (K == 19) s.a = rbf(rbf(__int_as_float(__builtin_amdgcn_readlane(__float_as_int(s.a), 63)) * (1.0f / DM)) + 1e-5f);
  if (K == 20) s.den = rbf(sqrtf(s.a));
  if (K == 21) { const float r = __builtin_amdgcn_rcpf(s.den); s.r = fmaf(fmaf(-s.den, r, 1.0f), r, r); }
  if (K >= 22 && K < 27) {
    const f32x2 a = s.v[K - 22], r2 = {s.r, s.r}, nd2 = {-s.den, -s.den};
    f32x2 t = a * r2;
    t = __builtin_elementwise_fma(__builtin_elementwise_fma(t, nd2, a), r2, t);
    out[K - 22] = pack2a(__builtin_amdgcn_div_fixupf(t[0], s.den, a[0]), __builtin_amdgcn_div_fixupf(t[1], s.den, a[1]));
  }
}
extern "C" __global__ __launch_bounds__(512, 2) void gemm1_kernel(const bf16_t* __restrict__ xn, const bf16_t* __restrict__ hbuf,
                                                       const bf16_t* __restrict__ ckv, const bf16_t* __restrict__ wq,
                                                       const bf16_t* __restrict__ wuk, const bf16_t* __restrict__ wuv,
                                                       const bf16_t* __restrict__ w1, const bf16_t* __restrict__ b1,
                                                       const bf16_t* __restrict__ wo, const bf16_t* __restrict__ w2,
                                                       bf16_t* __restrict__ Qo, bf16_t* __restrict__ Ko,
                                                       bf16_t* __restrict__ Vo, bf16_t* __restrict__ Uo,
                                                       const bf16_t* __restrict__ x, unsigned* __restrict__ cnt) {
  __shared__ __attribute__((aligned(16))) char smem[4 * STAGE256 + 32768];
  char LAS* lds = (char LAS*)smem;
  const int xcd = blockIdx.x & 7, c = blockIdx.x >> 3;
  const int l = threadIdx.x & 63, w = __builtin_amdgcn_readfirstlane(threadIdx.x >> 6);
  const unsigned sW = (unsigned)(uintptr_t)lds + w * 2048;
  const int r32 = l & 31, hi = l >> 5;
  TS(0, 0);
  unsigned keepc = 0, keept = 0, xb;
  u32x4 xa;
  unsigned long long ca, cb_, cc;
  CODE_ADDR("gemm1_kernel", ca); CODE_ADDR("attn_kernel", cb_); CODE_ADDR("gemm2_kernel", cc);
  {
    const unsigned long long p = ca + threadIdx.x * 128;
    if ((CODE_PF & 1) && cb_ > ca && p < cb_) asm volatile("global_load_dword %0, %1, off" : "+v"(keepc) : "v"(p) : "memory");
  }
  const int lnrow = blockIdx.x * 8 + w;
  asm volatile("global_load_dwordx4 %0, %2, %4\n\tglobal_load_dword %1, %3, %4 offset:1024"
               : "=&v"(xa), "=&v"(xb) : "v"(l * 16), "v"(l * 4), "s"(x + (size_t)lnrow * DM) : "memory");
  const int mbase = (xcd * 90) >> 3, mcnt = (((xcd + 1) * 90) >> 3) - mbase;
  constexpr bool hasMix = false;
  const bool single = c >= mcnt;
  const int mtile = mbase + c;
  const int spare = xcd * 32 - mbase + (c - mcnt);
  const int d0 = (mtile % 5) * 128, f0 = (mtile / 5) * 96;
  Desc MP, MR;
  {
    int po[2], ro[2];
#pragma unroll
    for (int i = 0; i < 2; ++i) {
      const int j = w * 2 + i;
      const int krow = 4 * j + (l >> 4), s16 = l & 15;
      po[i] = (krow * DM + ((s16 >> 2) ^ (krow & 3)) * 32 + (s16 & 3) * 8) * 2 - 1024 * i;
      const int cb = j >> 2, kr = (j & 3) * 16 + (l >> 2);
      ro[i] = j < 12 ? (kr * FF + cb * 32 + (l & 3) * 8) * 2 - 1024 * i : 0;
    }
    MP = Desc{(const char*)(hbuf + d0), 64u * DM * 2, po[0], po[1]};
    MR = Desc{(const char*)(w1 + f0), 64u * FF * 2, ro[0], ro[1]};
  }
  {
    struct Tile { const char* A; const char* W; bf16_t* O; int ngt; };
    auto kvt = [&](int id) {
      const int hp = xcd * 2 + (id & 1), tb = id >> 2;
      return Tile{(const char*)(ckv + (size_t)tb * 256 * (CDIR ? DC : 32)), (const char*)(((id & 2) ? wuv : wuk) + hp * 256),
                  ((id & 2) ? Vo : Ko) + ((size_t)hp * 2 * S_LEN + tb * 256) * HD, DC / 128};
    };
    auto qt = [&](int id) {
      const int hp = xcd * 2 + (id & 1), tb = id >> 1;
      return Tile{(const char*)(xn + (size_t)tb * 256 * 32), (const char*)(wq + hp * 256), Qo + ((size_t)hp * 2 * S_LEN + tb * 256) * HD, DM / 128};
    };
    const int ntile = 1;
    const Tile T0 = kvt(c);
    const Tile T1 = T0;
    Core256 g;
    g.init(CDIR ? DC : 32, HDN);
    constexpr unsigned PSTR = CDIR ? 64u : S_LEN * 64u;
    const char* Ap = T0.A;
    const char* Wp = T0.W;
    const int wp = w >> 2, wr = w & 3;
    constexpr unsigned RSTR = 32u * HDN * 2;
    f32x16 acc[4][2];
    Frag F0, F1;
    LnS lns;
    const char* tp = (const char*)w1;
    const int tv = (((int)blockIdx.x * 8 + w) * 4 + (l & 3)) * 128;
    unsigned u0[5], u1[5], u2[5];
    auto half = [&](auto first, auto dst, auto WS, const Frag& cur, Frag& nxt, const char LAS* nb, int nkk, const char* sp, int v0, int v1) {
      auto wk = [&](auto KK) {
        constexpr int k = (int)WS + (int)KK;
        if (LNPOS == 9 && WS >= 0) {
          if (k < 32) ln_slice<(k & 31)>(lns, u0, u1);
          else ln_slice<(k & 31)>(lns, u1, u2);
          SCHED0();
        }
      };
      auto mm = [&](int i, int j) {
        acc[i][j] = mf<X16_G1>(cur.rf[j], cur.pf[i], first ? (f32x16){} : acc[i][j], nkk ^ 1);
        SCHED0();
      };
      auto rdR = [&](int j) {
        nxt.rf[j] = cat8(tr_read(nb + g.rAddr[j] + nkk * 4096), tr_read(nb + g.rAddr[j] + nkk * 4096 + 1024));
        SCHED0();
      };
      auto rdP = [&](int i) {
        nxt.pf[i] = *(const LAS bf16x8*)(nb + g.pAddr[nkk] + i * 2048);
        SCHED0();
      };
      mm(0, 0); rdR(0); wk(IC<0>{});
      mm(0, 1); rdP(0); rdR(1);
      mm(1, 0); rdP(1); wk(IC<1>{});
      mm(1, 1); rdP(2);
      mm(2, 0); rdP(3); wk(IC<2>{}); if (dst >= 0) setm0<(dst >= 0 ? (int)dst : 0)>(sW); SCHED0();
      mm(2, 1); if (dst >= 0) gl<0>(sp, v0); SCHED0();
      mm(3, 0); if (dst >= 0) gl<1024>(sp, v1); SCHED0(); wk(IC<3>{});
      mm(3, 1);
    };
    auto group = [&](auto VAR, bool second, const Desc& Rc, const Desc& Pn, const Desc& Rn) {
      auto stp = [&](auto J) {
        constexpr int j = J, var = VAR;
        constexpr int dA = (var == 2 && j > 0) ? -1 : ((j + 3) & 3) * STAGE256 + 16384;
        constexpr int dB = (var == 2 && j > 0) ? -1 : j * STAGE256;
        const char LAS* cb = lds + j * STAGE256;
        const char LAS* nb = lds + ((j + 1) & 3) * STAGE256;
        constexpr int wsA = var == 1 ? -1 : var * 16 + j * 8, wsB = var == 1 ? -1 : var * 16 + j * 8 + 4;
        if (j == 0) half(std::integral_constant<bool, var == 0>{}, IC<dA>{}, IC<wsA>{}, F0, F1, cb, 1, Rc.p, Rc.v0, Rc.v1);
        else half(std::false_type{}, IC<dA>{}, IC<wsA>{}, F0, F1, cb, 1, Rn.p + (j - 1) * Rn.stride, Rn.v0, Rn.v1);
        if (var == 2) { if (j == 0) wait_vm<8>(); else if (j == 1) wait_vm<6>(); else if (j == 2) wait_vm<2>(); }
        else if (var == 0 && j < 2) { if (PLB == 2) wait_vm<(LNPOS < 0 ? 12 : 8) + K1PF>(); else if (second) wait_vm<24>(); else wait_vm<8>(); }
        else if (var == 1 && FUSE3 == 2 && W1PF == 4) wait_vm<8 + (j < 2 ? j : 2)>();
        else wait_vm<8>();
        if (var != 2 || j < 3) {
          WAIT_LGKM0();
          SCHED0();
          BARRIER();
        }
        SCHED0();
        if (var == 1 && FUSE3 == 2 && W1PF == 4) {
          touch4(keept, tv, tp);
          tp += 8192 * 128;
          if (tp >= (const char*)w1 + 6 * 8192 * 128 + (blockIdx.x < 192 ? 1 : 0)) tp = (const char*)w1;
          SCHED0();
        }
        half(std::false_type{}, IC<dB>{}, IC<wsB>{}, F1, F0, nb, 0, Pn.p + j * Pn.stride, Pn.v0, Pn.v1);
      };
      stp(IC<0>{}); stp(IC<1>{}); stp(IC<2>{}); stp(IC<3>{});
    };
    const int pv0 = g.pOff[0], pv1 = g.pOff[1] - 1024, rv0 = g.rOff[0], rv1 = g.rOff[1] - 1024;
    setm0<0>(sW); SCHED0(); asm volatile("s_nop 0"); gl<0>(Ap, pv0); gl<1024>(Ap, pv1);
    setm0<16384>(sW); SCHED0(); asm volatile("s_nop 0"); gl<0>(Wp, rv0); gl<1024>(Wp, rv1);
    setm0<STAGE256>(sW); SCHED0(); asm volatile("s_nop 0"); gl<0>(Ap + PSTR, pv0); gl<1024>(Ap + PSTR, pv1);
    setm0<STAGE256 + 16384>(sW); SCHED0(); asm volatile("s_nop 0"); gl<0>(Wp + RSTR, rv0); gl<1024>(Wp + RSTR, rv1);
    setm0<2 * STAGE256>(sW); SCHED0(); asm volatile("s_nop 0"); gl<0>(Ap + 2 * PSTR, pv0); gl<1024>(Ap + 2 * PSTR, pv1);
    setm0<2 * STAGE256 + 16384>(sW); SCHED0(); asm volatile("s_nop 0"); gl<0>(Wp + 2 * RSTR, rv0); gl<1024>(Wp + 2 * RSTR, rv1);
    setm0<3 * STAGE256>(sW); SCHED0(); asm volatile("s_nop 0"); gl<0>(Ap + 3 * PSTR, pv0); gl<1024>(Ap + 3 * PSTR, pv1);
    unsigned u[5];
    auto ln1 = [&]() {
      if (LNPOS == 9) {
#pragma unroll
        for (int i = 0; i < 5; ++i) u[i] = u1[i];
      } else {
        u[0] = xa[0]; u[1] = xa[1]; u[2] = xa[2]; u[3] = xa[3]; u[4] = xb;
        ln_row_pk(u);
      }
      bf16_t* xo = Qo + ((size_t)(lnrow >> 5) * 80 * 256 + (lnrow & 31) * 8);
      *(u32x4*)(xo + l * 256) = (u32x4){u[0], u[1], u[2], u[3]};
      *(unsigned*)(xo + (64 + (l >> 2)) * 256 + (l & 3) * 2) = u[4];
      SCHED0();
    };
    auto ln2 = [&]() {
      if (LNPOS == 9) {
#pragma unroll
        for (int i = 0; i < 5; ++i) u[i] = u2[i];
      } else ln_row_pk(u);
      bf16_t* ho = Uo + (size_t)lnrow * DM;
      *(u32x4*)(ho + l * 8) = (u32x4){u[0], u[1], u[2], u[3]};
      *(unsigned*)(ho + 512 + l * 2) = u[4];
      SCHED0();
    };
    if (LNPOS < 0) {
      asm volatile("s_waitcnt vmcnt(14)" : "+v"(xa), "+v"(xb), "+v"(keepc)::"memory");
      ln1();
      ln2();
    }
    Desc PT{Ap, 0u, 0, 0};
    if (w < 5) {
      const int o = (l >> 5) * HDN * 2 + ((l >> 2) & 7) * 128 + (l & 3) * 16;
      PT = Desc{(const char*)(wq + (size_t)(c * 20 + w * 4) * HDN + xcd * 512), 0u, o, o + 2 * HDN * 2 - 1024};
    } else if (w < 7) {
      if (FUSE3 == 2 && W1PF == 1) {
        const int i0 = ((int)blockIdx.x * 4 + (w - 5) * 2) * 64 + l, i1 = i0 + 64;
        PT = Desc{(const char*)w1, 0u, i0 < 55296 ? i0 * 128 : 0, (i1 < 55296 ? i1 * 128 : 1024) - 1024};
      }
    } else if (w == 7 && (CODE_PF & 2) && cc > cb_ && cc - cb_ <= 65536) {
      int o = (c * 16 + (l >> 2)) * 128;
      if (o > (int)(cc - cb_) - 16) o = 0;
      PT = Desc{(const char*)cb_ - 1024, 0u, o + 1024, o};
    }
    asm volatile("s_waitcnt vmcnt(%3)" : "+v"(xa), "+v"(xb), "+v"(keepc) : "n"(LNPOS < 0 ? 14 : 10) : "memory");
    BARRIER();
    u0[0] = xa[0]; u0[1] = xa[1]; u0[2] = xa[2]; u0[3] = xa[3]; u0[4] = xb;
    if (K1PF >= 1) asm volatile("global_load_dword %0, %1, %2" : "+v"(keept) : "v"((int)threadIdx.x * 128), "s"(Ap + (c & 3) * 65536) : "memory");
    if (K1PF >= 2)
      asm volatile("global_load_dword %0, %1, %2" : "+v"(keept)
                   : "v"(((int)threadIdx.x >> 3) * HDN * 2 + (((int)threadIdx.x >> 1) & 3) * 128), "s"(Wp + (size_t)(c >> 2) * 64 * HDN * 2) : "memory");
#pragma unroll
    for (int j = 0; j < 2; ++j) F0.rf[j] = cat8(tr_read(lds + g.rAddr[j]), tr_read(lds + g.rAddr[j] + 1024));
#pragma unroll
    for (int i = 0; i < 4; ++i) F0.pf[i] = *(const LAS bf16x8*)(lds + g.pAddr[0] + i * 2048);
    SCHED0();
    for (int t = 0; t < ntile; ++t) {
      const Tile T = t ? T1 : T0;
      const int ngt = T.ngt;
      auto run = [&](auto VAR, int gi) {
        const Desc Rc{T.W + (size_t)(4 * gi + 3) * RSTR, RSTR, rv0, rv1};
        const bool tl = gi + 1 == ngt;
        const bool sl = tl && t + 1 == ntile;
        Desc Pn{tl ? T1.A : T.A + (size_t)(gi + 1) * 4 * PSTR, PSTR, pv0, pv1};
        Desc Rn{tl ? T1.W : T.W + (size_t)(4 * gi + 4) * RSTR, RSTR, rv0, rv1};
        if (sl && hasMix) { Pn = MP; Rn = MR; }
        if (sl) Pn = PT;
        SCHED0();
        group(VAR, t > 0, Rc, Pn, Rn);
      };
      if (K1TS == 0) TS(0, 4);
      run(IC<0>{}, 0);
      if (K1TS == 2) TS(0, 4);
      asm volatile("" : "+v"(keept));
      for (int gi = 1; gi < 3; ++gi) {
        if (FUSE3 == 2 && W1PF >= 2 && W1PF < 4 && gi == W1PF - 1 && l < 27)
          asm volatile("global_load_dword %0, %1, %2" : "+v"(keept) : "v"((((int)blockIdx.x * 8 + w) * 27 + l) * 128), "s"(w1) : "memory");
        run(IC<1>{}, gi);
      }
      if (K1TS == 2) TS(0, 5);
      run(IC<2>{}, 3);
      asm volatile("" : "+v"(keept));
      if (K1TS == 0) TS(0, 5);
      if (LNPOS == 9) { ln1(); ln2(); }
      if (hasMix) {
        setm0<3 * STAGE256 + 16384>(sW); SCHED0(); asm volatile("s_nop 0");
        gl<0>(MR.p + 3 * MR.stride, MR.v0); gl<1024>(MR.p + 3 * MR.stride, MR.v1);
      }
      char LAS* slab = lds + 4 * STAGE256 + w * 4096;
      const int wa = r32 * 128, wsw = r32 & 7;
      const int rrow = l >> 3, ra = rrow * 128 + (((l & 7) ^ rrow) << 4);
      bf16_t* obase = T.O + ((size_t)(wr >> 1) * S_LEN + 128 * wp + rrow) * HD + 64 * (wr & 1) + (l & 7) * 8;
#pragma unroll
      for (int i = 0; i < 4; ++i) {
#pragma unroll
        for (int j = 0; j < 2; ++j) {
          const f32x16 a = acc[i][j];
          u32x4 v0 = {pack2(a[0], a[1]), pack2(a[2], a[3]), pack2(a[4], a[5]), pack2(a[6], a[7])};
          u32x4 v1 = {pack2(a[8], a[9]), pack2(a[10], a[11]), pack2(a[12], a[13]), pack2(a[14], a[15])};
          *(u32x4 LAS*)(slab + wa + (((4 * j + hi) ^ wsw) << 4)) = v0;
          *(u32x4 LAS*)(slab + wa + (((4 * j + 2 + hi) ^ wsw) << 4)) = v1;
        }
        u32x4 vv[4];
#pragma unroll
        for (int it = 0; it < 4; ++it) vv[it] = *(const u32x4 LAS*)(slab + ra + it * 1024);
#pragma unroll
        for (int it = 0; it < 4; ++it) {
          const u32x4 v = vv[it];
          if (PLB == 2 && K1SC) st16sc(obase + (size_t)(32 * i + 8 * it) * HD, v);
          else st16(obase + (size_t)(32 * i + 8 * it) * HD, v);
        }
        SCHED0();
        if (i == LNPOS) ln1();
        if (i == LNPOS2) ln2();
      }
      SCHED0();
    }
  }
  TS(0, 1);
  if (!hasMix) wait_vm<(ST_MODE == 9 ? 0 : (PLB == 2 && LNPOS >= 0) ? 20 : 16)>();
  if (blockIdx.x == 0 && threadIdx.x < 192) cnt[threadIdx.x * 32] = 0u;
  if (XWAVE && blockIdx.x == 1) cnt[(threadIdx.x >> 3) * 32 + (threadIdx.x & 7)] = 0u;
  if (hasMix) {
    const int ws = w & 3, kh = w >> 2;
    const int blk = (l >> 4) & 1, q = (l & 15) >> 2, p = l & 3;
    const int pB = (32 * kh + 8 * hi + q) * 256 + ((ws ^ q) * 64) + blk * 32 + p * 8;
    const int rA = 16384 + (32 * kh + 8 * hi + q) * 64 + blk * 32 + swapmid(p) * 8;
    f32x16 acc[3] = {};
    bf16x8 pa[2], ra[2][3], pb[2], rb[2][3];
    unsigned sc = 0, sn = STAGE256;
    const char* sp = MP.p + (size_t)5 * MP.stride;
    const char* sr = MR.p + (size_t)5 * MR.stride;
    const unsigned long long rmask = (MX_NOFILL && w >= 6) ? 1ull : ~0ull;
    int rleft = (MX_PF && w == 6) ? 26 - MX_PF : 32;
    auto mstep = [&](auto WN, auto ISS, const bf16x8 (&pc)[2], const bf16x8 (&rc)[2][3], bf16x8 (&pn)[2], bf16x8 (&rn)[2][3]) {
      constexpr int wn = WN, iss = ISS;
      if (wn >= 0) wait_vm<(wn >= 0 ? wn : 0)>();
      WAIT_LGKM0();
      SCHED0();
      BARRIER();
      SCHED0();
      const char LAS* nb = lds + sn;
      const unsigned sI = sW + sc;
      auto mm = [&](int k2, int jj) {
        acc[jj] = mf<X16_G1>(rc[k2][jj], pc[k2], acc[jj], k2);
        SCHED0();
      };
      auto rdP = [&](int k2) { if (wn >= 0) pn[k2] = cat8(tr_read(nb + pB + k2 * 4096), tr_read(nb + pB + k2 * 4096 + 1024)); SCHED0(); };
      auto rdR = [&](int k2, int jj) {
        if (wn >= 0) rn[k2][jj] = cat8(tr_read(nb + rA + jj * 4096 + k2 * 1024), tr_read(nb + rA + jj * 4096 + k2 * 1024 + 256));
        SCHED0();
      };
      if (iss) setm0<0>(sI); SCHED0();
      mm(0, 0); if (iss) gl<0>(sp, MP.v0); SCHED0(); rdP(0); rdR(0, 0); rdR(0, 1);
      mm(0, 1); if (iss) { gl<1024>(sp, MP.v1); setm0<16384>(sI); } SCHED0(); rdR(0, 2); rdP(1); rdR(1, 0);
      mm(0, 2); if (iss) glm<0>(sr, MR.v0, rmask); SCHED0(); rdR(1, 1); rdR(1, 2);
      mm(1, 0); if (iss) glm<1024>(sr, MR.v1, rmask); SCHED0();
      mm(1, 1);
      mm(1, 2);
      if (!iss) asm volatile("" : "+v"(acc[0]), "+v"(acc[1]), "+v"(acc[2]));
      sc = sn;
      sn = sn == 4 * STAGE256 ? 0 : sn + STAGE256;
      sp += MP.stride;
      sr += rleft > 0 ? MR.stride : 0u;
      --rleft;
    };
    wait_vm<28>();
    BARRIER();
    const int jk = kh == 0 ? 0 : 2;
    u32x4 bias[3];
    bias[0] = *(const u32x4*)(b1 + f0 + 8 * hi + 32 * jk);
    bias[1] = *(const u32x4*)(b1 + f0 + 8 * hi + 32 * jk + 16);
    bias[2] = *(const u32x4*)(b1 + f0 + 8 * hi + 32 + 16 * kh);
    SCHED0();
    setm0<4 * STAGE256>(sW); SCHED0(); asm volatile("s_nop 0");
    gl<0>(MP.p + 4 * MP.stride, MP.v0); gl<1024>(MP.p + 4 * MP.stride, MP.v1);
    setm0<4 * STAGE256 + 16384>(sW); SCHED0(); asm volatile("s_nop 0");
    glm<0>(MR.p + 4 * MR.stride, MR.v0, rmask); glm<1024>(MR.p + 4 * MR.stride, MR.v1, rmask);
    SCHED0();
#pragma unroll
    for (int k2 = 0; k2 < 2; ++k2) {
      pa[k2] = cat8(tr_read(lds + pB + k2 * 4096), tr_read(lds + pB + k2 * 4096 + 1024));
#pragma unroll
      for (int j = 0; j < 3; ++j) ra[k2][j] = cat8(tr_read(lds + rA + j * 4096 + k2 * 1024), tr_read(lds + rA + j * 4096 + k2 * 1024 + 256));
    }
    SCHED0();
    mstep(IC<31>{}, IC<1>{}, pa, ra, pb, rb);
    mstep(IC<31>{}, IC<1>{}, pb, rb, pa, ra);
    mstep(IC<31>{}, IC<1>{}, pa, ra, pb, rb);
#pragma unroll 1
    for (int m = 3; m < 27; m += 2) {
      mstep(IC<12>{}, IC<1>{}, pb, rb, pa, ra);
      mstep(IC<12>{}, IC<1>{}, pa, ra, pb, rb);
    }
    mstep(IC<12>{}, IC<0>{}, pb, rb, pa, ra);
    mstep(IC<8>{}, IC<0>{}, pa, ra, pb, rb);
    mstep(IC<4>{}, IC<0>{}, pb, rb, pa, ra);
    mstep(IC<0>{}, IC<0>{}, pa, ra, pb, rb);
    mstep(IC<-1>{}, IC<0>{}, pb, rb, pa, ra);
    WAIT_LGKM0();
    TS(0, 3);
    BARRIER();
    float LAS* dst = (float LAS*)(lds + w * 8192);
    const float LAS* src = (const float LAS*)(lds + (w ^ 4) * 8192);
    bf16_t* urow = Uo + (size_t)(d0 + 32 * ws + r32) * FF + f0 + 8 * hi;
    auto fin8 = [&](const float (&v)[8], int fo, const u32x4 bv) {
      float r[8];
#pragma unroll
      for (int e = 0; e < 4; ++e) {
        const unsigned t = pack2a(v[2 * e], v[2 * e + 1]);
        const unsigned u = pack2(__uint_as_float(t << 16) + __uint_as_float(bv[e] << 16),
                                 __uint_as_float(t & 0xffff0000u) + __uint_as_float(bv[e] & 0xffff0000u));
        r[2 * e] = gelu_tanh(__uint_as_float(u << 16));
        r[2 * e + 1] = gelu_tanh(__uint_as_float(u & 0xffff0000u));
      }
      st16(urow + fo, (u32x4){pack2(r[0], r[1]), pack2(r[2], r[3]), pack2(r[4], r[5]), pack2(r[6], r[7])});
    };
    auto fin = [&](auto KH_) {
      constexpr int KH = KH_, SA = KH == 0 ? 2 : 0, KA = 2 - SA, S1 = KH == 0 ? 8 : 0, K1 = 8 - S1;
#pragma unroll
      for (int g = 0; g < 4; ++g)
        *(f32x4 LAS*)(dst + (g * 64 + l) * 4) = (f32x4){acc[SA][4 * g], acc[SA][4 * g + 1], acc[SA][4 * g + 2], acc[SA][4 * g + 3]};
#pragma unroll
      for (int g = 0; g < 2; ++g)
        *(f32x4 LAS*)(dst + ((4 + g) * 64 + l) * 4) = (f32x4){acc[1][S1 + 4 * g], acc[1][S1 + 4 * g + 1], acc[1][S1 + 4 * g + 2], acc[1][S1 + 4 * g + 3]};
      WAIT_LGKM0();
      BARRIER();
      f32x4 o[6];
#pragma unroll
      for (int g = 0; g < 6; ++g) o[g] = *(const f32x4 LAS*)(src + (g * 64 + l) * 4);
#pragma unroll
      for (int hh = 0; hh < 2; ++hh) {
        float v[8];
#pragma unroll
        for (int e = 0; e < 8; ++e) v[e] = acc[KA][8 * hh + e] + o[2 * hh + (e >> 2)][e & 3];
        fin8(v, 32 * KA + 16 * hh, bias[hh]);
      }
      float v[8];
#pragma unroll
      for (int e = 0; e < 8; ++e) v[e] = acc[1][K1 + e] + o[4 + (e >> 2)][e & 3];
      fin8(v, 32 + 16 * KH, bias[2]);
    };
    if (kh == 0) fin(IC<0>{});
    else fin(IC<1>{});
  }
  TS(0, 2);
}

__device__ __forceinline__ int lane_id() {
  int l;
  asm volatile("v_mbcnt_lo_u32_b32 %0, -1, 0\n\tv_mbcnt_hi_u32_b32 %0, -1, %0" : "=v"(l));
  return l & 63;
}
__device__ __forceinline__ void ld_cnt(unsigned& d, const unsigned* p) {
  asm volatile("global_load_dword %0, %1, %2 sc1" : "=v"(d) : "v"(0), "s"(p) : "memory");
}
struct Poll { unsigned a, b, c, d; };
__device__ __forceinline__ void poll4(Poll& u, const unsigned* p, unsigned target, int skip) {
  int n = 1 << 15, t;
#define P4_LD(R) "global_load_dword " R ", %6, %7 sc1\n\t"
#define P4_CK(R) "s_waitcnt vmcnt(3)\n\tv_readfirstlane_b32 %5, " R "\n\ts_cmp_ge_u32 %5, %8\n\ts_cbranch_scc1 2f\n\t" P4_LD(R)
  asm volatile("s_cmp_lg_u32 %9, 0\n\ts_cbranch_scc1 2f\n\t"
               P4_LD("%0") "s_sleep 3\n\t" P4_LD("%1") "s_sleep 3\n\t" P4_LD("%2") "s_sleep 3\n\t" P4_LD("%3")
               "1:\n\t" P4_CK("%0") P4_CK("%1") P4_CK("%2") P4_CK("%3")
               "s_sub_u32 %4, %4, 1\n\ts_cbranch_scc0 1b\n\t"
               "2:"
               : "+&v"(u.a), "+&v"(u.b), "+&v"(u.c), "+&v"(u.d), "+&s"(n), "=&s"(t)
               : "v"(0), "s"(p), "s"(target), "s"(__builtin_amdgcn_readfirstlane(skip)) : "memory", "scc");
}
__device__ __forceinline__ void ld_cnt_m(unsigned& d, const unsigned* p, int mask) {
  asm volatile("s_mov_b32 exec_lo, %3\n\ts_mov_b32 exec_hi, 0\n\tglobal_load_dword %0, %1, %2 sc1\n\ts_mov_b64 exec, -1"
               : "+&v"(d) : "v"(0), "s"(p), "s"(__builtin_amdgcn_readfirstlane(mask)) : "memory");
}
__device__ __forceinline__ void keep4(Poll& u) { asm volatile("" : "+v"(u.a), "+v"(u.b), "+v"(u.c), "+v"(u.d)::"memory"); }
__device__ __forceinline__ void wait_cnt5(unsigned& u) { asm volatile("s_waitcnt vmcnt(5)" : "+v"(u)::"memory"); }
__device__ __forceinline__ void pin5(f32x16& a, f32x16& b, f32x16& c, f32x16& d, f32x16& e, unsigned& u) {
  asm volatile("" : "+v"(a), "+v"(b), "+v"(c), "+v"(d), "+v"(e), "+v"(u));
}
constexpr int G2_STAGE = 36864;
constexpr int G2_NST = 4;
template <class MID>
__device__ __forceinline__ void gemm2_body(char LAS* lds, const bf16_t* ctx, const bf16_t* __restrict__ wo, const bf16_t* U,
                                           const bf16_t* __restrict__ w2, float* __restrict__ part, unsigned* __restrict__ cnt,
                                           const bf16_t* __restrict__ x, const bf16_t* __restrict__ b2, bf16_t* __restrict__ out,
                                           const int w, const int l, unsigned* sig, MID mid) {
  const int b = blockIdx.x, xcd = b & 7, c = b >> 3;
  const int ks = xcd >> 1;
  const int s0 = (xcd & 1) * 1024 + (c >> 2) * 128, m0 = (c & 3) * 160;
  const int ws = w & 3, kh = w >> 2, r32 = l & 31, hi = l >> 5;
  const int blk = (l >> 4) & 1, q = (l & 15) >> 2, p = l & 3;
  int pOffA[2], pOffB[2], rOffA[3], rOffB[3];
#pragma unroll
  for (int i = 0; i < 2; ++i) {
    const int j = w * 2 + i;
    const int row = 8 * j + (l >> 3), slot = l & 7;
    pOffA[i] = (row * CTX_LD + (slot ^ ((row >> 1) & 7)) * 8) * 2;
    const int krow = 4 * j + (l >> 4), s16 = l & 15;
    pOffB[i] = (krow * S_LEN + ((s16 >> 2) ^ (krow & 3)) * 32 + (s16 & 3) * 8) * 2;
  }
#pragma unroll
  for (int i = 0; i < 3; ++i) {
    const int j = w + 8 * i;
    const int cb = j >> 2, kr = (j & 3) * 16 + (l >> 2);
    rOffA[i] = (kr * DM + cb * 32 + (l & 3) * 8) * 2;
    const int row = 8 * j + (l >> 3), slot = l & 7;
    rOffB[i] = (row * FF + (slot ^ ((row >> 1) & 7)) * 8) * 2;
  }
  const int f0 = ks * 7, nB = ks == 3 ? 6 : 7, total = 16 + nB;
  const bf16_t* PA = ctx + (size_t)s0 * CTX_LD + ks * 1024;
  const bf16_t* RA = wo + (size_t)ks * 1024 * DM + m0;
  const bf16_t* PB = w2 + (size_t)f0 * 64 * S_LEN + s0;
  const bf16_t* RB = U + (size_t)m0 * FF + f0 * 64;
  int pslot = 0, issued = 0;
  auto issue = [&]() {
    const unsigned bb = (unsigned)(uintptr_t)(lds + pslot * G2_STAGE);
    if (issued < 16) {
      const bf16_t* P = PA + issued * 64;
      const bf16_t* R = RA + (size_t)issued * 64 * DM;
      glds16(P, pOffA[0], bb + (w * 2) * 1024);
      glds16(P, pOffA[1], bb + (w * 2 + 1) * 1024);
      glds16(R, rOffA[0], bb + 16384 + w * 1024);
      glds16(R, rOffA[1], bb + 16384 + (w + 8) * 1024);
      if (w < 4) glds16(R, rOffA[2], bb + 16384 + (w + 16) * 1024);
    } else {
      const bf16_t* P = PB + (size_t)(issued - 16) * 64 * S_LEN;
      const bf16_t* R = RB + (issued - 16) * 64;
      glds16(P, pOffB[0], bb + (w * 2) * 1024);
      glds16(P, pOffB[1], bb + (w * 2 + 1) * 1024);
      glds16(R, rOffB[0], bb + 16384 + w * 1024);
      glds16(R, rOffB[1], bb + 16384 + (w + 8) * 1024);
      if (w < 4) glds16(R, rOffB[2], bb + 16384 + (w + 16) * 1024);
    }
    if (++pslot == G2_NST) pslot = 0;
    ++issued;
  };
  const int prow = 32 * ws + r32, psw = (prow >> 1) & 7;
  const int pA0 = prow * 128 + (((4 * kh + hi) ^ psw) << 4), pA1 = prow * 128 + (((4 * kh + 2 + hi) ^ psw) << 4);
  const int rA = 16384 + (32 * kh + 8 * hi + q) * 64 + blk * 32 + swapmid(p) * 8;
  const int pB = (32 * kh + 8 * hi + q) * 256 + ((ws ^ q) * 64) + blk * 32 + p * 8;
  const int rrow = rperm(r32), rsw = (rrow >> 1) & 7;
  const int rB0 = 16384 + rrow * 128 + (((4 * kh + hi) ^ rsw) << 4), rB1 = 16384 + rrow * 128 + (((4 * kh + 2 + hi) ^ rsw) << 4);
  unsigned ucnt = 0;
  {
    auto issueB = [&](int st, int slot) {
      const unsigned bb = (unsigned)(uintptr_t)lds + slot * G2_STAGE;
      const bf16_t* P = PB + (size_t)st * 64 * S_LEN;
      const bf16_t* R = RB + st * 64;
      glds16(P, pOffB[0], bb + (w * 2) * 1024);
      glds16(P, pOffB[1], bb + (w * 2 + 1) * 1024);
      glds16s(R, rOffB[0], bb + 16384 + w * 1024);
      glds16s(R, rOffB[1], bb + 16384 + (w + 8) * 1024);
      if (w < 4) glds16s(R, rOffB[2], bb + 16384 + (w + 16) * 1024);
    };
    issueB(0, 2);
    issueB(1, 3);
    SCHED0();
    mid();
    SCHED0();
    WAIT_LGKM0();
    BARRIER();
    TS(1, 2);
    TS(2, 0);
    issueB(2, 0);
  }
  f32x16 acc[5] = {};
  {
    bf16x8 pf[2] = {}, rf[2][5] = {};
    const bf16_t* Pn = FUSE3 == 2 ? PB + (size_t)(G2_NST - 1) * 64 * S_LEN : PA + (G2_NST - 1) * 64;
    const bf16_t* Rn = FUSE3 == 2 ? RB + (G2_NST - 1) * 64 : RA + (size_t)(G2_NST - 1) * 64 * DM;
    int buf = FUSE3 == 2 ? 2 : 0;
    unsigned long long smask = 0;
    Poll pu = {0u, 0u, 0u, 0u};
    int lmask = 0;
    const unsigned* cc = cnt + (64 + (s0 >> 7) * 4 + ks) * 32;
    auto body = [&](auto G_, auto ISS_, auto RD_, auto NW_) {
      constexpr int G = G_, ISS = ISS_, RD = RD_, NW = NW_, NP = G == 0 ? 5 : 4;
      asm volatile("s_waitcnt vmcnt(%0) lgkmcnt(0)" ::"n"(NP * NW) : "memory");
      BARRIER();
      if (ISS == 2 && G == 0) {
        atomic_inc_masked(sig, smask);
        if (CNT1) ld_cnt_m(ucnt, cc, lmask);
        else ld_cnt(ucnt, cc);
      }
      const char LAS* bp = lds + buf * G2_STAGE;
      const unsigned bb = (unsigned)(uintptr_t)(lds + ((buf + G2_NST - 1) & (G2_NST - 1)) * G2_STAGE);
      auto piece = [&](auto I_) {
        constexpr int I = I_;
        if (ISS != 0 && I < NP) {
          if (FUSE3 && ISS == 1 && I < 2) glds16s(Pn, pOffA[I], bb + (w * 2 + I) * 1024);
          else if (FUSE3 && ISS == 2 && I >= 2) glds16s(Rn, rOffB[I - 2], bb + 16384 + (w + 8 * (I - 2)) * 1024);
          else if (I < 2) glds16(Pn, ISS == 1 ? pOffA[I] : pOffB[I], bb + (w * 2 + I) * 1024);
          else glds16(Rn, ISS == 1 ? rOffA[I - 2] : rOffB[I - 2], bb + 16384 + (w + 8 * (I - 2)) * 1024);
        }
      };
      auto reads = [&]() {
        if (RD == 0) {
          pf[0] = *(const LAS bf16x8*)(bp + pA0);
#pragma unroll
          for (int j = 0; j < 5; ++j) rf[0][j] = cat8(tr_read(bp + rA + j * 4096), tr_read(bp + rA + j * 4096 + 256));
          pf[1] = *(const LAS bf16x8*)(bp + pA1);
#pragma unroll
          for (int j = 0; j < 5; ++j) rf[1][j] = cat8(tr_read(bp + rA + j * 4096 + 1024), tr_read(bp + rA + j * 4096 + 1024 + 256));
        } else {
          pf[0] = cat8(tr_read(bp + pB), tr_read(bp + pB + 1024));
#pragma unroll
          for (int j = 0; j < 5; ++j) rf[0][j] = *(const LAS bf16x8*)(bp + rB0 + j * 4096);
          pf[1] = cat8(tr_read(bp + pB + 4096), tr_read(bp + pB + 4096 + 1024));
#pragma unroll
          for (int j = 0; j < 5; ++j) rf[1][j] = *(const LAS bf16x8*)(bp + rB1 + j * 4096);
        }
      };
      auto mm = [&](auto A_, auto B_) {
#pragma unroll
        for (int i = A_; i < B_; ++i) acc[i % 5] = mf<X16_G2>(rf[i / 5][i % 5], pf[i / 5], acc[i % 5], i / 5);
      };
      if (G == 0) {
        reads();
        __builtin_amdgcn_sched_barrier(0);
        piece(IC<0>{}); piece(IC<1>{}); piece(IC<2>{}); piece(IC<3>{}); piece(IC<4>{});
        __builtin_amdgcn_sched_barrier(0);
        mm(IC<0>{}, IC<10>{});
        __builtin_amdgcn_sched_barrier(0);
      } else {
        mm(IC<0>{}, IC<1>{});
        __builtin_amdgcn_sched_barrier(0);
        piece(IC<0>{});
        __builtin_amdgcn_sched_barrier(0);
        mm(IC<1>{}, IC<3>{});
        __builtin_amdgcn_sched_barrier(0);
        piece(IC<1>{});
        __builtin_amdgcn_sched_barrier(0);
        mm(IC<3>{}, IC<5>{});
        __builtin_amdgcn_sched_barrier(0);
        piece(IC<2>{});
        __builtin_amdgcn_sched_barrier(0);
        mm(IC<5>{}, IC<7>{});
        __builtin_amdgcn_sched_barrier(0);
        piece(IC<3>{});
        __builtin_amdgcn_sched_barrier(0);
        mm(IC<7>{}, IC<10>{});
        __builtin_amdgcn_sched_barrier(0);
        reads();
        __builtin_amdgcn_sched_barrier(0);
      }
      if (ISS == 1) { Pn += 64; Rn += 64 * DM; }
      if (ISS == 2) { Pn += 64 * S_LEN; Rn += 64; }
      buf = (buf + 1) & (G2_NST - 1);
    };
    auto run = [&](auto G_) {
#pragma unroll 1
      for (int step = 0; step < nB - (G2_NST - 1); ++step) {
        smask = step == 1 && w == 0 ? (SSIG ? 1ull : 3ull) : 0ull;
        lmask = step == nB - G2_NST && w == 0 ? 1 : 0;
        body(G_, IC<2>{}, IC<1>{}, IC<2>{});
      }
      if (G_ == 0) {
        pin5(acc[0], acc[1], acc[2], acc[3], acc[4], ucnt);
        wait_cnt5(ucnt);
        if (POLL4 & 1) poll4(pu, cc, 8u, w != 0 || ucnt >= 8u);
        else if (w == 0) {
          if (__builtin_amdgcn_readfirstlane(ucnt) < 8u) {
            for (int spin = 0; spin < (1 << 17); ++spin) {
              const unsigned v = __builtin_amdgcn_readfirstlane(__hip_atomic_load(cc, __ATOMIC_RELAXED, __HIP_MEMORY_SCOPE_AGENT));
              if (v >= 8u) break;
              __builtin_amdgcn_s_sleep(PSLEEP);
            }
            asm volatile("" ::: "memory");
          }
        }
      }
      Pn = PA; Rn = RA;
#pragma unroll 1
      for (int step = 0; step < G2_NST - 1; ++step) body(G_, IC<1>{}, IC<1>{}, IC<2>{});
      if ((POLL4 & 1) && G_ == 0) keep4(pu);
#pragma unroll 1
      for (int step = 0; step < 16 - (G2_NST - 1); ++step) body(G_, IC<1>{}, IC<0>{}, IC<2>{});
      body(G_, IC<0>{}, IC<0>{}, IC<2>{});
      body(G_, IC<0>{}, IC<0>{}, IC<1>{});
      body(G_, IC<0>{}, IC<0>{}, IC<0>{});
      if (G_ == 1) {
        asm volatile("s_waitcnt lgkmcnt(0)" ::: "memory");
#pragma unroll
        for (int i = 0; i < 10; ++i) acc[i % 5] = mf<X16_G2>(rf[i / 5][i % 5], pf[i / 5], acc[i % 5], i / 5);
      }
    };
    if (kh == 0) run(IC<0>{});
    else run(IC<1>{});
  }
  TS(2, 1);
  BARRIER();
  {
    float LAS* dst = (float LAS*)(lds + w * 12288);
    if (kh == 0) {
#pragma unroll
      for (int j = 3; j < 5; ++j)
#pragma unroll
        for (int g = 0; g < 4; ++g)
          *(f32x4 LAS*)(dst + (((j - 3) * 4 + g) * 64 + l) * 4) = (f32x4){acc[j][4 * g], acc[j][4 * g + 1], acc[j][4 * g + 2], acc[j][4 * g + 3]};
    } else {
#pragma unroll
      for (int j = 0; j < 3; ++j)
#pragma unroll
        for (int g = 0; g < 4; ++g)
          *(f32x4 LAS*)(dst + ((j * 4 + g) * 64 + l) * 4) = (f32x4){acc[j][4 * g], acc[j][4 * g + 1], acc[j][4 * g + 2], acc[j][4 * g + 3]};
    }
    asm volatile("s_waitcnt lgkmcnt(0)" ::: "memory");
    BARRIER();
    const float LAS* src = (const float LAS*)(lds + (w ^ 4) * 12288);
    const int tile = (xcd & 1) * 32 + c;
    const __amdgpu_buffer_rsrc_t rs = __builtin_amdgcn_make_buffer_rsrc((void*)part, (short)0, 64 * 4 * 81920, 0x00020000);
    unsigned* ctr = cnt + tile * 32;
    auto fin = [&](auto NJ_, auto J0_) {
      constexpr int NJ = NJ_, J0 = J0_, SL = NJ * 4096;
      f32x4 r[NJ][4];
#pragma unroll
      for (int j = 0; j < NJ; ++j)
#pragma unroll
        for (int g = 0; g < 4; ++g) {
          const f32x4 o = *(const f32x4 LAS*)(src + ((j * 4 + g) * 64 + l) * 4);
          r[j][g] = (f32x4){acc[J0 + j][4 * g] + o[0], acc[J0 + j][4 * g + 1] + o[1], acc[J0 + j][4 * g + 2] + o[2], acc[J0 + j][4 * g + 3] + o[3]};
        }
      const int sb = (tile * 4 + ws) * 81920 + (J0 ? 49152 : 0);
      const size_t orow = (size_t)(s0 + 32 * ws + r32) * DM + m0 + 32 * J0 + 8 * hi;
      u32x4 xv[NJ][2];
      float bb = 0.f;
      TS(2, 4);
      if (ws != ks) {
#pragma unroll
        for (int j = 0; j < NJ; ++j)
#pragma unroll
          for (int g = 0; g < 4; ++g)
            __builtin_amdgcn_raw_buffer_store_b128(__builtin_bit_cast(u32x4, r[j][g]), rs, l * 16 + (j * 4 + g) * 1024, sb + ks * SL, 16);
        asm volatile("s_waitcnt vmcnt(0)" ::: "memory");
        if (XWAVE && l == 0) __hip_atomic_fetch_add(ctr + w, 1u, __ATOMIC_RELAXED, __HIP_MEMORY_SCOPE_AGENT);
      } else {
#pragma unroll
        for (int j = 0; j < NJ; ++j)
#pragma unroll
          for (int gp = 0; gp < 2; ++gp) xv[j][gp] = *(const u32x4*)(x + orow + 32 * j + 16 * gp);
        bb = bf2f(b2[s0 + 32 * ws + r32]);
      }
      if (!XWAVE) BARRIER();
      TS(2, 5);
      if (!XWAVE && w == 0 && l == 0) __hip_atomic_fetch_add(ctr, 1u, __ATOMIC_RELAXED, __HIP_MEMORY_SCOPE_AGENT);
      if (ws == ks) {
        const unsigned* cw = XWAVE ? ctr + w : ctr;
        constexpr unsigned need = XWAVE ? 3u : 4u;
        Poll qu = {0u, 0u, 0u, 0u};
        if (POLL4 & 2) poll4(qu, cw, need, 0);
        else
          for (int spin = 0; spin < (1 << 17); ++spin) {
            const unsigned v = __builtin_amdgcn_readfirstlane(__hip_atomic_load(cw, __ATOMIC_RELAXED, __HIP_MEMORY_SCOPE_AGENT));
            if (v >= need) break;
            __builtin_amdgcn_s_sleep(XSLEEP);
          }
        __builtin_amdgcn_fence(__ATOMIC_ACQUIRE, "wavefront");
        asm volatile("" ::: "memory");
        TS(2, 3);
        u32x4 pv[3][NJ][4];
#pragma unroll
        for (int t = 1; t < 4; ++t)
#pragma unroll
          for (int j = 0; j < NJ; ++j)
#pragma unroll
            for (int g = 0; g < 4; ++g)
              pv[t - 1][j][g] = __builtin_amdgcn_raw_buffer_load_b128(rs, l * 16 + (j * 4 + g) * 1024, sb + (ks ^ t) * SL, 16);
#pragma unroll
        for (int j = 0; j < NJ; ++j)
#pragma unroll
          for (int gp = 0; gp < 2; ++gp) {
            float o[8];
#pragma unroll
            for (int e = 0; e < 8; ++e) {
              const int g = 2 * gp + (e >> 2), q4 = e & 3;
              const float a = (r[j][g][q4] + __uint_as_float(pv[0][j][g][q4])) + (__uint_as_float(pv[1][j][g][q4]) + __uint_as_float(pv[2][j][g][q4]));
              const unsigned xw = xv[j][gp][e >> 1];
              o[e] = __uint_as_float((e & 1) ? (xw & 0xffff0000u) : (xw << 16)) + a + bb;
            }
            *(u32x4*)(out + orow + 32 * j + 16 * gp) = (u32x4){pack2(o[0], o[1]), pack2(o[2], o[3]), pack2(o[4], o[5]), pack2(o[6], o[7])};
          }
        if (POLL4 & 2) keep4(qu);
      }
    };
    if (kh == 0) fin(IC<3>{}, IC<0>{});
    else fin(IC<2>{}, IC<3>{});
  }
  TS(2, 2);
}

constexpr int AT_HALF = 16384;
#define LTAB 0x89ABCDEFu
#define STAB 0x21076543u
extern "C" __global__ __launch_bounds__(512, 2) void attn_kernel(const bf16_t* __restrict__ Q, const bf16_t* __restrict__ K,
                                                      const bf16_t* __restrict__ V, bf16_t* ctx,
                                                      const bf16_t* __restrict__ wo, const bf16_t* __restrict__ w2,
                                                      const bf16_t* __restrict__ wq, const bf16_t* __restrict__ hbuf,
                                                      const bf16_t* __restrict__ w1, const bf16_t* __restrict__ b1,
                                                      bf16_t* Uo, float* __restrict__ part, unsigned* __restrict__ cnt,
                                                      const bf16_t* __restrict__ x, const bf16_t* __restrict__ b2,
                                                      bf16_t* __restrict__ out) {
  __shared__ __attribute__((aligned(16))) char smem[8 * AT_HALF + 32768];
  char LAS* lds = (char LAS*)smem;
  const int tid = threadIdx.x, l = tid & 63, w = __builtin_amdgcn_readfirstlane(tid >> 6);
  const int rg = w & 3, g = w >> 2, r32 = l & 31, hi = l >> 5;
  const int xcd = blockIdx.x & 7, jj = blockIdx.x >> 3;
  const int head = xcd * 4 + (jj >> 3);
  const int LB = (LTAB >> (4 * (jj & 7))) & 15, pair = (STAB >> (4 * (jj & 7))) & 15;
  const int mbase = (xcd * 90) >> 3, mcnt = (((xcd + 1) * 90) >> 3) - mbase;
  const int mi = (jj >> 3) * 3 + (jj & 7) - 5;
  const bool hasMix = (jj & 7) >= 5 && mi < mcnt;
  const int nshort = 2 * pair + 2, NL = LB + pair + 1;
  const bf16_t* Kh = K + (size_t)head * S_LEN * HD;
  const bf16_t* Vh = V + (size_t)head * S_LEN * HD;
  const bf16_t* Qh = Q + (size_t)head * S_LEN * HD;
  char LAS* gl = lds + g * 4 * AT_HALF;
  int kOff[4], vOff[4];
#pragma unroll
  for (int i = 0; i < 4; ++i) {
    const int j = rg * 4 + i, row = 4 * j + (l >> 4), s16 = l & 15;
    kOff[i] = (row * HD + ((s16 ^ (row & 15)) * 8)) * 2 - 1024 * i;
    vOff[i] = (row * HD + ((((s16 >> 2) ^ (row & 3)) * 4 + (s16 & 3)) * 8)) * 2 - 1024 * i;
  }
  const unsigned glb = (unsigned)(uintptr_t)gl;
  auto htile = [&](int i) { return (g == 0 || i < nshort) ? i : i + LB - pair; };
  auto stageK = [&](int i) {
    const bf16_t* kp = Kh + (size_t)htile(i > NL ? NL : i) * 64 * HD;
    const unsigned bb = glb + (i & 1) * AT_HALF + rg * 4096;
    glds16x4(kp, kOff[0], kOff[1], kOff[2], kOff[3], bb);
  };
  auto stageV = [&](int i) {
    const bf16_t* vp = Vh + (size_t)htile(i) * 64 * HD;
    const unsigned bb = glb + (2 + (i & 1)) * AT_HALF + rg * 4096;
    glds16x4(vp, vOff[0], vOff[1], vOff[2], vOff[3], bb);
  };
  auto stageKp = [&](int i, auto J) {
    const bf16_t* kp = Kh + (size_t)htile(i > NL ? NL : i) * 64 * HD;
    glds16p<J>(kp, kOff[J], glb + (i & 1) * AT_HALF + rg * 4096);
  };
  auto stageVp = [&](int i, auto J) {
    const bf16_t* vp = Vh + (size_t)htile(i) * 64 * HD;
    glds16p<J>(vp, vOff[J], glb + (2 + (i & 1)) * AT_HALF + rg * 4096);
  };
  const int ksw = r32 & 15;
  const int blk = (l >> 4) & 1, q4 = (l & 15) >> 2, p4 = l & 3;
  int vAddr[4];
#pragma unroll
  for (int db = 0; db < 4; ++db)
    vAddr[db] = 2 * AT_HALF + (4 * hi + q4) * 256 + ((db ^ q4) * 64) + blk * 32 + swapmid(p4) * 8;
  constexpr float CS = 0.08838834764831845f * 1.4426950408889634f;

  TS(1, 0);
  int qb = g == 0 ? LB : pair;
  bf16x8 qf[8];
  u32x2 pfd = {0u, 0u};
  unsigned keepw = 0;
  char LAS* qsave = lds + 8 * AT_HALF + rg * 8192 + l * 16;
  {
    const unsigned sW = (unsigned)(uintptr_t)lds + w * 2048;
    int wOff[2];
#pragma unroll
    for (int i = 0; i < 2; ++i) {
      const int krow = 4 * (2 * w + i) + (l >> 4), s16 = l & 15;
      wOff[i] = (krow * HDN + ((s16 >> 2) ^ (krow & 3)) * 32 + (s16 & 3) * 8) * 2 - 1024 * i;
    }
    const char* wsrc = (const char*)(wq + head * HD);
    const char* xsrc = (const char*)(Q + (size_t)(qb * 4 + rg) * 40 * 512);
    const int xOff = l * 16;
    int wAddr[4];
#pragma unroll
    for (int db = 0; db < 4; ++db) wAddr[db] = (8 * hi + q4) * 256 + ((db ^ q4) * 64) + blk * 32 + swapmid(p4) * 8;
    bf16x8 xf[40], wf[8];
    Mix M;
    MixAcc MA;
    f32x16 qa[4];
    auto slot = [](int i) { return i < 8 ? (i & 1) * 4 * AT_HALF + ((i >> 1) == 1 ? 2 : (i >> 1) == 2 ? 1 : (i >> 1)) * AT_HALF : i * AT_HALF; };
    auto stW = [&](auto I) {
      constexpr int i = I;
      setm0<slot(i)>(sW); SCHED0(); asm volatile("s_nop 0");
      ::gl<0>(wsrc + (size_t)i * 64 * HDN * 2, wOff[0]); ::gl<1024>(wsrc + (size_t)i * 64 * HDN * 2, wOff[1]);
    };
    auto ldX = [&](auto I) {
      constexpr int i = I;
      const char* b4 = xsrc + i * 4096;
      gld16<0>(xf[4 * i], b4, xOff); gld16<1024>(xf[4 * i + 1], b4, xOff); gld16<2048>(xf[4 * i + 2], b4, xOff); gld16<3072>(xf[4 * i + 3], b4, xOff);
      return;
#pragma unroll
      for (int s = 0; s < 4; ++s) {
        const char* b = xsrc + (size_t)(2 * i + (s >> 1)) * S_LEN * 64;
        if (s & 1) gld16<32>(xf[4 * i + s], b, xOff);
        else gld16<0>(xf[4 * i + s], b, xOff);
      }
    };
    auto rdW = [&](int n) {
      const char LAS* a = lds + slot(n >> 4) + wAddr[n & 3] + ((n >> 2) & 3) * 4096;
      wf[n & 7] = cat8(tr_read(a), tr_read(a + 1024));
    };
    auto gate = [&](auto I) {
      constexpr int i = I;
      constexpr int vm = (i + QLEAD <= 10 ? 6 * (QLEAD - 1) : 6 * (9 - i) + 4) + (W1PF == 5 ? (i < QLEAD ? i : QLEAD) : 0);
      SCHED0();
      if (i > 0) pin4(qa[0], qa[1], qa[2], qa[3]);
      wait_vm4<vm>(xf[4 * i], xf[4 * i + 1], xf[4 * i + 2], xf[4 * i + 3]);
      BARRIER();
      if (i + QLEAD <= 9) { stW(IC<(i + QLEAD <= 9 ? i + QLEAD : 0)>{}); ldX(IC<(i + QLEAD <= 9 ? i + QLEAD : 0)>{}); }
      if (i + QLEAD == 10) {
        stageK(0);
      }
      if (W1PF == 5 && i < 9) touch3(keepw, (((int)blockIdx.x * 8 + w) * 3 + l) * 128, (const char*)w1 + i * 6144 * 128);
      if (FUSE3 == 2 && !MIXPOS && i == QLEAD + 1) atomic_inc_masked(cnt + 128 * 32, hasMix && w == 0 ? 1ull : 0ull);
      SCHED0();
    };
    if (hasMix) {
      M = mix_init(hbuf, w1, mbase + mi, w, l);
      mix_stage<0, 3>(M, sW); mix_stage<1, 4>(M, sW); mix_stage<2, 0>(M, sW); mix_stage<3, 1>(M, sW);
      mix_main<0>(lds, M, b1, MA, w, l, nullptr);
    }
    SCHED0();
    stW(IC<0>{}); ldX(IC<0>{}); stW(IC<1>{}); ldX(IC<1>{});
    if (QLEAD > 2) { stW(IC<2>{}); ldX(IC<2>{}); }
    if (QLEAD > 3) { stW(IC<3>{}); ldX(IC<3>{}); }
    SCHED0();
    if (hasMix) {
      mix_fin(lds + 7 * AT_HALF, M, MA, Uo, w, l);
      TS(1, 4);
    }
    constexpr int LA = 6;
    gate(IC<0>{});
#pragma unroll
    for (int n = 0; n < LA; ++n) rdW(n);
    auto stg = [&](auto I) {
      constexpr int i = I;
#pragma unroll
      for (int m = 0; m < 16; ++m) {
        const int n = 16 * i + m;
        if (m == 16 - LA && i < 9) gate(IC<(i < 9 ? i + 1 : 0)>{});
        if (n + LA < 160) rdW(n + LA);
        qa[n & 3] = __builtin_amdgcn_mfma_f32_32x32x16_bf16(wf[n & 7], xf[n >> 2], n < 4 ? (f32x16){} : qa[n & 3], 0, 0, 0);
        SCHED0();
      }
    };
    stg(IC<0>{}); stg(IC<1>{}); stg(IC<2>{}); stg(IC<3>{}); stg(IC<4>{}); stg(IC<5>{}); stg(IC<6>{}); stg(IC<7>{}); stg(IC<8>{}); stg(IC<9>{});
    pin4(qa[0], qa[1], qa[2], qa[3]);
#pragma unroll
    for (int db = 0; db < 4; ++db) {
      const f32x16 a = qa[db];
      u32x4 v0 = {pack2(a[0], a[1]), pack2(a[2], a[3]), pack2(a[4], a[5]), pack2(a[6], a[7])};
      u32x4 v1 = {pack2(a[8], a[9]), pack2(a[10], a[11]), pack2(a[12], a[13]), pack2(a[14], a[15])};
      qf[2 * db] = *(bf16x8*)&v0;
      qf[2 * db + 1] = *(bf16x8*)&v1;
    }
    SCHED0();
    BARRIER();
    stageK(1);
    stageV(0);
    SCHED0();
  }
  if (g == 0) {
#pragma unroll
    for (int d0 = 0; d0 < 8; ++d0) *(bf16x8 LAS*)(qsave + d0 * 1024) = qf[d0];
  }
  float m_run = -1e30f, l_run = 0.f, alpha = 1.f;
  f32x16 o[4] = {};
  f32x16 s0, s1;
  bf16x8 pf[4];
  constexpr int AT_VM = 5;
#define AT_W 6
  bf16x8 fr[32];
  const int kAddr0 = r32 * 256 + ((hi ^ ksw) << 4);
  auto rdK = [&](int k, int slot) {
    fr[k] = *(const LAS bf16x8*)(gl + slot * AT_HALF + (kAddr0 ^ ((k >> 1) << 5)) + (k & 1) * 8192);
  };
  auto rdV = [&](int k, int slot) {
    const char LAS* a = gl + slot * AT_HALF + vAddr[(k - 16) >> 2] + (k & 3) * 4096;
    fr[k] = cat8(tr_read(a), tr_read(a + 2048));
  };
  auto mm = [&](int k) {
    if (k < 16) {
      if (k & 1) s1 = mf<X16_AT>(fr[k], qf[k >> 1], k < 2 ? (f32x16){} : s1, (k >> 1) & 1);
      else s0 = mf<X16_AT>(fr[k], qf[k >> 1], k < 2 ? (f32x16){} : s0, (k >> 1) & 1);
    } else {
      o[(k - 16) >> 2] = mf<X16_AT>(fr[k], pf[k & 3], o[(k - 16) >> 2], k & 1);
    }
  };
  auto preK = [&](int slot) {
#pragma unroll
    for (int k = 0; k < AT_W; ++k) rdK(k, slot);
  };
  auto preV = [&](int slot) {
#pragma unroll
    for (int k = 16; k < 16 + AT_W; ++k) rdV(k, slot);
  };
  auto xhalf_max = [&](float v) {
    auto rr = __builtin_amdgcn_permlane32_swap(__float_as_uint(v), __float_as_uint(v), false, false);
    return fmaxf(__uint_as_float(rr[0]), __uint_as_float(rr[1]));
  };
  auto xhalf_sum = [&](float v) {
    auto rr = __builtin_amdgcn_permlane32_swap(__float_as_uint(v), __float_as_uint(v), false, false);
    return __uint_as_float(rr[0]) + __uint_as_float(rr[1]);
  };
#define AT_PREP 1
  const char* nx_k = nullptr; const char* nx_t = nullptr; unsigned nx_kd = 0; int nx_dh = 0, nx_sw = 0;
#define PREP(i_)                                                                                                        \
  do {                                                                                                                  \
    const int ii = (i_);                                                                                                \
    const char* a_k = (const char*)(Kh + (size_t)htile(ii + 2 > NL ? NL : ii + 2) * 64 * HD);                           \
    const unsigned a_kd = glb + (ii & 1) * AT_HALF + rg * 4096;                                                         \
    int idx = (ii * 256 + (int)blockIdx.x) * 8 + w;                                                                     \
    const char* a_t;                                                                                                    \
    if (FUSE3 == 2) {                                                                                                   \
      if (idx >= 24064) idx -= 24064;                                                                                   \
      if (idx >= 12032) idx -= 12032;                                                                                   \
      a_t = idx < 6912 ? (const char*)w2 + (size_t)idx * 1024 : (const char*)wo + (size_t)(idx - 6912) * 1024;          \
    } else if (PLB == 2) {                                                                                              \
      if (idx >= 18944) idx -= 18944;                                                                                   \
      if (idx >= 18944) idx -= 18944;                                                                                   \
      a_t = idx < 6912 ? (const char*)w1 + (size_t)idx * 1024                                                           \
          : idx < 12032 ? (const char*)wo + (size_t)(idx - 6912) * 1024 : (const char*)w2 + (size_t)(idx - 12032) * 1024; \
    } else {                                                                                                            \
      if (idx >= 24064) idx -= 24064;                                                                                   \
      if (idx >= 24064) idx -= 24064;                                                                                   \
      a_t = idx < 10240 ? (const char*)wo + (size_t)idx * 512 : (const char*)w2 + (size_t)(idx - 10240) * 512;          \
    }                                                                                                                   \
    const int a_dh = htile(ii) - 2 * ((g == 0 || ii >= nshort) ? LB : pair);                                            \
    const int a_sw = g == 1 ? ii - nshort : 1;                    \
    asm volatile("" ::"s"(a_k), "s"(a_t), "s"(a_kd), "s"(a_dh), "s"(a_sw));                                             \
    nx_k = a_k; nx_t = a_t; nx_kd = a_kd; nx_dh = a_dh; nx_sw = a_sw;                                                   \
  } while (0)
  auto softmax = [&](int i, const bool more) {
    if (AT_PREP ? nx_sw == 0 : (g == 1 && i == nshort)) {
      const float inv = 1.0f / xhalf_sum(l_run);
      bf16_t* orow = ctx + (size_t)(pair * 128 + 32 * rg + r32) * CTX_LD + head * HD + 8 * hi;
#pragma unroll
      for (int db = 0; db < 4; ++db) {
        const f32x16 a = o[db];
        u32x4 v0 = {pack2(a[0] * inv, a[1] * inv), pack2(a[2] * inv, a[3] * inv), pack2(a[4] * inv, a[5] * inv), pack2(a[6] * inv, a[7] * inv)};
        u32x4 v1 = {pack2(a[8] * inv, a[9] * inv), pack2(a[10] * inv, a[11] * inv), pack2(a[12] * inv, a[13] * inv), pack2(a[14] * inv, a[15] * inv)};
        if (LINEST & 2) {
          char LAS* slab = lds + 8 * AT_HALF + rg * 8192 + r32 * 256;
          *(u32x4 LAS*)(slab + (((4 * db + hi) ^ (r32 & 7)) << 4)) = v0;
          *(u32x4 LAS*)(slab + (((4 * db + 2 + hi) ^ (r32 & 7)) << 4)) = v1;
        } else {
          st16h(orow + 32 * db, v0);
          st16h(orow + 32 * db + 16, v1);
        }
        o[db] = (f32x16){};
      }
      if (LINEST & 2) {
        const int ll = lane_id();
        const char LAS* slab = lds + 8 * AT_HALF + rg * 8192 + (ll >> 4) * 256;
        bf16_t* ob = ctx + (size_t)(pair * 128 + 32 * rg + (ll >> 4)) * CTX_LD + head * HD + (ll & 15) * 8;
#pragma unroll
        for (int h = 0; h < 2; ++h) {
          u32x4 t[4];
#pragma unroll
          for (int i = 0; i < 4; ++i) t[i] = *(const u32x4 LAS*)(slab + (4 * h + i) * 1024 + (((ll & 15) ^ ((4 * i + (ll >> 4)) & 7)) << 4));
#pragma unroll
          for (int i = 0; i < 4; ++i) st16h(ob + (size_t)(4 * h + i) * 4 * CTX_LD, t[i]);
        }
      }
      if (FUSE3 == 2 && MIXPOS) atomic_inc_masked(cnt + 128 * 32, hasMix && w == 4 ? 1ull : 0ull);
      m_run = -1e30f;
      l_run = 0.f;
      qb = LB;
    }
    if (AT_PREP ? nx_sw == -1 : (g == 1 && i == nshort - 1)) {
#pragma unroll
      for (int d0 = 0; d0 < 8; ++d0) qf[d0] = *(const bf16x8 LAS*)(qsave + d0 * 1024);
    }
    SCHED0();
#define AT_SPREAD 0
#define AT_PK 0
    auto touch = [&]() {
      if (more) {
        int idx = (i * 256 + (int)blockIdx.x) * 8 + w;
        if (idx >= 24064) idx -= 24064;
        const char* pb = idx < 10240 ? (const char*)wo + (size_t)idx * 512 : (const char*)w2 + (size_t)(idx - 10240) * 512;
        if (AT_PREP) pb = nx_t;
        asm volatile("global_load_dwordx2 %0, %1, %2" : "+v"(pfd) : "v"(l * (PLB == 2 ? 16 : 8)), "s"(pb) : "memory");
      }
    };
    if (AT_SPREAD) {
      if (more) stageKp(i + 2, IC<0>{});
    } else {
      if (AT_PREP) { if (more) glds16x4(nx_k, kOff[0], kOff[1], kOff[2], kOff[3], nx_kd); }
      else if (more) stageK(i + 2);
      touch();
    }
    SCHED0(); PH(i, 5); SCHED0();
    const int dh = AT_PREP ? nx_dh : htile(i) - 2 * qb;
    if (dh >= 0) {
      const int qi = 32 * rg + r32 - 64 * dh;
#pragma unroll
      for (int r = 0; r < 16; ++r) {
        const int key = (r & 3) + 8 * (r >> 2) + 4 * hi;
        if (key > qi) s0[r] = -__builtin_inff();
        if (key + 32 > qi) s1[r] = -__builtin_inff();
      }
    }
    float pm = s0[0];
#pragma unroll
    for (int r = 1; r < 16; ++r) pm = fmaxf(pm, s0[r]);
#pragma unroll
    for (int r = 0; r < 16; ++r) pm = fmaxf(pm, s1[r]);
    pm = xhalf_max(pm);
    const float mn = fmaxf(m_run, pm);
    alpha = __builtin_amdgcn_exp2f((m_run - mn) * CS);
    m_run = mn;
    const float mc = -mn * CS;
    SCHED0(); PH(i, 6); SCHED0();
    float ps = 0.f;
    f32x2 ps2 = {0.f, 0.f};
    const f32x2 cs2 = {CS, CS}, mc2 = {mc, mc};
    auto exps = [&](f32x16& s, int r0) {
      if (AT_PK) {
#pragma unroll
        for (int r = r0; r < r0 + 8; r += 2) {
          f32x2 t = {s[r], s[r + 1]};
          t = __builtin_elementwise_fma(t, cs2, mc2);
          t[0] = __builtin_amdgcn_exp2f(t[0]);
          t[1] = __builtin_amdgcn_exp2f(t[1]);
          ps2 += t;
          s[r] = t[0];
          s[r + 1] = t[1];
        }
      } else {
#pragma unroll
        for (int r = r0; r < r0 + 8; ++r) { s[r] = __builtin_amdgcn_exp2f(fmaf(s[r], CS, mc)); ps += s[r]; }
      }
    };
    if (AT_SPREAD) { if (more) stageKp(i + 2, IC<1>{}); SCHED0(); }
    exps(s0, 0);
    if (AT_SPREAD) { SCHED0(); if (more) stageKp(i + 2, IC<2>{}); SCHED0(); }
    exps(s0, 8);
    if (AT_SPREAD) { SCHED0(); if (more) stageKp(i + 2, IC<3>{}); SCHED0(); }
    exps(s1, 0);
    if (AT_SPREAD) { SCHED0(); touch(); SCHED0(); }
    exps(s1, 8);
    if (AT_PK) ps = ps2[0] + ps2[1];
    l_run = l_run * alpha + ps;
    SCHED0(); PH(i, 7); SCHED0();
    u32x4 a0 = {pack2(s0[0], s0[1]), pack2(s0[2], s0[3]), pack2(s0[4], s0[5]), pack2(s0[6], s0[7])};
    u32x4 a1 = {pack2(s0[8], s0[9]), pack2(s0[10], s0[11]), pack2(s0[12], s0[13]), pack2(s0[14], s0[15])};
    u32x4 a2 = {pack2(s1[0], s1[1]), pack2(s1[2], s1[3]), pack2(s1[4], s1[5]), pack2(s1[6], s1[7])};
    u32x4 a3 = {pack2(s1[8], s1[9]), pack2(s1[10], s1[11]), pack2(s1[12], s1[13]), pack2(s1[14], s1[15])};
    pf[0] = *(bf16x8*)&a0; pf[1] = *(bf16x8*)&a1; pf[2] = *(bf16x8*)&a2; pf[3] = *(bf16x8*)&a3;
    asm volatile("" : "+v"(pf[0]), "+v"(pf[1]), "+v"(pf[2]), "+v"(pf[3]), "+v"(l_run), "+v"(alpha));
  };
#define AT_DK0 16
#define AT_DKS 3
  wait_vm<(QF ? 8 : 0)>();
  asm volatile("" : "+v"(keepw));
  WAIT_LGKM0();
  BARRIER();
  TS(1, 3);
  if (g == 1) BARRIER();
  SCHED0();
  preK(0);
#pragma unroll
  for (int k = 0; k < 16; ++k) {
    if (k + AT_W < 16) rdK(k + AT_W, 0);
    mm(k);
    SCHED0();
  }
  if (AT_PREP) PREP(0);
  SCHED0();
  if (QF) wait_vm<4>();
  BARRIER();
  for (int i = 0; i < NL; ++i) {
    SCHED0();
    PH(i, 0);
    softmax(i, true);
    SCHED0();
    const int ks = (i + 1) & 1, vs = i & 1;
    const f32x2 alpha2 = {alpha, alpha};
    preK(ks);
    SCHED0();
    wait_vm<AT_VM>();
    PH(i, 1);
    BARRIER();
    SCHED0();
    PH(i, 2);
    __builtin_amdgcn_s_setprio(1);
#pragma unroll
    for (int k = 0; k < 32; ++k) {
      if (k + AT_W < 16) rdK(k + AT_W, ks);
      else if (k + AT_W < 32) rdV(k + AT_W, vs);
      mm(k);
      if (k < 16) {
        if (AT_PK) {
#pragma unroll
          for (int e = 0; e < 4; e += 2) {
            f32x2 t = {o[k >> 2][4 * (k & 3) + e], o[k >> 2][4 * (k & 3) + e + 1]};
            asm volatile("v_pk_mul_f32 %0, %1, %0" : "+v"(t) : "v"(alpha2));
            o[k >> 2][4 * (k & 3) + e] = t[0];
            o[k >> 2][4 * (k & 3) + e + 1] = t[1];
          }
        } else {
#pragma unroll
          for (int e = 0; e < 4; ++e) {
            float t = o[k >> 2][4 * (k & 3) + e];
            asm volatile("v_mul_f32 %0, %1, %0" : "+v"(t) : "v"(alpha));
            o[k >> 2][4 * (k & 3) + e] = t;
          }
        }
      }
      if (AT_SPREAD) {
        if (k == AT_DK0) stageVp(i + 1, IC<0>{});
        if (k == AT_DK0 + AT_DKS) stageVp(i + 1, IC<1>{});
        if (k == AT_DK0 + 2 * AT_DKS) stageVp(i + 1, IC<2>{});
        if (k == AT_DK0 + 3 * AT_DKS) stageVp(i + 1, IC<3>{});
      } else {
        if (k == AT_DK0) stageV(i + 1);
      }
      SCHED0();
    }
    __builtin_amdgcn_s_setprio(0);
    if (AT_PREP) PREP(i + 1);
    SCHED0();
    PH(i, 3);
    wait_vm<AT_VM>();
    PH(i, 4);
    BARRIER();
  }
  SCHED0();
  unsigned ucnt = 0;
  if (FUSE3 == 2 && SSIG) atomic_inc_masked(cnt + (64 + pair * 4 + (head >> 3)) * 32, w == 4 ? 1ull : 0ull);
  if (FUSE3 == 2) ld_cnt(ucnt, cnt + 128 * 32);
  softmax(NL, false);
#pragma unroll
  for (int db = 0; db < 4; ++db)
#pragma unroll
    for (int r = 0; r < 16; ++r) o[db][r] *= alpha;
  SCHED0();
  WAIT_VM0();
  asm volatile("" : "+v"(ucnt) : "v"(pfd));
  BARRIER();
  SCHED0();
  preV(NL & 1);
#pragma unroll
  for (int k = 16; k < 32; ++k) {
    if (k + AT_W < 32) rdV(k + AT_W, NL & 1);
    mm(k);
    SCHED0();
  }
  BARRIER();
  if (g == 0) BARRIER();
  const unsigned sWm = (unsigned)(uintptr_t)lds + w * 2048;
  const int lm = FUSE3 ? lane_id() : l;
  unsigned* sig = cnt + (64 + ((lm & 1) ? pair : LB) * 4 + (head >> 3)) * 32;
  TS(1, 1);
  auto combine = [&]() {
    l_run = xhalf_sum(l_run);
    const int kh = g;
    float LAS* ml = (float LAS*)(lds + 4 * AT_HALF);
    float LAS* dst = (float LAS*)(lds + w * 8192);
#pragma unroll
    for (int dd = 0; dd < 2; ++dd) {
      f32x16 a;
#pragma unroll
      for (int r = 0; r < 16; ++r) a[r] = kh == 0 ? o[2 + dd][r] : o[dd][r];
#pragma unroll
      for (int gg = 0; gg < 4; ++gg)
        *(f32x4 LAS*)(dst + ((dd * 4 + gg) * 64 + l) * 4) = (f32x4){a[4 * gg], a[4 * gg + 1], a[4 * gg + 2], a[4 * gg + 3]};
    }
    ml[w * 128 + l] = m_run;
    ml[w * 128 + 64 + l] = l_run;
    asm volatile("s_waitcnt lgkmcnt(0)" ::: "memory");
    BARRIER();
    const int wo = w ^ 4;
    const float m2 = ml[wo * 128 + l], l2 = ml[wo * 128 + 64 + l];
    const float mt = fmaxf(m_run, m2);
    const float a1 = __builtin_amdgcn_exp2f((m_run - mt) * CS), a2 = __builtin_amdgcn_exp2f((m2 - mt) * CS);
    const float inv = 1.0f / (l_run * a1 + l2 * a2);
    const float f1 = a1 * inv, f2 = a2 * inv;
    const float LAS* src = (const float LAS*)(lds + wo * 8192);
    bf16_t* orow = ctx + (size_t)(LB * 128 + 32 * rg + r32) * CTX_LD + head * HD + 8 * hi;
    u32x4 pk[4];
#pragma unroll
    for (int dd = 0; dd < 2; ++dd) {
      f32x16 a;
#pragma unroll
      for (int r = 0; r < 16; ++r) a[r] = kh == 0 ? o[dd][r] : o[2 + dd][r];
      float r[16];
#pragma unroll
      for (int gg = 0; gg < 4; ++gg) {
        const f32x4 b = *(const f32x4 LAS*)(src + ((dd * 4 + gg) * 64 + l) * 4);
#pragma unroll
        for (int e = 0; e < 4; ++e) r[4 * gg + e] = a[4 * gg + e] * f1 + b[e] * f2;
      }
      u32x4 v0 = {pack2(r[0], r[1]), pack2(r[2], r[3]), pack2(r[4], r[5]), pack2(r[6], r[7])};
      u32x4 v1 = {pack2(r[8], r[9]), pack2(r[10], r[11]), pack2(r[12], r[13]), pack2(r[14], r[15])};
      const int dcol = 32 * (2 * kh + dd);
      if (LINEST & 1) {
        pk[2 * dd] = v0;
        pk[2 * dd + 1] = v1;
      } else {
        st16h(orow + dcol, v0);
        st16h(orow + dcol + 16, v1);
      }
    }
    if (LINEST & 1) {
      char LAS* slab = lds + wo * 8192;
      asm volatile("" ::: "memory");
#pragma unroll
      for (int i = 0; i < 4; ++i) *(u32x4 LAS*)(slab + r32 * 128 + (((2 * i + hi) ^ (r32 & 7)) << 4)) = pk[i];
      bf16_t* ob = ctx + (size_t)(LB * 128 + 32 * rg + (l >> 3)) * CTX_LD + head * HD + 64 * kh + (l & 7) * 8;
#pragma unroll
      for (int i = 0; i < 4; ++i) pk[i] = *(const u32x4 LAS*)(slab + i * 1024 + (l >> 3) * 128 + (((l & 7) ^ (l >> 3)) << 4));
#pragma unroll
      for (int i = 0; i < 4; ++i) st16h(ob + (size_t)i * 8 * CTX_LD, pk[i]);
    }
  };
  pin4(o[0], o[1], o[2], o[3]);
  if (__builtin_amdgcn_readfirstlane(ucnt) < 90u) {
    for (int spin = 0; spin < (1 << 17); ++spin) {
      const unsigned v = __builtin_amdgcn_readfirstlane(__hip_atomic_load(cnt + 128 * 32, __ATOMIC_RELAXED, __HIP_MEMORY_SCOPE_AGENT));
      if (v >= 90u) break;
      __builtin_amdgcn_s_sleep(1);
    }
    asm volatile("" ::: "memory");
  }
  gemm2_body(lds, ctx, wo, Uo, w2, part, cnt, x, b2, out, w, lm, sig, combine);
}

extern "C" __global__ __launch_bounds__(512, 2) void gemm2_kernel(const bf16_t* __restrict__ ctx, const bf16_t* __restrict__ wo,
                                                       const bf16_t* __restrict__ U, const bf16_t* __restrict__ w2,
                                                       float* __restrict__ part, unsigned* __restrict__ cnt,
                                                       const bf16_t* __restrict__ x, const bf16_t* __restrict__ b2,
                                                       bf16_t* __restrict__ out) {
}

extern "C" __global__ __launch_bounds__(256) void reduce_kernel(const float* __restrict__ part, const bf16_t* __restrict__ x,
                                                     const bf16_t* __restrict__ b2, bf16_t* __restrict__ out) {
  TS(4, 0);
  const int i = blockIdx.x * 256 + threadIdx.x;
  const int e = i * 4;
  const int s = e / DM;
  f32x4 a = *(const f32x4*)(part + e);
#pragma unroll
  for (int sp = 1; sp < 4; ++sp) a += *(const f32x4*)(part + (size_t)sp * S_LEN * DM + e);
  const u32x2 xv = *(const u32x2*)(x + e);
  const float bb = bf2f(b2[s]);
  float r0 = __uint_as_float(xv[0] << 16) + a[0] + bb;
  float r1 = __uint_as_float(xv[0] & 0xffff0000u) + a[1] + bb;
  float r2 = __uint_as_float(xv[1] << 16) + a[2] + bb;
  float r3 = __uint_as_float(xv[1] & 0xffff0000u) + a[3] + bb;
  u32x2 o = {pack2(r0, r1), pack2(r2, r3)};
  *(u32x2*)(out + e) = o;
  TS(4, 2);
}

extern "C" void kernel_launch(void* const* d_in, const int* in_sizes, int n_in, void* d_out, int out_size, void* d_ws,
                              size_t ws_size, hipStream_t stream) {
  const bf16_t* x = (const bf16_t*)d_in[0];
  const bf16_t* ckv = (const bf16_t*)d_in[1];
  const bf16_t* wq = (const bf16_t*)d_in[2];
  const bf16_t* wuk = (const bf16_t*)d_in[3];
  const bf16_t* wuv = (const bf16_t*)d_in[4];
  const bf16_t* wo = (const bf16_t*)d_in[5];
  const bf16_t* w1 = (const bf16_t*)d_in[7];
  const bf16_t* b1 = (const bf16_t*)d_in[8];
  const bf16_t* w2 = (const bf16_t*)d_in[9];
  const bf16_t* b2 = (const bf16_t*)d_in[10];
  char* ws = (char*)d_ws;
  bf16_t* xn = (bf16_t*)(ws + WS_XN);
  bf16_t* h = (bf16_t*)(ws + WS_H);
  bf16_t* Q = (bf16_t*)(ws + WS_Q);
  bf16_t* K = (bf16_t*)(ws + WS_K);
  bf16_t* V = (bf16_t*)(ws + WS_V);
  bf16_t* U = (bf16_t*)(ws + WS_U);
  bf16_t* ctx = (bf16_t*)(ws + WS_CTX);
  float* part = (float*)(ws + WS_PART);
  bf16_t* ct = (bf16_t*)(ws + WS_UP);
  unsigned* cnt = (unsigned*)(ws + WS_CNT);
  gemm1_kernel<<<256, 512, 0, stream>>>(nullptr, nullptr, ckv, wq, wuk, wuv, w1, b1, wo, w2, xn, K, V, h, x, cnt);
  attn_kernel<<<256, 512, 0, stream>>>(QF ? xn : Q, K, V, ctx, wo, w2, wq, h, w1, b1, U, part, cnt, x, b2, (bf16_t*)d_out);
  if (!FUSE3) gemm2_kernel<<<256, 512, 0, stream>>>(ctx, wo, U, w2, part, cnt, x, b2, (bf16_t*)d_out);
}
```
